```python
import torch

def _softmax(x, axis=-1):
    return torch.softmax(x, dim=axis)

def _gelu(x):
    return torch.nn.functional.gelu(x, approximate='tanh')

def _silu(x):
    return torch.nn.functional.silu(x)

def _sigmoid(x):
    return torch.sigmoid(x)

def _layernorm(x, axis=-1, eps=1e-05):
    m = x.mean(dim=axis, keepdim=True)
    v = x.var(dim=axis, keepdim=True, unbiased=False)
    return (x - m) / torch.sqrt(v + eps)

def _scatter_add(tgt, idx, src, axis=0):
    return tgt.clone().index_add_(axis, idx, src)

def _segment_sum(src, seg, n):
    out = torch.zeros((n,) + src.shape[1:], dtype=src.dtype)
    return out.index_add_(0, seg, src)

def _pad(x, pw):
    flat = tuple((v for pair in reversed(pw) for v in pair))
    return torch.nn.functional.pad(x, flat)
kernel_name = 'gated_mix__ssm_conv_s4d__mixer_gmlp__prompt'

def setup_inputs():

    _w = lambda a: (lambda t: t.to(torch.bfloat16) if t.is_floating_point() else t)(torch.as_tensor(a))

    def _A_setup():
        Bsz, L, D, N = (1, 2048, 576, 128)
        return {'u': _w((lambda *s: torch.randn(*s, dtype=torch.bfloat16) * 0.05)(Bsz, L, D)), 'log_A_real': _w((lambda *s: torch.randn(*s, dtype=torch.bfloat16) * 3.2)(D, N) * 0.5), 'A_imag': _w((lambda *s: torch.randn(*s, dtype=torch.bfloat16) * 0.05)(D, N) * 3.14), 'C_real': _w((lambda *s: torch.randn(*s, dtype=torch.bfloat16) * 3.2)(D, N) / N ** 0.5), 'C_imag': _w((lambda *s: torch.randn(*s, dtype=torch.bfloat16) * 3.2)(D, N) / N ** 0.5), 'log_dt': _w((lambda *s: torch.randn(*s, dtype=torch.bfloat16) * 0.05)(D) * 0.5 - 4.0), 'D_skip': _w((lambda *s: torch.randn(*s, dtype=torch.bfloat16) * 0.4)(D))}
    _A_in = _A_setup()

    def _B_setup():
        B, S, D, F = (1, 2048, 576, 4608)
        Fh = F // 2
        return {'x': _w((lambda *s: torch.randn(*s, dtype=torch.bfloat16) * 0.05)(B, S, D)), 'w_in': _w((lambda *s: torch.randn(*s, dtype=torch.bfloat16) * 0.2)(D, 2 * Fh)), 'w_sp': _w(0.01 * (lambda *s: torch.randn(*s, dtype=torch.bfloat16) * 0.2)(S, S)), 'b_sp': _w(1.0 + 0.01 * (lambda *s: torch.randn(*s, dtype=torch.bfloat16) * 0.05)(S)), 'w_out': _w((lambda *s: torch.randn(*s, dtype=torch.bfloat16) * 0.05)(Fh, D))}
    _B_in = _B_setup()
    return {'A_u': _A_in['u'], 'A_log_A_real': _A_in['log_A_real'], 'A_A_imag': _A_in['A_imag'], 'A_C_real': _A_in['C_real'], 'A_C_imag': _A_in['C_imag'], 'A_log_dt': _A_in['log_dt'], 'A_D_skip': _A_in['D_skip'], 'B_x': _B_in['x'], 'B_w_in': _B_in['w_in'], 'B_w_sp': _B_in['w_sp'], 'B_b_sp': _B_in['b_sp'], 'B_w_out': _B_in['w_out'], 'gate_w': _w((lambda *s: torch.randn(*s, dtype=torch.bfloat16) * 0.05)(576) * 16.0)}

def reference(A_u, A_log_A_real, A_A_imag, A_C_real, A_C_imag, A_log_dt, A_D_skip, B_x, B_w_in, B_w_sp, B_b_sp, B_w_out, gate_w):

    def _A_ref(u, log_A_real, A_imag, C_real, C_imag, log_dt, D_skip):
        L = u.shape[1]
        dt = torch.exp(log_dt)
        A = -torch.exp(log_A_real) + 1j * A_imag
        C = (C_real + 1j * C_imag) * dt[:, None]
        pos = (lambda x, dt: x.to(dt))(torch.arange(L), log_dt.dtype)
        P = torch.exp((A * dt[:, None])[:, :, None] * pos[None, None, :])
        K = 2.0 * torch.einsum('dn,dnl->dl', C, P).real
        L2 = 2 * L
        u_t = (lambda x, *d: x.permute(*d))(u, 0, 2, 1)
        Kf = (lambda x, *a, **k: torch.fft.rfft(x.float(), *a, **k))(K, L2)
        Uf = (lambda x, *a, **k: torch.fft.rfft(x.float(), *a, **k))(u_t, L2)
        y = (lambda x, *a, **k: torch.fft.irfft(x, *a, **k).to(torch.bfloat16))(Uf * Kf[None, :, :], L2)[:, :, :L]
        y = (lambda x, *d: x.permute(*d))(y, 0, 2, 1)
        return y + u * D_skip[None, None, :]

    def _B_ref(x, w_in, w_sp, b_sp, w_out):
        B, S, D = x.shape
        F = w_in.shape[-1]
        Fh = F // 2
        z = _gelu(torch.einsum('bsd,df->bsf', x, w_in))
        z1 = z[..., :Fh].clone()
        z2 = _layernorm(z[..., Fh:], axis=-1)
        g = torch.einsum('ts,bsf->btf', w_sp, z2) + b_sp[None, :, None]
        return torch.einsum('bsf,fd->bsd', z1 * g, w_out)
    x = A_u
    g = _sigmoid((x * gate_w).sum(-1, keepdims=True))
    a = _A_ref(x, A_log_A_real, A_A_imag, A_C_real, A_C_imag, A_log_dt, A_D_skip)
    b = _B_ref(x, B_w_in, B_w_sp, B_b_sp, B_w_out)
    return g * a + (1 - g) * b
```

```cpp
#include <hip/hip_runtime.h>
#include <hip/hip_bf16.h>
#include <stdint.h>
typedef unsigned short u16;
__device__ __forceinline__ float bf2f(u16 v){ return __uint_as_float(((unsigned)v)<<16); }
__device__ __forceinline__ u16 f2bf(float f){ unsigned u=__float_as_uint(f); u += 0x7FFFu + ((u>>16)&1u); return (u16)(u>>16); }
__device__ __forceinline__ float rbf(float f){ return bf2f(f2bf(f)); }
constexpr int L=2048, D=576, NN=128, F=4608, FH=2304;

__global__ void n_gemm(const u16* A, int lda, const u16* B, int ldb, u16* C, int ldc, int M, int N, int K){
  __shared__ float As[32][33], Bs[32][33];
  int tx=threadIdx.x, ty=threadIdx.y; int row=blockIdx.y*32+ty, col=blockIdx.x*32+tx;
  float acc=0.f;
  for(int k0=0;k0<K;k0+=32){
    As[ty][tx]=bf2f(A[(size_t)row*lda+k0+tx]);
    Bs[ty][tx]=bf2f(B[(size_t)(k0+ty)*ldb+col]);
    __syncthreads();
    for(int k=0;k<32;k++) acc+=As[ty][k]*Bs[k][tx];
    __syncthreads();
  }
  C[(size_t)row*ldc+col]=f2bf(acc);
}
__global__ void n_gelu(u16* z, int n){
  int i=blockIdx.x*blockDim.x+threadIdx.x; if(i>=n) return;
  float x=bf2f(z[i]);
  float t=tanhf(0.7978845608028654f*(x+0.044715f*x*x*x));
  z[i]=f2bf(0.5f*x*(1.f+t));
}
__global__ void n_ln(const u16* z, u16* z2n){
  int s=blockIdx.x; const u16* r=z+(size_t)s*F+FH; __shared__ float red[256];
  float a=0; for(int i=threadIdx.x;i<FH;i+=256) a+=bf2f(r[i]);
  red[threadIdx.x]=a; __syncthreads();
  for(int o=128;o>0;o>>=1){ if(threadIdx.x<o) red[threadIdx.x]+=red[threadIdx.x+o]; __syncthreads(); }
  float mean=red[0]/FH; __syncthreads();
  float q=0; for(int i=threadIdx.x;i<FH;i+=256){ float d=bf2f(r[i])-mean; q+=d*d; }
  red[threadIdx.x]=q; __syncthreads();
  for(int o=128;o>0;o>>=1){ if(threadIdx.x<o) red[threadIdx.x]+=red[threadIdx.x+o]; __syncthreads(); }
  float var=red[0]/FH;
  float m=rbf(mean), v=rbf(var); float sd=rbf(sqrtf(rbf(v+1e-5f)));
  for(int i=threadIdx.x;i<FH;i+=256) z2n[(size_t)s*FH+i]=f2bf(rbf(bf2f(r[i])-m)/sd);
}
__global__ void n_h(const u16* z, const u16* g, const u16* bsp, u16* h){
  int i=blockIdx.x*blockDim.x+threadIdx.x; if(i>=L*FH) return; int t=i/FH, f=i%FH;
  float gg=rbf(bf2f(g[i])+bf2f(bsp[t]));
  h[i]=f2bf(bf2f(z[(size_t)t*F+f])*gg);
}
__global__ void n_kgen(const u16* logA, const u16* Aim, const u16* Cre, const u16* Cim, const u16* logdt, float* K){
  int d=blockIdx.y; int l=blockIdx.x*blockDim.x+threadIdx.x;
  float dt=rbf(expf(bf2f(logdt[d])));
  float pos=rbf((float)l);
  float acc=0.f;
  for(int n=0;n<NN;n++){
    float ar=-rbf(expf(bf2f(logA[d*NN+n])))*dt, ai=bf2f(Aim[d*NN+n])*dt;
    float cr=bf2f(Cre[d*NN+n])*dt, ci=bf2f(Cim[d*NN+n])*dt;
    float e=expf(ar*pos); float s,c; sincosf(ai*pos,&s,&c);
    acc+=cr*(e*c)-ci*(e*s);
  }
  K[(size_t)d*L+l]=2.f*acc;
}
__global__ void n_conv(const float* K, const u16* u, float* y){
  int d=blockIdx.y; int l=blockIdx.x*blockDim.x+threadIdx.x;
  float acc=0.f;
  for(int j=0;j<=l;j++) acc+=K[(size_t)d*L+j]*bf2f(u[(size_t)(l-j)*D+d]);
  y[(size_t)l*D+d]=acc;
}
__global__ void n_final(const u16* x, const u16* gw, const u16* Dsk, const float* y, const u16* b, u16* out){
  int l=blockIdx.x; __shared__ float red[64];
  float a=0; for(int i=threadIdx.x;i<D;i+=64) a+=rbf(bf2f(x[(size_t)l*D+i])*bf2f(gw[i]));
  red[threadIdx.x]=a; __syncthreads();
  for(int o=32;o>0;o>>=1){ if(threadIdx.x<o) red[threadIdx.x]+=red[threadIdx.x+o]; __syncthreads(); }
  float s=rbf(red[0]); float g=rbf(1.f/(1.f+expf(-s)));
  for(int i=threadIdx.x;i<D;i+=64){
    float av=rbf(rbf(y[(size_t)l*D+i])+rbf(bf2f(x[(size_t)l*D+i])*bf2f(Dsk[i])));
    float o=rbf(g*av)+rbf(rbf(1.f-g)*bf2f(b[(size_t)l*D+i]));
    out[(size_t)l*D+i]=f2bf(o);
  }
}
#define NAIVE_NAME kernel_launch
extern "C" void NAIVE_NAME(void* const* d_in, const int* in_sizes, int n_in, void* d_out, int out_size, void* d_ws, size_t ws_size, hipStream_t stream){
  const u16* u=(const u16*)d_in[0]; const u16* logA=(const u16*)d_in[1]; const u16* Aim=(const u16*)d_in[2];
  const u16* Cre=(const u16*)d_in[3]; const u16* Cim=(const u16*)d_in[4]; const u16* logdt=(const u16*)d_in[5]; const u16* Dsk=(const u16*)d_in[6];
  const u16* win=(const u16*)d_in[8]; const u16* wsp=(const u16*)d_in[9]; const u16* bsp=(const u16*)d_in[10]; const u16* wout=(const u16*)d_in[11]; const u16* gw=(const u16*)d_in[12];
  char* ws=(char*)d_ws;
  u16* z=(u16*)ws; ws+=(size_t)L*F*2;
  u16* z2n=(u16*)ws; ws+=(size_t)L*FH*2;
  u16* g=(u16*)ws; ws+=(size_t)L*FH*2;
  u16* h=(u16*)ws; ws+=(size_t)L*FH*2;
  u16* b=(u16*)ws; ws+=(size_t)L*D*2;
  float* K=(float*)ws; ws+=(size_t)D*L*4;
  float* y=(float*)ws; ws+=(size_t)D*L*4;
  n_gemm<<<dim3(F/32,L/32),dim3(32,32),0,stream>>>(u,D,win,F,z,F,L,F,D);
  n_gelu<<<(L*F+255)/256,256,0,stream>>>(z,L*F);
  n_ln<<<L,256,0,stream>>>(z,z2n);
  n_gemm<<<dim3(FH/32,L/32),dim3(32,32),0,stream>>>(wsp,L,z2n,FH,g,FH,L,FH,L);
  n_h<<<(L*FH+255)/256,256,0,stream>>>(z,g,bsp,h);
  n_gemm<<<dim3(D/32,L/32),dim3(32,32),0,stream>>>(h,FH,wout,D,b,D,L,D,FH);
  n_kgen<<<dim3(L/256,D),256,0,stream>>>(logA,Aim,Cre,Cim,logdt,K);
  n_conv<<<dim3(L/256,D),256,0,stream>>>(K,u,y);
  n_final<<<L,64,0,stream>>>(u,gw,Dsk,y,b,(u16*)d_out);
}
```

```cpp
#include <hip/hip_runtime.h>
#include <stdint.h>

#define LAS __attribute__((address_space(3)))
typedef unsigned short u16;
typedef unsigned long long u64;
typedef short bf16x8 __attribute__((ext_vector_type(8)));
typedef short s16x4 __attribute__((ext_vector_type(4)));
typedef _Float16 f16x8 __attribute__((ext_vector_type(8)));
typedef _Float16 f16x2 __attribute__((ext_vector_type(2)));
typedef __bf16 bf16x2 __attribute__((ext_vector_type(2)));
typedef float f32x4 __attribute__((ext_vector_type(4)));
typedef float f32x2 __attribute__((ext_vector_type(2)));
typedef unsigned u32x4 __attribute__((ext_vector_type(4)));
typedef unsigned u32x2 __attribute__((ext_vector_type(2)));
typedef __amdgpu_buffer_rsrc_t rsrc_t;

namespace gm {
constexpr int SEQ = 2048, DM = 576, FH = 2304;
constexpr unsigned WS_E = 0, WS_FS = 4096, WS_FZ = WS_FS + 65536, WS_FH = WS_FZ + 65536, WS_FA = WS_FH + 131072;
constexpr unsigned WS_ST = 0x60000, WS_GATE = 0xA0000, WS_DBG = 0xB0000, WS_Z2F = 0x100000, WS_HF = 0xA00000, WS_WOF = 0x1300000, WS_UT = 0x1600000, WS_AT = 0x1900000, WS_END = 0x1C00000;
constexpr int STG = 34816, BOFF = 16384, Z0 = 3 * STG, ZST = 296, T0 = Z0 + 128 * ZST;
constexpr int KT_SZ = 4352, UT_SZ = 4608, Y_SZ = 10240;
constexpr int L_KT = Z0, L_UT = Z0 + 3 * KT_SZ, L_Y2 = L_UT + 3 * UT_SZ, L_PAR = L_Y2;
constexpr int L_Y0 = T0, L_KD = T0, L_MISC = T0 + 2 * Y_SZ;
constexpr int L_SP = T0, L_SR = T0 + 2048;
constexpr int L_AT = Z0, L_R1 = Z0 + 10240, L_R2 = L_R1 + 20480, L_R0 = 2 * STG, L_R3 = 0;
constexpr int LDS_BYTES = 163840;
static_assert(L_Y2 + Y_SZ <= T0 && L_MISC + 320 <= LDS_BYTES && L_R2 + 20480 <= L_MISC, "lds map");
#define GM_LAG 4

__device__ __forceinline__ float bf2f(unsigned v) { return __uint_as_float(v << 16); }
__device__ __forceinline__ float bflo(unsigned w) { return __uint_as_float(w << 16); }
__device__ __forceinline__ float bfhi(unsigned w) { return __uint_as_float(w & 0xffff0000u); }
__device__ __forceinline__ unsigned pk_bf16(float a, float b) { f32x2 v = {a, b}; return __builtin_bit_cast(unsigned, __builtin_convertvector(v, bf16x2)); }
__device__ __forceinline__ unsigned pk_f16(float a, float b) { f32x2 v = {a, b}; return __builtin_bit_cast(unsigned, __builtin_convertvector(v, f16x2)); }
__device__ __forceinline__ float rbf(float a) { return bflo(pk_bf16(a, a)); }
__device__ __forceinline__ f32x2 gelu2(f32x2 x) {
    const f32x2 w = (x * x) * (-0.10294324f) + (-2.30220819f);
    const f32x2 a = w * x;
    f32x2 e; e.x = __builtin_amdgcn_exp2f(a.x); e.y = __builtin_amdgcn_exp2f(a.y);
    e = e + 1.0f;
    f32x2 r; r.x = __builtin_amdgcn_rcpf(e.x); r.y = __builtin_amdgcn_rcpf(e.y);
    return x * r;
}
__device__ __forceinline__ f32x4 gelu4(f32x4 v) { const f32x2 a = gelu2((f32x2){v[0], v[1]}), b = gelu2((f32x2){v[2], v[3]}); return (f32x4){a.x, a.y, b.x, b.y}; }
__device__ __forceinline__ float allred_q(float v) {
    unsigned u = __float_as_uint(v);
    auto r = __builtin_amdgcn_permlane16_swap(u, u, false, false);
    v = __uint_as_float(r[0]) + __uint_as_float(r[1]);
    u = __float_as_uint(v);
    auto s = __builtin_amdgcn_permlane32_swap(u, u, false, false);
    return __uint_as_float(s[0]) + __uint_as_float(s[1]);
}
#define ACQ() __builtin_amdgcn_fence(__ATOMIC_ACQUIRE, "wavefront")
#define BAR() __builtin_amdgcn_s_barrier()
#define WAITV(n) asm volatile("s_waitcnt vmcnt(" #n ")" ::: "memory")
#define WAITL0() asm volatile("s_waitcnt lgkmcnt(0)" ::: "memory")
#define CFENCE() asm volatile("" ::: "memory")
#define SGB(mask, n, id) __builtin_amdgcn_sched_group_barrier(mask, n, id)
#define MFMA16(a, b, c) __builtin_amdgcn_mfma_f32_16x16x32_bf16(a, b, c, 0, 0, 0)
__device__ __forceinline__ s16x4 tr_read(const LAS unsigned char* p) { return __builtin_amdgcn_ds_read_tr16_b64_v4i16((LAS s16x4*)p); }
__device__ __forceinline__ bf16x8 cat8(s16x4 a, s16x4 b) { return __builtin_shufflevector(a, b, 0, 1, 2, 3, 4, 5, 6, 7); }
__device__ __forceinline__ int nextbuf(int so) { return so == 2 * STG ? 0 : so + STG; }

#define STAMP(i) do { } while (0)

__device__ __forceinline__ u32x2 flag_ld(rsrc_t ws, unsigned off) { return __builtin_amdgcn_raw_buffer_load_b64(ws, off, 0, 16); }
__device__ __forceinline__ bool flag_all(u32x2 v, u32x2 T, bool active) { return __builtin_amdgcn_ballot_w64(active && (v[0] != T[0] || v[1] != T[1])) == 0ull; }
__device__ __forceinline__ void flag_publish(rsrc_t ws, unsigned off, u32x2 T, int lane) {
    WAITV(0);
    if (lane == 0) __builtin_amdgcn_raw_buffer_store_b64(T, ws, off, 0, 16);
}
__device__ __forceinline__ void flag_wait(rsrc_t ws, unsigned base, int n, u32x2 T, int lane) {
    const unsigned off = base + (unsigned)(lane < n ? lane : 0) * 128u;
    const u64 t0 = __builtin_amdgcn_s_memrealtime();
    for (;;) {
        CFENCE();
        const u32x2 v = flag_ld(ws, off);
        if (flag_all(v, T, true)) break;
        if (__builtin_amdgcn_s_memrealtime() - t0 > 300000ull) break;
        __builtin_amdgcn_s_sleep(1);
    }
    ACQ();
}

#define SB0() __builtin_amdgcn_sched_barrier(0)
template <int NT> struct Frag { bf16x8 a[4]; s16x4 lo[NT], hi[NT]; };
template <int NT, bool TR, int HALF>
__device__ __forceinline__ void frag_read(Frag<NT>& f, int i, const LAS unsigned char* pa, const LAS unsigned char* pb) {
    if (i < 4) f.a[i] = *(const LAS bf16x8*)(pa + i * 2048);
    else if (TR) { const int n = (i - 4) >> 1; if ((i - 4) & 1) f.hi[n] = tr_read(pb + HALF * 9216 + 4608 + n * 32); else f.lo[n] = tr_read(pb + HALF * 9216 + n * 32); }
    else { const int n = i - 4; const bf16x8 v = *(const LAS bf16x8*)(pb + HALF * 9216 + n * 1024);
           f.lo[n] = __builtin_shufflevector(v, v, 0, 1, 2, 3); f.hi[n] = __builtin_shufflevector(v, v, 4, 5, 6, 7); }
}
template <int NT, bool TR, int LHALF, int RPS, bool FIRST, bool UPD>
__device__ __forceinline__ void half_step(f32x4 (&acc)[NT][4], const Frag<NT>& c, Frag<NT>& l, const LAS unsigned char*& pa0, const LAS unsigned char*& pa1, const LAS unsigned char*& pb, int delta) {
    constexpr int NRD = TR ? 4 + 2 * NT : 4 + NT, NM = 4 * NT, LAST = (NRD + RPS - 1) / RPS;
#pragma unroll
    for (int i = 0; i < NM; ++i) {
        const int n = i >> 2, m = i & 3;
        acc[n][m] = MFMA16(cat8(c.lo[n], c.hi[n]), c.a[m], FIRST ? ((f32x4){0.f, 0.f, 0.f, 0.f}) : acc[n][m]);
        if (LHALF >= 0) {
#pragma unroll
            for (int r = 0; r < RPS; ++r) if (i * RPS + r < NRD) frag_read<NT, TR, (LHALF > 0 ? 1 : 0)>(l, i * RPS + r, LHALF > 0 ? pa1 : pa0, pb);
        }
        if (UPD) { if (i == LAST) { pa0 += delta; asm volatile("" : "+v"(pa0)); } if (i == LAST + 1) { pb += delta; asm volatile("" : "+v"(pb)); } if (i == LAST + 2) { pa1 += delta; asm volatile("" : "+v"(pa1)); } }
        SB0();
    }
}
template <int NT, bool TR>
__device__ __forceinline__ void gemm_phase(int nsteps, f32x4 (&acc)[NT][4], int& so, const LAS unsigned char* pa0, const LAS unsigned char* pa1, const LAS unsigned char* pb) {
    constexpr int NRD = TR ? 4 + 2 * NT : 4 + NT;
    Frag<NT> f0, f1;
    pa0 += so; pa1 += so; pb += so;
    asm volatile("" : "+v"(pa0), "+v"(pa1), "+v"(pb));
    __builtin_amdgcn_s_waitcnt(0xC07F);
    BAR();
#pragma unroll
    for (int i = 0; i < NRD; ++i) frag_read<NT, TR, 0>(f0, i, pa0, pb);
    SB0();
    int delta = so == 2 * STG ? -2 * STG : STG; so += delta;
    half_step<NT, TR, 1, 2, true, true>(acc, f0, f1, pa0, pa1, pb, delta);
#pragma nounroll
    for (int s = 1; s < nsteps; ++s) {
        WAITL0();
        BAR();
        delta = so == 2 * STG ? -2 * STG : STG; so += delta;
        half_step<NT, TR, 0, 1, false, false>(acc, f1, f0, pa0, pa1, pb, 0);
        half_step<NT, TR, 1, 2, false, true>(acc, f0, f1, pa0, pa1, pb, delta);
    }
    half_step<NT, TR, -1, 1, false, false>(acc, f1, f0, pa0, pa1, pb, 0);
}

template <int NT>
__device__ __forceinline__ void consumer_front(LAS unsigned char* lds, rsrc_t ws, u32x2 T, const u16* __restrict__ bsp, int pm, int pn, int wm, int wn, int lane, unsigned char* wsp_) {
    const int fr = lane & 15, fq = lane >> 4;
    constexpr int N0 = (NT == 5) ? 0 : 5;
    f32x4 acc[NT][4];
    int so = 0;
    const int sw = (fr >> 1) & 7;
    const int a0 = (wm * 64 + fr) * 128 + ((fq ^ sw) << 4), a1 = (wm * 64 + fr) * 128 + (((4 + fq) ^ sw) << 4);
    const int bw = BOFF + (4 * fq + (fr >> 2)) * 288 + (lane & 3) * 8 + N0 * 32;
    u32x2 z1[NT][4];
#pragma nounroll
    for (int it = 0; it < 2; ++it) {
        gemm_phase<NT, true>(9, acc, so, lds + a0, lds + a1, lds + bw);
        STAMP(1 + 2 * it);
#pragma unroll
        for (int m = 0; m < 4; ++m)
#pragma unroll
            for (int n = 0; n < NT; ++n) { acc[n][m] = gelu4(acc[n][m]); z1[n][m] = (u32x2){pk_bf16(acc[n][m][0], acc[n][m][1]), pk_bf16(acc[n][m][2], acc[n][m][3])}; }
        if (it == 0) {
#pragma unroll
            for (int m = 0; m < 4; ++m) {
                f32x4 s4 = acc[0][m];
#pragma unroll
                for (int n = 1; n < NT; ++n) s4 += acc[n][m];
                const float mean = allred_q((s4[0] + s4[1]) + (s4[2] + s4[3])) * (1.0f / (16 * NT));
                f32x4 q4 = {0.f, 0.f, 0.f, 0.f};
#pragma unroll
                for (int n = 0; n < NT; ++n) { const f32x4 d = acc[n][m] - mean; q4 += d * d; }
                const float m2 = allred_q((q4[0] + q4[1]) + (q4[2] + q4[3]));
                if (fq == 0) *(LAS f32x2*)(lds + L_SP + (wm * 64 + m * 16 + fr) * 16 + wn * 8) = (f32x2){mean, m2};
#pragma unroll
                for (int n = 0; n < NT; ++n) *(LAS u32x2*)(lds + Z0 + (wm * 64 + m * 16 + fr) * ZST + (N0 + n) * 32 + fq * 8) = z1[n][m];
            }
            WAITL0();
            STAMP(2);
        }
    }
    float bs[4];
#pragma unroll
    for (int m = 0; m < 4; ++m) bs[m] = bf2f(bsp[pm * 128 + wm * 64 + m * 16 + fr]);
    STAMP(4);
    const int bz = BOFF + N0 * 1024 + lane * 16;
    gemm_phase<NT, false>(32, acc, so, lds + a0, lds + a1, lds + bz);
    STAMP(5);
    {
        const int qm = 2 * pm + wm;
        const unsigned hb = WS_HF + (unsigned)qm * (18u * 16384u);
#pragma unroll
        for (int m = 0; m < 4; ++m) {
            u32x2 hp[NT];
#pragma unroll
            for (int n = 0; n < NT; ++n) {
                const f32x4 g = acc[n][m] + bs[m];
                hp[n] = (u32x2){pk_bf16(bflo(z1[n][m][0]) * g[0], bfhi(z1[n][m][0]) * g[1]), pk_bf16(bflo(z1[n][m][1]) * g[2], bfhi(z1[n][m][1]) * g[3])};
            }
#pragma unroll
            for (int j = 0; j < 2; ++j)
                __builtin_amdgcn_raw_buffer_store_b128((u32x4){hp[2 * j][0], hp[2 * j][1], hp[2 * j + 1][0], hp[2 * j + 1][1]}, ws,
                                                       hb + (unsigned)pn * 16384u + (unsigned)(m * 4 + wn * 2 + j) * 1024u + lane * 16, 0, 16);
            if (NT == 5) __builtin_amdgcn_raw_buffer_store_b64(hp[NT - 1], ws, hb + (unsigned)(16 + (pn >> 3)) * 16384u + m * 4096u + (unsigned)(pn & 7) * 512u + lane * 8, 0, 16);
        }
        flag_publish(ws, WS_FH + (unsigned)(qm * 32 + pn * 2 + wn) * 128u, T, lane);
    }
    STAMP(6);
}

template <int TLO, int THI>
__device__ __forceinline__ void conv_task(LAS unsigned char* lds, int kt, int ut, int yt, int tl, int lane) {
    constexpr int NTL = THI - TLO + 1, ND = THI + 2;
    const int i = lane & 15, g = lane >> 4;
    const LAS unsigned char* pk = lds + kt + (i & 3) * 1088 + ((i >> 2) + g) * 16;
    const LAS unsigned char* pu = lds + ut + (128 + 16 * tl - 32 * g - 4 * (i >> 2) + 4 * (i & 3)) * 2;
    bf16x8 ub[ND];
#pragma unroll
    for (int d = 0; d < ND; ++d) ub[d] = cat8(tr_read(pu + 256 * d), tr_read(pu + 256 * d - 32));
    f32x4 acc[NTL];
#pragma unroll
    for (int t = 0; t < NTL; ++t) acc[t] = (f32x4){0.f, 0.f, 0.f, 0.f};
#pragma unroll
    for (int s = -1; s <= THI; ++s) {
        const bf16x8 kf = *(const LAS bf16x8*)(pk + 64 * (s + 1));
#pragma unroll
        for (int t = TLO; t <= THI; ++t) if (t >= s) acc[t - TLO] = MFMA16(kf, ub[t - s], acc[t - TLO]);
    }
    LAS float* y = (LAS float*)(lds + yt) + 32 * g + 16 * tl + i;
    LAS float* cy = (LAS float*)(lds + yt + 8192) + (2 * g + tl) * 4 + (i & 3);
#define DPPF(v, ctl) __int_as_float(__builtin_amdgcn_update_dpp(0, __float_as_int(v), ctl, 0xf, 0xf, true))
#pragma unroll
    for (int t = TLO; t <= THI; ++t) {
        const f32x4 a = acc[t - TLO];
        y[128 * t] = (a[0] + DPPF(a[1], 0x111)) + (DPPF(a[2], 0x112) + DPPF(a[3], 0x113));
        const float cv = DPPF(a[1], 0x10f) + (DPPF(a[2], 0x10e) + DPPF(a[3], 0x10d));
        if (i < 4) cy[32 * t] = cv;
    }
#undef DPPF
}
__device__ __forceinline__ int ytab(int c) { return c == 2 ? L_Y2 : L_Y0 + c * Y_SZ; }

__global__ __launch_bounds__(512, 2) void gm_kernel(const u16* __restrict__ x, const u16* __restrict__ logA, const u16* __restrict__ Aim, const u16* __restrict__ Cre,
                                                    const u16* __restrict__ Cim, const u16* __restrict__ logdt, const u16* __restrict__ Dsk, const u16* __restrict__ win,
                                                    const u16* __restrict__ wsp, const u16* __restrict__ bsp, const u16* __restrict__ wout, const u16* __restrict__ gw,
                                                    u16* __restrict__ out, unsigned char* wsp_) {
    extern __shared__ __attribute__((aligned(16))) unsigned char lds_[];
    LAS unsigned char* lds = (LAS unsigned char*)lds_;
    const int wid = __builtin_amdgcn_readfirstlane(threadIdx.x >> 6), lane = threadIdx.x & 63;
    const int bid = blockIdx.x, xg = bid & 7, rr = bid >> 3, bi = rr >> 2, bj = rr & 3;
    const int pm = (xg >> 2) * 8 + bi, pn = (xg & 3) * 4 + bj;
    const int qm = 2 * pm + (pn >> 3), qn = pn & 7;
    const int nch = pm < 4 ? 3 : 2;
    const rsrc_t ws = __builtin_amdgcn_make_buffer_rsrc(wsp_, 0, WS_END, 0x00020000);
    STAMP(0);

    if (wid == 4 || wid == 5) {
        const int p = wid - 4;
        const int phi = (-(bi + bj)) & 3;
        const unsigned rsw = (unsigned)(((lane & 7) ^ ((4 * p + (lane >> 4)) & 7)) * 16);
        const unsigned vx = (unsigned)(lane >> 3) * 1152u + rsw, vs = (unsigned)(lane >> 3) * 4096u + rsw;
        unsigned wv[9];
#pragma unroll
        for (int i = 0; i < 9; ++i) { const int c = (p + 2 * i) * 64 + lane, rho = c / 18, ch = c - rho * 18;
            const int kap = (rho & 32) + 8 * ((rho >> 2) & 3) + 4 * ((rho >> 4) & 1) + (rho & 3); wv[i] = (unsigned)kap * 9216u + (unsigned)ch * 16u; }
        const char* xb = (const char*)x + (size_t)(pm * 128 + p * 8) * 1152;
        const char* sb = (const char*)wsp + (size_t)(pm * 128 + p * 8) * 4096;
        const char* wb = (const char*)win + pn * 288;
        const char* lin = (const char*)wsp_ + p * 1024 + lane * 16;
        int so = 0;
#define GLDS(src, dst, aux) __builtin_amdgcn_global_load_lds((const unsigned*)(src), (LAS unsigned*)(dst), 16, 0, aux)
        auto issueX = [&](int k, int s) {
#pragma unroll
            for (int i = 0; i < 8; ++i) GLDS(xb + (size_t)i * (16 * 1152) + k * 128 + vx, lds + s + (p + 2 * i) * 1024, 0); };
        auto issueS = [&](int k, int s) {
#pragma unroll
            for (int i = 0; i < 8; ++i) GLDS(sb + (size_t)i * (16 * 4096) + k * 128 + vs, lds + s + (p + 2 * i) * 1024, 0); };
        auto issueW = [&](int k, int coloff, int s) {
            const char* b = wb + (size_t)k * (64 * 9216) + coloff;
#pragma unroll
            for (int i = 0; i < 9; ++i) GLDS(b + wv[i], lds + s + BOFF + (p + 2 * i) * 1024, 0); };
        auto issueLA = [&](unsigned off, int s) {
#pragma unroll
            for (int i = 0; i < 8; ++i) GLDS(lin + off + i * 2048, lds + s + (p + 2 * i) * 1024, 16); };
        auto issueLB = [&](unsigned off, int s) {
#pragma unroll
            for (int i = 0; i < 9; ++i) GLDS(lin + off + i * 2048, lds + s + BOFF + (p + 2 * i) * 1024, 16); };
        auto ord2 = [&](int s) { return GM_LAG == 0 ? s : (((s & 3) == phi) ? s : ((s - GM_LAG) & 31)); };
        auto ord3 = [&](int s) { return (GM_LAG == 0 || s >= 16) ? s : (((s & 3) == phi) ? s : ((s - GM_LAG) & 15)); };
        issueX(0, 0); issueW(0, FH * 2, 0); issueX(1, STG); issueW(1, FH * 2, STG);
        const u32x2 E = flag_ld(ws, WS_E);
        so = 2 * STG;
#pragma nounroll
        for (int S = 0; S < 16; ++S) {
            WAITV(17); BAR();
            const int t = S + 2, k = t >= 9 ? t - 9 : t;
            issueX(k, so); issueW(k, t >= 9 ? 0 : FH * 2, so); so = nextbuf(so);
        }
        u32x2 T; { u64 z = (((u64)__builtin_amdgcn_readfirstlane(E[1]) << 32) | __builtin_amdgcn_readfirstlane(E[0])) + 0x9E3779B97F4A7C15ull;
            z = (z ^ (z >> 30)) * 0xBF58476D1CE4E5B9ull; z = (z ^ (z >> 27)) * 0x94D049BB133111EBull; z ^= z >> 31; T = (u32x2){(unsigned)z, (unsigned)(z >> 32)}; }
        const unsigned zb = WS_Z2F + (unsigned)pn * (64u * 9216u);
        WAITV(17); BAR(); issueS(ord2(0), 0);
        WAITV(8); BAR(); issueS(ord2(1), STG);
        STAMP(1);
        flag_wait(ws, WS_FZ + (unsigned)pn * 32u * 128u, 32, T, lane);
        STAMP(2);
        issueLB(zb + (unsigned)ord2(0) * 18432u, 0); issueLB(zb + (unsigned)ord2(1) * 18432u, STG);
        WAITV(9); BAR();
        so = 2 * STG;
        { const int k = ord2(2); issueS(k, so); issueLB(zb + (unsigned)k * 18432u, so); so = nextbuf(so); }
#pragma nounroll
        for (int S = 19; S < 48; ++S) {
            WAITV(17); BAR();
            const int k = ord2(S - 16); issueS(k, so); issueLB(zb + (unsigned)k * 18432u, so); so = nextbuf(so);
        }
        const unsigned hb = WS_HF + (unsigned)qm * (18u * 16384u), ob = WS_WOF + (unsigned)qn * (18u * 18432u);
        WAITV(17); BAR();
        flag_wait(ws, WS_FZ + (unsigned)(pn ^ 8) * 32u * 128u, 32, T, lane);
        issueLB(ob + (unsigned)ord3(0) * 18432u, 2 * STG);
        BAR();
        issueLB(ob + (unsigned)ord3(1) * 18432u, 0);
        STAMP(3);
        flag_wait(ws, WS_FH + (unsigned)qm * 32u * 128u, 32, T, lane);
        STAMP(4);
        issueLA(hb + (unsigned)ord3(0) * 16384u, 2 * STG); issueLA(hb + (unsigned)ord3(1) * 16384u, 0);
        WAITV(8); BAR();
        so = STG;
        { const int k = ord3(2); issueLA(hb + (unsigned)k * 16384u, so); issueLB(ob + (unsigned)k * 18432u, so); so = nextbuf(so); }
#pragma nounroll
        for (int S = 51; S < 66; ++S) {
            WAITV(17); BAR();
            const int k = ord3(S - 48); issueLA(hb + (unsigned)k * 16384u, so); issueLB(ob + (unsigned)k * 18432u, so); so = nextbuf(so);
        }
        WAITV(17); BAR();
        WAITV(0); BAR();
        BAR();
        STAMP(5);
        if (bid == 0 && p == 0 && lane == 0) __builtin_amdgcn_raw_buffer_store_b64(T, ws, WS_E, 0, 16);
        return;
    }

    u32x2 T; { const u32x2 E = flag_ld(ws, WS_E);
        u64 z = (((u64)__builtin_amdgcn_readfirstlane(E[1]) << 32) | __builtin_amdgcn_readfirstlane(E[0])) + 0x9E3779B97F4A7C15ull;
        z = (z ^ (z >> 30)) * 0xBF58476D1CE4E5B9ull; z = (z ^ (z >> 27)) * 0x94D049BB133111EBull; z ^= z >> 31; T = (u32x2){(unsigned)z, (unsigned)(z >> 32)}; }
    const int fr = lane & 15, fq = lane >> 4;
    const int h = wid - 6;

    if (wid < 4) {
        if (wid >> 1) consumer_front<4>(lds, ws, T, bsp, pm, pn, wid & 1, 1, lane, wsp_);
        else consumer_front<5>(lds, ws, T, bsp, pm, pn, wid & 1, 0, lane, wsp_);
    } else {
#define GM_HPRIO 1
        __builtin_amdgcn_s_setprio(GM_HPRIO);
        int nb = 0;
#define STEP(lim) do { if (nb < (lim)) { BAR(); ++nb; } } while (0)
        {
        if (h == 0) {
            u32x2 uc[18];
#pragma unroll
            for (int i = 0; i < 18; ++i) { const int c = i * 64 + lane, row = c / 9, ch = c - row * 9; uc[i] = *(const u32x2*)((const char*)x + (size_t)(pm * 128 + row) * 1152 + pn * 72 + ch * 8); }
            u32x4 xv[9], gv[9];
#pragma unroll
            for (int i = 0; i < 9; ++i) { xv[i] = *(const u32x4*)((const char*)x + (size_t)(pm * 128 + pn * 8 + (lane >> 3)) * 1152 + ((lane & 7) + 8 * i) * 16);
                                          gv[i] = *(const u32x4*)((const char*)gw + ((lane & 7) + 8 * i) * 16); }
            if (lane == 0) *(LAS unsigned*)(lds + L_MISC) = 0u;
            STEP(9); STEP(9);
#pragma unroll
            for (int i = 0; i < 18; ++i) *(LAS u32x2*)(lds + Z0 + (i * 64 + lane) * 8) = uc[i];
            STEP(9);
            { const LAS unsigned char* pt = lds + Z0 + (8 * (4 * fq + (lane & 3)) + (fr >> 2)) * 72;
              const unsigned ub = WS_UT + (unsigned)(pn * 36 + (lane & 3)) * 4096u + (unsigned)(pm * 128 + 8 * (4 * fq + (fr >> 2))) * 2u;
#pragma unroll
              for (int cc = 0; cc < 9; ++cc) __builtin_amdgcn_raw_buffer_store_b128(__builtin_bit_cast(u32x4, cat8(tr_read(pt + cc * 8), tr_read(pt + 288 + cc * 8))), ws, ub + cc * 16384u, 0, 16); }
            STEP(9);
            float a = 0.f;
#pragma unroll
            for (int i = 0; i < 9; ++i) {
#pragma unroll
                for (int j = 0; j < 4; ++j) { const unsigned pr = pk_bf16(bflo(xv[i][j]) * bflo(gv[i][j]), bfhi(xv[i][j]) * bfhi(gv[i][j])); a += bflo(pr) + bfhi(pr); }
                if (i == 2 || i == 5) { asm volatile("" : "+v"(a)); STEP(9); }
            }
            a += __shfl_xor(a, 1); a += __shfl_xor(a, 2); a += __shfl_xor(a, 4);
            const float gt = rbf(1.0f / (1.0f + __expf(-rbf(a))));
            if ((lane & 7) == 0) __builtin_amdgcn_raw_buffer_store_b32(__float_as_uint(gt), ws, WS_GATE + (unsigned)(pm * 128 + pn * 8 + (lane >> 3)) * 4u, 0, 16);
        } else {
            const int u = (pn >> 3) * 16 + pm, nblk = u < 8 ? 3 : 2;
            u32x4 wv[3][5];
#pragma unroll
            for (int b = 0; b < 3; ++b) {
                const int blk = (b < nblk) ? u + 32 * b : u, st = blk >> 2, jb = blk & 3;
                const int fA = st < 16 ? st * 144 + (jb < 2 ? 32 * jb : 16 + 32 * jb) : ((st - 16) * 8 + 2 * jb) * 144 + 64, dB = st < 16 ? 16 : 144;
#pragma unroll
                for (int i = 0; i < 5; ++i) { int c = i * 64 + lane; c = c < 288 ? c : 287; const int row = c / 9, ch = c - row * 9;
                    wv[b][i] = *(const u32x4*)((const char*)wout + (size_t)(fA + (row >> 4) * dB + (row & 15)) * 1152 + qn * 144 + ch * 16); }
            }
            STEP(9); STEP(9);
#pragma unroll
            for (int b = 0; b < 3; ++b)
#pragma unroll
                for (int i = 0; i < 5; ++i) if (i < 4 || lane < 32) *(LAS u32x4*)(lds + Z0 + 9216 + b * 4608 + (i * 64 + lane) * 16) = wv[b][i];
            STEP(9);
#pragma unroll
            for (int b = 0; b < 3; ++b) {
                if (b < nblk) {
                    const LAS unsigned char* pt = lds + Z0 + 9216 + b * 4608 + (4 * fq + (fr >> 2)) * 144 + (lane & 3) * 8;
                    const unsigned ob = WS_WOF + (unsigned)qn * (18u * 18432u) + (unsigned)(u + 32 * b) * 4608u;
#pragma unroll
                    for (int nn = 0; nn < 5; ++nn) {
                        const u32x4 v = __builtin_bit_cast(u32x4, cat8(tr_read(pt + nn * 32), tr_read(pt + 2304 + nn * 32)));
                        if (nn < 4) __builtin_amdgcn_raw_buffer_store_b128(v, ws, ob + nn * 1024 + lane * 16, 0, 16);
                        else if (fr < 8) __builtin_amdgcn_raw_buffer_store_b128(v, ws, ob + 4096 + (fq * 8 + fr) * 16, 0, 16);
                    }
                }
                STEP(9);
            }
        }
        }
        WAITL0();
        _Pragma("nounroll") while (nb < 10) { BAR(); ++nb; }
        STAMP(1);
        {
            const f32x4 sp = *(const LAS f32x4*)(lds + L_SP + (h * 64 + lane) * 16);
            const float dm = sp[0] - sp[2];
            const f32x2 mine = {(80.f * sp[0] + 64.f * sp[2]) * (1.0f / 144.f), sp[1] + sp[3] + (80.f * 64.f / 144.f) * dm * dm};
            __builtin_amdgcn_raw_buffer_store_b64(__builtin_bit_cast(u32x2, mine), ws, WS_ST + (unsigned)((pm * 16 + pn) * 128 + h * 64 + lane) * 8u, 0, 16);
            STEP(18);
            flag_publish(ws, WS_FS + (unsigned)((pm * 2 + h) * 16 + pn) * 128u, T, lane);
            const unsigned fo = WS_FS + (unsigned)((pm * 2 + h) * 16 + fr) * 128u;
            const u64 t0 = __builtin_amdgcn_s_memrealtime();
            for (;;) {
                CFENCE();
                const u32x2 v = flag_ld(ws, fo);
                STEP(18);
                if (flag_all(v, T, true)) break;
                if (__builtin_amdgcn_s_memrealtime() - t0 > 300000ull) break;
            }
            ACQ();
            STAMP(2);
            u32x2 st[16];
#pragma unroll
            for (int j = 0; j < 16; ++j) st[j] = __builtin_amdgcn_raw_buffer_load_b64(ws, WS_ST + (unsigned)((pm * 16 + j) * 128 + h * 64 + lane) * 8u, 0, 16);
            STEP(18);
            float mean = 0.f, m2 = 0.f;
#pragma unroll
            for (int j = 0; j < 16; ++j) mean += __uint_as_float(st[j][0]);
            mean *= (1.0f / 16.f);
#pragma unroll
            for (int j = 0; j < 16; ++j) { const float d = __uint_as_float(st[j][0]) - mean; m2 += __uint_as_float(st[j][1]) + 144.f * d * d; }
            const float rstd = 1.0f / sqrtf(m2 * (1.0f / FH) + 1e-5f);
            *(LAS f32x2*)(lds + L_SR + (h * 64 + lane) * 8) = (f32x2){rstd, -mean * rstd};
#pragma unroll
            for (int kb = 0; kb < 2; ++kb) {
                f32x4 sr[4];
#pragma unroll
                for (int j = 0; j < 4; ++j) sr[j] = *(const LAS f32x4*)(lds + L_SR + (h * 64 + kb * 32 + fq * 8) * 8 + j * 16);
                const LAS unsigned char* pt = lds + Z0 + (h * 64 + kb * 32 + 8 * fq + (fr >> 2)) * ZST + (lane & 3) * 8;
                const unsigned zo = WS_Z2F + (unsigned)((pn * 64 + pm * 4 + h * 2 + kb) * 9) * 1024u + lane * 16;
#pragma unroll
                for (int n = 0; n < 9; ++n) {
                    const u32x2 lo = __builtin_bit_cast(u32x2, tr_read(pt + n * 32)), hi = __builtin_bit_cast(u32x2, tr_read(pt + 4 * ZST + n * 32));
                    u32x4 o;
                    o[0] = pk_bf16(bflo(lo[0]) * sr[0][0] + sr[0][1], bfhi(lo[0]) * sr[0][2] + sr[0][3]);
                    o[1] = pk_bf16(bflo(lo[1]) * sr[1][0] + sr[1][1], bfhi(lo[1]) * sr[1][2] + sr[1][3]);
                    o[2] = pk_bf16(bflo(hi[0]) * sr[2][0] + sr[2][1], bfhi(hi[0]) * sr[2][2] + sr[2][3]);
                    o[3] = pk_bf16(bflo(hi[1]) * sr[3][0] + sr[3][1], bfhi(hi[1]) * sr[3][2] + sr[3][3]);
                    __builtin_amdgcn_raw_buffer_store_b128(o, ws, zo + n * 1024, 0, 16);
                    if (n == 4) STEP(18);
                }
                STEP(18);
            }
            flag_publish(ws, WS_FZ + (unsigned)(pn * 32 + pm * 2 + h) * 128u, T, lane);
        }
        STAMP(3);
        _Pragma("nounroll") while (nb < 18) { BAR(); ++nb; }
        const float ex0 = (float)(128 * (fr >> 3) + (fr & 7));
        const float ex1 = (float)(fr < 8 ? 256 + 128 * (fr >> 2) + 2 * (fr & 3) : 512 + 128 * ((fr - 8) >> 1) + 4 * (fr & 1));
        const float ex2 = (float)(fr < 9 ? 1024 + 128 * fr : 0);
        const float exb = (float)(8 * fr);
        unsigned gi[8];
#pragma unroll
        for (int j = 0; j < 8; ++j) {
            unsigned w = 0;
#pragma unroll
            for (int r2 = 0; r2 < 2; ++r2) {
                const int lag = 4 * (8 * lane + j) + 2 * h + r2, pos = (int)rbf((float)lag), hh = pos >> 7, jj = (pos >> 3) & 15, r8 = pos & 7;
                const int R = hh < 2 ? 8 * hh + r8 : hh < 4 ? 16 + 4 * (hh - 2) + (r8 >> 1) : hh < 8 ? 24 + 2 * (hh - 4) + (r8 >> 2) : 32 + (hh - 8);
                w |= (unsigned)((R * 16 + jj) * 4) << (16 * r2);
            }
            gi[j] = w;
        }
        int d = pn * 36 + pm;
        unsigned pA = logA[d * 128 + h * 64 + lane], pI = Aim[d * 128 + h * 64 + lane], pR = Cre[d * 128 + h * 64 + lane], pC = Cim[d * 128 + h * 64 + lane], pT = logdt[d];
#pragma nounroll
        for (int c = 0; c < nch; ++c) {
            BAR(); ++nb;
            u32x4 uu[2];
#pragma unroll
            for (int j = 0; j < 2; ++j) uu[j] = __builtin_amdgcn_raw_buffer_load_b128(ws, WS_UT + (unsigned)d * 4096u + h * 2048 + j * 1024 + lane * 16, 0, 16);
            const float dt = rbf(expf(bf2f(pT)));
            const float ar = -rbf(expf(bf2f(pA))) * dt, ai = bf2f(pI) * dt, cr = bf2f(pR) * dt, ci = bf2f(pC) * dt;
            const float lm = 1.0f + 0.5f * __log2f(cr * cr + ci * ci);
            float lmax = lm;
#pragma unroll
            for (int o = 1; o < 64; o <<= 1) lmax = fmaxf(lmax, __shfl_xor(lmax, o));
            lmax = fmaxf(lmax, -120.f);
            *(LAS f32x4*)(lds + L_PAR + h * 1024 + lane * 16) = (f32x4){ar * 1.44269504f, ai * 0.15915494f, lm - lmax, atan2f(ci, cr) * 0.15915494f};
            const float scale = __builtin_amdgcn_exp2f(lmax);
            const int dn = (c + 1 < nch) ? d + 16 : d;
            pA = logA[dn * 128 + h * 64 + lane]; pI = Aim[dn * 128 + h * 64 + lane]; pR = Cre[dn * 128 + h * 64 + lane]; pC = Cim[dn * 128 + h * 64 + lane]; pT = logdt[dn];
            f32x4 kd[3] = {{0.f, 0.f, 0.f, 0.f}, {0.f, 0.f, 0.f, 0.f}, {0.f, 0.f, 0.f, 0.f}};
#pragma nounroll
            for (int ks = 0; ks < 4; ++ks) {
                u32x4 fa[3], fb;
#pragma unroll
                for (int part = 0; part < 2; ++part) {
                    BAR(); ++nb;
                    if (part == 0 && ks == 0) {
#pragma unroll
                        for (int j = 0; j < 2; ++j) *(LAS u32x4*)(lds + L_UT + c * UT_SZ + 256 + h * 2048 + j * 1024 + lane * 16) = uu[j];
                        if (lane < 16) *(LAS u32x4*)(lds + L_UT + c * UT_SZ + h * 4352 + lane * 16) = (u32x4){0u, 0u, 0u, 0u};
                    }
#pragma unroll
                    for (int mm = 0; mm < 2; ++mm) {
                        const f32x4 pr = *(const LAS f32x4*)(lds + L_PAR + h * 1024 + (ks * 16 + fq * 4 + part * 2 + mm) * 16);
                        const float exs[3] = {ex0, ex1, ex2};
#pragma unroll
                        for (int tt = 0; tt < 3; ++tt) {
                            const float mg = __builtin_amdgcn_exp2f(pr[0] * exs[tt] + pr[2]), th = __builtin_amdgcn_fractf(pr[1] * exs[tt] + pr[3]);
                            fa[tt][part * 2 + mm] = pk_f16(mg * __builtin_amdgcn_cosf(th), mg * __builtin_amdgcn_sinf(th));
                        }
                        const float mg = __builtin_amdgcn_exp2f(pr[0] * exb), th = __builtin_amdgcn_fractf(pr[1] * exb);
                        fb[part * 2 + mm] = pk_f16(mg * __builtin_amdgcn_cosf(th), -mg * __builtin_amdgcn_sinf(th));
                    }
                }
#pragma unroll
                for (int tt = 0; tt < 3; ++tt) kd[tt] = __builtin_amdgcn_mfma_f32_16x16x32_f16(__builtin_bit_cast(f16x8, fa[tt]), __builtin_bit_cast(f16x8, fb), kd[tt], 0, 0, 0);
            }
#pragma unroll
            for (int tt = 0; tt < 3; ++tt)
#pragma unroll
                for (int e = 0; e < 4; ++e) *((LAS float*)(lds + L_KD + h * 3072) + tt * 256 + (4 * fq + e) * 16 + fr) = kd[tt][e] * scale;
            WAITL0();
            BAR(); ++nb;
#pragma unroll
            for (int r2 = 0; r2 < 2; ++r2) {
                float kv[8];
#pragma unroll
                for (int j = 0; j < 8; ++j) { const LAS unsigned char* q = lds + L_KD + ((gi[j] >> (16 * r2)) & 0xffffu); kv[j] = *(const LAS float*)q + *(const LAS float*)(q + 3072); }
                *(LAS u32x4*)(lds + L_KT + c * KT_SZ + (2 * h + r2) * 1088 + 64 + lane * 16) = (u32x4){pk_bf16(kv[0], kv[1]), pk_bf16(kv[2], kv[3]), pk_bf16(kv[4], kv[5]), pk_bf16(kv[6], kv[7])};
            }
            if (lane < 8 + 4 * h) *(LAS u32x4*)(lds + L_KT + c * KT_SZ + (2 * h + (lane >> 2)) * 1088 + (lane & 3) * 16) = (u32x4){0u, 0u, 0u, 0u};
            WAITL0();
            d += 16;
        }
        STAMP(4);
        _Pragma("nounroll") while (nb < 50) { BAR(); ++nb; }
#undef STEP
    }

    for (;;) {
        unsigned tk = 0;
        if (lane == 0) tk = __hip_atomic_fetch_add((LAS unsigned*)(lds + L_MISC), 1u, __ATOMIC_RELAXED, __HIP_MEMORY_SCOPE_WORKGROUP);
        tk = __builtin_amdgcn_readfirstlane(tk);
        if (tk >= (unsigned)(4 * nch)) break;
        const int c = tk >> 2, tl = tk & 1;
        if (tk & 2) conv_task<0, 10>(lds, L_KT + c * KT_SZ, L_UT + c * UT_SZ, ytab(c), tl, lane);
        else conv_task<11, 15>(lds, L_KT + c * KT_SZ, L_UT + c * UT_SZ, ytab(c), tl, lane);
    }
    WAITL0();
    STAMP(7);

    if (wid < 4) {
        f32x4 acc[5][4];
#pragma unroll
        for (int n = 0; n < 5; ++n)
#pragma unroll
            for (int m = 0; m < 4; ++m) acc[n][m] = (f32x4){0.f, 0.f, 0.f, 0.f};
        bf16x8 fa0[4], fb0[5], fa1[4], fb1[5];
        const int ha = wid * 1024 + lane * 16, hs = wid * 1024 + lane * 8, wo = BOFF + wid * 4608 + lane * 16, wo4 = BOFF + wid * 4608 + 4096 + (fq * 8 + (fr & 7)) * 16;
        auto ld3 = [&](bool single, int s, bf16x8 (&fa)[4], bf16x8 (&fb)[5]) {
#pragma unroll
            for (int m = 0; m < 4; ++m) {
                if (!single) fa[m] = *(const LAS bf16x8*)(lds + s + ha + m * 4096);
                else fa[m] = cat8(*(const LAS s16x4*)(lds + s + hs + m * 4096), *(const LAS s16x4*)(lds + s + hs + m * 4096 + 512));
            }
#pragma unroll
            for (int n = 0; n < 4; ++n) fb[n] = *(const LAS bf16x8*)(lds + s + wo + n * 1024);
            fb[4] = *(const LAS bf16x8*)(lds + s + wo4);
        };
#define G3_MMA(fa, fb) _Pragma("unroll") for (int n = 0; n < 5; ++n) _Pragma("unroll") for (int m = 0; m < 4; ++m) acc[n][m] = MFMA16(fb[n], fa[m], acc[n][m]);
#define G3_SCHED(id) do { _Pragma("unroll") for (int i = 0; i < 9; ++i) { SGB(0x8, 1, id); SGB(0x100, 1, id); } SGB(0x8, 11, id); __builtin_amdgcn_sched_barrier(0); } while (0)
        int so = 2 * STG;
        __builtin_amdgcn_s_waitcnt(0xC07F);
        BAR(); ld3(false, so, fa0, fb0); so = nextbuf(so);
#pragma nounroll
        for (int j = 0; j < 7; ++j) {
            WAITL0(); BAR(); ld3(false, so, fa1, fb1); G3_MMA(fa0, fb0); so = nextbuf(so); G3_SCHED(3);
            WAITL0(); BAR(); ld3(false, so, fa0, fb0); G3_MMA(fa1, fb1); so = nextbuf(so); G3_SCHED(4);
        }
        WAITL0(); BAR(); ld3(false, so, fa1, fb1); G3_MMA(fa0, fb0); so = nextbuf(so); G3_SCHED(5);
        WAITL0(); BAR(); ld3(true, so, fa0, fb0); G3_MMA(fa1, fb1); so = nextbuf(so);
        WAITL0(); BAR(); ld3(true, so, fa1, fb1); G3_MMA(fa0, fb0);
        G3_MMA(fa1, fb1);
        STAMP(8);
        LAS unsigned char* rb = lds + (wid == 0 ? L_R0 : wid == 1 ? L_R1 : wid == 2 ? L_R2 : L_R3) + lane * 16;
#pragma unroll
        for (int n = 0; n < 5; ++n)
#pragma unroll
            for (int m = 0; m < 4; ++m) *(LAS f32x4*)(rb + (n * 4 + m) * 1024) = acc[n][m];
    } else {
        int nb = 50, d = pn * 36 + pm;
#pragma nounroll
        for (int c = 0; c < nch; ++c) {
            const float dsk = bf2f(Dsk[d]);
            BAR(); ++nb;
#pragma unroll
            for (int k = 0; k < 2; ++k) {
                const int l0 = h * 1024 + k * 512 + lane * 8;
                f32x4 cv = *(const LAS f32x4*)(lds + ytab(c) + 8192 + (l0 >= 16 ? (l0 >> 4) - 1 : 0) * 16);
                if ((lane & 1) || l0 == 0) cv = (f32x4){0.f, 0.f, 0.f, 0.f};
                const f32x4 y0 = *(const LAS f32x4*)(lds + ytab(c) + l0 * 4) + cv, y1 = *(const LAS f32x4*)(lds + ytab(c) + l0 * 4 + 16);
                const u32x4 uv = *(const LAS u32x4*)(lds + L_UT + c * UT_SZ + 256 + l0 * 2);
                u32x4 o;
#pragma unroll
                for (int j = 0; j < 4; ++j) {
                    const float ya = j < 2 ? y0[2 * j] : y1[2 * j - 4], yb = j < 2 ? y0[2 * j + 1] : y1[2 * j - 3];
                    const unsigned yp = pk_bf16(ya, yb), up = pk_bf16(bflo(uv[j]) * dsk, bfhi(uv[j]) * dsk);
                    o[j] = pk_bf16(bflo(yp) + bflo(up), bfhi(yp) + bfhi(up));
                }
                __builtin_amdgcn_raw_buffer_store_b128(o, ws, WS_AT + (unsigned)d * 4096u + l0 * 2, 0, 16);
            }
            d += 16;
        }
        BAR(); ++nb;
        flag_publish(ws, WS_FA + (unsigned)((pn >> 1) * 64 + (pn & 1) * 32 + pm * 2 + h) * 128u, T, lane);
        STAMP(5);
        _Pragma("nounroll") while (nb < 60) { BAR(); ++nb; }
        if (h == 0) {
            const unsigned fo = WS_FA + (unsigned)(qn * 64 + lane) * 128u;
            const u64 t0 = __builtin_amdgcn_s_memrealtime();
            for (;;) {
                CFENCE();
                const u32x2 v = flag_ld(ws, fo);
                if (nb < 68) { BAR(); ++nb; }
                if (flag_all(v, T, true)) break;
                if (__builtin_amdgcn_s_memrealtime() - t0 > 300000ull) break;
            }
            ACQ();
            u32x4 av[9];
#pragma unroll
            for (int i = 0; i < 9; ++i) av[i] = __builtin_amdgcn_raw_buffer_load_b128(ws, WS_AT + (unsigned)(qn * 72 + i * 8 + (lane >> 3)) * 4096u + qm * 128 + (lane & 7) * 16, 0, 16);
            if (nb < 68) { BAR(); ++nb; }
#pragma unroll
            for (int i = 0; i < 9; ++i) *(LAS u32x4*)(lds + L_AT + i * 1024 + lane * 16) = av[i];
        } else {
            const unsigned gv = __builtin_amdgcn_raw_buffer_load_b32(ws, WS_GATE + (unsigned)(qm * 64 + lane) * 4u, 0, 16);
            BAR(); ++nb;
            *(LAS unsigned*)(lds + L_MISC + 64 + lane * 4) = gv;
        }
        _Pragma("nounroll") while (nb < 68) { BAR(); ++nb; }
    }
    WAITL0();
    BAR();
    {
        const int v = wid < 4 ? wid : wid - 2;
#pragma nounroll
        for (int tile = v; tile < 20; tile += 6) {
            const int n = tile >> 2, m = tile & 3;
            const f32x4 b = (*(const LAS f32x4*)(lds + L_R0 + tile * 1024 + lane * 16) + *(const LAS f32x4*)(lds + L_R1 + tile * 1024 + lane * 16)) +
                            (*(const LAS f32x4*)(lds + L_R2 + tile * 1024 + lane * 16) + *(const LAS f32x4*)(lds + L_R3 + tile * 1024 + lane * 16));
            const u32x2 a = __builtin_bit_cast(u32x2, tr_read(lds + L_AT + (16 * n + 4 * fq + (fr >> 2)) * 128 + (16 * m + 4 * (lane & 3)) * 2));
            const float g = *(const LAS float*)(lds + L_MISC + 64 + (16 * m + fr) * 4), g1 = rbf(1.0f - g);
            const u32x2 o = {pk_bf16(g * bflo(a[0]) + g1 * b[0], g * bfhi(a[0]) + g1 * b[1]), pk_bf16(g * bflo(a[1]) + g1 * b[2], g * bfhi(a[1]) + g1 * b[3])};
            if (n < 4 || fq < 2) *(u32x2*)((char*)out + (size_t)(qm * 64 + 16 * m + fr) * 1152 + (qn * 72 + 16 * n + 4 * fq) * 2) = o;
        }
    }
    STAMP(9);
}
}

extern "C" void kernel_launch(void* const* d_in, const int* in_sizes, int n_in, void* d_out, int out_size, void* d_ws, size_t ws_size, hipStream_t stream) {
    (void)in_sizes; (void)n_in; (void)out_size; (void)ws_size;
    (void)hipFuncSetAttribute((const void*)gm::gm_kernel, hipFuncAttributeMaxDynamicSharedMemorySize, gm::LDS_BYTES);
    hipLaunchKernelGGL(gm::gm_kernel, dim3(256), dim3(512), gm::LDS_BYTES, stream, (const u16*)d_in[0], (const u16*)d_in[1], (const u16*)d_in[2], (const u16*)d_in[3],
                       (const u16*)d_in[4], (const u16*)d_in[5], (const u16*)d_in[6], (const u16*)d_in[8], (const u16*)d_in[9], (const u16*)d_in[10],
                       (const u16*)d_in[11], (const u16*)d_in[12], (u16*)d_out, (unsigned char*)d_ws);
}
```

```cpp
#include <hip/hip_runtime.h>
#include <stdint.h>
#include <type_traits>

#define LAS __attribute__((address_space(3)))
typedef unsigned short u16;
typedef unsigned long long u64;
typedef short bf16x8 __attribute__((ext_vector_type(8)));
typedef short s16x4 __attribute__((ext_vector_type(4)));
typedef _Float16 f16x8 __attribute__((ext_vector_type(8)));
typedef _Float16 f16x2 __attribute__((ext_vector_type(2)));
typedef __bf16 bf16x2 __attribute__((ext_vector_type(2)));
typedef float f32x4 __attribute__((ext_vector_type(4)));
typedef float f32x2 __attribute__((ext_vector_type(2)));
typedef unsigned u32x4 __attribute__((ext_vector_type(4)));
typedef unsigned u32x2 __attribute__((ext_vector_type(2)));
typedef __amdgpu_buffer_rsrc_t rsrc_t;

namespace gm {
constexpr int SEQ = 2048, DM = 576, FH = 2304;
constexpr unsigned WS_E = 0, WS_FS = 4096, WS_FZ = WS_FS + 65536, WS_FH = WS_FZ + 65536, WS_FA = WS_FH + 131072;
constexpr unsigned WS_ST = 0x60000, WS_GATE = 0xE0000, WS_DBG = 0x1E00000, WS_Z2F = 0x100000, WS_HF = 0xA00000, WS_WOF = 0x1300000, WS_UT = 0x1600000, WS_AT = 0x1900000, WS_END = 0x1C00000, WS_TRC = 0x1C00000;
constexpr int STG = 34816, BOFF = 16384, Z0 = 3 * STG, ZST = 296, T0 = Z0 + 128 * ZST;
constexpr int KT_SZ = 4352, UT_SZ = 4608, Y_SZ = 10240;
constexpr int L_MISC = 163840 - 320;
constexpr int L_KT = L_MISC - 3 * KT_SZ, L_SP = T0, L_KD = Z0 + 3 * 8448, L_UT = Z0 + 3 * Y_SZ;
constexpr int L_AT = Z0, L_R = Z0 + 10240;
constexpr int LDS_BYTES = 163840;
static_assert(L_UT + 3 * UT_SZ <= L_KT && L_KD + 3 * 3072 <= T0 && L_SP + 2048 <= L_KT, "lds map");
#define GM_HPRIO 1
#define GM_LAG 4

__device__ __forceinline__ float bf2f(unsigned v) { return __uint_as_float(v << 16); }
__device__ __forceinline__ float bflo(unsigned w) { return __uint_as_float(w << 16); }
__device__ __forceinline__ float bfhi(unsigned w) { return __uint_as_float(w & 0xffff0000u); }
__device__ __forceinline__ unsigned pk_bf16(float a, float b) { f32x2 v = {a, b}; return __builtin_bit_cast(unsigned, __builtin_convertvector(v, bf16x2)); }
__device__ __forceinline__ unsigned pk_f16(float a, float b) { f32x2 v = {a, b}; return __builtin_bit_cast(unsigned, __builtin_convertvector(v, f16x2)); }
__device__ __forceinline__ float rbf(float a) { return bflo(pk_bf16(a, a)); }
__device__ __forceinline__ f32x2 gelu2(f32x2 x) {
    const f32x2 w = (x * x) * (-0.10294324f) + (-2.30220819f);
    const f32x2 a = w * x;
    f32x2 e; e.x = __builtin_amdgcn_exp2f(a.x); e.y = __builtin_amdgcn_exp2f(a.y);
    e = e + 1.0f;
    f32x2 r; r.x = __builtin_amdgcn_rcpf(e.x); r.y = __builtin_amdgcn_rcpf(e.y);
    return x * r;
}
__device__ __forceinline__ f32x4 gelu4(f32x4 v) { const f32x2 a = gelu2((f32x2){v[0], v[1]}), b = gelu2((f32x2){v[2], v[3]}); return (f32x4){a.x, a.y, b.x, b.y}; }
__device__ __forceinline__ float allred_q(float v) {
    unsigned u = __float_as_uint(v);
    auto r = __builtin_amdgcn_permlane16_swap(u, u, false, false);
    v = __uint_as_float(r[0]) + __uint_as_float(r[1]);
    u = __float_as_uint(v);
    auto s = __builtin_amdgcn_permlane32_swap(u, u, false, false);
    return __uint_as_float(s[0]) + __uint_as_float(s[1]);
}
#define ACQ() __builtin_amdgcn_fence(__ATOMIC_ACQUIRE, "wavefront")
#define TRP
#define TRA
#define TRDUMP() do { } while (0)
#define BAR() __builtin_amdgcn_s_barrier()
#define PBAR() BAR()
#define PDUMP() TRDUMP()
#define WAITV(n) asm volatile("s_waitcnt vmcnt(" #n ")" ::: "memory")
#define WAITL0() asm volatile("s_waitcnt lgkmcnt(0)" ::: "memory")
#define CFENCE() asm volatile("" ::: "memory")
#define SGB(mask, n, id) __builtin_amdgcn_sched_group_barrier(mask, n, id)
#define MFMA16(a, b, c) __builtin_amdgcn_mfma_f32_16x16x32_bf16(a, b, c, 0, 0, 0)
__device__ __forceinline__ s16x4 tr_read(const LAS unsigned char* p) { return __builtin_amdgcn_ds_read_tr16_b64_v4i16((LAS s16x4*)p); }
__device__ __forceinline__ bf16x8 cat8(s16x4 a, s16x4 b) { return __builtin_shufflevector(a, b, 0, 1, 2, 3, 4, 5, 6, 7); }
template <class P> __device__ __forceinline__ P opq(P p) { asm volatile("" : "+v"(p)); return p; }
__device__ __forceinline__ int nextbuf(int so) { return so == 2 * STG ? 0 : so + STG; }

#define STAMP(i) do { } while (0)

__device__ __forceinline__ u32x2 flag_ld(rsrc_t ws, unsigned off) { return __builtin_amdgcn_raw_buffer_load_b64(ws, off, 0, 16); }
__device__ __forceinline__ bool flag_all(u32x2 v, u32x2 T, bool active) { return __builtin_amdgcn_ballot_w64(active && (v[0] != T[0] || v[1] != T[1])) == 0ull; }
__device__ __forceinline__ void flag_publish(rsrc_t ws, unsigned off, u32x2 T, int lane) {
    WAITV(0);
    if (lane == 0) __builtin_amdgcn_raw_buffer_store_b64(T, ws, off, 0, 16);
}
__device__ __forceinline__ void flag_wait(rsrc_t ws, unsigned base, int n, u32x2 T, int lane) {
    const unsigned off = base + (unsigned)(lane < n ? lane : 0) * 128u;
    const u64 t0 = __builtin_amdgcn_s_memrealtime();
    for (;;) {
        CFENCE();
        const u32x2 v = flag_ld(ws, off);
        if (flag_all(v, T, true)) break;
        if (__builtin_amdgcn_s_memrealtime() - t0 > 300000ull) break;
        __builtin_amdgcn_s_sleep(1);
    }
    ACQ();
}

#define SB0() __builtin_amdgcn_sched_barrier(0)
template <int NT> struct Frag { bf16x8 a[4]; s16x4 lo[NT], hi[NT]; };
template <int NT, bool TR, int HALF>
__device__ __forceinline__ void frag_read(Frag<NT>& f, int i, const LAS unsigned char* pa, const LAS unsigned char* pb) {
    if (i < 4) f.a[i] = *(const LAS bf16x8*)(pa + i * 2048);
    else if (TR) { const int n = (i - 4) >> 1; if ((i - 4) & 1) f.hi[n] = tr_read(pb + HALF * 9216 + 4608 + n * 32); else f.lo[n] = tr_read(pb + HALF * 9216 + n * 32); }
    else { const int n = i - 4; const bf16x8 v = *(const LAS bf16x8*)(pb + HALF * 9216 + n * 1024);
           f.lo[n] = __builtin_shufflevector(v, v, 0, 1, 2, 3); f.hi[n] = __builtin_shufflevector(v, v, 4, 5, 6, 7); }
}
template <int NT, bool TR, int LHALF, int RPS, bool FIRST, bool UPD>
__device__ __forceinline__ void half_step(f32x4 (&acc)[NT][4], const Frag<NT>& c, Frag<NT>& l, const LAS unsigned char*& pa0, const LAS unsigned char*& pa1, const LAS unsigned char*& pb, int delta, const float* ini = nullptr) {
    constexpr int NRD = TR ? 4 + 2 * NT : 4 + NT, NM = 4 * NT, LAST = (NRD + RPS - 1) / RPS;
#pragma unroll
    for (int i = 0; i < NM; ++i) {
        const int n = i >> 2, m = i & 3;
        acc[n][m] = MFMA16(cat8(c.lo[n], c.hi[n]), c.a[m], FIRST ? (TR ? ((f32x4){0.f, 0.f, 0.f, 0.f}) : ((f32x4){ini[m], ini[m], ini[m], ini[m]})) : acc[n][m]);
        if (LHALF >= 0) {
#pragma unroll
            for (int r = 0; r < RPS; ++r) if (i * RPS + r < NRD) frag_read<NT, TR, (LHALF > 0 ? 1 : 0)>(l, i * RPS + r, LHALF > 0 ? pa1 : pa0, pb);
        }
        if (UPD) { if (i == LAST) { pa0 += delta; asm volatile("" : "+v"(pa0)); } if (i == LAST + 1) { pb += delta; asm volatile("" : "+v"(pb)); } if (i == LAST + 2) { pa1 += delta; asm volatile("" : "+v"(pa1)); } }
        SB0();
    }
}
template <int NT, bool TR, class HK>
__device__ __forceinline__ void gemm_phase(int nsteps, f32x4 (&acc)[NT][4], int& so, const LAS unsigned char* pa0, const LAS unsigned char* pa1, const LAS unsigned char* pb,
                                           const float* ini, const volatile LAS unsigned* rdy, HK hook TRP) {
    constexpr int NRD = TR ? 4 + 2 * NT : 4 + NT;
    Frag<NT> f0, f1;
    pa0 += so; pa1 += so; pb += so;
    asm volatile("" : "+v"(pa0), "+v"(pa1), "+v"(pb));
    __builtin_amdgcn_s_waitcnt(0xC07F);
    BAR();
#pragma unroll
    for (int i = 0; i < NRD; ++i) frag_read<NT, TR, 0>(f0, i, pa0, pb);
    SB0();
    int delta = so == 2 * STG ? -2 * STG : STG; so += delta;
    half_step<NT, TR, 1, 2, true, true>(acc, f0, f1, pa0, pa1, pb, delta, ini);
#pragma nounroll
    for (int s = 1; s < nsteps; ++s) {
        WAITL0();
        BAR();
        unsigned rv = 0;
        if (TR) { rv = *rdy; SB0(); }
        delta = so == 2 * STG ? -2 * STG : STG; so += delta;
        half_step<NT, TR, 0, 1, false, false>(acc, f1, f0, pa0, pa1, pb, 0);
        half_step<NT, TR, 1, 2, false, true>(acc, f0, f1, pa0, pa1, pb, delta);
        if (TR) hook(rv);
    }
    half_step<NT, TR, -1, 1, false, false>(acc, f1, f0, pa0, pa1, pb, 0);
}

template <int NT, class LT>
__device__ __forceinline__ void consumer_a(LAS unsigned char* lds, u32x2 (&zp)[5][4], LT late, int wm, int wn, int lane, unsigned char* wsp_ TRP) {
    const int fr = lane & 15, fq = lane >> 4;
    constexpr int N0 = (NT == 5) ? 0 : 5;
    f32x4 acc[NT][4];
    int so = 0;
    const int sw = (fr >> 1) & 7;
    const int a0 = (wm * 64 + fr) * 128 + ((fq ^ sw) << 4), a1 = (wm * 64 + fr) * 128 + (((4 + fq) ^ sw) << 4);
    const int bw = BOFF + (4 * fq + (fr >> 2)) * 288 + (lane & 3) * 8 + N0 * 32;
    const volatile LAS unsigned* rdy = (const volatile LAS unsigned*)(lds + L_MISC + 4 + wm * 4);
    bool done = false;
    auto normalise = [&]() {
        const LAS unsigned char* ps = opq(lds + L_SP + (wm * 64 + fr) * 16);
        LAS unsigned char* pz = opq(lds + Z0 + (wm * 64 + fr) * ZST + N0 * 32 + fq * 8);
#pragma unroll
        for (int m = 0; m < 4; ++m) {
            const f32x2 sr = *(const LAS f32x2*)(ps + m * 256);
#pragma unroll
            for (int n = 0; n < NT; ++n) {
                const f32x2 u = (f32x2){bflo(zp[n][m][0]), bfhi(zp[n][m][0])} * sr.x + sr.y, v = (f32x2){bflo(zp[n][m][1]), bfhi(zp[n][m][1])} * sr.x + sr.y;
                *(LAS u32x2*)(pz + m * 16 * ZST + n * 32) = (u32x2){pk_bf16(u.x, u.y), pk_bf16(v.x, v.y)};
            }
        }
        WAITL0();
        if (lane == 0) *(volatile LAS unsigned*)(lds + L_MISC + 16 + (wm * 2 + wn) * 4) = 1u;
        done = true;
    };
    auto hook = [&](unsigned rv) { if (__builtin_amdgcn_readfirstlane(rv) != 0u && !done) normalise(); };
#pragma unroll
    for (int n = 0; n < NT; ++n)
#pragma unroll
        for (int m = 0; m < 4; ++m) zp[n][m] = (u32x2){0u, 0u};
#pragma nounroll
    for (int it = 0; it < 2; ++it) {
        gemm_phase<NT, true>(9, acc, so, lds + a0, lds + a1, lds + bw, nullptr, rdy, hook TRA);
        STAMP(1 + 2 * it);
        late();
        if (it == 1 && !done) {
            const u64 t0 = __builtin_amdgcn_s_memrealtime();
            while (__builtin_amdgcn_readfirstlane(*rdy) == 0u && __builtin_amdgcn_s_memrealtime() - t0 < 400000ull) __builtin_amdgcn_s_sleep(1);
            normalise();
        }
#pragma unroll
        for (int m = 0; m < 4; ++m)
#pragma unroll
            for (int n = 0; n < NT; ++n) { acc[n][m] = gelu4(acc[n][m]); zp[n][m] = (u32x2){pk_bf16(acc[n][m][0], acc[n][m][1]), pk_bf16(acc[n][m][2], acc[n][m][3])}; }
        if (it == 0) {
            LAS unsigned char* ps = opq(lds + L_SP + (wm * 64 + fr) * 16 + wn * 8);
#pragma unroll
            for (int m = 0; m < 4; ++m) {
                f32x4 s4 = acc[0][m];
#pragma unroll
                for (int n = 1; n < NT; ++n) s4 += acc[n][m];
                const float mean = opq(allred_q((s4[0] + s4[1]) + (s4[2] + s4[3])) * (1.0f / (16 * NT)));
                f32x4 q4 = {0.f, 0.f, 0.f, 0.f};
#pragma unroll
                for (int n = 0; n < NT; ++n) { const f32x4 d = acc[n][m] - mean; q4 += d * d; }
                const float m2 = allred_q((q4[0] + q4[1]) + (q4[2] + q4[3]));
                if (fq == 0) *(LAS f32x2*)(ps + m * 256) = (f32x2){mean, m2};
            }
            STAMP(2);
        }
    }
    STAMP(4);
}

template <int NT>
__device__ __forceinline__ void consumer_b(LAS unsigned char* lds, rsrc_t ws, u32x2 T, const u32x2 (&z1)[5][4], const float (&bs_)[4], int pm, int pn, int wm, int wn, int lane, unsigned char* wsp_ TRP) {
    float bs[4] = {bf2f(__float_as_uint(opq(bs_[0]))), bf2f(__float_as_uint(opq(bs_[1]))), bf2f(__float_as_uint(opq(bs_[2]))), bf2f(__float_as_uint(opq(bs_[3])))};
    const int fr = lane & 15, fq = lane >> 4;
    constexpr int N0 = (NT == 5) ? 0 : 5;
    f32x4 acc[NT][4];
    int so = 0;
    const int sw = (fr >> 1) & 7;
    const int a0 = (wm * 64 + fr) * 128 + ((fq ^ sw) << 4), a1 = (wm * 64 + fr) * 128 + (((4 + fq) ^ sw) << 4);
    const int bz = BOFF + N0 * 1024 + lane * 16;
    gemm_phase<NT, false>(32, acc, so, lds + a0, lds + a1, lds + bz, bs, nullptr, [](unsigned) {} TRA);
    STAMP(5);
    __builtin_amdgcn_s_setprio(3);
    const int qm = 2 * pm + wm;
    const unsigned hb = WS_HF + (unsigned)qm * (18u * 16384u);
#pragma unroll
    for (int m = 0; m < 4; ++m) {
        u32x2 hp[NT];
#pragma unroll
        for (int n = 0; n < NT; ++n) {
            const f32x2 u = (f32x2){bflo(z1[n][m][0]), bfhi(z1[n][m][0])} * (f32x2){acc[n][m][0], acc[n][m][1]}, v = (f32x2){bflo(z1[n][m][1]), bfhi(z1[n][m][1])} * (f32x2){acc[n][m][2], acc[n][m][3]};
            hp[n] = (u32x2){pk_bf16(u.x, u.y), pk_bf16(v.x, v.y)};
        }
#pragma unroll
        for (int j = 0; j < 2; ++j)
            __builtin_amdgcn_raw_buffer_store_b128((u32x4){hp[2 * j][0], hp[2 * j][1], hp[2 * j + 1][0], hp[2 * j + 1][1]}, ws,
                                                   hb + (unsigned)pn * 16384u + (unsigned)(m * 4 + wn * 2 + j) * 1024u + lane * 16, 0, 16);
        if (NT == 5) __builtin_amdgcn_raw_buffer_store_b64(hp[NT - 1], ws, hb + (unsigned)(16 + (pn >> 3)) * 16384u + m * 4096u + (unsigned)(pn & 7) * 512u + lane * 8, 0, 16);
    }
    flag_publish(ws, WS_FH + (unsigned)(qm * 32 + pn * 2 + wn) * 128u, T, lane);
    __builtin_amdgcn_s_setprio(0);
    STAMP(6);
}

template <int TLO, int THI>
__device__ __forceinline__ void conv_task(LAS unsigned char* lds, int kt, int ut, int yt, int tl, int lane, int& nb TRP) {
#define CSL() do { if (nb < 50) { BAR(); ++nb; } } while (0)
    constexpr int NTL = THI - TLO + 1, ND = THI + 2;
    const int i = lane & 15, g = lane >> 4;
    const LAS unsigned char* pk = opq(lds + kt + (i & 3) * 1088 + ((i >> 2) + g) * 16);
    const LAS unsigned char* pu = opq(lds + ut + (128 + 16 * tl - 32 * g - 4 * (i >> 2) + 4 * (i & 3)) * 2 - 32);
    bf16x8 ub[ND];
#pragma unroll
    for (int d = 0; d < ND; ++d) { if (d == 0 || d == 8) CSL(); ub[d] = cat8(tr_read(pu + 256 * d + 32), tr_read(pu + 256 * d)); }
    f32x4 acc[NTL];
#pragma unroll
    for (int t = 0; t < NTL; ++t) acc[t] = (f32x4){0.f, 0.f, 0.f, 0.f};
#pragma unroll
    for (int s = -1; s <= THI; ++s) {
        if (TLO == 0 ? (s <= 3 || s == 5 || s == 7) : (s & 1) != 0 && s < 12) CSL();
        const bf16x8 kf = *(const LAS bf16x8*)(pk + 64 * (s + 1));
#pragma unroll
        for (int t = TLO; t <= THI; ++t) if (t >= s) acc[t - TLO] = MFMA16(kf, ub[t - s], acc[t - TLO]);
    }
    LAS float* y = opq((LAS float*)(lds + yt) + 32 * g + 16 * tl + i);
    LAS float* cy = opq((LAS float*)(lds + yt + 8192) + (2 * g + tl) * 4 + (i & 3));
#define DPPF(v, ctl) __int_as_float(__builtin_amdgcn_update_dpp(0, __float_as_int(v), ctl, 0xf, 0xf, true))
#pragma unroll
    for (int t = TLO; t <= THI; ++t) {
        if (((t - TLO) % 2) == 0) CSL();
        const f32x4 a = acc[t - TLO];
        y[128 * t] = (a[0] + DPPF(a[1], 0x111)) + (DPPF(a[2], 0x112) + DPPF(a[3], 0x113));
        const float cv = DPPF(a[1], 0x10f) + (DPPF(a[2], 0x10e) + DPPF(a[3], 0x10d));
        if (i < 4) cy[32 * t] = cv;
    }
#undef DPPF
#undef CSL
}
__device__ __forceinline__ int ytab(int c) { return Z0 + c * Y_SZ; }

__global__ __launch_bounds__(512, 2) void gm_kernel(const u16* __restrict__ x, const u16* __restrict__ logA, const u16* __restrict__ Aim, const u16* __restrict__ Cre,
                                                    const u16* __restrict__ Cim, const u16* __restrict__ logdt, const u16* __restrict__ Dsk, const u16* __restrict__ win,
                                                    const u16* __restrict__ wsp, const u16* __restrict__ bsp, const u16* __restrict__ wout, const u16* __restrict__ gw,
                                                    u16* __restrict__ out, unsigned char* wsp_) {
    extern __shared__ __attribute__((aligned(16))) unsigned char lds_[];
    LAS unsigned char* lds = (LAS unsigned char*)lds_;
    const int wid = __builtin_amdgcn_readfirstlane(threadIdx.x >> 6), lane = threadIdx.x & 63;
    const int bid = blockIdx.x, xg = bid & 7, rr = bid >> 3, bi = rr >> 2, bj = rr & 3;
    const int pm = (xg >> 2) * 8 + bi, pn = (xg & 3) * 4 + bj;
    const int qm = 2 * pm + (pn >> 3), qn = pn & 7;
    const int nch = pm < 4 ? 3 : 2;
    const rsrc_t ws = __builtin_amdgcn_make_buffer_rsrc(wsp_, 0, WS_END, 0x00020000);
    STAMP(0);

    const int fr = lane & 15, fq = lane >> 4;
    const int h = wid - 6, p = wid - 4;
    const bool is_prod = (wid >> 1) == 2;
    const int ch0 = pn * 36 + pm;
    float bs[4];
    auto late = [&]() {
#pragma unroll
        for (int m = 0; m < 4; ++m) bs[m] = __uint_as_float((unsigned)bsp[pm * 128 + (wid & 1) * 64 + m * 16 + fr]);
    };
    const int phi = (-(bi + bj)) & 3;
    const unsigned rsw = (unsigned)(((lane & 7) ^ ((4 * p + (lane >> 4)) & 7)) * 16);
#define GLDS(src, dst, aux) __builtin_amdgcn_global_load_lds((const unsigned*)(src), (LAS unsigned*)(dst), 16, 0, aux)
    auto issueS = [&](int k, int s) {
        const char* sb = (const char*)wsp + (size_t)(pm * 128 + p * 8) * 4096 + k * 128; const unsigned vs = (unsigned)(lane >> 3) * 4096u + rsw;
#pragma unroll
        for (int i = 0; i < 8; ++i) GLDS(sb + (size_t)i * (16 * 4096) + vs, lds + s + (p + 2 * i) * 1024, 0); };
    auto ord2 = [&](int s) { return GM_LAG == 0 ? s : (((s & 3) == phi) ? s : ((s - GM_LAG) & 31)); };
    u32x2 E, T;
    auto mk_tag = [&]() { u64 z = (((u64)__builtin_amdgcn_readfirstlane(E[1]) << 32) | __builtin_amdgcn_readfirstlane(E[0])) + 0x9E3779B97F4A7C15ull;
        z = (z ^ (z >> 30)) * 0xBF58476D1CE4E5B9ull; z = (z ^ (z >> 27)) * 0x94D049BB133111EBull; z ^= z >> 31; T = (u32x2){(unsigned)z, (unsigned)(z >> 32)}; };
    u32x2 zp[5][4];
#define STEP(lim) do { if (nb < (lim)) { BAR(); ++nb; } } while (0)
#define STEPS_TO(lim) _Pragma("nounroll") while (nb < (lim)) { BAR(); ++nb; }
    int nb = 0;
    if (wid < nch) {
        const int kch = ch0 + 16 * wid, ki = kch * 64 + lane;
        const unsigned pA = ((const unsigned*)logA)[ki], pI = ((const unsigned*)Aim)[ki], pR = ((const unsigned*)Cre)[ki], pC = ((const unsigned*)Cim)[ki], pT = logdt[kch];
        LAS u32x2* tw = opq((LAS u32x2*)(lds + Z0 + wid * 8448) + lane);
        const LAS unsigned char* trd = opq(lds + Z0 + wid * 8448 + fr * 528 + fq * 16);
        const float dt = rbf(__expf(bf2f(pT)));
        const f32x2 ar = (f32x2){rbf(__expf(bflo(pA))), rbf(__expf(bfhi(pA)))} * dt * -1.44269504f, ai = (f32x2){bflo(pI), bfhi(pI)} * dt * 0.15915494f;
        const f32x2 cr = (f32x2){bflo(pR), bfhi(pR)} * dt, ci = (f32x2){bflo(pC), bfhi(pC)} * dt;
        float cm = fmaxf(fmaxf(fabsf(cr.x), fabsf(ci.x)), fmaxf(fabsf(cr.y), fabsf(ci.y)));
#define DPPF(v, ctl) __int_as_float(__builtin_amdgcn_update_dpp(0, __float_as_int(v), ctl, 0xf, 0xf, true))
        cm = fmaxf(cm, DPPF(cm, 0x121)); cm = fmaxf(cm, DPPF(cm, 0x122)); cm = fmaxf(cm, DPPF(cm, 0x124)); cm = fmaxf(cm, DPPF(cm, 0x128));
#undef DPPF
        { auto r = __builtin_amdgcn_permlane16_swap(__float_as_uint(cm), __float_as_uint(cm), false, false); cm = fmaxf(__uint_as_float(r[0]), __uint_as_float(r[1]));
          auto q = __builtin_amdgcn_permlane32_swap(__float_as_uint(cm), __float_as_uint(cm), false, false); cm = fmaxf(fmaxf(__uint_as_float(q[0]), __uint_as_float(q[1])), 1e-30f); }
        struct C2 { f32x2 x, y; };
        auto cexp = [&](float p_) { const f32x2 e = ar * p_, t = ai * p_;
            const f32x2 mg = {__builtin_amdgcn_exp2f(e.x), __builtin_amdgcn_exp2f(e.y)}; const float t0 = __builtin_amdgcn_fractf(t.x), t1 = __builtin_amdgcn_fractf(t.y);
            return C2{mg * (f32x2){__builtin_amdgcn_cosf(t0), __builtin_amdgcn_cosf(t1)}, mg * (f32x2){__builtin_amdgcn_sinf(t0), __builtin_amdgcn_sinf(t1)}}; };
        auto cmul = [](C2 a, C2 b) { return C2{__builtin_elementwise_fma(a.x, b.x, -(a.y * b.y)), __builtin_elementwise_fma(a.x, b.y, a.y * b.x)}; };
        auto put = [&](int row, C2 v, bool conj) { tw[row * 66] = conj ? (u32x2){pk_f16(v.x.x, -v.y.x), pk_f16(v.x.y, -v.y.y)} : (u32x2){pk_f16(v.x.x, v.y.x), pk_f16(v.x.y, v.y.y)}; };
        C2 wp[16], zr[8], vp[4], cv[17];
        wp[1] = cexp(8.f); zr[1] = cexp(1.f); vp[0] = cexp(128.f);
        tw[0] = (u32x2){pk_f16(1.f, 0.f), pk_f16(1.f, 0.f)};
#pragma unroll
        for (int j = 1; j < 16; ++j) { if (j > 1) wp[j] = cmul(wp[j >> 1], wp[j - (j >> 1)]); put(j, wp[j], true); }
        CFENCE();
        u32x4 fb[8];
#pragma unroll
        for (int ks = 0; ks < 8; ++ks) fb[ks] = *(const LAS u32x4*)(trd + ks * 64);
        CFENCE();
#pragma unroll
        for (int r = 2; r < 8; ++r) zr[r] = cmul(zr[r >> 1], zr[r - (r >> 1)]);
#pragma unroll
        for (int k = 1; k < 4; ++k) vp[k] = cmul(vp[k - 1], vp[k - 1]);
        const float rc = __builtin_amdgcn_rcpf(cm);
        cv[0] = C2{cr * rc, ci * rc};
#pragma unroll
        for (int k = 1; k < 8; ++k) { const int lg = k >= 4 ? 2 : k >= 2 ? 1 : 0; cv[k] = cmul(cv[k - (1 << lg)], vp[lg]); }
        LAS float* kb_ = (LAS float*)(lds + L_KD + wid * 3072);
        const float sc = 2.0f * cm;
        u32x4 fa[3][8];
#pragma unroll
        for (int tt = 0; tt < 3; ++tt) {
#pragma unroll
            for (int i = 0; i < (tt == 2 ? 9 : 16); ++i) {
                const int r = tt == 0 ? (i & 7) : tt == 1 ? (i < 8 ? 2 * (i & 3) : 4 * (i & 1)) : 0;
                const int hh = tt == 0 ? (i >> 3) : tt == 1 ? (i < 8 ? 2 + (i >> 2) : 4 + ((i - 8) >> 1)) : 8 + i;
                if (tt == 2) cv[hh] = cmul(cv[hh - 8], vp[3]);
                put(i, r == 0 ? cv[hh] : cmul(cv[hh], zr[r]), false);
            }
            CFENCE();
#pragma unroll
            for (int ks = 0; ks < 8; ++ks) fa[tt][ks] = *(const LAS u32x4*)(trd + ks * 64);
            CFENCE();
            SB0();
        }
        f32x4 kd[3] = {{0.f, 0.f, 0.f, 0.f}, {0.f, 0.f, 0.f, 0.f}, {0.f, 0.f, 0.f, 0.f}};
#pragma unroll
        for (int ks = 0; ks < 8; ++ks)
#pragma unroll
            for (int tt = 0; tt < 3; ++tt) kd[tt] = __builtin_amdgcn_mfma_f32_16x16x32_f16(__builtin_bit_cast(f16x8, fa[tt][ks]), __builtin_bit_cast(f16x8, fb[ks]), kd[tt], 0, 0, 0);
#pragma unroll
        for (int tt = 0; tt < 3; ++tt) {
            LAS float* kw = opq(kb_ + (tt == 0 ? 128 * (fq >> 1) + 4 * (fq & 1) + 8 * fr : tt == 1 ? 256 + 64 * fq + (fq < 2 ? 4 : 2) * fr : 512 + 64 * fq + fr));
            LAS float* kwb = tt == 1 ? opq(kw + (fq < 2 ? 2 : 32)) : kw + (tt == 0 ? 2 : 32);
            kw[0] = kd[tt][0] * sc; kw[tt == 2 ? 16 : 1] = kd[tt][1] * sc; kwb[0] = kd[tt][2] * sc; kwb[tt == 2 ? 16 : 1] = kd[tt][3] * sc;
        }
        WAITL0();
    }
#define IC(n) std::integral_constant<int, n>{}
    if (wid < 6) STAMP(10);
    if (is_prod) {
        const unsigned vx = (unsigned)(lane >> 3) * 1152u + rsw;
        unsigned wv[9];
#pragma unroll
        for (int i = 0; i < 9; ++i) { const int c = (p + 2 * i) * 64 + lane, rho = c / 18, ch = c - rho * 18;
            const int kap = (rho & 32) + 8 * ((rho >> 2) & 3) + 4 * ((rho >> 4) & 1) + (rho & 3); wv[i] = (unsigned)kap * 9216u + (unsigned)ch * 16u; }
        const char* xb = (const char*)x + (size_t)(pm * 128 + p * 8) * 1152;
        const char* wb = (const char*)win + pn * 288;
        auto issueX = [&](int k, int s) {
#pragma unroll
            for (int i = 0; i < 8; ++i) GLDS(xb + (size_t)i * (16 * 1152) + k * 128 + vx, lds + s + (p + 2 * i) * 1024, 0); };
        auto issueW = [&](int k, int coloff, int s) {
            const char* b = wb + (size_t)k * (64 * 9216) + coloff;
#pragma unroll
            for (int i = 0; i < 9; ++i) GLDS(b + wv[i], lds + s + BOFF + (p + 2 * i) * 1024, 0); };
        auto ord1 = [&](int s) { return (GM_LAG == 0 || (s & 3) == phi) ? s : (s >= 4 ? s - 4 : (s == 0 ? 8 : s + 4)); };
        issueX(ord1(0), 0); issueW(ord1(0), FH * 2, 0); issueX(ord1(1), STG); issueW(ord1(1), FH * 2, STG);
        issueX(ord1(2), 2 * STG); issueW(ord1(2), FH * 2, 2 * STG);
        E = flag_ld(ws, WS_E);
        int so = 0;
        WAITV(35); PBAR();
#pragma nounroll
        for (int S = 1; S < 16; ++S) {
            WAITV(17); PBAR();
            const int t = S + 2, k = ord1(t >= 9 ? t - 9 : t);
            issueX(k, so); issueW(k, t >= 9 ? 0 : FH * 2, so); so = nextbuf(so);
        }
        WAITV(17); PBAR(); issueS(ord2(0), 0);
        WAITV(8); PBAR(); issueS(ord2(1), STG);
        STAMP(1);
    } else if (wid < 4) {
        E = flag_ld(ws, WS_E);
        if (wid >> 1) consumer_a<4>(lds, zp, late, wid & 1, 1, lane, wsp_ TRA);
        else consumer_a<5>(lds, zp, late, wid & 1, 0, lane, wsp_ TRA);
    } else {
        if (h == 0 && lane < 12) *(LAS unsigned*)(lds + L_MISC + lane * 4) = 0u;
        const int row0 = lane / 9, chk0 = lane - row0 * 9;
        const int u = (pn >> 3) * 16 + pm, nblk = u < 8 ? 3 : 2;
        u32x2 uc[18]; u32x4 xv[9], gv[9], wv[3][5];
        if (h == 0) {
            { const char* xb = (const char*)x + (size_t)pm * 128 * 1152 + pn * 72; unsigned off = row0 * 1152 + chk0 * 8; int ch = chk0;
#pragma unroll
              for (int i = 0; i < 9; ++i) { uc[i] = *(const u32x2*)(xb + off); uc[9 + i] = *(const u32x2*)(xb + 64 * 1152 + off); off += 7 * 1152 + 8; if (++ch == 9) { ch = 0; off += 1152 - 72; } } }
            { const char* xr = (const char*)x + (size_t)(pm * 128 + pn * 8 + (lane >> 3)) * 1152 + (lane & 7) * 16; const char* gr = (const char*)gw + (lane & 7) * 16;
#pragma unroll
              for (int i = 0; i < 9; ++i) { xv[i] = *(const u32x4*)(xr + i * 128); gv[i] = *(const u32x4*)(gr + i * 128); } }
        } else {
            unsigned wo_[5], hi_[5];
            { int row = row0, ch = chk0;
#pragma unroll
              for (int i = 0; i < 5; ++i) { const int rw = row < 32 ? row : 31; wo_[i] = (rw & 15) * 1152 + ch * 16; hi_[i] = rw >> 4; row += 7; if (++ch == 9) { ch = 0; ++row; } } }
#pragma unroll
            for (int b = 0; b < 3; ++b) {
                const int blk = (b < nblk) ? u + 32 * b : u, st = blk >> 2, jb = blk & 3;
                const int fA = st < 16 ? st * 144 + (jb < 2 ? 32 * jb : 16 + 32 * jb) : ((st - 16) * 8 + 2 * jb) * 144 + 64, dB = (st < 16 ? 16 : 144) * 1152;
                const char* wb_ = (const char*)wout + (size_t)fA * 1152 + qn * 144;
#pragma unroll
                for (int i = 0; i < 5; ++i) wv[b][i] = *(const u32x4*)(wb_ + (wo_[i] + hi_[i] * dB));
            }
        }
        E = flag_ld(ws, WS_E);
        if (lane < 24) *(LAS u32x4*)(lds + L_KT + (lane >> 3) * KT_SZ + (2 * h + ((lane >> 2) & 1)) * 1088 + (lane & 3) * 16) = (u32x4){0u, 0u, 0u, 0u};
        WAITL0();
        STAMP(10);
        __builtin_amdgcn_s_setprio(GM_HPRIO);
        {
            float kv[2][3];
#pragma nounroll
            for (int ei = 0; ei < 9; ++ei) {
                BAR();
                if (ei > 0) {
                    LAS unsigned char* kt = opq(lds + L_KT + (2 * h + ((ei - 1) >> 2)) * 1088 + 64 + ((ei - 1) & 3) * 256 + lane * 2);
#pragma unroll
                    for (int j = 0; j < 2; ++j)
#pragma unroll
                        for (int c = 0; c < 3; ++c) *(LAS u16*)(kt + c * KT_SZ + j * 128) = (u16)pk_bf16(kv[j][c], 0.f);
                }
#pragma unroll
                for (int j = 0; j < 2; ++j) {
                    const int jj = (ei & 3) * 2 + j, e = jj >= 4 ? 3 : jj >= 2 ? 2 : jj, lag = 256 * jj + 4 * lane + 2 * h + ((ei >> 2) & 1);
                    const int idx = e == 0 ? lag : 128 * e + ((lag + (1 << (e - 1)) - 1 + ((lag >> e) & 1)) >> e);
                    const LAS float* q = opq((const LAS float*)(lds + L_KD) + idx);
#pragma unroll
                    for (int c = 0; c < 3; ++c) kv[j][c] = q[c * 768];
                }
            }
        }
        if (h == 0) {
            LAS unsigned char* pzs = opq(lds + Z0 + lane * 8);
#pragma unroll
            for (int i = 0; i < 18; ++i) *(LAS u32x2*)(pzs + i * 512) = uc[i];
            CFENCE();
            { const LAS unsigned char* pt = opq(lds + Z0 + (8 * (4 * fq + (lane & 3)) + (fr >> 2)) * 72);
              const unsigned ub = WS_UT + (unsigned)(pn * 36 + (lane & 3)) * 4096u + (unsigned)(pm * 128 + 8 * (4 * fq + (fr >> 2))) * 2u;
#pragma unroll
              for (int cc = 0; cc < 9; ++cc) __builtin_amdgcn_raw_buffer_store_b128(__builtin_bit_cast(u32x4, cat8(tr_read(pt + cc * 8), tr_read(pt + 288 + cc * 8))), ws, ub + cc * 16384u, 0, 16); }
            float a = 0.f;
#pragma unroll
            for (int i = 0; i < 9; ++i)
#pragma unroll
                for (int j = 0; j < 4; ++j) { const unsigned xa = xv[i][j], ga = gv[i][j]; a = __builtin_amdgcn_fdot2_f32_bf16(__builtin_bit_cast(bf16x2, xa), __builtin_bit_cast(bf16x2, ga), a, false); }
#define DPPF(v, ctl) __int_as_float(__builtin_amdgcn_update_dpp(0, __float_as_int(v), ctl, 0xf, 0xf, true))
            a += DPPF(a, 0xb1); a += DPPF(a, 0x4e); a += DPPF(a, 0x141);
#undef DPPF
            const float gt = rbf(__builtin_amdgcn_rcpf(1.0f + __expf(-rbf(a))));
            if ((lane & 7) == 0) __builtin_amdgcn_raw_buffer_store_b32(__float_as_uint(gt), ws, WS_GATE + (unsigned)(pm * 128 + pn * 8 + (lane >> 3)) * 4u, 0, 16);
        } else {
            LAS unsigned char* pws = opq(lds + Z0 + 9216 + lane * 16);
            const LAS unsigned char* ptw = opq(lds + Z0 + 9216 + (4 * fq + (fr >> 2)) * 144 + (lane & 3) * 8);
#pragma unroll
            for (int b = 0; b < 3; ++b) {
                if (b < nblk) {
#pragma unroll
                    for (int i = 0; i < 5; ++i) if (i < 4 || lane < 32) *(LAS u32x4*)(pws + i * 1024) = wv[b][i];
                    CFENCE();
                    const unsigned ob = WS_WOF + (unsigned)qn * (18u * 18432u) + (unsigned)(u + 32 * b) * 4608u;
#pragma unroll
                    for (int nn = 0; nn < 5; ++nn) {
                        const u32x4 v = __builtin_bit_cast(u32x4, cat8(tr_read(ptw + nn * 32), tr_read(ptw + 2304 + nn * 32)));
                        if (nn < 4) __builtin_amdgcn_raw_buffer_store_b128(v, ws, ob + nn * 1024 + lane * 16, 0, 16);
                        else if (fr < 8) __builtin_amdgcn_raw_buffer_store_b128(v, ws, ob + 4096 + (fq * 8 + fr) * 16, 0, 16);
                    }
                    CFENCE();
                }
            }
        }
        nb = 9;
        mk_tag();
        WAITL0();
        STEPS_TO(10);
        STAMP(1);
        {
            const f32x4 sp = *(const LAS f32x4*)(lds + L_SP + (h * 64 + lane) * 16);
            const float dm = sp[0] - sp[2];
            const f32x2 mine = {(80.f * sp[0] + 64.f * sp[2]) * (1.0f / 144.f), sp[1] + sp[3] + (80.f * 64.f / 144.f) * dm * dm};
            __builtin_amdgcn_raw_buffer_store_b128((u32x4){T[0], __float_as_uint(mine.x), T[1], __float_as_uint(mine.y)}, ws, WS_ST + (unsigned)((pm * 16 + pn) * 128 + h * 64 + lane) * 16u, 0, 16);
            STEP(18);
            const unsigned so_ = WS_ST + (unsigned)(pm * 16 * 128 + h * 64 + lane) * 16u;
            const u64 t0 = __builtin_amdgcn_s_memrealtime();
            u32x4 st[16];
            for (;;) {
                CFENCE();
#pragma unroll
                for (int j = 0; j < 16; ++j) st[j] = __builtin_amdgcn_raw_buffer_load_b128(ws, so_ + j * 2048u, 0, 16);
                STEP(18);
                unsigned bad = 0;
#pragma unroll
                for (int j = 0; j < 16; ++j) bad |= (st[j][0] ^ T[0]) | (st[j][2] ^ T[1]);
                if (__builtin_amdgcn_ballot_w64(bad != 0u) == 0ull) break;
                if (__builtin_amdgcn_s_memrealtime() - t0 > 300000ull) break;
            }
            STAMP(2);
            STEP(18);
            float mean = 0.f, m2 = 0.f;
#pragma unroll
            for (int j = 0; j < 16; ++j) mean += __uint_as_float(st[j][1]);
            mean *= (1.0f / 16.f);
#pragma unroll
            for (int j = 0; j < 16; ++j) { const float d = __uint_as_float(st[j][1]) - mean; m2 += __uint_as_float(st[j][3]) + 144.f * d * d; }
            const float rstd = __builtin_amdgcn_rsqf(m2 * (1.0f / FH) + 1e-5f);
            *(LAS f32x2*)(lds + L_SP + (h * 64 + lane) * 16) = (f32x2){rstd, -mean * rstd};
            WAITL0();
            if (lane == 0) *(volatile LAS unsigned*)(lds + L_MISC + 4 + h * 4) = 1u;
            const volatile LAS unsigned* zd = (const volatile LAS unsigned*)(lds + L_MISC + 16 + h * 8 + (lane & 1) * 4);
            for (;;) {
                STEP(18);
                if (__builtin_amdgcn_ballot_w64(*zd == 0u) == 0ull) break;
                if (__builtin_amdgcn_s_memrealtime() - t0 > 800000ull) break;
            }
            CFENCE();
            const LAS unsigned char* ptz = opq(lds + Z0 + (h * 64 + 8 * fq + (fr >> 2)) * ZST + (lane & 3) * 8);
            const unsigned zo = WS_Z2F + (unsigned)((pn * 64 + pm * 4 + h * 2) * 9) * 1024u + lane * 16;
#pragma unroll
            for (int i = 0; i < 18; ++i) {
                const LAS unsigned char* pt = ptz + (i / 9) * 32 * ZST + (i % 9) * 32;
                __builtin_amdgcn_raw_buffer_store_b128(__builtin_bit_cast(u32x4, cat8(tr_read(pt), tr_read(pt + 4 * ZST))), ws, zo + i * 1024, 0, 16);
                if (i == 5 || i == 11) STEP(18);
            }
            STEP(18);
            flag_publish(ws, WS_FZ + (unsigned)(pn * 32 + pm * 2 + h) * 128u, T, lane);
            STAMP(3);
            STEPS_TO(18);
            __builtin_amdgcn_s_setprio(0);
        }
    }

    if (is_prod) {
        mk_tag();
        const char* lin = (const char*)wsp_ + p * 1024 + lane * 16;
        auto issueLA = [&](unsigned off, int s) {
#pragma unroll
            for (int i = 0; i < 8; ++i) GLDS(lin + off + i * 2048, lds + s + (p + 2 * i) * 1024, 16); };
        auto issueLB = [&](unsigned off, int s) {
#pragma unroll
            for (int i = 0; i < 9; ++i) GLDS(lin + off + i * 2048, lds + s + BOFF + (p + 2 * i) * 1024, 16); };
        auto ord3 = [&](int s) { return (GM_LAG == 0 || s >= 16) ? s : (((s & 3) == phi) ? s : ((s - GM_LAG) & 15)); };
        const unsigned zb = WS_Z2F + (unsigned)pn * (64u * 9216u);
        flag_wait(ws, WS_FZ + (unsigned)pn * 32u * 128u, 32, T, lane);
        STAMP(2);
        issueLB(zb + (unsigned)ord2(0) * 18432u, 0); issueLB(zb + (unsigned)ord2(1) * 18432u, STG);
        WAITV(9); PBAR();
        int so = 2 * STG;
        { const int k = ord2(2); issueS(k, so); issueLB(zb + (unsigned)k * 18432u, so); so = nextbuf(so); }
#pragma nounroll
        for (int S = 19; S < 48; ++S) {
            WAITV(17); PBAR();
            const int k = ord2(S - 16); issueS(k, so); issueLB(zb + (unsigned)k * 18432u, so); so = nextbuf(so);
        }
        const unsigned hb = WS_HF + (unsigned)qm * (18u * 16384u), ob = WS_WOF + (unsigned)qn * (18u * 18432u);
        WAITV(17); PBAR();
        flag_wait(ws, WS_FZ + (unsigned)(pn ^ 8) * 32u * 128u, 32, T, lane);
        issueLB(ob + (unsigned)ord3(0) * 18432u, 2 * STG);
        PBAR();
        issueLB(ob + (unsigned)ord3(1) * 18432u, 0);
        STAMP(3);
        flag_wait(ws, WS_FH + (unsigned)qm * 32u * 128u, 32, T, lane);
        STAMP(4);
        issueLA(hb + (unsigned)ord3(0) * 16384u, 2 * STG); issueLA(hb + (unsigned)ord3(1) * 16384u, 0);
        WAITV(8); PBAR();
        so = STG;
        { const int k = ord3(2); issueLA(hb + (unsigned)k * 16384u, so); issueLB(ob + (unsigned)k * 18432u, so); so = nextbuf(so); }
#pragma nounroll
        for (int S = 51; S < 66; ++S) {
            WAITV(17); PBAR();
            const int k = ord3(S - 48); issueLA(hb + (unsigned)k * 16384u, so); issueLB(ob + (unsigned)k * 18432u, so); so = nextbuf(so);
        }
        WAITV(17); PBAR();
        WAITV(0); PBAR();
        PBAR();
        STAMP(5); PDUMP();
        if (bid == 0 && p == 0 && lane == 0) __builtin_amdgcn_raw_buffer_store_b64(T, ws, WS_E, 0, 16);
        return;
    }

    if (wid < 4) {
        mk_tag();
        if (wid >> 1) consumer_b<4>(lds, ws, T, zp, bs, pm, pn, wid & 1, 1, lane, wsp_ TRA);
        else consumer_b<5>(lds, ws, T, zp, bs, pm, pn, wid & 1, 0, lane, wsp_ TRA);
    } else {
        __builtin_amdgcn_s_setprio(GM_HPRIO);
        STEPS_TO(19);
        u32x4 uu[3][2];
#pragma unroll
        for (int c = 0; c < 3; ++c)
#pragma unroll
            for (int j = 0; j < 2; ++j) uu[c][j] = __builtin_amdgcn_raw_buffer_load_b128(ws, WS_UT + (unsigned)(ch0 + (c < nch ? 16 * c : 0)) * 4096u + h * 2048 + j * 1024 + lane * 16, 0, 16);
        STEPS_TO(22);
#pragma unroll
        for (int c = 0; c < 3; ++c) {
            BAR(); ++nb;
#pragma unroll
            for (int j = 0; j < 2; ++j) *(LAS u32x4*)(lds + L_UT + c * UT_SZ + 256 + h * 2048 + j * 1024 + lane * 16) = uu[c][j];
            if (lane < 16) *(LAS u32x4*)(lds + L_UT + c * UT_SZ + h * 4352 + lane * 16) = (u32x4){0u, 0u, 0u, 0u};
        }
        WAITL0();
        BAR(); ++nb;
        STAMP(4);
    }

    if (wid < 4) nb = 50;
    for (;;) {
        if (nb > 45) STEPS_TO(50);
        unsigned tk = 0;
        if (lane == 0) tk = __hip_atomic_fetch_add((LAS unsigned*)(lds + L_MISC), 1u, __ATOMIC_RELAXED, __HIP_MEMORY_SCOPE_WORKGROUP);
        tk = __builtin_amdgcn_readfirstlane(tk);
        if (tk >= (unsigned)(4 * nch)) break;
        const int c = tk >> 2, tl = tk & 1;
        if (tk & 2) conv_task<0, 10>(lds, L_KT + c * KT_SZ, L_UT + c * UT_SZ, ytab(c), tl, lane, nb TRA);
        else conv_task<11, 15>(lds, L_KT + c * KT_SZ, L_UT + c * UT_SZ, ytab(c), tl, lane, nb TRA);
    }
    STEPS_TO(50);
    WAITL0();
    STAMP(7);

    if (wid < 4) {
        const int kq = wid >> 1, mh = wid & 1;
        f32x4 acc[5][2];
        struct F3 { s16x4 alo[4], ahi[4]; bf16x8 b[10]; } f0, f1;
        const LAS unsigned char* ph = lds + 2 * STG + mh * 8192 + kq * 2048 + lane * 16;
        const LAS unsigned char* pw = lds + 2 * STG + BOFF + kq * 9216 + lane * 16;
        const LAS unsigned char* pw4 = lds + 2 * STG + BOFF + kq * 9216 + 4096 + (fq * 8 + (fr & 7)) * 16;
        auto rd3 = [&](F3& f, int i, bool single) {
            const int j = i / 7, r = i % 7;
            if (r < 2) {
                if (!single) { const bf16x8 v = *(const LAS bf16x8*)(ph + r * 4096 + j * 1024); f.alo[j * 2 + r] = __builtin_shufflevector(v, v, 0, 1, 2, 3); f.ahi[j * 2 + r] = __builtin_shufflevector(v, v, 4, 5, 6, 7); }
                else { const LAS unsigned char* q = ph - lane * 8 + r * 4096 + j * 1024; f.alo[j * 2 + r] = *(const LAS s16x4*)q; f.ahi[j * 2 + r] = *(const LAS s16x4*)(q + 512); }
            } else if (r < 6) f.b[j * 5 + r - 2] = *(const LAS bf16x8*)(pw + j * 4608 + (r - 2) * 1024);
            else f.b[j * 5 + 4] = *(const LAS bf16x8*)(pw4 + j * 4608);
        };
        int so = 2 * STG;
        auto step3 = [&](const F3& c, F3& l, int mode  , bool first) {
            const int delta = so == 2 * STG ? -2 * STG : STG; so += delta;
#pragma unroll
            for (int i = 0; i < 20; ++i) {
                const int j = i / 10, n = (i % 10) >> 1, m = i & 1;
                acc[n][m] = MFMA16(c.b[j * 5 + n], cat8(c.alo[j * 2 + m], c.ahi[j * 2 + m]), (first && j == 0) ? ((f32x4){0.f, 0.f, 0.f, 0.f}) : acc[n][m]);
                if (mode < 2 && i < 7) { rd3(l, 2 * i, mode == 1); rd3(l, 2 * i + 1, mode == 1); }
                if (mode < 2) { if (i == 8) { ph += delta; asm volatile("" : "+v"(ph)); } if (i == 9) { pw += delta; asm volatile("" : "+v"(pw)); } if (i == 10) { pw4 += delta; asm volatile("" : "+v"(pw4)); } }
                SB0();
            }
        };
        __builtin_amdgcn_s_waitcnt(0xC07F);
        BAR();
#pragma unroll
        for (int i = 0; i < 14; ++i) rd3(f0, i, false);
        ph -= 2 * STG; pw -= 2 * STG; pw4 -= 2 * STG; so = 0;
        WAITL0(); BAR(); step3(f0, f1, 0, true);
#pragma nounroll
        for (int j = 0; j < 7; ++j) {
            WAITL0(); BAR(); step3(f1, f0, 0, false);
            WAITL0(); BAR(); step3(f0, f1, 0, false);
        }
        WAITL0(); BAR(); step3(f1, f0, 1, false);
        WAITL0(); BAR(); step3(f0, f1, 1, false);
        step3(f1, f0, 2, false);
        STAMP(8);
        LAS unsigned char* prw = opq(lds + L_R + wid * 5120 + lane * 16);
#pragma unroll
        for (int n = 0; n < 5; ++n) { const f32x4 x0 = acc[n][0], x1 = acc[n][1]; *(LAS f32x4*)(prw + n * 1024) = kq ? x0 : x1; acc[n][0] = kq ? x1 : x0; }
        WAITL0();
        BAR();
        const int m = mh * 2 + kq;
        const float g = *(const LAS float*)(lds + L_MISC + 64 + (16 * m + fr) * 4), g1 = rbf(1.0f - g);
        const LAS unsigned char* prr = opq(lds + L_R + (wid ^ 2) * 5120 + lane * 16);
        const LAS unsigned char* pat = opq(lds + L_AT + (4 * fq + (fr >> 2)) * 128 + (16 * m + 4 * (lane & 3)) * 2);
#pragma unroll
        for (int n = 0; n < 5; ++n) {
            const f32x4 b = acc[n][0] + *(const LAS f32x4*)(prr + n * 1024);
            const u32x2 a = __builtin_bit_cast(u32x2, tr_read(pat + n * 2048));
            const u32x2 o = {pk_bf16(g * bflo(a[0]) + g1 * b[0], g * bfhi(a[0]) + g1 * b[1]), pk_bf16(g * bflo(a[1]) + g1 * b[2], g * bfhi(a[1]) + g1 * b[3])};
            if (n < 4 || fq < 2) *(u32x2*)((char*)out + (size_t)(qm * 64 + 16 * m + fr) * 1152 + (qn * 72 + 16 * n + 4 * fq) * 2) = o;
        }
    } else {
        int d = pn * 36 + pm;
#pragma nounroll
        for (int c = 0; c < nch; ++c) {
            const float dsk = bf2f(Dsk[d]);
#pragma unroll
            for (int k = 0; k < 2; ++k) {
                BAR(); ++nb;
                const int l0 = h * 1024 + k * 512 + lane * 8;
                f32x4 cv = *(const LAS f32x4*)(lds + ytab(c) + 8192 + (l0 >= 16 ? (l0 >> 4) - 1 : 0) * 16);
                if ((lane & 1) || l0 == 0) cv = (f32x4){0.f, 0.f, 0.f, 0.f};
                const f32x4 y0 = *(const LAS f32x4*)(lds + ytab(c) + l0 * 4) + cv, y1 = *(const LAS f32x4*)(lds + ytab(c) + l0 * 4 + 16);
                const u32x4 uv = *(const LAS u32x4*)(lds + L_UT + c * UT_SZ + 256 + l0 * 2);
                u32x4 o;
#pragma unroll
                for (int j = 0; j < 4; ++j) {
                    const float ya = j < 2 ? y0[2 * j] : y1[2 * j - 4], yb = j < 2 ? y0[2 * j + 1] : y1[2 * j - 3];
                    const unsigned yp = pk_bf16(ya, yb), up = pk_bf16(bflo(uv[j]) * dsk, bfhi(uv[j]) * dsk);
                    o[j] = pk_bf16(bflo(yp) + bflo(up), bfhi(yp) + bfhi(up));
                }
                __builtin_amdgcn_raw_buffer_store_b128(o, ws, WS_AT + (unsigned)d * 4096u + l0 * 2, 0, 16);
            }
            d += 16;
        }
        STEPS_TO(58);
        flag_publish(ws, WS_FA + (unsigned)((pn >> 1) * 64 + (pn & 1) * 32 + pm * 2 + h) * 128u, T, lane);
        STAMP(5);
        STEPS_TO(62);
        if (h == 0) {
            const unsigned fo = WS_FA + (unsigned)(qn * 64 + lane) * 128u;
            const u64 t0 = __builtin_amdgcn_s_memrealtime();
            for (;;) {
                CFENCE();
                const u32x2 v = flag_ld(ws, fo);
                STEP(68); STEP(68);
                if (flag_all(v, T, true)) break;
                if (__builtin_amdgcn_s_memrealtime() - t0 > 300000ull) break;
            }
            ACQ();
            u32x4 av[9];
#pragma unroll
            for (int i = 0; i < 9; ++i) av[i] = __builtin_amdgcn_raw_buffer_load_b128(ws, WS_AT + (unsigned)(qn * 72 + i * 8 + (lane >> 3)) * 4096u + qm * 128 + (lane & 7) * 16, 0, 16);
            STEP(68); STEP(68);
            LAS unsigned char* pa_ = opq(lds + L_AT + lane * 16);
#pragma unroll
            for (int i = 0; i < 9; ++i) *(LAS u32x4*)(pa_ + i * 1024) = av[i];
        } else {
            const unsigned gv = __builtin_amdgcn_raw_buffer_load_b32(ws, WS_GATE + (unsigned)(qm * 64 + lane) * 4u, 0, 16);
            BAR(); BAR(); nb += 2;
            *(LAS unsigned*)(lds + L_MISC + 64 + lane * 4) = gv;
        }
        STEPS_TO(68);
        WAITL0();
        BAR();
    }
    STAMP(9); TRDUMP();
}
}

extern "C" void kernel_launch(void* const* d_in, const int* in_sizes, int n_in, void* d_out, int out_size, void* d_ws, size_t ws_size, hipStream_t stream) {
    (void)in_sizes; (void)n_in; (void)out_size; (void)ws_size;
    (void)hipFuncSetAttribute((const void*)gm::gm_kernel, hipFuncAttributeMaxDynamicSharedMemorySize, gm::LDS_BYTES);
    hipLaunchKernelGGL(gm::gm_kernel, dim3(256), dim3(512), gm::LDS_BYTES, stream, (const u16*)d_in[0], (const u16*)d_in[1], (const u16*)d_in[2], (const u16*)d_in[3],
                       (const u16*)d_in[4], (const u16*)d_in[5], (const u16*)d_in[6], (const u16*)d_in[8], (const u16*)d_in[9], (const u16*)d_in[10],
                       (const u16*)d_in[11], (const u16*)d_in[12], (u16*)d_out, (unsigned char*)d_ws);
}
```

```cpp
#define GM_PF 1
#include <hip/hip_runtime.h>
#include <stdint.h>
#include <type_traits>

#define LAS __attribute__((address_space(3)))
typedef unsigned short u16;
typedef unsigned long long u64;
typedef short bf16x8 __attribute__((ext_vector_type(8)));
typedef short s16x4 __attribute__((ext_vector_type(4)));
typedef _Float16 f16x8 __attribute__((ext_vector_type(8)));
typedef _Float16 f16x2 __attribute__((ext_vector_type(2)));
typedef __bf16 bf16x2 __attribute__((ext_vector_type(2)));
typedef float f32x4 __attribute__((ext_vector_type(4)));
typedef float f32x2 __attribute__((ext_vector_type(2)));
typedef unsigned u32x4 __attribute__((ext_vector_type(4)));
typedef unsigned u32x2 __attribute__((ext_vector_type(2)));
typedef __amdgpu_buffer_rsrc_t rsrc_t;

namespace gm {
constexpr int SEQ = 2048, DM = 576, FH = 2304;
constexpr unsigned WS_E = 0, WS_FS = 4096, WS_FZ = WS_FS + 65536, WS_FH = WS_FZ + 65536, WS_FA = WS_FH + 131072;
constexpr unsigned WS_ST = 0x60000, WS_GATE = 0xE0000, WS_DBG = 0x1E00000, WS_Z2F = 0x100000, WS_HF = 0xA00000, WS_WOF = 0x1300000, WS_UT = 0x1600000, WS_AT = 0x1900000, WS_END = 0x1C00000, WS_TRC = 0x1C00000;
constexpr int STG = 34816, BOFF = 16384, Z0 = 3 * STG, ZST = 296, T0 = Z0 + 128 * ZST;
constexpr int KT_SZ = 4352, UT_SZ = 4608, Y_SZ = 10240;
constexpr int L_MISC = 163840 - 320;
constexpr int L_KT = L_MISC - 3 * KT_SZ, L_SP = T0, L_KD = Z0 + 3 * 8448, L_UT = Z0 + 3 * Y_SZ;
constexpr int L_AT = Z0, L_R = Z0 + 10240;
constexpr int LDS_BYTES = 163840;
static_assert(L_UT + 3 * UT_SZ <= L_KT && L_KD + 3 * 3072 <= T0 && L_SP + 2048 <= L_KT, "lds map");
#define GM_HPRIO 1
#define GM_LAG 4

__device__ __forceinline__ float bf2f(unsigned v) { return __uint_as_float(v << 16); }
__device__ __forceinline__ float bflo(unsigned w) { return __uint_as_float(w << 16); }
__device__ __forceinline__ float bfhi(unsigned w) { return __uint_as_float(w & 0xffff0000u); }
__device__ __forceinline__ unsigned pk_bf16(float a, float b) { f32x2 v = {a, b}; return __builtin_bit_cast(unsigned, __builtin_convertvector(v, bf16x2)); }
__device__ __forceinline__ unsigned pk_f16(float a, float b) { f32x2 v = {a, b}; return __builtin_bit_cast(unsigned, __builtin_convertvector(v, f16x2)); }
__device__ __forceinline__ float rbf(float a) { return bflo(pk_bf16(a, a)); }
__device__ __forceinline__ f32x2 gelu2(f32x2 x) {
    const f32x2 w = (x * x) * (-0.10294324f) + (-2.30220819f);
    const f32x2 a = w * x;
    f32x2 e; e.x = __builtin_amdgcn_exp2f(a.x); e.y = __builtin_amdgcn_exp2f(a.y);
    e = e + 1.0f;
    f32x2 r; r.x = __builtin_amdgcn_rcpf(e.x); r.y = __builtin_amdgcn_rcpf(e.y);
    return x * r;
}
__device__ __forceinline__ f32x4 gelu4(f32x4 v) { const f32x2 a = gelu2((f32x2){v[0], v[1]}), b = gelu2((f32x2){v[2], v[3]}); return (f32x4){a.x, a.y, b.x, b.y}; }
__device__ __forceinline__ float allred_q(float v) {
    unsigned u = __float_as_uint(v);
    auto r = __builtin_amdgcn_permlane16_swap(u, u, false, false);
    v = __uint_as_float(r[0]) + __uint_as_float(r[1]);
    u = __float_as_uint(v);
    auto s = __builtin_amdgcn_permlane32_swap(u, u, false, false);
    return __uint_as_float(s[0]) + __uint_as_float(s[1]);
}
#define ACQ() __builtin_amdgcn_fence(__ATOMIC_ACQUIRE, "wavefront")
#define TRP
#define TRA
#define TRDUMP() do { } while (0)
#define BAR() __builtin_amdgcn_s_barrier()
#define PBAR() BAR()
#define PDUMP() TRDUMP()
#define WAITV(n) asm volatile("s_waitcnt vmcnt(" #n ")" ::: "memory")
#define WAITL0() asm volatile("s_waitcnt lgkmcnt(0)" ::: "memory")
#define CFENCE() asm volatile("" ::: "memory")
#define SGB(mask, n, id) __builtin_amdgcn_sched_group_barrier(mask, n, id)
#define MFMA16(a, b, c) __builtin_amdgcn_mfma_f32_16x16x32_bf16(a, b, c, 0, 0, 0)
__device__ __forceinline__ s16x4 tr_read(const LAS unsigned char* p) { return __builtin_amdgcn_ds_read_tr16_b64_v4i16((LAS s16x4*)p); }
__device__ __forceinline__ bf16x8 cat8(s16x4 a, s16x4 b) { return __builtin_shufflevector(a, b, 0, 1, 2, 3, 4, 5, 6, 7); }
template <class P> __device__ __forceinline__ P opq(P p) { asm volatile("" : "+v"(p)); return p; }
__device__ __forceinline__ int nextbuf(int so) { return so == 2 * STG ? 0 : so + STG; }

#define STAMP(i) do { } while (0)

__device__ __forceinline__ u32x2 flag_ld(rsrc_t ws, unsigned off) { return __builtin_amdgcn_raw_buffer_load_b64(ws, off, 0, 16); }
__device__ __forceinline__ bool flag_all(u32x2 v, u32x2 T, bool active) { return __builtin_amdgcn_ballot_w64(active && (v[0] != T[0] || v[1] != T[1])) == 0ull; }
__device__ __forceinline__ void flag_publish(rsrc_t ws, unsigned off, u32x2 T, int lane) {
    WAITV(0);
    if (lane == 0) __builtin_amdgcn_raw_buffer_store_b64(T, ws, off, 0, 16);
}
__device__ __forceinline__ void flag_wait(rsrc_t ws, unsigned base, int n, u32x2 T, int lane) {
    const unsigned off = base + (unsigned)(lane < n ? lane : 0) * 128u;
    const u64 t0 = __builtin_amdgcn_s_memrealtime();
    for (;;) {
        CFENCE();
        const u32x2 v = flag_ld(ws, off);
        if (flag_all(v, T, true)) break;
        if (__builtin_amdgcn_s_memrealtime() - t0 > 300000ull) break;
        __builtin_amdgcn_s_sleep(1);
    }
    ACQ();
}

#define SB0() __builtin_amdgcn_sched_barrier(0)
template <int NT> struct Frag { bf16x8 a[4]; s16x4 lo[NT], hi[NT]; };
template <int NT, bool TR, int HALF>
__device__ __forceinline__ void frag_read(Frag<NT>& f, int i, const LAS unsigned char* pa, const LAS unsigned char* pb) {
    if (i < 4) f.a[i] = *(const LAS bf16x8*)(pa + i * 2048);
    else if (TR) { const int n = (i - 4) >> 1; if ((i - 4) & 1) f.hi[n] = tr_read(pb + HALF * 9216 + 4608 + n * 32); else f.lo[n] = tr_read(pb + HALF * 9216 + n * 32); }
    else { const int n = i - 4; const bf16x8 v = *(const LAS bf16x8*)(pb + HALF * 9216 + n * 1024);
           f.lo[n] = __builtin_shufflevector(v, v, 0, 1, 2, 3); f.hi[n] = __builtin_shufflevector(v, v, 4, 5, 6, 7); }
}
template <int NT, bool TR, int LHALF, int RPS, bool FIRST, bool UPD>
__device__ __forceinline__ void half_step(f32x4 (&acc)[NT][4], const Frag<NT>& c, Frag<NT>& l, const LAS unsigned char*& pa0, const LAS unsigned char*& pa1, const LAS unsigned char*& pb, int delta, const float* ini = nullptr) {
    constexpr int NRD = TR ? 4 + 2 * NT : 4 + NT, NM = 4 * NT, LAST = (NRD + RPS - 1) / RPS;
#pragma unroll
    for (int i = 0; i < NM; ++i) {
        const int n = i >> 2, m = i & 3;
        acc[n][m] = MFMA16(cat8(c.lo[n], c.hi[n]), c.a[m], FIRST ? (TR ? ((f32x4){0.f, 0.f, 0.f, 0.f}) : ((f32x4){ini[m], ini[m], ini[m], ini[m]})) : acc[n][m]);
        if (LHALF >= 0) {
#pragma unroll
            for (int r = 0; r < RPS; ++r) if (i * RPS + r < NRD) frag_read<NT, TR, (LHALF > 0 ? 1 : 0)>(l, i * RPS + r, LHALF > 0 ? pa1 : pa0, pb);
        }
        if (UPD) { if (i == LAST) { pa0 += delta; asm volatile("" : "+v"(pa0)); } if (i == LAST + 1) { pb += delta; asm volatile("" : "+v"(pb)); } if (i == LAST + 2) { pa1 += delta; asm volatile("" : "+v"(pa1)); } }
        SB0();
    }
}
template <int NT, bool TR, class HK>
__device__ __forceinline__ void gemm_phase(int nsteps, f32x4 (&acc)[NT][4], int& so, const LAS unsigned char* pa0, const LAS unsigned char* pa1, const LAS unsigned char* pb,
                                           const float* ini, const volatile LAS unsigned* rdy, HK hook TRP) {
    constexpr int NRD = TR ? 4 + 2 * NT : 4 + NT;
    Frag<NT> f0, f1;
    pa0 += so; pa1 += so; pb += so;
    asm volatile("" : "+v"(pa0), "+v"(pa1), "+v"(pb));
    __builtin_amdgcn_s_waitcnt(0xC07F);
    BAR();
#pragma unroll
    for (int i = 0; i < NRD; ++i) frag_read<NT, TR, 0>(f0, i, pa0, pb);
    SB0();
    int delta = so == 2 * STG ? -2 * STG : STG; so += delta;
    half_step<NT, TR, 1, 2, true, true>(acc, f0, f1, pa0, pa1, pb, delta, ini);
#pragma nounroll
    for (int s = 1; s < nsteps; ++s) {
        WAITL0();
        BAR();
        unsigned rv = 0;
        if (TR) { rv = *rdy; SB0(); }
        delta = so == 2 * STG ? -2 * STG : STG; so += delta;
        half_step<NT, TR, 0, 1, false, false>(acc, f1, f0, pa0, pa1, pb, 0);
        half_step<NT, TR, 1, 2, false, true>(acc, f0, f1, pa0, pa1, pb, delta);
        if (TR) hook(rv);
    }
    half_step<NT, TR, -1, 1, false, false>(acc, f1, f0, pa0, pa1, pb, 0);
}

template <int NT, class LT>
__device__ __forceinline__ void consumer_a(LAS unsigned char* lds, u32x2 (&zp)[5][4], LT late, int wm, int wn, int lane, unsigned char* wsp_ TRP) {
    const int fr = lane & 15, fq = lane >> 4;
    constexpr int N0 = (NT == 5) ? 0 : 5;
    f32x4 acc[NT][4];
    int so = 0;
    const int sw = (fr >> 1) & 7;
    const int a0 = (wm * 64 + fr) * 128 + ((fq ^ sw) << 4), a1 = (wm * 64 + fr) * 128 + (((4 + fq) ^ sw) << 4);
    const int bw = BOFF + (4 * fq + (fr >> 2)) * 288 + (lane & 3) * 8 + N0 * 32;
    const volatile LAS unsigned* rdy = (const volatile LAS unsigned*)(lds + L_MISC + 4 + wm * 4);
    bool done = false;
    auto normalise = [&]() {
        const LAS unsigned char* ps = opq(lds + L_SP + (wm * 64 + fr) * 16);
        LAS unsigned char* pz = opq(lds + Z0 + (wm * 64 + fr) * ZST + N0 * 32 + fq * 8);
#pragma unroll
        for (int m = 0; m < 4; ++m) {
            const f32x2 sr = *(const LAS f32x2*)(ps + m * 256);
#pragma unroll
            for (int n = 0; n < NT; ++n) {
                const f32x2 u = (f32x2){bflo(zp[n][m][0]), bfhi(zp[n][m][0])} * sr.x + sr.y, v = (f32x2){bflo(zp[n][m][1]), bfhi(zp[n][m][1])} * sr.x + sr.y;
                *(LAS u32x2*)(pz + m * 16 * ZST + n * 32) = (u32x2){pk_bf16(u.x, u.y), pk_bf16(v.x, v.y)};
            }
        }
        WAITL0();
        if (lane == 0) *(volatile LAS unsigned*)(lds + L_MISC + 16 + (wm * 2 + wn) * 4) = 1u;
        done = true;
    };
    auto hook = [&](unsigned rv) { if (__builtin_amdgcn_readfirstlane(rv) != 0u && !done) normalise(); };
#pragma unroll
    for (int n = 0; n < NT; ++n)
#pragma unroll
        for (int m = 0; m < 4; ++m) zp[n][m] = (u32x2){0u, 0u};
#pragma nounroll
    for (int it = 0; it < 2; ++it) {
        gemm_phase<NT, true>(9, acc, so, lds + a0, lds + a1, lds + bw, nullptr, rdy, hook TRA);
        STAMP(1 + 2 * it);
        late();
        if (it == 1 && !done) {
            const u64 t0 = __builtin_amdgcn_s_memrealtime();
            while (__builtin_amdgcn_readfirstlane(*rdy) == 0u && __builtin_amdgcn_s_memrealtime() - t0 < 400000ull) __builtin_amdgcn_s_sleep(1);
            normalise();
        }
#pragma unroll
        for (int m = 0; m < 4; ++m)
#pragma unroll
            for (int n = 0; n < NT; ++n) { acc[n][m] = gelu4(acc[n][m]); zp[n][m] = (u32x2){pk_bf16(acc[n][m][0], acc[n][m][1]), pk_bf16(acc[n][m][2], acc[n][m][3])}; }
        if (it == 0) {
            LAS unsigned char* ps = opq(lds + L_SP + (wm * 64 + fr) * 16 + wn * 8);
#pragma unroll
            for (int m = 0; m < 4; ++m) {
                f32x4 s4 = acc[0][m];
#pragma unroll
                for (int n = 1; n < NT; ++n) s4 += acc[n][m];
                const float mean = opq(allred_q((s4[0] + s4[1]) + (s4[2] + s4[3])) * (1.0f / (16 * NT)));
                f32x4 q4 = {0.f, 0.f, 0.f, 0.f};
#pragma unroll
                for (int n = 0; n < NT; ++n) { const f32x4 d = acc[n][m] - mean; q4 += d * d; }
                const float m2 = allred_q((q4[0] + q4[1]) + (q4[2] + q4[3]));
                if (fq == 0) *(LAS f32x2*)(ps + m * 256) = (f32x2){mean, m2};
            }
            STAMP(2);
        }
    }
    STAMP(4);
}

template <int NT>
__device__ __forceinline__ void consumer_b(LAS unsigned char* lds, rsrc_t ws, u32x2 T, const u32x2 (&z1)[5][4], const float (&bs_)[4], int pm, int pn, int wm, int wn, int lane, unsigned char* wsp_ TRP) {
    float bs[4] = {bf2f(__float_as_uint(opq(bs_[0]))), bf2f(__float_as_uint(opq(bs_[1]))), bf2f(__float_as_uint(opq(bs_[2]))), bf2f(__float_as_uint(opq(bs_[3])))};
    const int fr = lane & 15, fq = lane >> 4;
    constexpr int N0 = (NT == 5) ? 0 : 5;
    f32x4 acc[NT][4];
    int so = 0;
    const int sw = (fr >> 1) & 7;
    const int a0 = (wm * 64 + fr) * 128 + ((fq ^ sw) << 4), a1 = (wm * 64 + fr) * 128 + (((4 + fq) ^ sw) << 4);
    const int bz = BOFF + N0 * 1024 + lane * 16;
    gemm_phase<NT, false>(32, acc, so, lds + a0, lds + a1, lds + bz, bs, nullptr, [](unsigned) {} TRA);
    STAMP(5);
    __builtin_amdgcn_s_setprio(3);
    const int qm = 2 * pm + wm;
    const unsigned hb = WS_HF + (unsigned)qm * (18u * 16384u);
#pragma unroll
    for (int m = 0; m < 4; ++m) {
        u32x2 hp[NT];
#pragma unroll
        for (int n = 0; n < NT; ++n) {
            const f32x2 u = (f32x2){bflo(z1[n][m][0]), bfhi(z1[n][m][0])} * (f32x2){acc[n][m][0], acc[n][m][1]}, v = (f32x2){bflo(z1[n][m][1]), bfhi(z1[n][m][1])} * (f32x2){acc[n][m][2], acc[n][m][3]};
            hp[n] = (u32x2){pk_bf16(u.x, u.y), pk_bf16(v.x, v.y)};
        }
#pragma unroll
        for (int j = 0; j < 2; ++j)
            __builtin_amdgcn_raw_buffer_store_b128((u32x4){hp[2 * j][0], hp[2 * j][1], hp[2 * j + 1][0], hp[2 * j + 1][1]}, ws,
                                                   hb + (unsigned)pn * 16384u + (unsigned)(m * 4 + wn * 2 + j) * 1024u + lane * 16, 0, 16);
        if (NT == 5) __builtin_amdgcn_raw_buffer_store_b64(hp[NT - 1], ws, hb + (unsigned)(16 + (pn >> 3)) * 16384u + m * 4096u + (unsigned)(pn & 7) * 512u + lane * 8, 0, 16);
    }
    flag_publish(ws, WS_FH + (unsigned)(qm * 32 + pn * 2 + wn) * 128u, T, lane);
    __builtin_amdgcn_s_setprio(0);
    STAMP(6);
}

template <int TLO, int THI>
__device__ __forceinline__ void conv_task(LAS unsigned char* lds, int kt, int ut, int yt, int tl, int lane, int& nb TRP) {
#define CSL() do { if (nb < 50) { BAR(); ++nb; } } while (0)
    constexpr int NTL = THI - TLO + 1, ND = THI + 2;
    const int i = lane & 15, g = lane >> 4;
    const LAS unsigned char* pk = opq(lds + kt + (i & 3) * 1088 + ((i >> 2) + g) * 16);
    const LAS unsigned char* pu = opq(lds + ut + (128 + 16 * tl - 32 * g - 4 * (i >> 2) + 4 * (i & 3)) * 2 - 32);
    bf16x8 ub[ND];
#pragma unroll
    for (int d = 0; d < ND; ++d) { if (d == 0 || d == 8) CSL(); ub[d] = cat8(tr_read(pu + 256 * d + 32), tr_read(pu + 256 * d)); }
    f32x4 acc[NTL];
#pragma unroll
    for (int t = 0; t < NTL; ++t) acc[t] = (f32x4){0.f, 0.f, 0.f, 0.f};
#pragma unroll
    for (int s = -1; s <= THI; ++s) {
        if (TLO == 0 ? (s <= 3 || s == 5 || s == 7) : (s & 1) != 0 && s < 12) CSL();
        const bf16x8 kf = *(const LAS bf16x8*)(pk + 64 * (s + 1));
#pragma unroll
        for (int t = TLO; t <= THI; ++t) if (t >= s) acc[t - TLO] = MFMA16(kf, ub[t - s], acc[t - TLO]);
    }
    LAS float* y = opq((LAS float*)(lds + yt) + 32 * g + 16 * tl + i);
    LAS float* cy = opq((LAS float*)(lds + yt + 8192) + (2 * g + tl) * 4 + (i & 3));
#define DPPF(v, ctl) __int_as_float(__builtin_amdgcn_update_dpp(0, __float_as_int(v), ctl, 0xf, 0xf, true))
#pragma unroll
    for (int t = TLO; t <= THI; ++t) {
        if (((t - TLO) % 2) == 0) CSL();
        const f32x4 a = acc[t - TLO];
        y[128 * t] = (a[0] + DPPF(a[1], 0x111)) + (DPPF(a[2], 0x112) + DPPF(a[3], 0x113));
        const float cv = DPPF(a[1], 0x10f) + (DPPF(a[2], 0x10e) + DPPF(a[3], 0x10d));
        if (i < 4) cy[32 * t] = cv;
    }
#undef DPPF
#undef CSL
}
__device__ __forceinline__ int ytab(int c) { return Z0 + c * Y_SZ; }

__global__ __launch_bounds__(512, 2) void gm_kernel(const u16* __restrict__ x, const u16* __restrict__ logA, const u16* __restrict__ Aim, const u16* __restrict__ Cre,
                                                    const u16* __restrict__ Cim, const u16* __restrict__ logdt, const u16* __restrict__ Dsk, const u16* __restrict__ win,
                                                    const u16* __restrict__ wsp, const u16* __restrict__ bsp, const u16* __restrict__ wout, const u16* __restrict__ gw,
                                                    u16* __restrict__ out, unsigned char* wsp_) {
    extern __shared__ __attribute__((aligned(16))) unsigned char lds_[];
    LAS unsigned char* lds = (LAS unsigned char*)lds_;
    const int wid = __builtin_amdgcn_readfirstlane(threadIdx.x >> 6), lane = threadIdx.x & 63;
    const int bid = blockIdx.x, xg = bid & 7, rr = bid >> 3, bi = rr >> 2, bj = rr & 3;
    const int pm = (xg >> 2) * 8 + bi, pn = (xg & 3) * 4 + bj;
    const int qm = 2 * pm + (pn >> 3), qn = pn & 7;
    const int nch = pm < 4 ? 3 : 2;
    const rsrc_t ws = __builtin_amdgcn_make_buffer_rsrc(wsp_, 0, WS_END, 0x00020000);
    STAMP(0);

    const int fr = lane & 15, fq = lane >> 4;
    const int h = wid - 6, p = wid - 4;
    const bool is_prod = (wid >> 1) == 2;
    const int ch0 = pn * 36 + pm;
    float bs[4];
    auto late = [&]() {
#pragma unroll
        for (int m = 0; m < 4; ++m) bs[m] = __uint_as_float((unsigned)bsp[pm * 128 + (wid & 1) * 64 + m * 16 + fr]);
    };
    const int phi = (-(bi + bj)) & 3;
    const unsigned rsw = (unsigned)(((lane & 7) ^ ((4 * p + (lane >> 4)) & 7)) * 16);
#define GLDS(src, dst, aux) __builtin_amdgcn_global_load_lds((const unsigned*)(src), (LAS unsigned*)(dst), 16, 0, aux)
    auto issueS = [&](int k, int s) {
        const char* sb = (const char*)wsp + (size_t)(pm * 128 + p * 8) * 4096 + k * 128; const unsigned vs = (unsigned)(lane >> 3) * 4096u + rsw;
#pragma unroll
        for (int i = 0; i < 8; ++i) GLDS(sb + (size_t)i * (16 * 4096) + vs, lds + s + (p + 2 * i) * 1024, 0); };
    auto ord2 = [&](int s) { return GM_LAG == 0 ? s : (((s & 3) == phi) ? s : ((s - GM_LAG) & 31)); };
    u32x2 E, T;
    auto mk_tag = [&]() { u64 z = (((u64)__builtin_amdgcn_readfirstlane(E[1]) << 32) | __builtin_amdgcn_readfirstlane(E[0])) + 0x9E3779B97F4A7C15ull;
        z = (z ^ (z >> 30)) * 0xBF58476D1CE4E5B9ull; z = (z ^ (z >> 27)) * 0x94D049BB133111EBull; z ^= z >> 31; T = (u32x2){(unsigned)z, (unsigned)(z >> 32)}; };
    u32x2 zp[5][4];
#define STEP(lim) do { if (nb < (lim)) { BAR(); ++nb; } } while (0)
#define STEPS_TO(lim) _Pragma("nounroll") while (nb < (lim)) { BAR(); ++nb; }
    int nb = 0;
    if (wid < nch) {
        const int kch = ch0 + 16 * wid, ki = kch * 64 + lane;
        const unsigned pA = ((const unsigned*)logA)[ki], pI = ((const unsigned*)Aim)[ki], pR = ((const unsigned*)Cre)[ki], pC = ((const unsigned*)Cim)[ki], pT = logdt[kch];
        LAS u32x2* tw = opq((LAS u32x2*)(lds + Z0 + wid * 8448) + lane);
        const LAS unsigned char* trd = opq(lds + Z0 + wid * 8448 + fr * 528 + fq * 16);
        const float dt = rbf(__expf(bf2f(pT)));
        const f32x2 ar = (f32x2){rbf(__expf(bflo(pA))), rbf(__expf(bfhi(pA)))} * dt * -1.44269504f, ai = (f32x2){bflo(pI), bfhi(pI)} * dt * 0.15915494f;
        const f32x2 cr = (f32x2){bflo(pR), bfhi(pR)} * dt, ci = (f32x2){bflo(pC), bfhi(pC)} * dt;
        float cm = fmaxf(fmaxf(fabsf(cr.x), fabsf(ci.x)), fmaxf(fabsf(cr.y), fabsf(ci.y)));
#define DPPF(v, ctl) __int_as_float(__builtin_amdgcn_update_dpp(0, __float_as_int(v), ctl, 0xf, 0xf, true))
        cm = fmaxf(cm, DPPF(cm, 0x121)); cm = fmaxf(cm, DPPF(cm, 0x122)); cm = fmaxf(cm, DPPF(cm, 0x124)); cm = fmaxf(cm, DPPF(cm, 0x128));
#undef DPPF
        { auto r = __builtin_amdgcn_permlane16_swap(__float_as_uint(cm), __float_as_uint(cm), false, false); cm = fmaxf(__uint_as_float(r[0]), __uint_as_float(r[1]));
          auto q = __builtin_amdgcn_permlane32_swap(__float_as_uint(cm), __float_as_uint(cm), false, false); cm = fmaxf(fmaxf(__uint_as_float(q[0]), __uint_as_float(q[1])), 1e-30f); }
        struct C2 { f32x2 x, y; };
        auto cexp = [&](float p_) { const f32x2 e = ar * p_, t = ai * p_;
            const f32x2 mg = {__builtin_amdgcn_exp2f(e.x), __builtin_amdgcn_exp2f(e.y)}; const float t0 = __builtin_amdgcn_fractf(t.x), t1 = __builtin_amdgcn_fractf(t.y);
            return C2{mg * (f32x2){__builtin_amdgcn_cosf(t0), __builtin_amdgcn_cosf(t1)}, mg * (f32x2){__builtin_amdgcn_sinf(t0), __builtin_amdgcn_sinf(t1)}}; };
        auto cmul = [](C2 a, C2 b) { return C2{__builtin_elementwise_fma(a.x, b.x, -(a.y * b.y)), __builtin_elementwise_fma(a.x, b.y, a.y * b.x)}; };
        auto put = [&](int row, C2 v, bool conj) { tw[row * 66] = conj ? (u32x2){pk_f16(v.x.x, -v.y.x), pk_f16(v.x.y, -v.y.y)} : (u32x2){pk_f16(v.x.x, v.y.x), pk_f16(v.x.y, v.y.y)}; };
        C2 wp[16], zr[8], vp[4], cv[17];
        wp[1] = cexp(8.f); zr[1] = cexp(1.f); vp[0] = cexp(128.f);
        tw[0] = (u32x2){pk_f16(1.f, 0.f), pk_f16(1.f, 0.f)};
#pragma unroll
        for (int j = 1; j < 16; ++j) { if (j > 1) wp[j] = cmul(wp[j >> 1], wp[j - (j >> 1)]); put(j, wp[j], true); }
        CFENCE();
        u32x4 fb[8];
#pragma unroll
        for (int ks = 0; ks < 8; ++ks) fb[ks] = *(const LAS u32x4*)(trd + ks * 64);
        CFENCE();
#pragma unroll
        for (int r = 2; r < 8; ++r) zr[r] = cmul(zr[r >> 1], zr[r - (r >> 1)]);
#pragma unroll
        for (int k = 1; k < 4; ++k) vp[k] = cmul(vp[k - 1], vp[k - 1]);
        const float rc = __builtin_amdgcn_rcpf(cm);
        cv[0] = C2{cr * rc, ci * rc};
#pragma unroll
        for (int k = 1; k < 8; ++k) { const int lg = k >= 4 ? 2 : k >= 2 ? 1 : 0; cv[k] = cmul(cv[k - (1 << lg)], vp[lg]); }
        LAS float* kb_ = (LAS float*)(lds + L_KD + wid * 3072);
        const float sc = 2.0f * cm;
        u32x4 fa[3][8];
#pragma unroll
        for (int tt = 0; tt < 3; ++tt) {
#pragma unroll
            for (int i = 0; i < (tt == 2 ? 9 : 16); ++i) {
                const int r = tt == 0 ? (i & 7) : tt == 1 ? (i < 8 ? 2 * (i & 3) : 4 * (i & 1)) : 0;
                const int hh = tt == 0 ? (i >> 3) : tt == 1 ? (i < 8 ? 2 + (i >> 2) : 4 + ((i - 8) >> 1)) : 8 + i;
                if (tt == 2) cv[hh] = cmul(cv[hh - 8], vp[3]);
                put(i, r == 0 ? cv[hh] : cmul(cv[hh], zr[r]), false);
            }
            CFENCE();
#pragma unroll
            for (int ks = 0; ks < 8; ++ks) fa[tt][ks] = *(const LAS u32x4*)(trd + ks * 64);
            CFENCE();
            SB0();
        }
        f32x4 kd[3] = {{0.f, 0.f, 0.f, 0.f}, {0.f, 0.f, 0.f, 0.f}, {0.f, 0.f, 0.f, 0.f}};
#pragma unroll
        for (int ks = 0; ks < 8; ++ks)
#pragma unroll
            for (int tt = 0; tt < 3; ++tt) kd[tt] = __builtin_amdgcn_mfma_f32_16x16x32_f16(__builtin_bit_cast(f16x8, fa[tt][ks]), __builtin_bit_cast(f16x8, fb[ks]), kd[tt], 0, 0, 0);
#pragma unroll
        for (int tt = 0; tt < 3; ++tt) {
            LAS float* kw = opq(kb_ + (tt == 0 ? 128 * (fq >> 1) + 4 * (fq & 1) + 8 * fr : tt == 1 ? 256 + 64 * fq + (fq < 2 ? 4 : 2) * fr : 512 + 64 * fq + fr));
            LAS float* kwb = tt == 1 ? opq(kw + (fq < 2 ? 2 : 32)) : kw + (tt == 0 ? 2 : 32);
            kw[0] = kd[tt][0] * sc; kw[tt == 2 ? 16 : 1] = kd[tt][1] * sc; kwb[0] = kd[tt][2] * sc; kwb[tt == 2 ? 16 : 1] = kd[tt][3] * sc;
        }
        WAITL0();
    }
    if (GM_PF && wid == 3) {
        unsigned acc_ = 0;
        if (GM_PF & 1) { const char* pc = (const char*)(__builtin_amdgcn_s_getpc() & ~127ull) - 1024; if (lane < 11) acc_ += *(const unsigned*)(pc + (rr * 11 + lane) * 128); }
        if (GM_PF & 2) {
            acc_ += *(const unsigned*)((const char*)wsp + (size_t)(pm * 128 + lane) * 4096) + *(const unsigned*)((const char*)wsp + (size_t)(pm * 128 + 64 + lane) * 4096);
#pragma unroll
            for (int i = 0; i < 3; ++i) if (i * 64 + lane < 144) acc_ += __builtin_amdgcn_raw_buffer_load_b32(ws, WS_Z2F + (unsigned)pn * (64u * 9216u) + (i * 64 + lane) * 4096u, 0, 16);
            acc_ += __builtin_amdgcn_raw_buffer_load_b32(ws, WS_HF + (unsigned)qm * (18u * 16384u) + lane * 4096u, 0, 16);
            if (lane < 8) acc_ += __builtin_amdgcn_raw_buffer_load_b32(ws, WS_HF + (unsigned)qm * (18u * 16384u) + (64 + lane) * 4096u, 0, 16);
            acc_ += __builtin_amdgcn_raw_buffer_load_b32(ws, WS_HF + (unsigned)(qm ^ 1) * (18u * 16384u) + lane * 4096u, 0, 16);
            acc_ += __builtin_amdgcn_raw_buffer_load_b32(ws, WS_WOF + (unsigned)qn * (18u * 18432u) + lane * 4096u, 0, 16);
            if (lane < 17) acc_ += __builtin_amdgcn_raw_buffer_load_b32(ws, WS_WOF + (unsigned)qn * (18u * 18432u) + (64 + lane) * 4096u, 0, 16);
            acc_ += __builtin_amdgcn_raw_buffer_load_b32(ws, WS_AT + (unsigned)(qn * 72 + lane) * 4096u, 0, 16);
            if (lane < 8) acc_ += __builtin_amdgcn_raw_buffer_load_b32(ws, WS_AT + (unsigned)(qn * 72 + 64 + lane) * 4096u, 0, 16);
            if (lane < 36) acc_ += __builtin_amdgcn_raw_buffer_load_b32(ws, WS_UT + (unsigned)(pn * 36 + lane) * 4096u, 0, 16) + __builtin_amdgcn_raw_buffer_load_b32(ws, WS_AT + (unsigned)(pn * 36 + lane) * 4096u, 0, 16);
            acc_ += __builtin_amdgcn_raw_buffer_load_b32(ws, lane * 16384u, 0, 16);
            if (lane < 18) acc_ += *(const unsigned*)((const char*)out + (size_t)qm * 64 * 1152 + lane * 4096);
        }
        if (GM_PF & 4) {
#pragma unroll
            for (int i = 0; i < 4; ++i) acc_ += *(const unsigned*)((const char*)wsp + (size_t)bid * 32768 + (i * 64 + lane) * 128);
        }
        asm volatile("" :: "v"(acc_));
    }
#define IC(n) std::integral_constant<int, n>{}
    if (wid < 6) STAMP(10);
    if (is_prod) {
        const unsigned vx = (unsigned)(lane >> 3) * 1152u + rsw;
        unsigned wv[9];
#pragma unroll
        for (int i = 0; i < 9; ++i) { const int c = (p + 2 * i) * 64 + lane, rho = c / 18, ch = c - rho * 18;
            const int kap = (rho & 32) + 8 * ((rho >> 2) & 3) + 4 * ((rho >> 4) & 1) + (rho & 3); wv[i] = (unsigned)kap * 9216u + (unsigned)ch * 16u; }
        const char* xb = (const char*)x + (size_t)(pm * 128 + p * 8) * 1152;
        const char* wb = (const char*)win + pn * 288;
        auto issueX = [&](int k, int s) {
#pragma unroll
            for (int i = 0; i < 8; ++i) GLDS(xb + (size_t)i * (16 * 1152) + k * 128 + vx, lds + s + (p + 2 * i) * 1024, 0); };
        auto issueW = [&](int k, int coloff, int s) {
            const char* b = wb + (size_t)k * (64 * 9216) + coloff;
#pragma unroll
            for (int i = 0; i < 9; ++i) GLDS(b + wv[i], lds + s + BOFF + (p + 2 * i) * 1024, 0); };
        auto ord1 = [&](int s) { return (GM_LAG == 0 || (s & 3) == phi) ? s : (s >= 4 ? s - 4 : (s == 0 ? 8 : s + 4)); };
        issueX(ord1(0), 0); issueW(ord1(0), FH * 2, 0); issueX(ord1(1), STG); issueW(ord1(1), FH * 2, STG);
        issueX(ord1(2), 2 * STG); issueW(ord1(2), FH * 2, 2 * STG);
        E = flag_ld(ws, WS_E);
        int so = 0;
        WAITV(35); PBAR();
#pragma nounroll
        for (int S = 1; S < 16; ++S) {
            WAITV(17); PBAR();
            const int t = S + 2, k = ord1(t >= 9 ? t - 9 : t);
            issueX(k, so); issueW(k, t >= 9 ? 0 : FH * 2, so); so = nextbuf(so);
        }
        WAITV(17); PBAR(); issueS(ord2(0), 0);
        WAITV(8); PBAR(); issueS(ord2(1), STG);
        STAMP(1);
    } else if (wid < 4) {
        E = flag_ld(ws, WS_E);
        if (wid >> 1) consumer_a<4>(lds, zp, late, wid & 1, 1, lane, wsp_ TRA);
        else consumer_a<5>(lds, zp, late, wid & 1, 0, lane, wsp_ TRA);
    } else {
        if (h == 0 && lane < 12) *(LAS unsigned*)(lds + L_MISC + lane * 4) = 0u;
        const int row0 = lane / 9, chk0 = lane - row0 * 9;
        const int u = (pn >> 3) * 16 + pm, nblk = u < 8 ? 3 : 2;
        u32x2 uc[18]; u32x4 xv[9], gv[9], wv[3][5];
        if (h == 0) {
            { const char* xb = (const char*)x + (size_t)pm * 128 * 1152 + pn * 72; unsigned off = row0 * 1152 + chk0 * 8; int ch = chk0;
#pragma unroll
              for (int i = 0; i < 9; ++i) { uc[i] = *(const u32x2*)(xb + off); uc[9 + i] = *(const u32x2*)(xb + 64 * 1152 + off); off += 7 * 1152 + 8; if (++ch == 9) { ch = 0; off += 1152 - 72; } } }
            { const char* xr = (const char*)x + (size_t)(pm * 128 + pn * 8 + (lane >> 3)) * 1152 + (lane & 7) * 16; const char* gr = (const char*)gw + (lane & 7) * 16;
#pragma unroll
              for (int i = 0; i < 9; ++i) { xv[i] = *(const u32x4*)(xr + i * 128); gv[i] = *(const u32x4*)(gr + i * 128); } }
        } else {
            unsigned wo_[5], hi_[5];
            { int row = row0, ch = chk0;
#pragma unroll
              for (int i = 0; i < 5; ++i) { const int rw = row < 32 ? row : 31; wo_[i] = (rw & 15) * 1152 + ch * 16; hi_[i] = rw >> 4; row += 7; if (++ch == 9) { ch = 0; ++row; } } }
#pragma unroll
            for (int b = 0; b < 3; ++b) {
                const int blk = (b < nblk) ? u + 32 * b : u, st = blk >> 2, jb = blk & 3;
                const int fA = st < 16 ? st * 144 + (jb < 2 ? 32 * jb : 16 + 32 * jb) : ((st - 16) * 8 + 2 * jb) * 144 + 64, dB = (st < 16 ? 16 : 144) * 1152;
                const char* wb_ = (const char*)wout + (size_t)fA * 1152 + qn * 144;
#pragma unroll
                for (int i = 0; i < 5; ++i) wv[b][i] = *(const u32x4*)(wb_ + (wo_[i] + hi_[i] * dB));
            }
        }
        E = flag_ld(ws, WS_E);
        if (lane < 24) *(LAS u32x4*)(lds + L_KT + (lane >> 3) * KT_SZ + (2 * h + ((lane >> 2) & 1)) * 1088 + (lane & 3) * 16) = (u32x4){0u, 0u, 0u, 0u};
        WAITL0();
        STAMP(10);
        __builtin_amdgcn_s_setprio(GM_HPRIO);
        {
            float kv[2][3];
#pragma nounroll
            for (int ei = 0; ei < 9; ++ei) {
                BAR();
                if (ei > 0) {
                    LAS unsigned char* kt = opq(lds + L_KT + (2 * h + ((ei - 1) >> 2)) * 1088 + 64 + ((ei - 1) & 3) * 256 + lane * 2);
#pragma unroll
                    for (int j = 0; j < 2; ++j)
#pragma unroll
                        for (int c = 0; c < 3; ++c) *(LAS u16*)(kt + c * KT_SZ + j * 128) = (u16)pk_bf16(kv[j][c], 0.f);
                }
#pragma unroll
                for (int j = 0; j < 2; ++j) {
                    const int jj = (ei & 3) * 2 + j, e = jj >= 4 ? 3 : jj >= 2 ? 2 : jj, lag = 256 * jj + 4 * lane + 2 * h + ((ei >> 2) & 1);
                    const int idx = e == 0 ? lag : 128 * e + ((lag + (1 << (e - 1)) - 1 + ((lag >> e) & 1)) >> e);
                    const LAS float* q = opq((const LAS float*)(lds + L_KD) + idx);
#pragma unroll
                    for (int c = 0; c < 3; ++c) kv[j][c] = q[c * 768];
                }
            }
        }
        if (h == 0) {
            LAS unsigned char* pzs = opq(lds + Z0 + lane * 8);
#pragma unroll
            for (int i = 0; i < 18; ++i) *(LAS u32x2*)(pzs + i * 512) = uc[i];
            CFENCE();
            { const LAS unsigned char* pt = opq(lds + Z0 + (8 * (4 * fq + (lane & 3)) + (fr >> 2)) * 72);
              const unsigned ub = WS_UT + (unsigned)(pn * 36 + (lane & 3)) * 4096u + (unsigned)(pm * 128 + 8 * (4 * fq + (fr >> 2))) * 2u;
#pragma unroll
              for (int cc = 0; cc < 9; ++cc) __builtin_amdgcn_raw_buffer_store_b128(__builtin_bit_cast(u32x4, cat8(tr_read(pt + cc * 8), tr_read(pt + 288 + cc * 8))), ws, ub + cc * 16384u, 0, 16); }
            float a = 0.f;
#pragma unroll
            for (int i = 0; i < 9; ++i)
#pragma unroll
                for (int j = 0; j < 4; ++j) { const unsigned xa = xv[i][j], ga = gv[i][j]; a = __builtin_amdgcn_fdot2_f32_bf16(__builtin_bit_cast(bf16x2, xa), __builtin_bit_cast(bf16x2, ga), a, false); }
#define DPPF(v, ctl) __int_as_float(__builtin_amdgcn_update_dpp(0, __float_as_int(v), ctl, 0xf, 0xf, true))
            a += DPPF(a, 0xb1); a += DPPF(a, 0x4e); a += DPPF(a, 0x141);
#undef DPPF
            const float gt = rbf(__builtin_amdgcn_rcpf(1.0f + __expf(-rbf(a))));
            if ((lane & 7) == 0) __builtin_amdgcn_raw_buffer_store_b32(__float_as_uint(gt), ws, WS_GATE + (unsigned)(pm * 128 + pn * 8 + (lane >> 3)) * 4u, 0, 16);
        } else {
            LAS unsigned char* pws = opq(lds + Z0 + 9216 + lane * 16);
            const LAS unsigned char* ptw = opq(lds + Z0 + 9216 + (4 * fq + (fr >> 2)) * 144 + (lane & 3) * 8);
#pragma unroll
            for (int b = 0; b < 3; ++b) {
                if (b < nblk) {
#pragma unroll
                    for (int i = 0; i < 5; ++i) if (i < 4 || lane < 32) *(LAS u32x4*)(pws + i * 1024) = wv[b][i];
                    CFENCE();
                    const unsigned ob = WS_WOF + (unsigned)qn * (18u * 18432u) + (unsigned)(u + 32 * b) * 4608u;
#pragma unroll
                    for (int nn = 0; nn < 5; ++nn) {
                        const u32x4 v = __builtin_bit_cast(u32x4, cat8(tr_read(ptw + nn * 32), tr_read(ptw + 2304 + nn * 32)));
                        if (nn < 4) __builtin_amdgcn_raw_buffer_store_b128(v, ws, ob + nn * 1024 + lane * 16, 0, 16);
                        else if (fr < 8) __builtin_amdgcn_raw_buffer_store_b128(v, ws, ob + 4096 + (fq * 8 + fr) * 16, 0, 16);
                    }
                    CFENCE();
                }
            }
        }
        nb = 9;
        mk_tag();
        WAITL0();
        STEPS_TO(10);
        STAMP(1);
        {
            const f32x4 sp = *(const LAS f32x4*)(lds + L_SP + (h * 64 + lane) * 16);
            const float dm = sp[0] - sp[2];
            const f32x2 mine = {(80.f * sp[0] + 64.f * sp[2]) * (1.0f / 144.f), sp[1] + sp[3] + (80.f * 64.f / 144.f) * dm * dm};
            __builtin_amdgcn_raw_buffer_store_b128((u32x4){T[0], __float_as_uint(mine.x), T[1], __float_as_uint(mine.y)}, ws, WS_ST + (unsigned)((pm * 16 + pn) * 128 + h * 64 + lane) * 16u, 0, 16);
            STEP(18);
            const unsigned so_ = WS_ST + (unsigned)(pm * 16 * 128 + h * 64 + lane) * 16u;
            const u64 t0 = __builtin_amdgcn_s_memrealtime();
            u32x4 st[16];
            for (;;) {
                CFENCE();
#pragma unroll
                for (int j = 0; j < 16; ++j) st[j] = __builtin_amdgcn_raw_buffer_load_b128(ws, so_ + j * 2048u, 0, 16);
                STEP(18);
                unsigned bad = 0;
#pragma unroll
                for (int j = 0; j < 16; ++j) bad |= (st[j][0] ^ T[0]) | (st[j][2] ^ T[1]);
                if (__builtin_amdgcn_ballot_w64(bad != 0u) == 0ull) break;
                if (__builtin_amdgcn_s_memrealtime() - t0 > 300000ull) break;
            }
            STAMP(2);
            STEP(18);
            float mean = 0.f, m2 = 0.f;
#pragma unroll
            for (int j = 0; j < 16; ++j) mean += __uint_as_float(st[j][1]);
            mean *= (1.0f / 16.f);
#pragma unroll
            for (int j = 0; j < 16; ++j) { const float d = __uint_as_float(st[j][1]) - mean; m2 += __uint_as_float(st[j][3]) + 144.f * d * d; }
            const float rstd = __builtin_amdgcn_rsqf(m2 * (1.0f / FH) + 1e-5f);
            *(LAS f32x2*)(lds + L_SP + (h * 64 + lane) * 16) = (f32x2){rstd, -mean * rstd};
            WAITL0();
            if (lane == 0) *(volatile LAS unsigned*)(lds + L_MISC + 4 + h * 4) = 1u;
            const volatile LAS unsigned* zd = (const volatile LAS unsigned*)(lds + L_MISC + 16 + h * 8 + (lane & 1) * 4);
            for (;;) {
                STEP(18);
                if (__builtin_amdgcn_ballot_w64(*zd == 0u) == 0ull) break;
                if (__builtin_amdgcn_s_memrealtime() - t0 > 800000ull) break;
            }
            CFENCE();
            const LAS unsigned char* ptz = opq(lds + Z0 + (h * 64 + 8 * fq + (fr >> 2)) * ZST + (lane & 3) * 8);
            const unsigned zo = WS_Z2F + (unsigned)((pn * 64 + pm * 4 + h * 2) * 9) * 1024u + lane * 16;
#pragma unroll
            for (int i = 0; i < 18; ++i) {
                const LAS unsigned char* pt = ptz + (i / 9) * 32 * ZST + (i % 9) * 32;
                __builtin_amdgcn_raw_buffer_store_b128(__builtin_bit_cast(u32x4, cat8(tr_read(pt), tr_read(pt + 4 * ZST))), ws, zo + i * 1024, 0, 16);
                if (i == 5 || i == 11) STEP(18);
            }
            STEP(18);
            flag_publish(ws, WS_FZ + (unsigned)(pn * 32 + pm * 2 + h) * 128u, T, lane);
            STAMP(3);
            STEPS_TO(18);
            __builtin_amdgcn_s_setprio(0);
        }
    }

    if (is_prod) {
        mk_tag();
        const char* lin = (const char*)wsp_ + p * 1024 + lane * 16;
        auto issueLA = [&](unsigned off, int s) {
#pragma unroll
            for (int i = 0; i < 8; ++i) GLDS(lin + off + i * 2048, lds + s + (p + 2 * i) * 1024, 16); };
        auto issueLB = [&](unsigned off, int s) {
#pragma unroll
            for (int i = 0; i < 9; ++i) GLDS(lin + off + i * 2048, lds + s + BOFF + (p + 2 * i) * 1024, 16); };
        auto ord3 = [&](int s) { return (GM_LAG == 0 || s >= 16) ? s : (((s & 3) == phi) ? s : ((s - GM_LAG) & 15)); };
        const unsigned zb = WS_Z2F + (unsigned)pn * (64u * 9216u);
        flag_wait(ws, WS_FZ + (unsigned)pn * 32u * 128u, 32, T, lane);
        STAMP(2);
        issueLB(zb + (unsigned)ord2(0) * 18432u, 0); issueLB(zb + (unsigned)ord2(1) * 18432u, STG);
        WAITV(9); PBAR();
        int so = 2 * STG;
        { const int k = ord2(2); issueS(k, so); issueLB(zb + (unsigned)k * 18432u, so); so = nextbuf(so); }
#pragma nounroll
        for (int S = 19; S < 48; ++S) {
            WAITV(17); PBAR();
            const int k = ord2(S - 16); issueS(k, so); issueLB(zb + (unsigned)k * 18432u, so); so = nextbuf(so);
        }
        const unsigned hb = WS_HF + (unsigned)qm * (18u * 16384u), ob = WS_WOF + (unsigned)qn * (18u * 18432u);
        WAITV(17); PBAR();
        flag_wait(ws, WS_FZ + (unsigned)(pn ^ 8) * 32u * 128u, 32, T, lane);
        issueLB(ob + (unsigned)ord3(0) * 18432u, 2 * STG);
        PBAR();
        issueLB(ob + (unsigned)ord3(1) * 18432u, 0);
        STAMP(3);
        flag_wait(ws, WS_FH + (unsigned)qm * 32u * 128u, 32, T, lane);
        STAMP(4);
        issueLA(hb + (unsigned)ord3(0) * 16384u, 2 * STG); issueLA(hb + (unsigned)ord3(1) * 16384u, 0);
        WAITV(8); PBAR();
        so = STG;
        { const int k = ord3(2); issueLA(hb + (unsigned)k * 16384u, so); issueLB(ob + (unsigned)k * 18432u, so); so = nextbuf(so); }
#pragma nounroll
        for (int S = 51; S < 66; ++S) {
            WAITV(17); PBAR();
            const int k = ord3(S - 48); issueLA(hb + (unsigned)k * 16384u, so); issueLB(ob + (unsigned)k * 18432u, so); so = nextbuf(so);
        }
        WAITV(17); PBAR();
        WAITV(0); PBAR();
        PBAR();
        STAMP(5); PDUMP();
        if (bid == 0 && p == 0 && lane == 0) __builtin_amdgcn_raw_buffer_store_b64(T, ws, WS_E, 0, 16);
        return;
    }

    if (wid < 4) {
        mk_tag();
        if (wid >> 1) consumer_b<4>(lds, ws, T, zp, bs, pm, pn, wid & 1, 1, lane, wsp_ TRA);
        else consumer_b<5>(lds, ws, T, zp, bs, pm, pn, wid & 1, 0, lane, wsp_ TRA);
    } else {
        __builtin_amdgcn_s_setprio(GM_HPRIO);
        STEPS_TO(19);
        u32x4 uu[3][2];
#pragma unroll
        for (int c = 0; c < 3; ++c)
#pragma unroll
            for (int j = 0; j < 2; ++j) uu[c][j] = __builtin_amdgcn_raw_buffer_load_b128(ws, WS_UT + (unsigned)(ch0 + (c < nch ? 16 * c : 0)) * 4096u + h * 2048 + j * 1024 + lane * 16, 0, 16);
        STEPS_TO(22);
#pragma unroll
        for (int c = 0; c < 3; ++c) {
            BAR(); ++nb;
#pragma unroll
            for (int j = 0; j < 2; ++j) *(LAS u32x4*)(lds + L_UT + c * UT_SZ + 256 + h * 2048 + j * 1024 + lane * 16) = uu[c][j];
            if (lane < 16) *(LAS u32x4*)(lds + L_UT + c * UT_SZ + h * 4352 + lane * 16) = (u32x4){0u, 0u, 0u, 0u};
        }
        WAITL0();
        BAR(); ++nb;
        STAMP(4);
    }

    if (wid < 4) nb = 50;
    for (;;) {
        if (nb > 45) STEPS_TO(50);
        unsigned tk = 0;
        if (lane == 0) tk = __hip_atomic_fetch_add((LAS unsigned*)(lds + L_MISC), 1u, __ATOMIC_RELAXED, __HIP_MEMORY_SCOPE_WORKGROUP);
        tk = __builtin_amdgcn_readfirstlane(tk);
        if (tk >= (unsigned)(4 * nch)) break;
        const int c = tk >> 2, tl = tk & 1;
        if (tk & 2) conv_task<0, 10>(lds, L_KT + c * KT_SZ, L_UT + c * UT_SZ, ytab(c), tl, lane, nb TRA);
        else conv_task<11, 15>(lds, L_KT + c * KT_SZ, L_UT + c * UT_SZ, ytab(c), tl, lane, nb TRA);
    }
    STEPS_TO(50);
    WAITL0();
    STAMP(7);

    if (wid < 4) {
        const int kq = wid >> 1, mh = wid & 1;
        f32x4 acc[5][2];
        struct F3 { s16x4 alo[4], ahi[4]; bf16x8 b[10]; } f0, f1;
        const LAS unsigned char* ph = lds + 2 * STG + mh * 8192 + kq * 2048 + lane * 16;
        const LAS unsigned char* pw = lds + 2 * STG + BOFF + kq * 9216 + lane * 16;
        const LAS unsigned char* pw4 = lds + 2 * STG + BOFF + kq * 9216 + 4096 + (fq * 8 + (fr & 7)) * 16;
        auto rd3 = [&](F3& f, int i, bool single) {
            const int j = i / 7, r = i % 7;
            if (r < 2) {
                if (!single) { const bf16x8 v = *(const LAS bf16x8*)(ph + r * 4096 + j * 1024); f.alo[j * 2 + r] = __builtin_shufflevector(v, v, 0, 1, 2, 3); f.ahi[j * 2 + r] = __builtin_shufflevector(v, v, 4, 5, 6, 7); }
                else { const LAS unsigned char* q = ph - lane * 8 + r * 4096 + j * 1024; f.alo[j * 2 + r] = *(const LAS s16x4*)q; f.ahi[j * 2 + r] = *(const LAS s16x4*)(q + 512); }
            } else if (r < 6) f.b[j * 5 + r - 2] = *(const LAS bf16x8*)(pw + j * 4608 + (r - 2) * 1024);
            else f.b[j * 5 + 4] = *(const LAS bf16x8*)(pw4 + j * 4608);
        };
        int so = 2 * STG;
        auto step3 = [&](const F3& c, F3& l, int mode  , bool first) {
            const int delta = so == 2 * STG ? -2 * STG : STG; so += delta;
#pragma unroll
            for (int i = 0; i < 20; ++i) {
                const int j = i / 10, n = (i % 10) >> 1, m = i & 1;
                acc[n][m] = MFMA16(c.b[j * 5 + n], cat8(c.alo[j * 2 + m], c.ahi[j * 2 + m]), (first && j == 0) ? ((f32x4){0.f, 0.f, 0.f, 0.f}) : acc[n][m]);
                if (mode < 2 && i < 7) { rd3(l, 2 * i, mode == 1); rd3(l, 2 * i + 1, mode == 1); }
                if (mode < 2) { if (i == 8) { ph += delta; asm volatile("" : "+v"(ph)); } if (i == 9) { pw += delta; asm volatile("" : "+v"(pw)); } if (i == 10) { pw4 += delta; asm volatile("" : "+v"(pw4)); } }
                SB0();
            }
        };
        __builtin_amdgcn_s_waitcnt(0xC07F);
        BAR();
#pragma unroll
        for (int i = 0; i < 14; ++i) rd3(f0, i, false);
        ph -= 2 * STG; pw -= 2 * STG; pw4 -= 2 * STG; so = 0;
        WAITL0(); BAR(); step3(f0, f1, 0, true);
#pragma nounroll
        for (int j = 0; j < 7; ++j) {
            WAITL0(); BAR(); step3(f1, f0, 0, false);
            WAITL0(); BAR(); step3(f0, f1, 0, false);
        }
        WAITL0(); BAR(); step3(f1, f0, 1, false);
        WAITL0(); BAR(); step3(f0, f1, 1, false);
        step3(f1, f0, 2, false);
        STAMP(8);
        LAS unsigned char* prw = opq(lds + L_R + wid * 5120 + lane * 16);
#pragma unroll
        for (int n = 0; n < 5; ++n) { const f32x4 x0 = acc[n][0], x1 = acc[n][1]; *(LAS f32x4*)(prw + n * 1024) = kq ? x0 : x1; acc[n][0] = kq ? x1 : x0; }
        WAITL0();
        BAR();
        const int m = mh * 2 + kq;
        const float g = *(const LAS float*)(lds + L_MISC + 64 + (16 * m + fr) * 4), g1 = rbf(1.0f - g);
        const LAS unsigned char* prr = opq(lds + L_R + (wid ^ 2) * 5120 + lane * 16);
        const LAS unsigned char* pat = opq(lds + L_AT + (4 * fq + (fr >> 2)) * 128 + (16 * m + 4 * (lane & 3)) * 2);
#pragma unroll
        for (int n = 0; n < 5; ++n) {
            const f32x4 b = acc[n][0] + *(const LAS f32x4*)(prr + n * 1024);
            const u32x2 a = __builtin_bit_cast(u32x2, tr_read(pat + n * 2048));
            const u32x2 o = {pk_bf16(g * bflo(a[0]) + g1 * b[0], g * bfhi(a[0]) + g1 * b[1]), pk_bf16(g * bflo(a[1]) + g1 * b[2], g * bfhi(a[1]) + g1 * b[3])};
            if (n < 4 || fq < 2) *(u32x2*)((char*)out + (size_t)(qm * 64 + 16 * m + fr) * 1152 + (qn * 72 + 16 * n + 4 * fq) * 2) = o;
        }
    } else {
        int d = pn * 36 + pm;
#pragma nounroll
        for (int c = 0; c < nch; ++c) {
            const float dsk = bf2f(Dsk[d]);
#pragma unroll
            for (int k = 0; k < 2; ++k) {
                BAR(); ++nb;
                const int l0 = h * 1024 + k * 512 + lane * 8;
                f32x4 cv = *(const LAS f32x4*)(lds + ytab(c) + 8192 + (l0 >= 16 ? (l0 >> 4) - 1 : 0) * 16);
                if ((lane & 1) || l0 == 0) cv = (f32x4){0.f, 0.f, 0.f, 0.f};
                const f32x4 y0 = *(const LAS f32x4*)(lds + ytab(c) + l0 * 4) + cv, y1 = *(const LAS f32x4*)(lds + ytab(c) + l0 * 4 + 16);
                const u32x4 uv = *(const LAS u32x4*)(lds + L_UT + c * UT_SZ + 256 + l0 * 2);
                u32x4 o;
#pragma unroll
                for (int j = 0; j < 4; ++j) {
                    const float ya = j < 2 ? y0[2 * j] : y1[2 * j - 4], yb = j < 2 ? y0[2 * j + 1] : y1[2 * j - 3];
                    const unsigned yp = pk_bf16(ya, yb), up = pk_bf16(bflo(uv[j]) * dsk, bfhi(uv[j]) * dsk);
                    o[j] = pk_bf16(bflo(yp) + bflo(up), bfhi(yp) + bfhi(up));
                }
                __builtin_amdgcn_raw_buffer_store_b128(o, ws, WS_AT + (unsigned)d * 4096u + l0 * 2, 0, 16);
            }
            d += 16;
        }
        STEPS_TO(58);
        flag_publish(ws, WS_FA + (unsigned)((pn >> 1) * 64 + (pn & 1) * 32 + pm * 2 + h) * 128u, T, lane);
        STAMP(5);
        STEPS_TO(62);
        if (h == 0) {
            const unsigned fo = WS_FA + (unsigned)(qn * 64 + lane) * 128u;
            const u64 t0 = __builtin_amdgcn_s_memrealtime();
            for (;;) {
                CFENCE();
                const u32x2 v = flag_ld(ws, fo);
                STEP(68); STEP(68);
                if (flag_all(v, T, true)) break;
                if (__builtin_amdgcn_s_memrealtime() - t0 > 300000ull) break;
            }
            ACQ();
            u32x4 av[9];
#pragma unroll
            for (int i = 0; i < 9; ++i) av[i] = __builtin_amdgcn_raw_buffer_load_b128(ws, WS_AT + (unsigned)(qn * 72 + i * 8 + (lane >> 3)) * 4096u + qm * 128 + (lane & 7) * 16, 0, 16);
            STEP(68); STEP(68);
            LAS unsigned char* pa_ = opq(lds + L_AT + lane * 16);
#pragma unroll
            for (int i = 0; i < 9; ++i) *(LAS u32x4*)(pa_ + i * 1024) = av[i];
        } else {
            const unsigned gv = __builtin_amdgcn_raw_buffer_load_b32(ws, WS_GATE + (unsigned)(qm * 64 + lane) * 4u, 0, 16);
            BAR(); BAR(); nb += 2;
            *(LAS unsigned*)(lds + L_MISC + 64 + lane * 4) = gv;
        }
        STEPS_TO(68);
        WAITL0();
        BAR();
    }
    STAMP(9); TRDUMP();
}
}

extern "C" void kernel_launch(void* const* d_in, const int* in_sizes, int n_in, void* d_out, int out_size, void* d_ws, size_t ws_size, hipStream_t stream) {
    (void)in_sizes; (void)n_in; (void)out_size; (void)ws_size;
    (void)hipFuncSetAttribute((const void*)gm::gm_kernel, hipFuncAttributeMaxDynamicSharedMemorySize, gm::LDS_BYTES);
    hipLaunchKernelGGL(gm::gm_kernel, dim3(256), dim3(512), gm::LDS_BYTES, stream, (const u16*)d_in[0], (const u16*)d_in[1], (const u16*)d_in[2], (const u16*)d_in[3],
                       (const u16*)d_in[4], (const u16*)d_in[5], (const u16*)d_in[6], (const u16*)d_in[8], (const u16*)d_in[9], (const u16*)d_in[10],
                       (const u16*)d_in[11], (const u16*)d_in[12], (u16*)d_out, (unsigned char*)d_ws);
}
```

```cpp
#define GM_PF 1
#define GM_DA 0
#define GM_DB 1
#include <hip/hip_runtime.h>
#include <stdint.h>
#include <type_traits>

#define LAS __attribute__((address_space(3)))
typedef unsigned short u16;
typedef unsigned long long u64;
typedef short bf16x8 __attribute__((ext_vector_type(8)));
typedef short s16x4 __attribute__((ext_vector_type(4)));
typedef _Float16 f16x8 __attribute__((ext_vector_type(8)));
typedef _Float16 f16x2 __attribute__((ext_vector_type(2)));
typedef __bf16 bf16x2 __attribute__((ext_vector_type(2)));
typedef float f32x4 __attribute__((ext_vector_type(4)));
typedef float f32x2 __attribute__((ext_vector_type(2)));
typedef unsigned u32x4 __attribute__((ext_vector_type(4)));
typedef unsigned u32x2 __attribute__((ext_vector_type(2)));
typedef __amdgpu_buffer_rsrc_t rsrc_t;

namespace gm {
constexpr int SEQ = 2048, DM = 576, FH = 2304;
constexpr unsigned WS_E = 0, WS_FS = 4096, WS_FZ = WS_FS + 65536, WS_FH = WS_FZ + 65536, WS_FA = WS_FH + 131072;
constexpr unsigned WS_ST = 0x60000, WS_GATE = 0xE0000, WS_DBG = 0x1E00000, WS_Z2F = 0x100000, WS_HF = 0xA00000, WS_WOF = 0x1300000, WS_UT = 0x1600000, WS_AT = 0x1900000, WS_END = 0x1C00000, WS_TRC = 0x1C00000;
constexpr int STG = 34816, BOFF = 16384, Z0 = 3 * STG, ZST = 296, T0 = Z0 + 128 * ZST;
constexpr int KT_SZ = 4352, UT_SZ = 4608, Y_SZ = 10240;
constexpr int L_MISC = 163840 - 320;
constexpr int L_KT = L_MISC - 3 * KT_SZ, L_SP = T0, L_KD = Z0 + 3 * 8448, L_UT = Z0 + 3 * Y_SZ;
constexpr int L_AT = Z0, L_R = Z0 + 10240;
constexpr int LDS_BYTES = 163840;
static_assert(L_UT + 3 * UT_SZ <= L_KT && L_KD + 3 * 3072 <= T0 && L_SP + 2048 <= L_KT, "lds map");
#define GM_HPRIO 1
#define GM_LAG 4

__device__ __forceinline__ float bf2f(unsigned v) { return __uint_as_float(v << 16); }
__device__ __forceinline__ float bflo(unsigned w) { return __uint_as_float(w << 16); }
__device__ __forceinline__ float bfhi(unsigned w) { return __uint_as_float(w & 0xffff0000u); }
__device__ __forceinline__ unsigned pk_bf16(float a, float b) { f32x2 v = {a, b}; return __builtin_bit_cast(unsigned, __builtin_convertvector(v, bf16x2)); }
__device__ __forceinline__ unsigned pk_f16(float a, float b) { f32x2 v = {a, b}; return __builtin_bit_cast(unsigned, __builtin_convertvector(v, f16x2)); }
__device__ __forceinline__ float rbf(float a) { return bflo(pk_bf16(a, a)); }
__device__ __forceinline__ f32x2 gelu2(f32x2 x) {
    const f32x2 w = (x * x) * (-0.10294324f) + (-2.30220819f);
    const f32x2 a = w * x;
    f32x2 e; e.x = __builtin_amdgcn_exp2f(a.x); e.y = __builtin_amdgcn_exp2f(a.y);
    e = e + 1.0f;
    f32x2 r; r.x = __builtin_amdgcn_rcpf(e.x); r.y = __builtin_amdgcn_rcpf(e.y);
    return x * r;
}
__device__ __forceinline__ f32x4 gelu4(f32x4 v) { const f32x2 a = gelu2((f32x2){v[0], v[1]}), b = gelu2((f32x2){v[2], v[3]}); return (f32x4){a.x, a.y, b.x, b.y}; }
__device__ __forceinline__ float allred_q(float v) {
    unsigned u = __float_as_uint(v);
    auto r = __builtin_amdgcn_permlane16_swap(u, u, false, false);
    v = __uint_as_float(r[0]) + __uint_as_float(r[1]);
    u = __float_as_uint(v);
    auto s = __builtin_amdgcn_permlane32_swap(u, u, false, false);
    return __uint_as_float(s[0]) + __uint_as_float(s[1]);
}
#define ACQ() __builtin_amdgcn_fence(__ATOMIC_ACQUIRE, "wavefront")
#define TRP
#define TRA
#define TRDUMP() do { } while (0)
#define BAR() __builtin_amdgcn_s_barrier()
#define PBAR() BAR()
#define PDUMP() TRDUMP()
#define WAITV(n) asm volatile("s_waitcnt vmcnt(" #n ")" ::: "memory")
#define WAITL0() asm volatile("s_waitcnt lgkmcnt(0)" ::: "memory")
#define CFENCE() asm volatile("" ::: "memory")
#define SGB(mask, n, id) __builtin_amdgcn_sched_group_barrier(mask, n, id)
#define MFMA16(a, b, c) __builtin_amdgcn_mfma_f32_16x16x32_bf16(a, b, c, 0, 0, 0)
__device__ __forceinline__ s16x4 tr_read(const LAS unsigned char* p) { return __builtin_amdgcn_ds_read_tr16_b64_v4i16((LAS s16x4*)p); }
__device__ __forceinline__ bf16x8 cat8(s16x4 a, s16x4 b) { return __builtin_shufflevector(a, b, 0, 1, 2, 3, 4, 5, 6, 7); }
template <class P> __device__ __forceinline__ P opq(P p) { asm volatile("" : "+v"(p)); return p; }
__device__ __forceinline__ int nextbuf(int so) { return so == 2 * STG ? 0 : so + STG; }

#define STAMP(i) do { } while (0)

__device__ __forceinline__ u32x2 flag_ld(rsrc_t ws, unsigned off) { return __builtin_amdgcn_raw_buffer_load_b64(ws, off, 0, 16); }
__device__ __forceinline__ bool flag_all(u32x2 v, u32x2 T, bool active) { return __builtin_amdgcn_ballot_w64(active && (v[0] != T[0] || v[1] != T[1])) == 0ull; }
__device__ __forceinline__ void flag_publish(rsrc_t ws, unsigned off, u32x2 T, int lane) {
    WAITV(0);
    if (lane == 0) __builtin_amdgcn_raw_buffer_store_b64(T, ws, off, 0, 16);
}
__device__ __forceinline__ void flag_wait(rsrc_t ws, unsigned base, int n, u32x2 T, int lane) {
    const unsigned off = base + (unsigned)(lane < n ? lane : 0) * 128u;
    const u64 t0 = __builtin_amdgcn_s_memrealtime();
    for (;;) {
        CFENCE();
        const u32x2 v = flag_ld(ws, off);
        if (flag_all(v, T, true)) break;
        if (__builtin_amdgcn_s_memrealtime() - t0 > 300000ull) break;
        __builtin_amdgcn_s_sleep(1);
    }
    ACQ();
}

#define SB0() __builtin_amdgcn_sched_barrier(0)
template <int NT> struct Frag { bf16x8 a[4]; s16x4 lo[NT], hi[NT]; };
template <int NT, bool TR, int HALF>
__device__ __forceinline__ void frag_read(Frag<NT>& f, int i, const LAS unsigned char* pa, const LAS unsigned char* pb) {
    if (i < 4) f.a[i] = *(const LAS bf16x8*)(pa + i * 2048);
    else if (TR) { const int n = (i - 4) >> 1; if ((i - 4) & 1) f.hi[n] = tr_read(pb + HALF * 9216 + 4608 + n * 32); else f.lo[n] = tr_read(pb + HALF * 9216 + n * 32); }
    else { const int n = i - 4; const bf16x8 v = *(const LAS bf16x8*)(pb + HALF * 9216 + n * 1024);
           f.lo[n] = __builtin_shufflevector(v, v, 0, 1, 2, 3); f.hi[n] = __builtin_shufflevector(v, v, 4, 5, 6, 7); }
}
template <int NT, bool TR, int LHALF, int RPS, bool FIRST, bool UPD>
__device__ __forceinline__ void half_step(f32x4 (&acc)[NT][4], const Frag<NT>& c, Frag<NT>& l, const LAS unsigned char*& pa0, const LAS unsigned char*& pa1, const LAS unsigned char*& pb, int delta, const float* ini = nullptr) {
    constexpr int NRD = TR ? 4 + 2 * NT : 4 + NT, NM = 4 * NT, LAST = (NRD + RPS - 1) / RPS;
#pragma unroll
    for (int i = 0; i < NM; ++i) {
        const int n = i >> 2, m = i & 3;
        acc[n][m] = MFMA16(cat8(c.lo[n], c.hi[n]), c.a[m], FIRST ? (TR ? ((f32x4){0.f, 0.f, 0.f, 0.f}) : ((f32x4){ini[m], ini[m], ini[m], ini[m]})) : acc[n][m]);
        if (LHALF >= 0) {
#pragma unroll
            for (int r = 0; r < RPS; ++r) if (i * RPS + r < NRD) frag_read<NT, TR, (LHALF > 0 ? 1 : 0)>(l, i * RPS + r, LHALF > 0 ? pa1 : pa0, pb);
        }
        if (UPD) { if (i == LAST) { pa0 += delta; asm volatile("" : "+v"(pa0)); } if (i == LAST + 1) { pb += delta; asm volatile("" : "+v"(pb)); } if (i == LAST + 2) { pa1 += delta; asm volatile("" : "+v"(pa1)); } }
        SB0();
    }
}
template <int NT, bool TR, class HK>
__device__ __forceinline__ void gemm_phase(int nsteps, f32x4 (&acc)[NT][4], int& so, const LAS unsigned char* pa0, const LAS unsigned char* pa1, const LAS unsigned char* pb,
                                           const float* ini, const volatile LAS unsigned* rdy, HK hook TRP) {
    constexpr int NRD = TR ? 4 + 2 * NT : 4 + NT;
    Frag<NT> f0, f1;
    pa0 += so; pa1 += so; pb += so;
    asm volatile("" : "+v"(pa0), "+v"(pa1), "+v"(pb));
    __builtin_amdgcn_s_waitcnt(0xC07F);
    BAR();
#pragma unroll
    for (int i = 0; i < NRD; ++i) frag_read<NT, TR, 0>(f0, i, pa0, pb);
    SB0();
    int delta = so == 2 * STG ? -2 * STG : STG; so += delta;
    half_step<NT, TR, 1, 2, true, true>(acc, f0, f1, pa0, pa1, pb, delta, ini);
#pragma nounroll
    for (int s = 1; s < nsteps; ++s) {
        WAITL0();
        BAR();
        unsigned rv = 0;
        if (TR) { rv = *rdy; SB0(); }
        delta = so == 2 * STG ? -2 * STG : STG; so += delta;
        half_step<NT, TR, 0, 1, false, false>(acc, f1, f0, pa0, pa1, pb, 0);
        half_step<NT, TR, 1, 2, false, true>(acc, f0, f1, pa0, pa1, pb, delta);
        if (TR) hook(rv);
    }
    half_step<NT, TR, -1, 1, false, false>(acc, f1, f0, pa0, pa1, pb, 0);
}

template <int NT, class LT>
__device__ __forceinline__ void consumer_a(LAS unsigned char* lds, u32x2 (&zp)[5][4], LT late, int wm, int wn, int lane, unsigned char* wsp_ TRP) {
    const int fr = lane & 15, fq = lane >> 4;
    constexpr int N0 = (NT == 5) ? 0 : 5;
    f32x4 acc[NT][4];
    int so = 0;
    const int sw = (fr >> 1) & 7;
    const int a0 = (wm * 64 + fr) * 128 + ((fq ^ sw) << 4), a1 = (wm * 64 + fr) * 128 + (((4 + fq) ^ sw) << 4);
    const int bw = BOFF + (4 * fq + (fr >> 2)) * 288 + (lane & 3) * 8 + N0 * 32;
    const volatile LAS unsigned* rdy = (const volatile LAS unsigned*)(lds + L_MISC + 4 + wm * 4);
    bool done = false;
    auto normalise = [&]() {
        const LAS unsigned char* ps = opq(lds + L_SP + (wm * 64 + fr) * 16);
        LAS unsigned char* pz = opq(lds + Z0 + (wm * 64 + fr) * ZST + N0 * 32 + fq * 8);
#pragma unroll
        for (int m = 0; m < 4; ++m) {
            const f32x2 sr = *(const LAS f32x2*)(ps + m * 256);
#pragma unroll
            for (int n = 0; n < NT; ++n) {
                const f32x2 u = (f32x2){bflo(zp[n][m][0]), bfhi(zp[n][m][0])} * sr.x + sr.y, v = (f32x2){bflo(zp[n][m][1]), bfhi(zp[n][m][1])} * sr.x + sr.y;
                *(LAS u32x2*)(pz + m * 16 * ZST + n * 32) = (u32x2){pk_bf16(u.x, u.y), pk_bf16(v.x, v.y)};
            }
        }
        WAITL0();
        if (lane == 0) *(volatile LAS unsigned*)(lds + L_MISC + 16 + (wm * 2 + wn) * 4) = 1u;
        done = true;
    };
    auto hook = [&](unsigned rv) { if (__builtin_amdgcn_readfirstlane(rv) != 0u && !done) normalise(); };
#pragma unroll
    for (int n = 0; n < NT; ++n)
#pragma unroll
        for (int m = 0; m < 4; ++m) zp[n][m] = (u32x2){0u, 0u};
#pragma nounroll
    for (int it = 0; it < 2; ++it) {
        gemm_phase<NT, true>(9, acc, so, lds + a0, lds + a1, lds + bw, nullptr, rdy, hook TRA);
        STAMP(1 + 2 * it);
        late();
        if (it == 1 && !done) {
            const u64 t0 = __builtin_amdgcn_s_memrealtime();
            while (__builtin_amdgcn_readfirstlane(*rdy) == 0u && __builtin_amdgcn_s_memrealtime() - t0 < 400000ull) __builtin_amdgcn_s_sleep(1);
            normalise();
        }
#pragma unroll
        for (int m = 0; m < 4; ++m)
#pragma unroll
            for (int n = 0; n < NT; ++n) { acc[n][m] = gelu4(acc[n][m]); zp[n][m] = (u32x2){pk_bf16(acc[n][m][0], acc[n][m][1]), pk_bf16(acc[n][m][2], acc[n][m][3])}; }
        if (it == 0) {
            LAS unsigned char* ps = opq(lds + L_SP + (wm * 64 + fr) * 16 + wn * 8);
#pragma unroll
            for (int m = 0; m < 4; ++m) {
                f32x4 s4 = acc[0][m];
#pragma unroll
                for (int n = 1; n < NT; ++n) s4 += acc[n][m];
                const float mean = opq(allred_q((s4[0] + s4[1]) + (s4[2] + s4[3])) * (1.0f / (16 * NT)));
                f32x4 q4 = {0.f, 0.f, 0.f, 0.f};
#pragma unroll
                for (int n = 0; n < NT; ++n) { const f32x4 d = acc[n][m] - mean; q4 += d * d; }
                const float m2 = allred_q((q4[0] + q4[1]) + (q4[2] + q4[3]));
                if (fq == 0) *(LAS f32x2*)(ps + m * 256) = (f32x2){mean, m2};
            }
            STAMP(2);
        }
    }
    STAMP(4);
}

template <int NT>
__device__ __forceinline__ void consumer_b(LAS unsigned char* lds, rsrc_t ws, u32x2 T, const u32x2 (&z1)[5][4], const float (&bs_)[4], int pm, int pn, int wm, int wn, int lane, unsigned char* wsp_ TRP) {
    float bs[4] = {bf2f(__float_as_uint(opq(bs_[0]))), bf2f(__float_as_uint(opq(bs_[1]))), bf2f(__float_as_uint(opq(bs_[2]))), bf2f(__float_as_uint(opq(bs_[3])))};
    const int fr = lane & 15, fq = lane >> 4;
    constexpr int N0 = (NT == 5) ? 0 : 5;
    f32x4 acc[NT][4];
    int so = 0;
    const int sw = (fr >> 1) & 7;
    const int a0 = (wm * 64 + fr) * 128 + ((fq ^ sw) << 4), a1 = (wm * 64 + fr) * 128 + (((4 + fq) ^ sw) << 4);
    const int bz = BOFF + N0 * 1024 + lane * 16;
    gemm_phase<NT, false>(32, acc, so, lds + a0, lds + a1, lds + bz, bs, nullptr, [](unsigned) {} TRA);
    STAMP(5);
    __builtin_amdgcn_s_setprio(3);
    const int qm = 2 * pm + wm;
    const unsigned hb = WS_HF + (unsigned)qm * (18u * 16384u);
#pragma unroll
    for (int m = 0; m < 4; ++m) {
        u32x2 hp[NT];
#pragma unroll
        for (int n = 0; n < NT; ++n) {
            const f32x2 u = (f32x2){bflo(z1[n][m][0]), bfhi(z1[n][m][0])} * (f32x2){acc[n][m][0], acc[n][m][1]}, v = (f32x2){bflo(z1[n][m][1]), bfhi(z1[n][m][1])} * (f32x2){acc[n][m][2], acc[n][m][3]};
            hp[n] = (u32x2){pk_bf16(u.x, u.y), pk_bf16(v.x, v.y)};
        }
#pragma unroll
        for (int j = 0; j < 2; ++j)
            __builtin_amdgcn_raw_buffer_store_b128((u32x4){hp[2 * j][0], hp[2 * j][1], hp[2 * j + 1][0], hp[2 * j + 1][1]}, ws,
                                                   hb + (unsigned)pn * 16384u + (unsigned)(m * 4 + wn * 2 + j) * 1024u + lane * 16, 0, 16);
        if (NT == 5) __builtin_amdgcn_raw_buffer_store_b64(hp[NT - 1], ws, hb + (unsigned)(16 + (pn >> 3)) * 16384u + m * 4096u + (unsigned)(pn & 7) * 512u + lane * 8, 0, 16);
    }
    flag_publish(ws, WS_FH + (unsigned)(qm * 32 + pn * 2 + wn) * 128u, T, lane);
    __builtin_amdgcn_s_setprio(0);
    STAMP(6);
}

template <int TLO, int THI>
__device__ __forceinline__ void conv_task(LAS unsigned char* lds, int kt, int ut, int yt, int tl, int lane, int& nb TRP) {
#define CSL() do { if (nb < 50) { BAR(); ++nb; } } while (0)
    constexpr int NTL = THI - TLO + 1, ND = THI + 2;
    const int i = lane & 15, g = lane >> 4;
    const LAS unsigned char* pk = opq(lds + kt + (i & 3) * 1088 + ((i >> 2) + g) * 16);
    const LAS unsigned char* pu = opq(lds + ut + (128 + 16 * tl - 32 * g - 4 * (i >> 2) + 4 * (i & 3)) * 2 - 32);
    bf16x8 ub[ND];
#pragma unroll
    for (int d = 0; d < ND; ++d) { if (d == 0 || d == 8) CSL(); ub[d] = cat8(tr_read(pu + 256 * d + 32), tr_read(pu + 256 * d)); }
    f32x4 acc[NTL];
#pragma unroll
    for (int t = 0; t < NTL; ++t) acc[t] = (f32x4){0.f, 0.f, 0.f, 0.f};
#pragma unroll
    for (int s = -1; s <= THI; ++s) {
        if (TLO == 0 ? (s <= 3 || s == 5 || s == 7) : (s & 1) != 0 && s < 12) CSL();
        const bf16x8 kf = *(const LAS bf16x8*)(pk + 64 * (s + 1));
#pragma unroll
        for (int t = TLO; t <= THI; ++t) if (t >= s) acc[t - TLO] = MFMA16(kf, ub[t - s], acc[t - TLO]);
    }
    LAS float* y = opq((LAS float*)(lds + yt) + 32 * g + 16 * tl + i);
    LAS float* cy = opq((LAS float*)(lds + yt + 8192) + (2 * g + tl) * 4 + (i & 3));
#define DPPF(v, ctl) __int_as_float(__builtin_amdgcn_update_dpp(0, __float_as_int(v), ctl, 0xf, 0xf, true))
#pragma unroll
    for (int t = TLO; t <= THI; ++t) {
        if (((t - TLO) % 2) == 0) CSL();
        const f32x4 a = acc[t - TLO];
        y[128 * t] = (a[0] + DPPF(a[1], 0x111)) + (DPPF(a[2], 0x112) + DPPF(a[3], 0x113));
        const float cv = DPPF(a[1], 0x10f) + (DPPF(a[2], 0x10e) + DPPF(a[3], 0x10d));
        if (i < 4) cy[32 * t] = cv;
    }
#undef DPPF
#undef CSL
}
__device__ __forceinline__ int ytab(int c) { return Z0 + c * Y_SZ; }

__global__ __launch_bounds__(512, 2) void gm_kernel(const u16* __restrict__ x, const u16* __restrict__ logA, const u16* __restrict__ Aim, const u16* __restrict__ Cre,
                                                    const u16* __restrict__ Cim, const u16* __restrict__ logdt, const u16* __restrict__ Dsk, const u16* __restrict__ win,
                                                    const u16* __restrict__ wsp, const u16* __restrict__ bsp, const u16* __restrict__ wout, const u16* __restrict__ gw,
                                                    u16* __restrict__ out, unsigned char* wsp_) {
    extern __shared__ __attribute__((aligned(16))) unsigned char lds_[];
    LAS unsigned char* lds = (LAS unsigned char*)lds_;
    const int wid = __builtin_amdgcn_readfirstlane(threadIdx.x >> 6), lane = threadIdx.x & 63;
    const int bid = blockIdx.x, xg = bid & 7, rr = bid >> 3, bi = rr >> 2, bj = rr & 3;
    const int pm = (xg >> 2) * 8 + bi, pn = (xg & 3) * 4 + bj;
    const int qm = 2 * pm + (pn >> 3), qn = pn & 7;
    const int nch = pm < 4 ? 3 : 2;
    const rsrc_t ws = __builtin_amdgcn_make_buffer_rsrc(wsp_, 0, WS_END, 0x00020000);
    STAMP(0);
    const u64 dg0 = __builtin_amdgcn_s_memrealtime(); u64 dgA = dg0, dgB = dg0;
#define MARK(i) do { if (GM_DA == (i)) dgA = __builtin_amdgcn_s_memrealtime(); if (GM_DB == (i)) dgB = __builtin_amdgcn_s_memrealtime(); } while (0)

    const int fr = lane & 15, fq = lane >> 4;
    const int h = wid - 6, p = wid - 4;
    const bool is_prod = (wid >> 1) == 2;
    const int ch0 = pn * 36 + pm;
    float bs[4];
    auto late = [&]() {
#pragma unroll
        for (int m = 0; m < 4; ++m) bs[m] = __uint_as_float((unsigned)bsp[pm * 128 + (wid & 1) * 64 + m * 16 + fr]);
    };
    const int phi = (-(bi + bj)) & 3;
    const unsigned rsw = (unsigned)(((lane & 7) ^ ((4 * p + (lane >> 4)) & 7)) * 16);
#define GLDS(src, dst, aux) __builtin_amdgcn_global_load_lds((const unsigned*)(src), (LAS unsigned*)(dst), 16, 0, aux)
    auto issueS = [&](int k, int s) {
        const char* sb = (const char*)wsp + (size_t)(pm * 128 + p * 8) * 4096 + k * 128; const unsigned vs = (unsigned)(lane >> 3) * 4096u + rsw;
#pragma unroll
        for (int i = 0; i < 8; ++i) GLDS(sb + (size_t)i * (16 * 4096) + vs, lds + s + (p + 2 * i) * 1024, 0); };
    auto ord2 = [&](int s) { return GM_LAG == 0 ? s : (((s & 3) == phi) ? s : ((s - GM_LAG) & 31)); };
    u32x2 E, T;
    auto mk_tag = [&]() { u64 z = (((u64)__builtin_amdgcn_readfirstlane(E[1]) << 32) | __builtin_amdgcn_readfirstlane(E[0])) + 0x9E3779B97F4A7C15ull;
        z = (z ^ (z >> 30)) * 0xBF58476D1CE4E5B9ull; z = (z ^ (z >> 27)) * 0x94D049BB133111EBull; z ^= z >> 31; T = (u32x2){(unsigned)z, (unsigned)(z >> 32)}; };
    u32x2 zp[5][4];
#define STEP(lim) do { if (nb < (lim)) { BAR(); ++nb; } } while (0)
#define STEPS_TO(lim) _Pragma("nounroll") while (nb < (lim)) { BAR(); ++nb; }
    int nb = 0;
    if (wid < nch) {
        const int kch = ch0 + 16 * wid, ki = kch * 64 + lane;
        const unsigned pA = ((const unsigned*)logA)[ki], pI = ((const unsigned*)Aim)[ki], pR = ((const unsigned*)Cre)[ki], pC = ((const unsigned*)Cim)[ki], pT = logdt[kch];
        LAS u32x2* tw = opq((LAS u32x2*)(lds + Z0 + wid * 8448) + lane);
        const LAS unsigned char* trd = opq(lds + Z0 + wid * 8448 + fr * 528 + fq * 16);
        const float dt = rbf(__expf(bf2f(pT)));
        const f32x2 ar = (f32x2){rbf(__expf(bflo(pA))), rbf(__expf(bfhi(pA)))} * dt * -1.44269504f, ai = (f32x2){bflo(pI), bfhi(pI)} * dt * 0.15915494f;
        const f32x2 cr = (f32x2){bflo(pR), bfhi(pR)} * dt, ci = (f32x2){bflo(pC), bfhi(pC)} * dt;
        float cm = fmaxf(fmaxf(fabsf(cr.x), fabsf(ci.x)), fmaxf(fabsf(cr.y), fabsf(ci.y)));
#define DPPF(v, ctl) __int_as_float(__builtin_amdgcn_update_dpp(0, __float_as_int(v), ctl, 0xf, 0xf, true))
        cm = fmaxf(cm, DPPF(cm, 0x121)); cm = fmaxf(cm, DPPF(cm, 0x122)); cm = fmaxf(cm, DPPF(cm, 0x124)); cm = fmaxf(cm, DPPF(cm, 0x128));
#undef DPPF
        { auto r = __builtin_amdgcn_permlane16_swap(__float_as_uint(cm), __float_as_uint(cm), false, false); cm = fmaxf(__uint_as_float(r[0]), __uint_as_float(r[1]));
          auto q = __builtin_amdgcn_permlane32_swap(__float_as_uint(cm), __float_as_uint(cm), false, false); cm = fmaxf(fmaxf(__uint_as_float(q[0]), __uint_as_float(q[1])), 1e-30f); }
        struct C2 { f32x2 x, y; };
        auto cexp = [&](float p_) { const f32x2 e = ar * p_, t = ai * p_;
            const f32x2 mg = {__builtin_amdgcn_exp2f(e.x), __builtin_amdgcn_exp2f(e.y)}; const float t0 = __builtin_amdgcn_fractf(t.x), t1 = __builtin_amdgcn_fractf(t.y);
            return C2{mg * (f32x2){__builtin_amdgcn_cosf(t0), __builtin_amdgcn_cosf(t1)}, mg * (f32x2){__builtin_amdgcn_sinf(t0), __builtin_amdgcn_sinf(t1)}}; };
        auto cmul = [](C2 a, C2 b) { return C2{__builtin_elementwise_fma(a.x, b.x, -(a.y * b.y)), __builtin_elementwise_fma(a.x, b.y, a.y * b.x)}; };
        auto put = [&](int row, C2 v, bool conj) { tw[row * 66] = conj ? (u32x2){pk_f16(v.x.x, -v.y.x), pk_f16(v.x.y, -v.y.y)} : (u32x2){pk_f16(v.x.x, v.y.x), pk_f16(v.x.y, v.y.y)}; };
        C2 wp[16], zr[8], vp[4], cv[17];
        wp[1] = cexp(8.f); zr[1] = cexp(1.f); vp[0] = cexp(128.f);
        tw[0] = (u32x2){pk_f16(1.f, 0.f), pk_f16(1.f, 0.f)};
#pragma unroll
        for (int j = 1; j < 16; ++j) { if (j > 1) wp[j] = cmul(wp[j >> 1], wp[j - (j >> 1)]); put(j, wp[j], true); }
        CFENCE();
        u32x4 fb[8];
#pragma unroll
        for (int ks = 0; ks < 8; ++ks) fb[ks] = *(const LAS u32x4*)(trd + ks * 64);
        CFENCE();
#pragma unroll
        for (int r = 2; r < 8; ++r) zr[r] = cmul(zr[r >> 1], zr[r - (r >> 1)]);
#pragma unroll
        for (int k = 1; k < 4; ++k) vp[k] = cmul(vp[k - 1], vp[k - 1]);
        const float rc = __builtin_amdgcn_rcpf(cm);
        cv[0] = C2{cr * rc, ci * rc};
#pragma unroll
        for (int k = 1; k < 8; ++k) { const int lg = k >= 4 ? 2 : k >= 2 ? 1 : 0; cv[k] = cmul(cv[k - (1 << lg)], vp[lg]); }
        LAS float* kb_ = (LAS float*)(lds + L_KD + wid * 3072);
        const float sc = 2.0f * cm;
        u32x4 fa[3][8];
#pragma unroll
        for (int tt = 0; tt < 3; ++tt) {
#pragma unroll
            for (int i = 0; i < (tt == 2 ? 9 : 16); ++i) {
                const int r = tt == 0 ? (i & 7) : tt == 1 ? (i < 8 ? 2 * (i & 3) : 4 * (i & 1)) : 0;
                const int hh = tt == 0 ? (i >> 3) : tt == 1 ? (i < 8 ? 2 + (i >> 2) : 4 + ((i - 8) >> 1)) : 8 + i;
                if (tt == 2) cv[hh] = cmul(cv[hh - 8], vp[3]);
                put(i, r == 0 ? cv[hh] : cmul(cv[hh], zr[r]), false);
            }
            CFENCE();
#pragma unroll
            for (int ks = 0; ks < 8; ++ks) fa[tt][ks] = *(const LAS u32x4*)(trd + ks * 64);
            CFENCE();
            SB0();
        }
        f32x4 kd[3] = {{0.f, 0.f, 0.f, 0.f}, {0.f, 0.f, 0.f, 0.f}, {0.f, 0.f, 0.f, 0.f}};
#pragma unroll
        for (int ks = 0; ks < 8; ++ks)
#pragma unroll
            for (int tt = 0; tt < 3; ++tt) kd[tt] = __builtin_amdgcn_mfma_f32_16x16x32_f16(__builtin_bit_cast(f16x8, fa[tt][ks]), __builtin_bit_cast(f16x8, fb[ks]), kd[tt], 0, 0, 0);
#pragma unroll
        for (int tt = 0; tt < 3; ++tt) {
            LAS float* kw = opq(kb_ + (tt == 0 ? 128 * (fq >> 1) + 4 * (fq & 1) + 8 * fr : tt == 1 ? 256 + 64 * fq + (fq < 2 ? 4 : 2) * fr : 512 + 64 * fq + fr));
            LAS float* kwb = tt == 1 ? opq(kw + (fq < 2 ? 2 : 32)) : kw + (tt == 0 ? 2 : 32);
            kw[0] = kd[tt][0] * sc; kw[tt == 2 ? 16 : 1] = kd[tt][1] * sc; kwb[0] = kd[tt][2] * sc; kwb[tt == 2 ? 16 : 1] = kd[tt][3] * sc;
        }
        WAITL0();
    }
    if (GM_PF && wid == 3) {
        unsigned acc_ = 0;
        if (GM_PF & 1) { const char* pc = (const char*)(__builtin_amdgcn_s_getpc() & ~127ull) - 1024; if (lane < 11) acc_ += *(const unsigned*)(pc + (rr * 11 + lane) * 128); }
        if (GM_PF & 2) {
            acc_ += *(const unsigned*)((const char*)wsp + (size_t)(pm * 128 + lane) * 4096) + *(const unsigned*)((const char*)wsp + (size_t)(pm * 128 + 64 + lane) * 4096);
#pragma unroll
            for (int i = 0; i < 3; ++i) if (i * 64 + lane < 144) acc_ += __builtin_amdgcn_raw_buffer_load_b32(ws, WS_Z2F + (unsigned)pn * (64u * 9216u) + (i * 64 + lane) * 4096u, 0, 16);
            acc_ += __builtin_amdgcn_raw_buffer_load_b32(ws, WS_HF + (unsigned)qm * (18u * 16384u) + lane * 4096u, 0, 16);
            if (lane < 8) acc_ += __builtin_amdgcn_raw_buffer_load_b32(ws, WS_HF + (unsigned)qm * (18u * 16384u) + (64 + lane) * 4096u, 0, 16);
            acc_ += __builtin_amdgcn_raw_buffer_load_b32(ws, WS_HF + (unsigned)(qm ^ 1) * (18u * 16384u) + lane * 4096u, 0, 16);
            acc_ += __builtin_amdgcn_raw_buffer_load_b32(ws, WS_WOF + (unsigned)qn * (18u * 18432u) + lane * 4096u, 0, 16);
            if (lane < 17) acc_ += __builtin_amdgcn_raw_buffer_load_b32(ws, WS_WOF + (unsigned)qn * (18u * 18432u) + (64 + lane) * 4096u, 0, 16);
            acc_ += __builtin_amdgcn_raw_buffer_load_b32(ws, WS_AT + (unsigned)(qn * 72 + lane) * 4096u, 0, 16);
            if (lane < 8) acc_ += __builtin_amdgcn_raw_buffer_load_b32(ws, WS_AT + (unsigned)(qn * 72 + 64 + lane) * 4096u, 0, 16);
            if (lane < 36) acc_ += __builtin_amdgcn_raw_buffer_load_b32(ws, WS_UT + (unsigned)(pn * 36 + lane) * 4096u, 0, 16) + __builtin_amdgcn_raw_buffer_load_b32(ws, WS_AT + (unsigned)(pn * 36 + lane) * 4096u, 0, 16);
            acc_ += __builtin_amdgcn_raw_buffer_load_b32(ws, lane * 16384u, 0, 16);
            if (lane < 18) acc_ += *(const unsigned*)((const char*)out + (size_t)qm * 64 * 1152 + lane * 4096);
        }
        if (GM_PF & 4) {
#pragma unroll
            for (int i = 0; i < 4; ++i) acc_ += *(const unsigned*)((const char*)wsp + (size_t)bid * 32768 + (i * 64 + lane) * 128);
        }
        asm volatile("" :: "v"(acc_));
    }
#define IC(n) std::integral_constant<int, n>{}
    if (wid < 6) STAMP(10);
    if (is_prod) {
        const unsigned vx = (unsigned)(lane >> 3) * 1152u + rsw;
        unsigned wv[9];
#pragma unroll
        for (int i = 0; i < 9; ++i) { const int c = (p + 2 * i) * 64 + lane, rho = c / 18, ch = c - rho * 18;
            const int kap = (rho & 32) + 8 * ((rho >> 2) & 3) + 4 * ((rho >> 4) & 1) + (rho & 3); wv[i] = (unsigned)kap * 9216u + (unsigned)ch * 16u; }
        const char* xb = (const char*)x + (size_t)(pm * 128 + p * 8) * 1152;
        const char* wb = (const char*)win + pn * 288;
        auto issueX = [&](int k, int s) {
#pragma unroll
            for (int i = 0; i < 8; ++i) GLDS(xb + (size_t)i * (16 * 1152) + k * 128 + vx, lds + s + (p + 2 * i) * 1024, 0); };
        auto issueW = [&](int k, int coloff, int s) {
            const char* b = wb + (size_t)k * (64 * 9216) + coloff;
#pragma unroll
            for (int i = 0; i < 9; ++i) GLDS(b + wv[i], lds + s + BOFF + (p + 2 * i) * 1024, 0); };
        auto ord1 = [&](int s) { return (GM_LAG == 0 || (s & 3) == phi) ? s : (s >= 4 ? s - 4 : (s == 0 ? 8 : s + 4)); };
        issueX(ord1(0), 0); issueW(ord1(0), FH * 2, 0); issueX(ord1(1), STG); issueW(ord1(1), FH * 2, STG);
        issueX(ord1(2), 2 * STG); issueW(ord1(2), FH * 2, 2 * STG);
        E = flag_ld(ws, WS_E);
        int so = 0;
        WAITV(35); PBAR(); MARK(1);
#pragma nounroll
        for (int S = 1; S < 16; ++S) {
            WAITV(17); PBAR(); if (S == 9) MARK(2);
            const int t = S + 2, k = ord1(t >= 9 ? t - 9 : t);
            issueX(k, so); issueW(k, t >= 9 ? 0 : FH * 2, so); so = nextbuf(so);
        }
        WAITV(17); PBAR(); issueS(ord2(0), 0);
        WAITV(8); PBAR(); issueS(ord2(1), STG);
        STAMP(1);
    } else if (wid < 4) {
        E = flag_ld(ws, WS_E);
        if (wid >> 1) consumer_a<4>(lds, zp, late, wid & 1, 1, lane, wsp_ TRA);
        else consumer_a<5>(lds, zp, late, wid & 1, 0, lane, wsp_ TRA);
    } else {
        if (h == 0 && lane < 12) *(LAS unsigned*)(lds + L_MISC + lane * 4) = 0u;
        const int row0 = lane / 9, chk0 = lane - row0 * 9;
        const int u = (pn >> 3) * 16 + pm, nblk = u < 8 ? 3 : 2;
        u32x2 uc[18]; u32x4 xv[9], gv[9], wv[3][5];
        if (h == 0) {
            { const char* xb = (const char*)x + (size_t)pm * 128 * 1152 + pn * 72; unsigned off = row0 * 1152 + chk0 * 8; int ch = chk0;
#pragma unroll
              for (int i = 0; i < 9; ++i) { uc[i] = *(const u32x2*)(xb + off); uc[9 + i] = *(const u32x2*)(xb + 64 * 1152 + off); off += 7 * 1152 + 8; if (++ch == 9) { ch = 0; off += 1152 - 72; } } }
            { const char* xr = (const char*)x + (size_t)(pm * 128 + pn * 8 + (lane >> 3)) * 1152 + (lane & 7) * 16; const char* gr = (const char*)gw + (lane & 7) * 16;
#pragma unroll
              for (int i = 0; i < 9; ++i) { xv[i] = *(const u32x4*)(xr + i * 128); gv[i] = *(const u32x4*)(gr + i * 128); } }
        } else {
            unsigned wo_[5], hi_[5];
            { int row = row0, ch = chk0;
#pragma unroll
              for (int i = 0; i < 5; ++i) { const int rw = row < 32 ? row : 31; wo_[i] = (rw & 15) * 1152 + ch * 16; hi_[i] = rw >> 4; row += 7; if (++ch == 9) { ch = 0; ++row; } } }
#pragma unroll
            for (int b = 0; b < 3; ++b) {
                const int blk = (b < nblk) ? u + 32 * b : u, st = blk >> 2, jb = blk & 3;
                const int fA = st < 16 ? st * 144 + (jb < 2 ? 32 * jb : 16 + 32 * jb) : ((st - 16) * 8 + 2 * jb) * 144 + 64, dB = (st < 16 ? 16 : 144) * 1152;
                const char* wb_ = (const char*)wout + (size_t)fA * 1152 + qn * 144;
#pragma unroll
                for (int i = 0; i < 5; ++i) wv[b][i] = *(const u32x4*)(wb_ + (wo_[i] + hi_[i] * dB));
            }
        }
        E = flag_ld(ws, WS_E);
        if (lane < 24) *(LAS u32x4*)(lds + L_KT + (lane >> 3) * KT_SZ + (2 * h + ((lane >> 2) & 1)) * 1088 + (lane & 3) * 16) = (u32x4){0u, 0u, 0u, 0u};
        WAITL0();
        STAMP(10);
        __builtin_amdgcn_s_setprio(GM_HPRIO);
        {
            float kv[2][3];
#pragma nounroll
            for (int ei = 0; ei < 9; ++ei) {
                BAR();
                if (ei > 0) {
                    LAS unsigned char* kt = opq(lds + L_KT + (2 * h + ((ei - 1) >> 2)) * 1088 + 64 + ((ei - 1) & 3) * 256 + lane * 2);
#pragma unroll
                    for (int j = 0; j < 2; ++j)
#pragma unroll
                        for (int c = 0; c < 3; ++c) *(LAS u16*)(kt + c * KT_SZ + j * 128) = (u16)pk_bf16(kv[j][c], 0.f);
                }
#pragma unroll
                for (int j = 0; j < 2; ++j) {
                    const int jj = (ei & 3) * 2 + j, e = jj >= 4 ? 3 : jj >= 2 ? 2 : jj, lag = 256 * jj + 4 * lane + 2 * h + ((ei >> 2) & 1);
                    const int idx = e == 0 ? lag : 128 * e + ((lag + (1 << (e - 1)) - 1 + ((lag >> e) & 1)) >> e);
                    const LAS float* q = opq((const LAS float*)(lds + L_KD) + idx);
#pragma unroll
                    for (int c = 0; c < 3; ++c) kv[j][c] = q[c * 768];
                }
            }
        }
        if (h == 0) {
            LAS unsigned char* pzs = opq(lds + Z0 + lane * 8);
#pragma unroll
            for (int i = 0; i < 18; ++i) *(LAS u32x2*)(pzs + i * 512) = uc[i];
            CFENCE();
            { const LAS unsigned char* pt = opq(lds + Z0 + (8 * (4 * fq + (lane & 3)) + (fr >> 2)) * 72);
              const unsigned ub = WS_UT + (unsigned)(pn * 36 + (lane & 3)) * 4096u + (unsigned)(pm * 128 + 8 * (4 * fq + (fr >> 2))) * 2u;
#pragma unroll
              for (int cc = 0; cc < 9; ++cc) __builtin_amdgcn_raw_buffer_store_b128(__builtin_bit_cast(u32x4, cat8(tr_read(pt + cc * 8), tr_read(pt + 288 + cc * 8))), ws, ub + cc * 16384u, 0, 16); }
            float a = 0.f;
#pragma unroll
            for (int i = 0; i < 9; ++i)
#pragma unroll
                for (int j = 0; j < 4; ++j) { const unsigned xa = xv[i][j], ga = gv[i][j]; a = __builtin_amdgcn_fdot2_f32_bf16(__builtin_bit_cast(bf16x2, xa), __builtin_bit_cast(bf16x2, ga), a, false); }
#define DPPF(v, ctl) __int_as_float(__builtin_amdgcn_update_dpp(0, __float_as_int(v), ctl, 0xf, 0xf, true))
            a += DPPF(a, 0xb1); a += DPPF(a, 0x4e); a += DPPF(a, 0x141);
#undef DPPF
            const float gt = rbf(__builtin_amdgcn_rcpf(1.0f + __expf(-rbf(a))));
            if ((lane & 7) == 0) __builtin_amdgcn_raw_buffer_store_b32(__float_as_uint(gt), ws, WS_GATE + (unsigned)(pm * 128 + pn * 8 + (lane >> 3)) * 4u, 0, 16);
        } else {
            LAS unsigned char* pws = opq(lds + Z0 + 9216 + lane * 16);
            const LAS unsigned char* ptw = opq(lds + Z0 + 9216 + (4 * fq + (fr >> 2)) * 144 + (lane & 3) * 8);
#pragma unroll
            for (int b = 0; b < 3; ++b) {
                if (b < nblk) {
#pragma unroll
                    for (int i = 0; i < 5; ++i) if (i < 4 || lane < 32) *(LAS u32x4*)(pws + i * 1024) = wv[b][i];
                    CFENCE();
                    const unsigned ob = WS_WOF + (unsigned)qn * (18u * 18432u) + (unsigned)(u + 32 * b) * 4608u;
#pragma unroll
                    for (int nn = 0; nn < 5; ++nn) {
                        const u32x4 v = __builtin_bit_cast(u32x4, cat8(tr_read(ptw + nn * 32), tr_read(ptw + 2304 + nn * 32)));
                        if (nn < 4) __builtin_amdgcn_raw_buffer_store_b128(v, ws, ob + nn * 1024 + lane * 16, 0, 16);
                        else if (fr < 8) __builtin_amdgcn_raw_buffer_store_b128(v, ws, ob + 4096 + (fq * 8 + fr) * 16, 0, 16);
                    }
                    CFENCE();
                }
            }
        }
        nb = 9;
        mk_tag();
        WAITL0();
        STEPS_TO(10);
        STAMP(1);
        {
            const f32x4 sp = *(const LAS f32x4*)(lds + L_SP + (h * 64 + lane) * 16);
            const float dm = sp[0] - sp[2];
            const f32x2 mine = {(80.f * sp[0] + 64.f * sp[2]) * (1.0f / 144.f), sp[1] + sp[3] + (80.f * 64.f / 144.f) * dm * dm};
            __builtin_amdgcn_raw_buffer_store_b128((u32x4){T[0], __float_as_uint(mine.x), T[1], __float_as_uint(mine.y)}, ws, WS_ST + (unsigned)((pm * 16 + pn) * 128 + h * 64 + lane) * 16u, 0, 16);
            STEP(18);
            const unsigned so_ = WS_ST + (unsigned)(pm * 16 * 128 + h * 64 + lane) * 16u;
            const u64 t0 = __builtin_amdgcn_s_memrealtime();
            u32x4 st[16];
            for (;;) {
                CFENCE();
#pragma unroll
                for (int j = 0; j < 16; ++j) st[j] = __builtin_amdgcn_raw_buffer_load_b128(ws, so_ + j * 2048u, 0, 16);
                STEP(18);
                unsigned bad = 0;
#pragma unroll
                for (int j = 0; j < 16; ++j) bad |= (st[j][0] ^ T[0]) | (st[j][2] ^ T[1]);
                if (__builtin_amdgcn_ballot_w64(bad != 0u) == 0ull) break;
                if (__builtin_amdgcn_s_memrealtime() - t0 > 300000ull) break;
            }
            STAMP(2);
            STEP(18);
            float mean = 0.f, m2 = 0.f;
#pragma unroll
            for (int j = 0; j < 16; ++j) mean += __uint_as_float(st[j][1]);
            mean *= (1.0f / 16.f);
#pragma unroll
            for (int j = 0; j < 16; ++j) { const float d = __uint_as_float(st[j][1]) - mean; m2 += __uint_as_float(st[j][3]) + 144.f * d * d; }
            const float rstd = __builtin_amdgcn_rsqf(m2 * (1.0f / FH) + 1e-5f);
            *(LAS f32x2*)(lds + L_SP + (h * 64 + lane) * 16) = (f32x2){rstd, -mean * rstd};
            WAITL0();
            if (lane == 0) *(volatile LAS unsigned*)(lds + L_MISC + 4 + h * 4) = 1u;
            const volatile LAS unsigned* zd = (const volatile LAS unsigned*)(lds + L_MISC + 16 + h * 8 + (lane & 1) * 4);
            for (;;) {
                STEP(18);
                if (__builtin_amdgcn_ballot_w64(*zd == 0u) == 0ull) break;
                if (__builtin_amdgcn_s_memrealtime() - t0 > 800000ull) break;
            }
            CFENCE();
            const LAS unsigned char* ptz = opq(lds + Z0 + (h * 64 + 8 * fq + (fr >> 2)) * ZST + (lane & 3) * 8);
            const unsigned zo = WS_Z2F + (unsigned)((pn * 64 + pm * 4 + h * 2) * 9) * 1024u + lane * 16;
#pragma unroll
            for (int i = 0; i < 18; ++i) {
                const LAS unsigned char* pt = ptz + (i / 9) * 32 * ZST + (i % 9) * 32;
                __builtin_amdgcn_raw_buffer_store_b128(__builtin_bit_cast(u32x4, cat8(tr_read(pt), tr_read(pt + 4 * ZST))), ws, zo + i * 1024, 0, 16);
                if (i == 5 || i == 11) STEP(18);
            }
            STEP(18);
            flag_publish(ws, WS_FZ + (unsigned)(pn * 32 + pm * 2 + h) * 128u, T, lane);
            STAMP(3);
            STEPS_TO(18);
            __builtin_amdgcn_s_setprio(0);
        }
    }

    if (is_prod) {
        mk_tag();
        const char* lin = (const char*)wsp_ + p * 1024 + lane * 16;
        auto issueLA = [&](unsigned off, int s) {
#pragma unroll
            for (int i = 0; i < 8; ++i) GLDS(lin + off + i * 2048, lds + s + (p + 2 * i) * 1024, 16); };
        auto issueLB = [&](unsigned off, int s) {
#pragma unroll
            for (int i = 0; i < 9; ++i) GLDS(lin + off + i * 2048, lds + s + BOFF + (p + 2 * i) * 1024, 16); };
        auto ord3 = [&](int s) { return (GM_LAG == 0 || s >= 16) ? s : (((s & 3) == phi) ? s : ((s - GM_LAG) & 15)); };
        const unsigned zb = WS_Z2F + (unsigned)pn * (64u * 9216u);
        flag_wait(ws, WS_FZ + (unsigned)pn * 32u * 128u, 32, T, lane);
        STAMP(2);
        issueLB(zb + (unsigned)ord2(0) * 18432u, 0); issueLB(zb + (unsigned)ord2(1) * 18432u, STG);
        WAITV(9); PBAR(); MARK(3);
        int so = 2 * STG;
        { const int k = ord2(2); issueS(k, so); issueLB(zb + (unsigned)k * 18432u, so); so = nextbuf(so); }
#pragma nounroll
        for (int S = 19; S < 48; ++S) {
            WAITV(17); PBAR();
            const int k = ord2(S - 16); issueS(k, so); issueLB(zb + (unsigned)k * 18432u, so); so = nextbuf(so);
        }
        const unsigned hb = WS_HF + (unsigned)qm * (18u * 16384u), ob = WS_WOF + (unsigned)qn * (18u * 18432u);
        WAITV(17); PBAR();
        flag_wait(ws, WS_FZ + (unsigned)(pn ^ 8) * 32u * 128u, 32, T, lane);
        issueLB(ob + (unsigned)ord3(0) * 18432u, 2 * STG);
        PBAR(); MARK(4);
        issueLB(ob + (unsigned)ord3(1) * 18432u, 0);
        STAMP(3);
        flag_wait(ws, WS_FH + (unsigned)qm * 32u * 128u, 32, T, lane);
        STAMP(4);
        issueLA(hb + (unsigned)ord3(0) * 16384u, 2 * STG); issueLA(hb + (unsigned)ord3(1) * 16384u, 0);
        WAITV(8); PBAR(); MARK(5);
        so = STG;
        { const int k = ord3(2); issueLA(hb + (unsigned)k * 16384u, so); issueLB(ob + (unsigned)k * 18432u, so); so = nextbuf(so); }
#pragma nounroll
        for (int S = 51; S < 66; ++S) {
            WAITV(17); PBAR();
            const int k = ord3(S - 48); issueLA(hb + (unsigned)k * 16384u, so); issueLB(ob + (unsigned)k * 18432u, so); so = nextbuf(so);
        }
        WAITV(17); PBAR();
        WAITV(0); PBAR();
        PBAR(); MARK(6);
        { const u64 now = __builtin_amdgcn_s_memrealtime(); const u64 until = GM_DA < 0 ? dg0 + 10000ull : now + (dgB - dgA);
          while (__builtin_amdgcn_s_memrealtime() < until) __builtin_amdgcn_s_sleep(1); }
        STAMP(5); PDUMP();
        if (bid == 0 && p == 0 && lane == 0) __builtin_amdgcn_raw_buffer_store_b64(T, ws, WS_E, 0, 16);
        return;
    }

    if (wid < 4) {
        mk_tag();
        if (wid >> 1) consumer_b<4>(lds, ws, T, zp, bs, pm, pn, wid & 1, 1, lane, wsp_ TRA);
        else consumer_b<5>(lds, ws, T, zp, bs, pm, pn, wid & 1, 0, lane, wsp_ TRA);
    } else {
        __builtin_amdgcn_s_setprio(GM_HPRIO);
        STEPS_TO(19);
        u32x4 uu[3][2];
#pragma unroll
        for (int c = 0; c < 3; ++c)
#pragma unroll
            for (int j = 0; j < 2; ++j) uu[c][j] = __builtin_amdgcn_raw_buffer_load_b128(ws, WS_UT + (unsigned)(ch0 + (c < nch ? 16 * c : 0)) * 4096u + h * 2048 + j * 1024 + lane * 16, 0, 16);
        STEPS_TO(22);
#pragma unroll
        for (int c = 0; c < 3; ++c) {
            BAR(); ++nb;
#pragma unroll
            for (int j = 0; j < 2; ++j) *(LAS u32x4*)(lds + L_UT + c * UT_SZ + 256 + h * 2048 + j * 1024 + lane * 16) = uu[c][j];
            if (lane < 16) *(LAS u32x4*)(lds + L_UT + c * UT_SZ + h * 4352 + lane * 16) = (u32x4){0u, 0u, 0u, 0u};
        }
        WAITL0();
        BAR(); ++nb;
        STAMP(4);
    }

    if (wid < 4) nb = 50;
    for (;;) {
        if (nb > 45) STEPS_TO(50);
        unsigned tk = 0;
        if (lane == 0) tk = __hip_atomic_fetch_add((LAS unsigned*)(lds + L_MISC), 1u, __ATOMIC_RELAXED, __HIP_MEMORY_SCOPE_WORKGROUP);
        tk = __builtin_amdgcn_readfirstlane(tk);
        if (tk >= (unsigned)(4 * nch)) break;
        const int c = tk >> 2, tl = tk & 1;
        if (tk & 2) conv_task<0, 10>(lds, L_KT + c * KT_SZ, L_UT + c * UT_SZ, ytab(c), tl, lane, nb TRA);
        else conv_task<11, 15>(lds, L_KT + c * KT_SZ, L_UT + c * UT_SZ, ytab(c), tl, lane, nb TRA);
    }
    STEPS_TO(50);
    WAITL0();
    STAMP(7);

    if (wid < 4) {
        const int kq = wid >> 1, mh = wid & 1;
        f32x4 acc[5][2];
        struct F3 { s16x4 alo[4], ahi[4]; bf16x8 b[10]; } f0, f1;
        const LAS unsigned char* ph = lds + 2 * STG + mh * 8192 + kq * 2048 + lane * 16;
        const LAS unsigned char* pw = lds + 2 * STG + BOFF + kq * 9216 + lane * 16;
        const LAS unsigned char* pw4 = lds + 2 * STG + BOFF + kq * 9216 + 4096 + (fq * 8 + (fr & 7)) * 16;
        auto rd3 = [&](F3& f, int i, bool single) {
            const int j = i / 7, r = i % 7;
            if (r < 2) {
                if (!single) { const bf16x8 v = *(const LAS bf16x8*)(ph + r * 4096 + j * 1024); f.alo[j * 2 + r] = __builtin_shufflevector(v, v, 0, 1, 2, 3); f.ahi[j * 2 + r] = __builtin_shufflevector(v, v, 4, 5, 6, 7); }
                else { const LAS unsigned char* q = ph - lane * 8 + r * 4096 + j * 1024; f.alo[j * 2 + r] = *(const LAS s16x4*)q; f.ahi[j * 2 + r] = *(const LAS s16x4*)(q + 512); }
            } else if (r < 6) f.b[j * 5 + r - 2] = *(const LAS bf16x8*)(pw + j * 4608 + (r - 2) * 1024);
            else f.b[j * 5 + 4] = *(const LAS bf16x8*)(pw4 + j * 4608);
        };
        int so = 2 * STG;
        auto step3 = [&](const F3& c, F3& l, int mode  , bool first) {
            const int delta = so == 2 * STG ? -2 * STG : STG; so += delta;
#pragma unroll
            for (int i = 0; i < 20; ++i) {
                const int j = i / 10, n = (i % 10) >> 1, m = i & 1;
                acc[n][m] = MFMA16(c.b[j * 5 + n], cat8(c.alo[j * 2 + m], c.ahi[j * 2 + m]), (first && j == 0) ? ((f32x4){0.f, 0.f, 0.f, 0.f}) : acc[n][m]);
                if (mode < 2 && i < 7) { rd3(l, 2 * i, mode == 1); rd3(l, 2 * i + 1, mode == 1); }
                if (mode < 2) { if (i == 8) { ph += delta; asm volatile("" : "+v"(ph)); } if (i == 9) { pw += delta; asm volatile("" : "+v"(pw)); } if (i == 10) { pw4 += delta; asm volatile("" : "+v"(pw4)); } }
                SB0();
            }
        };
        __builtin_amdgcn_s_waitcnt(0xC07F);
        BAR();
#pragma unroll
        for (int i = 0; i < 14; ++i) rd3(f0, i, false);
        ph -= 2 * STG; pw -= 2 * STG; pw4 -= 2 * STG; so = 0;
        WAITL0(); BAR(); step3(f0, f1, 0, true);
#pragma nounroll
        for (int j = 0; j < 7; ++j) {
            WAITL0(); BAR(); step3(f1, f0, 0, false);
            WAITL0(); BAR(); step3(f0, f1, 0, false);
        }
        WAITL0(); BAR(); step3(f1, f0, 1, false);
        WAITL0(); BAR(); step3(f0, f1, 1, false);
        step3(f1, f0, 2, false);
        STAMP(8);
        LAS unsigned char* prw = opq(lds + L_R + wid * 5120 + lane * 16);
#pragma unroll
        for (int n = 0; n < 5; ++n) { const f32x4 x0 = acc[n][0], x1 = acc[n][1]; *(LAS f32x4*)(prw + n * 1024) = kq ? x0 : x1; acc[n][0] = kq ? x1 : x0; }
        WAITL0();
        BAR();
        const int m = mh * 2 + kq;
        const float g = *(const LAS float*)(lds + L_MISC + 64 + (16 * m + fr) * 4), g1 = rbf(1.0f - g);
        const LAS unsigned char* prr = opq(lds + L_R + (wid ^ 2) * 5120 + lane * 16);
        const LAS unsigned char* pat = opq(lds + L_AT + (4 * fq + (fr >> 2)) * 128 + (16 * m + 4 * (lane & 3)) * 2);
#pragma unroll
        for (int n = 0; n < 5; ++n) {
            const f32x4 b = acc[n][0] + *(const LAS f32x4*)(prr + n * 1024);
            const u32x2 a = __builtin_bit_cast(u32x2, tr_read(pat + n * 2048));
            const u32x2 o = {pk_bf16(g * bflo(a[0]) + g1 * b[0], g * bfhi(a[0]) + g1 * b[1]), pk_bf16(g * bflo(a[1]) + g1 * b[2], g * bfhi(a[1]) + g1 * b[3])};
            if (n < 4 || fq < 2) *(u32x2*)((char*)out + (size_t)(qm * 64 + 16 * m + fr) * 1152 + (qn * 72 + 16 * n + 4 * fq) * 2) = o;
        }
    } else {
        int d = pn * 36 + pm;
#pragma nounroll
        for (int c = 0; c < nch; ++c) {
            const float dsk = bf2f(Dsk[d]);
#pragma unroll
            for (int k = 0; k < 2; ++k) {
                BAR(); ++nb;
                const int l0 = h * 1024 + k * 512 + lane * 8;
                f32x4 cv = *(const LAS f32x4*)(lds + ytab(c) + 8192 + (l0 >= 16 ? (l0 >> 4) - 1 : 0) * 16);
                if ((lane & 1) || l0 == 0) cv = (f32x4){0.f, 0.f, 0.f, 0.f};
                const f32x4 y0 = *(const LAS f32x4*)(lds + ytab(c) + l0 * 4) + cv, y1 = *(const LAS f32x4*)(lds + ytab(c) + l0 * 4 + 16);
                const u32x4 uv = *(const LAS u32x4*)(lds + L_UT + c * UT_SZ + 256 + l0 * 2);
                u32x4 o;
#pragma unroll
                for (int j = 0; j < 4; ++j) {
                    const float ya = j < 2 ? y0[2 * j] : y1[2 * j - 4], yb = j < 2 ? y0[2 * j + 1] : y1[2 * j - 3];
                    const unsigned yp = pk_bf16(ya, yb), up = pk_bf16(bflo(uv[j]) * dsk, bfhi(uv[j]) * dsk);
                    o[j] = pk_bf16(bflo(yp) + bflo(up), bfhi(yp) + bfhi(up));
                }
                __builtin_amdgcn_raw_buffer_store_b128(o, ws, WS_AT + (unsigned)d * 4096u + l0 * 2, 0, 16);
            }
            d += 16;
        }
        STEPS_TO(58);
        flag_publish(ws, WS_FA + (unsigned)((pn >> 1) * 64 + (pn & 1) * 32 + pm * 2 + h) * 128u, T, lane);
        STAMP(5);
        STEPS_TO(62);
        if (h == 0) {
            const unsigned fo = WS_FA + (unsigned)(qn * 64 + lane) * 128u;
            const u64 t0 = __builtin_amdgcn_s_memrealtime();
            for (;;) {
                CFENCE();
                const u32x2 v = flag_ld(ws, fo);
                STEP(68); STEP(68);
                if (flag_all(v, T, true)) break;
                if (__builtin_amdgcn_s_memrealtime() - t0 > 300000ull) break;
            }
            ACQ();
            u32x4 av[9];
#pragma unroll
            for (int i = 0; i < 9; ++i) av[i] = __builtin_amdgcn_raw_buffer_load_b128(ws, WS_AT + (unsigned)(qn * 72 + i * 8 + (lane >> 3)) * 4096u + qm * 128 + (lane & 7) * 16, 0, 16);
            STEP(68); STEP(68);
            LAS unsigned char* pa_ = opq(lds + L_AT + lane * 16);
#pragma unroll
            for (int i = 0; i < 9; ++i) *(LAS u32x4*)(pa_ + i * 1024) = av[i];
        } else {
            const unsigned gv = __builtin_amdgcn_raw_buffer_load_b32(ws, WS_GATE + (unsigned)(qm * 64 + lane) * 4u, 0, 16);
            BAR(); BAR(); nb += 2;
            *(LAS unsigned*)(lds + L_MISC + 64 + lane * 4) = gv;
        }
        STEPS_TO(68);
        WAITL0();
        BAR();
    }
    STAMP(9); TRDUMP();
}
}

extern "C" void kernel_launch(void* const* d_in, const int* in_sizes, int n_in, void* d_out, int out_size, void* d_ws, size_t ws_size, hipStream_t stream) {
    (void)in_sizes; (void)n_in; (void)out_size; (void)ws_size;
    (void)hipFuncSetAttribute((const void*)gm::gm_kernel, hipFuncAttributeMaxDynamicSharedMemorySize, gm::LDS_BYTES);
    hipLaunchKernelGGL(gm::gm_kernel, dim3(256), dim3(512), gm::LDS_BYTES, stream, (const u16*)d_in[0], (const u16*)d_in[1], (const u16*)d_in[2], (const u16*)d_in[3],
                       (const u16*)d_in[4], (const u16*)d_in[5], (const u16*)d_in[6], (const u16*)d_in[8], (const u16*)d_in[9], (const u16*)d_in[10],
                       (const u16*)d_in[11], (const u16*)d_in[12], (u16*)d_out, (unsigned char*)d_ws);
}
```

```cpp
#define GM_PF 1
#define GM_OAUX 0
#include <hip/hip_runtime.h>
#include <stdint.h>
#include <type_traits>

#define LAS __attribute__((address_space(3)))
typedef unsigned short u16;
typedef unsigned long long u64;
typedef short bf16x8 __attribute__((ext_vector_type(8)));
typedef short s16x4 __attribute__((ext_vector_type(4)));
typedef _Float16 f16x8 __attribute__((ext_vector_type(8)));
typedef _Float16 f16x2 __attribute__((ext_vector_type(2)));
typedef __bf16 bf16x2 __attribute__((ext_vector_type(2)));
typedef float f32x4 __attribute__((ext_vector_type(4)));
typedef float f32x2 __attribute__((ext_vector_type(2)));
typedef unsigned u32x4 __attribute__((ext_vector_type(4)));
typedef unsigned u32x2 __attribute__((ext_vector_type(2)));
typedef __amdgpu_buffer_rsrc_t rsrc_t;

namespace gm {
constexpr int SEQ = 2048, DM = 576, FH = 2304;
constexpr unsigned WS_E = 0, WS_FS = 4096, WS_FZ = WS_FS + 65536, WS_FH = WS_FZ + 65536, WS_FA = WS_FH + 131072;
constexpr unsigned WS_ST = 0x60000, WS_GATE = 0xE0000, WS_DBG = 0x1E00000, WS_Z2F = 0x100000, WS_HF = 0xA00000, WS_WOF = 0x1300000, WS_UT = 0x1600000, WS_AT = 0x1900000, WS_END = 0x1C00000, WS_TRC = 0x1C00000;
constexpr int STG = 34816, BOFF = 16384, Z0 = 3 * STG, ZST = 296, T0 = Z0 + 128 * ZST;
constexpr int KT_SZ = 4352, UT_SZ = 4608, Y_SZ = 10240;
constexpr int L_MISC = 163840 - 320;
constexpr int L_KT = L_MISC - 3 * KT_SZ, L_SP = T0, L_KD = Z0 + 3 * 8448, L_UT = Z0 + 3 * Y_SZ;
constexpr int L_AT = Z0, L_R = Z0 + 10240;
constexpr int LDS_BYTES = 163840;
static_assert(L_UT + 3 * UT_SZ <= L_KT && L_KD + 3 * 3072 <= T0 && L_SP + 2048 <= L_KT, "lds map");
#define GM_HPRIO 1
#define GM_LAG 4

__device__ __forceinline__ float bf2f(unsigned v) { return __uint_as_float(v << 16); }
__device__ __forceinline__ float bflo(unsigned w) { return __uint_as_float(w << 16); }
__device__ __forceinline__ float bfhi(unsigned w) { return __uint_as_float(w & 0xffff0000u); }
__device__ __forceinline__ unsigned pk_bf16(float a, float b) { f32x2 v = {a, b}; return __builtin_bit_cast(unsigned, __builtin_convertvector(v, bf16x2)); }
__device__ __forceinline__ unsigned pk_f16(float a, float b) { f32x2 v = {a, b}; return __builtin_bit_cast(unsigned, __builtin_convertvector(v, f16x2)); }
__device__ __forceinline__ float rbf(float a) { return bflo(pk_bf16(a, a)); }
__device__ __forceinline__ f32x2 gelu2(f32x2 x) {
    const f32x2 w = (x * x) * (-0.10294324f) + (-2.30220819f);
    const f32x2 a = w * x;
    f32x2 e; e.x = __builtin_amdgcn_exp2f(a.x); e.y = __builtin_amdgcn_exp2f(a.y);
    e = e + 1.0f;
    f32x2 r; r.x = __builtin_amdgcn_rcpf(e.x); r.y = __builtin_amdgcn_rcpf(e.y);
    return x * r;
}
__device__ __forceinline__ f32x4 gelu4(f32x4 v) { const f32x2 a = gelu2((f32x2){v[0], v[1]}), b = gelu2((f32x2){v[2], v[3]}); return (f32x4){a.x, a.y, b.x, b.y}; }
__device__ __forceinline__ float allred_q(float v) {
    unsigned u = __float_as_uint(v);
    auto r = __builtin_amdgcn_permlane16_swap(u, u, false, false);
    v = __uint_as_float(r[0]) + __uint_as_float(r[1]);
    u = __float_as_uint(v);
    auto s = __builtin_amdgcn_permlane32_swap(u, u, false, false);
    return __uint_as_float(s[0]) + __uint_as_float(s[1]);
}
#define ACQ() __builtin_amdgcn_fence(__ATOMIC_ACQUIRE, "wavefront")
#define TRP
#define TRA
#define TRDUMP() do { } while (0)
#define BAR() __builtin_amdgcn_s_barrier()
#define PBAR() BAR()
#define PDUMP() TRDUMP()
#define WAITV(n) asm volatile("s_waitcnt vmcnt(" #n ")" ::: "memory")
#define WAITL0() asm volatile("s_waitcnt lgkmcnt(0)" ::: "memory")
#define CFENCE() asm volatile("" ::: "memory")
#define SGB(mask, n, id) __builtin_amdgcn_sched_group_barrier(mask, n, id)
#define MFMA16(a, b, c) __builtin_amdgcn_mfma_f32_16x16x32_bf16(a, b, c, 0, 0, 0)
__device__ __forceinline__ s16x4 tr_read(const LAS unsigned char* p) { return __builtin_amdgcn_ds_read_tr16_b64_v4i16((LAS s16x4*)p); }
__device__ __forceinline__ bf16x8 cat8(s16x4 a, s16x4 b) { return __builtin_shufflevector(a, b, 0, 1, 2, 3, 4, 5, 6, 7); }
template <class P> __device__ __forceinline__ P opq(P p) { asm volatile("" : "+v"(p)); return p; }
__device__ __forceinline__ int nextbuf(int so) { return so == 2 * STG ? 0 : so + STG; }

#define STAMP(i) do { } while (0)

__device__ __forceinline__ u32x2 flag_ld(rsrc_t ws, unsigned off) { return __builtin_amdgcn_raw_buffer_load_b64(ws, off, 0, 16); }
__device__ __forceinline__ bool flag_all(u32x2 v, u32x2 T, bool active) { return __builtin_amdgcn_ballot_w64(active && (v[0] != T[0] || v[1] != T[1])) == 0ull; }
__device__ __forceinline__ void flag_publish(rsrc_t ws, unsigned off, u32x2 T, int lane) {
    WAITV(0);
    if (lane == 0) __builtin_amdgcn_raw_buffer_store_b64(T, ws, off, 0, 16);
}
__device__ __forceinline__ void flag_wait(rsrc_t ws, unsigned base, int n, u32x2 T, int lane) {
    const unsigned off = base + (unsigned)(lane < n ? lane : 0) * 128u;
    const u64 t0 = __builtin_amdgcn_s_memrealtime();
    for (;;) {
        CFENCE();
        const u32x2 v = flag_ld(ws, off);
        if (flag_all(v, T, true)) break;
        if (__builtin_amdgcn_s_memrealtime() - t0 > 300000ull) break;
        __builtin_amdgcn_s_sleep(1);
    }
    ACQ();
}

#define SB0() __builtin_amdgcn_sched_barrier(0)
template <int NT> struct Frag { bf16x8 a[4]; s16x4 lo[NT], hi[NT]; };
template <int NT, bool TR, int HALF>
__device__ __forceinline__ void frag_read(Frag<NT>& f, int i, const LAS unsigned char* pa, const LAS unsigned char* pb) {
    if (i < 4) f.a[i] = *(const LAS bf16x8*)(pa + i * 2048);
    else if (TR) { const int n = (i - 4) >> 1; if ((i - 4) & 1) f.hi[n] = tr_read(pb + HALF * 9216 + 4608 + n * 32); else f.lo[n] = tr_read(pb + HALF * 9216 + n * 32); }
    else { const int n = i - 4; const bf16x8 v = *(const LAS bf16x8*)(pb + HALF * 9216 + n * 1024);
           f.lo[n] = __builtin_shufflevector(v, v, 0, 1, 2, 3); f.hi[n] = __builtin_shufflevector(v, v, 4, 5, 6, 7); }
}
template <int NT, bool TR, int LHALF, int RPS, bool FIRST, bool UPD>
__device__ __forceinline__ void half_step(f32x4 (&acc)[NT][4], const Frag<NT>& c, Frag<NT>& l, const LAS unsigned char*& pa0, const LAS unsigned char*& pa1, const LAS unsigned char*& pb, int delta, const float* ini = nullptr) {
    constexpr int NRD = TR ? 4 + 2 * NT : 4 + NT, NM = 4 * NT, LAST = (NRD + RPS - 1) / RPS;
#pragma unroll
    for (int i = 0; i < NM; ++i) {
        const int n = i >> 2, m = i & 3;
        acc[n][m] = MFMA16(cat8(c.lo[n], c.hi[n]), c.a[m], FIRST ? (TR ? ((f32x4){0.f, 0.f, 0.f, 0.f}) : ((f32x4){ini[m], ini[m], ini[m], ini[m]})) : acc[n][m]);
        if (LHALF >= 0) {
#pragma unroll
            for (int r = 0; r < RPS; ++r) if (i * RPS + r < NRD) frag_read<NT, TR, (LHALF > 0 ? 1 : 0)>(l, i * RPS + r, LHALF > 0 ? pa1 : pa0, pb);
        }
        if (UPD) { if (i == LAST) { pa0 += delta; asm volatile("" : "+v"(pa0)); } if (i == LAST + 1) { pb += delta; asm volatile("" : "+v"(pb)); } if (i == LAST + 2) { pa1 += delta; asm volatile("" : "+v"(pa1)); } }
        SB0();
    }
}
template <int NT, bool TR, class HK>
__device__ __forceinline__ void gemm_phase(int nsteps, f32x4 (&acc)[NT][4], int& so, const LAS unsigned char* pa0, const LAS unsigned char* pa1, const LAS unsigned char* pb,
                                           const float* ini, const volatile LAS unsigned* rdy, HK hook TRP) {
    constexpr int NRD = TR ? 4 + 2 * NT : 4 + NT;
    Frag<NT> f0, f1;
    pa0 += so; pa1 += so; pb += so;
    asm volatile("" : "+v"(pa0), "+v"(pa1), "+v"(pb));
    __builtin_amdgcn_s_waitcnt(0xC07F);
    BAR();
#pragma unroll
    for (int i = 0; i < NRD; ++i) frag_read<NT, TR, 0>(f0, i, pa0, pb);
    SB0();
    int delta = so == 2 * STG ? -2 * STG : STG; so += delta;
    half_step<NT, TR, 1, 2, true, true>(acc, f0, f1, pa0, pa1, pb, delta, ini);
#pragma nounroll
    for (int s = 1; s < nsteps; ++s) {
        WAITL0();
        BAR();
        unsigned rv = 0;
        if (TR) { rv = *rdy; SB0(); }
        delta = so == 2 * STG ? -2 * STG : STG; so += delta;
        half_step<NT, TR, 0, 1, false, false>(acc, f1, f0, pa0, pa1, pb, 0);
        half_step<NT, TR, 1, 2, false, true>(acc, f0, f1, pa0, pa1, pb, delta);
        if (TR) hook(rv);
    }
    half_step<NT, TR, -1, 1, false, false>(acc, f1, f0, pa0, pa1, pb, 0);
}

template <int NT, class LT>
__device__ __forceinline__ void consumer_a(LAS unsigned char* lds, u32x2 (&zp)[5][4], LT late, int wm, int wn, int lane, unsigned char* wsp_ TRP) {
    const int fr = lane & 15, fq = lane >> 4;
    constexpr int N0 = (NT == 5) ? 0 : 5;
    f32x4 acc[NT][4];
    int so = 0;
    const int sw = (fr >> 1) & 7;
    const int a0 = (wm * 64 + fr) * 128 + ((fq ^ sw) << 4), a1 = (wm * 64 + fr) * 128 + (((4 + fq) ^ sw) << 4);
    const int bw = BOFF + (4 * fq + (fr >> 2)) * 288 + (lane & 3) * 8 + N0 * 32;
    const volatile LAS unsigned* rdy = (const volatile LAS unsigned*)(lds + L_MISC + 4 + wm * 4);
    bool done = false;
    auto normalise = [&]() {
        const LAS unsigned char* ps = opq(lds + L_SP + (wm * 64 + fr) * 16);
        LAS unsigned char* pz = opq(lds + Z0 + (wm * 64 + fr) * ZST + N0 * 32 + fq * 8);
#pragma unroll
        for (int m = 0; m < 4; ++m) {
            const f32x2 sr = *(const LAS f32x2*)(ps + m * 256);
#pragma unroll
            for (int n = 0; n < NT; ++n) {
                const f32x2 u = (f32x2){bflo(zp[n][m][0]), bfhi(zp[n][m][0])} * sr.x + sr.y, v = (f32x2){bflo(zp[n][m][1]), bfhi(zp[n][m][1])} * sr.x + sr.y;
                *(LAS u32x2*)(pz + m * 16 * ZST + n * 32) = (u32x2){pk_bf16(u.x, u.y), pk_bf16(v.x, v.y)};
            }
        }
        WAITL0();
        if (lane == 0) *(volatile LAS unsigned*)(lds + L_MISC + 16 + (wm * 2 + wn) * 4) = 1u;
        done = true;
    };
    auto hook = [&](unsigned rv) { if (__builtin_amdgcn_readfirstlane(rv) != 0u && !done) normalise(); };
#pragma unroll
    for (int n = 0; n < NT; ++n)
#pragma unroll
        for (int m = 0; m < 4; ++m) zp[n][m] = (u32x2){0u, 0u};
#pragma nounroll
    for (int it = 0; it < 2; ++it) {
        gemm_phase<NT, true>(9, acc, so, lds + a0, lds + a1, lds + bw, nullptr, rdy, hook TRA);
        STAMP(1 + 2 * it);
        late();
        if (it == 1 && !done) {
            const u64 t0 = __builtin_amdgcn_s_memrealtime();
            while (__builtin_amdgcn_readfirstlane(*rdy) == 0u && __builtin_amdgcn_s_memrealtime() - t0 < 400000ull) __builtin_amdgcn_s_sleep(1);
            normalise();
        }
#pragma unroll
        for (int m = 0; m < 4; ++m)
#pragma unroll
            for (int n = 0; n < NT; ++n) { acc[n][m] = gelu4(acc[n][m]); zp[n][m] = (u32x2){pk_bf16(acc[n][m][0], acc[n][m][1]), pk_bf16(acc[n][m][2], acc[n][m][3])}; }
        if (it == 0) {
            LAS unsigned char* ps = opq(lds + L_SP + (wm * 64 + fr) * 16 + wn * 8);
#pragma unroll
            for (int m = 0; m < 4; ++m) {
                f32x4 s4 = acc[0][m];
#pragma unroll
                for (int n = 1; n < NT; ++n) s4 += acc[n][m];
                const float mean = opq(allred_q((s4[0] + s4[1]) + (s4[2] + s4[3])) * (1.0f / (16 * NT)));
                f32x4 q4 = {0.f, 0.f, 0.f, 0.f};
#pragma unroll
                for (int n = 0; n < NT; ++n) { const f32x4 d = acc[n][m] - mean; q4 += d * d; }
                const float m2 = allred_q((q4[0] + q4[1]) + (q4[2] + q4[3]));
                if (fq == 0) *(LAS f32x2*)(ps + m * 256) = (f32x2){mean, m2};
            }
            STAMP(2);
        }
    }
    STAMP(4);
}

template <int NT>
__device__ __forceinline__ void consumer_b(LAS unsigned char* lds, rsrc_t ws, u32x2 T, const u32x2 (&z1)[5][4], const float (&bs_)[4], int pm, int pn, int wm, int wn, int lane, unsigned char* wsp_ TRP) {
    float bs[4] = {bf2f(__float_as_uint(opq(bs_[0]))), bf2f(__float_as_uint(opq(bs_[1]))), bf2f(__float_as_uint(opq(bs_[2]))), bf2f(__float_as_uint(opq(bs_[3])))};
    const int fr = lane & 15, fq = lane >> 4;
    constexpr int N0 = (NT == 5) ? 0 : 5;
    f32x4 acc[NT][4];
    int so = 0;
    const int sw = (fr >> 1) & 7;
    const int a0 = (wm * 64 + fr) * 128 + ((fq ^ sw) << 4), a1 = (wm * 64 + fr) * 128 + (((4 + fq) ^ sw) << 4);
    const int bz = BOFF + N0 * 1024 + lane * 16;
    gemm_phase<NT, false>(32, acc, so, lds + a0, lds + a1, lds + bz, bs, nullptr, [](unsigned) {} TRA);
    STAMP(5);
    __builtin_amdgcn_s_setprio(3);
    const int qm = 2 * pm + wm;
    const unsigned hb = WS_HF + (unsigned)qm * (18u * 16384u);
#pragma unroll
    for (int m = 0; m < 4; ++m) {
        u32x2 hp[NT];
#pragma unroll
        for (int n = 0; n < NT; ++n) {
            const f32x2 u = (f32x2){bflo(z1[n][m][0]), bfhi(z1[n][m][0])} * (f32x2){acc[n][m][0], acc[n][m][1]}, v = (f32x2){bflo(z1[n][m][1]), bfhi(z1[n][m][1])} * (f32x2){acc[n][m][2], acc[n][m][3]};
            hp[n] = (u32x2){pk_bf16(u.x, u.y), pk_bf16(v.x, v.y)};
        }
#pragma unroll
        for (int j = 0; j < 2; ++j)
            __builtin_amdgcn_raw_buffer_store_b128((u32x4){hp[2 * j][0], hp[2 * j][1], hp[2 * j + 1][0], hp[2 * j + 1][1]}, ws,
                                                   hb + (unsigned)pn * 16384u + (unsigned)(m * 4 + wn * 2 + j) * 1024u + lane * 16, 0, 16);
        if (NT == 5) __builtin_amdgcn_raw_buffer_store_b64(hp[NT - 1], ws, hb + (unsigned)(16 + (pn >> 3)) * 16384u + m * 4096u + (unsigned)(pn & 7) * 512u + lane * 8, 0, 16);
    }
    flag_publish(ws, WS_FH + (unsigned)(qm * 32 + pn * 2 + wn) * 128u, T, lane);
    __builtin_amdgcn_s_setprio(0);
    STAMP(6);
}

template <int TLO, int THI>
__device__ __forceinline__ void conv_task(LAS unsigned char* lds, int kt, int ut, int yt, int tl, int lane, int& nb TRP) {
#define CSL() do { if (nb < 50) { BAR(); ++nb; } } while (0)
    constexpr int NTL = THI - TLO + 1, ND = THI + 2;
    const int i = lane & 15, g = lane >> 4;
    const LAS unsigned char* pk = opq(lds + kt + (i & 3) * 1088 + ((i >> 2) + g) * 16);
    const LAS unsigned char* pu = opq(lds + ut + (128 + 16 * tl - 32 * g - 4 * (i >> 2) + 4 * (i & 3)) * 2 - 32);
    bf16x8 ub[ND];
#pragma unroll
    for (int d = 0; d < ND; ++d) { if (d == 0 || d == 8) CSL(); ub[d] = cat8(tr_read(pu + 256 * d + 32), tr_read(pu + 256 * d)); }
    f32x4 acc[NTL];
#pragma unroll
    for (int t = 0; t < NTL; ++t) acc[t] = (f32x4){0.f, 0.f, 0.f, 0.f};
#pragma unroll
    for (int s = -1; s <= THI; ++s) {
        if (TLO == 0 ? (s <= 3 || s == 5 || s == 7) : (s & 1) != 0 && s < 12) CSL();
        const bf16x8 kf = *(const LAS bf16x8*)(pk + 64 * (s + 1));
#pragma unroll
        for (int t = TLO; t <= THI; ++t) if (t >= s) acc[t - TLO] = MFMA16(kf, ub[t - s], acc[t - TLO]);
    }
    LAS float* y = opq((LAS float*)(lds + yt) + 32 * g + 16 * tl + i);
    LAS float* cy = opq((LAS float*)(lds + yt + 8192) + (2 * g + tl) * 4 + (i & 3));
#define DPPF(v, ctl) __int_as_float(__builtin_amdgcn_update_dpp(0, __float_as_int(v), ctl, 0xf, 0xf, true))
#pragma unroll
    for (int t = TLO; t <= THI; ++t) {
        if (((t - TLO) % 2) == 0) CSL();
        const f32x4 a = acc[t - TLO];
        y[128 * t] = (a[0] + DPPF(a[1], 0x111)) + (DPPF(a[2], 0x112) + DPPF(a[3], 0x113));
        const float cv = DPPF(a[1], 0x10f) + (DPPF(a[2], 0x10e) + DPPF(a[3], 0x10d));
        if (i < 4) cy[32 * t] = cv;
    }
#undef DPPF
#undef CSL
}
__device__ __forceinline__ int ytab(int c) { return Z0 + c * Y_SZ; }

__global__ __launch_bounds__(512, 2) void gm_kernel(const u16* __restrict__ x, const u16* __restrict__ logA, const u16* __restrict__ Aim, const u16* __restrict__ Cre,
                                                    const u16* __restrict__ Cim, const u16* __restrict__ logdt, const u16* __restrict__ Dsk, const u16* __restrict__ win,
                                                    const u16* __restrict__ wsp, const u16* __restrict__ bsp, const u16* __restrict__ wout, const u16* __restrict__ gw,
                                                    u16* __restrict__ out, unsigned char* wsp_) {
    extern __shared__ __attribute__((aligned(16))) unsigned char lds_[];
    LAS unsigned char* lds = (LAS unsigned char*)lds_;
    const int wid = __builtin_amdgcn_readfirstlane(threadIdx.x >> 6), lane = threadIdx.x & 63;
    const int bid = blockIdx.x, xg = bid & 7, rr = bid >> 3, bi = rr >> 2, bj = rr & 3;
    const int pm = (xg >> 2) * 8 + bi, pn = (xg & 3) * 4 + bj;
    const int qm = 2 * pm + (pn >> 3), qn = pn & 7;
    const int nch = pm < 4 ? 3 : 2;
    const rsrc_t ws = __builtin_amdgcn_make_buffer_rsrc(wsp_, 0, WS_END, 0x00020000);
    STAMP(0);

    const int fr = lane & 15, fq = lane >> 4;
    const int h = wid - 6, p = wid - 4;
    const bool is_prod = (wid >> 1) == 2;
    const int ch0 = pn * 36 + pm;
    float bs[4];
    auto late = [&]() {
#pragma unroll
        for (int m = 0; m < 4; ++m) bs[m] = __uint_as_float((unsigned)bsp[pm * 128 + (wid & 1) * 64 + m * 16 + fr]);
    };
    const int phi = (-(bi + bj)) & 3;
    const unsigned rsw = (unsigned)(((lane & 7) ^ ((4 * p + (lane >> 4)) & 7)) * 16);
#define GLDS(src, dst, aux) __builtin_amdgcn_global_load_lds((const unsigned*)(src), (LAS unsigned*)(dst), 16, 0, aux)
    auto issueS = [&](int k, int s) {
        const char* sb = (const char*)wsp + (size_t)(pm * 128 + p * 8) * 4096 + k * 128; const unsigned vs = (unsigned)(lane >> 3) * 4096u + rsw;
#pragma unroll
        for (int i = 0; i < 8; ++i) GLDS(sb + (size_t)i * (16 * 4096) + vs, lds + s + (p + 2 * i) * 1024, 0); };
    auto ord2 = [&](int s) { return GM_LAG == 0 ? s : (((s & 3) == phi) ? s : ((s - GM_LAG) & 31)); };
    u32x2 E, T;
    auto mk_tag = [&]() { u64 z = (((u64)__builtin_amdgcn_readfirstlane(E[1]) << 32) | __builtin_amdgcn_readfirstlane(E[0])) + 0x9E3779B97F4A7C15ull;
        z = (z ^ (z >> 30)) * 0xBF58476D1CE4E5B9ull; z = (z ^ (z >> 27)) * 0x94D049BB133111EBull; z ^= z >> 31; T = (u32x2){(unsigned)z, (unsigned)(z >> 32)}; };
    u32x2 zp[5][4];
#define STEP(lim) do { if (nb < (lim)) { BAR(); ++nb; } } while (0)
#define STEPS_TO(lim) _Pragma("nounroll") while (nb < (lim)) { BAR(); ++nb; }
    int nb = 0;
    if (wid < nch) {
        const int kch = ch0 + 16 * wid, ki = kch * 64 + lane;
        const unsigned pA = ((const unsigned*)logA)[ki], pI = ((const unsigned*)Aim)[ki], pR = ((const unsigned*)Cre)[ki], pC = ((const unsigned*)Cim)[ki], pT = logdt[kch];
        LAS u32x2* tw = opq((LAS u32x2*)(lds + Z0 + wid * 8448) + lane);
        const LAS unsigned char* trd = opq(lds + Z0 + wid * 8448 + fr * 528 + fq * 16);
        const float dt = rbf(__expf(bf2f(pT)));
        const f32x2 ar = (f32x2){rbf(__expf(bflo(pA))), rbf(__expf(bfhi(pA)))} * dt * -1.44269504f, ai = (f32x2){bflo(pI), bfhi(pI)} * dt * 0.15915494f;
        const f32x2 cr = (f32x2){bflo(pR), bfhi(pR)} * dt, ci = (f32x2){bflo(pC), bfhi(pC)} * dt;
        float cm = fmaxf(fmaxf(fabsf(cr.x), fabsf(ci.x)), fmaxf(fabsf(cr.y), fabsf(ci.y)));
#define DPPF(v, ctl) __int_as_float(__builtin_amdgcn_update_dpp(0, __float_as_int(v), ctl, 0xf, 0xf, true))
        cm = fmaxf(cm, DPPF(cm, 0x121)); cm = fmaxf(cm, DPPF(cm, 0x122)); cm = fmaxf(cm, DPPF(cm, 0x124)); cm = fmaxf(cm, DPPF(cm, 0x128));
#undef DPPF
        { auto r = __builtin_amdgcn_permlane16_swap(__float_as_uint(cm), __float_as_uint(cm), false, false); cm = fmaxf(__uint_as_float(r[0]), __uint_as_float(r[1]));
          auto q = __builtin_amdgcn_permlane32_swap(__float_as_uint(cm), __float_as_uint(cm), false, false); cm = fmaxf(fmaxf(__uint_as_float(q[0]), __uint_as_float(q[1])), 1e-30f); }
        struct C2 { f32x2 x, y; };
        auto cexp = [&](float p_) { const f32x2 e = ar * p_, t = ai * p_;
            const f32x2 mg = {__builtin_amdgcn_exp2f(e.x), __builtin_amdgcn_exp2f(e.y)}; const float t0 = __builtin_amdgcn_fractf(t.x), t1 = __builtin_amdgcn_fractf(t.y);
            return C2{mg * (f32x2){__builtin_amdgcn_cosf(t0), __builtin_amdgcn_cosf(t1)}, mg * (f32x2){__builtin_amdgcn_sinf(t0), __builtin_amdgcn_sinf(t1)}}; };
        auto cmul = [](C2 a, C2 b) { return C2{__builtin_elementwise_fma(a.x, b.x, -(a.y * b.y)), __builtin_elementwise_fma(a.x, b.y, a.y * b.x)}; };
        auto put = [&](int row, C2 v, bool conj) { tw[row * 66] = conj ? (u32x2){pk_f16(v.x.x, -v.y.x), pk_f16(v.x.y, -v.y.y)} : (u32x2){pk_f16(v.x.x, v.y.x), pk_f16(v.x.y, v.y.y)}; };
        C2 wp[16], zr[8], vp[4], cv[17];
        wp[1] = cexp(8.f); zr[1] = cexp(1.f); vp[0] = cexp(128.f);
        tw[0] = (u32x2){pk_f16(1.f, 0.f), pk_f16(1.f, 0.f)};
#pragma unroll
        for (int j = 1; j < 16; ++j) { if (j > 1) wp[j] = cmul(wp[j >> 1], wp[j - (j >> 1)]); put(j, wp[j], true); }
        CFENCE();
        u32x4 fb[8];
#pragma unroll
        for (int ks = 0; ks < 8; ++ks) fb[ks] = *(const LAS u32x4*)(trd + ks * 64);
        CFENCE();
#pragma unroll
        for (int r = 2; r < 8; ++r) zr[r] = cmul(zr[r >> 1], zr[r - (r >> 1)]);
#pragma unroll
        for (int k = 1; k < 4; ++k) vp[k] = cmul(vp[k - 1], vp[k - 1]);
        const float rc = __builtin_amdgcn_rcpf(cm);
        cv[0] = C2{cr * rc, ci * rc};
#pragma unroll
        for (int k = 1; k < 8; ++k) { const int lg = k >= 4 ? 2 : k >= 2 ? 1 : 0; cv[k] = cmul(cv[k - (1 << lg)], vp[lg]); }
        LAS float* kb_ = (LAS float*)(lds + L_KD + wid * 3072);
        const float sc = 2.0f * cm;
        u32x4 fa[3][8];
#pragma unroll
        for (int tt = 0; tt < 3; ++tt) {
#pragma unroll
            for (int i = 0; i < (tt == 2 ? 9 : 16); ++i) {
                const int r = tt == 0 ? (i & 7) : tt == 1 ? (i < 8 ? 2 * (i & 3) : 4 * (i & 1)) : 0;
                const int hh = tt == 0 ? (i >> 3) : tt == 1 ? (i < 8 ? 2 + (i >> 2) : 4 + ((i - 8) >> 1)) : 8 + i;
                if (tt == 2) cv[hh] = cmul(cv[hh - 8], vp[3]);
                put(i, r == 0 ? cv[hh] : cmul(cv[hh], zr[r]), false);
            }
            CFENCE();
#pragma unroll
            for (int ks = 0; ks < 8; ++ks) fa[tt][ks] = *(const LAS u32x4*)(trd + ks * 64);
            CFENCE();
            SB0();
        }
        f32x4 kd[3] = {{0.f, 0.f, 0.f, 0.f}, {0.f, 0.f, 0.f, 0.f}, {0.f, 0.f, 0.f, 0.f}};
#pragma unroll
        for (int ks = 0; ks < 8; ++ks)
#pragma unroll
            for (int tt = 0; tt < 3; ++tt) kd[tt] = __builtin_amdgcn_mfma_f32_16x16x32_f16(__builtin_bit_cast(f16x8, fa[tt][ks]), __builtin_bit_cast(f16x8, fb[ks]), kd[tt], 0, 0, 0);
#pragma unroll
        for (int tt = 0; tt < 3; ++tt) {
            LAS float* kw = opq(kb_ + (tt == 0 ? 128 * (fq >> 1) + 4 * (fq & 1) + 8 * fr : tt == 1 ? 256 + 64 * fq + (fq < 2 ? 4 : 2) * fr : 512 + 64 * fq + fr));
            LAS float* kwb = tt == 1 ? opq(kw + (fq < 2 ? 2 : 32)) : kw + (tt == 0 ? 2 : 32);
            kw[0] = kd[tt][0] * sc; kw[tt == 2 ? 16 : 1] = kd[tt][1] * sc; kwb[0] = kd[tt][2] * sc; kwb[tt == 2 ? 16 : 1] = kd[tt][3] * sc;
        }
        WAITL0();
    }
    if (GM_PF && wid == 3) {
        unsigned acc_ = 0;
        if (GM_PF & 1) { const char* pc = (const char*)(__builtin_amdgcn_s_getpc() & ~127ull) - 1024; if (lane < 11) acc_ += *(const unsigned*)(pc + (rr * 11 + lane) * 128); }
        if (GM_PF & 2) {
            acc_ += *(const unsigned*)((const char*)wsp + (size_t)(pm * 128 + lane) * 4096) + *(const unsigned*)((const char*)wsp + (size_t)(pm * 128 + 64 + lane) * 4096);
#pragma unroll
            for (int i = 0; i < 3; ++i) if (i * 64 + lane < 144) acc_ += __builtin_amdgcn_raw_buffer_load_b32(ws, WS_Z2F + (unsigned)pn * (64u * 9216u) + (i * 64 + lane) * 4096u, 0, 16);
            acc_ += __builtin_amdgcn_raw_buffer_load_b32(ws, WS_HF + (unsigned)qm * (18u * 16384u) + lane * 4096u, 0, 16);
            if (lane < 8) acc_ += __builtin_amdgcn_raw_buffer_load_b32(ws, WS_HF + (unsigned)qm * (18u * 16384u) + (64 + lane) * 4096u, 0, 16);
            acc_ += __builtin_amdgcn_raw_buffer_load_b32(ws, WS_HF + (unsigned)(qm ^ 1) * (18u * 16384u) + lane * 4096u, 0, 16);
            acc_ += __builtin_amdgcn_raw_buffer_load_b32(ws, WS_WOF + (unsigned)qn * (18u * 18432u) + lane * 4096u, 0, 16);
            if (lane < 17) acc_ += __builtin_amdgcn_raw_buffer_load_b32(ws, WS_WOF + (unsigned)qn * (18u * 18432u) + (64 + lane) * 4096u, 0, 16);
            acc_ += __builtin_amdgcn_raw_buffer_load_b32(ws, WS_AT + (unsigned)(qn * 72 + lane) * 4096u, 0, 16);
            if (lane < 8) acc_ += __builtin_amdgcn_raw_buffer_load_b32(ws, WS_AT + (unsigned)(qn * 72 + 64 + lane) * 4096u, 0, 16);
            if (lane < 36) acc_ += __builtin_amdgcn_raw_buffer_load_b32(ws, WS_UT + (unsigned)(pn * 36 + lane) * 4096u, 0, 16) + __builtin_amdgcn_raw_buffer_load_b32(ws, WS_AT + (unsigned)(pn * 36 + lane) * 4096u, 0, 16);
            acc_ += __builtin_amdgcn_raw_buffer_load_b32(ws, lane * 16384u, 0, 16);
            if (lane < 18) acc_ += *(const unsigned*)((const char*)out + (size_t)qm * 64 * 1152 + lane * 4096);
        }
        if (GM_PF & 4) {
#pragma unroll
            for (int i = 0; i < 4; ++i) acc_ += *(const unsigned*)((const char*)wsp + (size_t)bid * 32768 + (i * 64 + lane) * 128);
        }
        asm volatile("" :: "v"(acc_));
    }
#define IC(n) std::integral_constant<int, n>{}
    if (wid < 6) STAMP(10);
    if (is_prod) {
        const unsigned vx = (unsigned)(lane >> 3) * 1152u + rsw;
        unsigned wv[9];
#pragma unroll
        for (int i = 0; i < 9; ++i) { const int c = (p + 2 * i) * 64 + lane, rho = c / 18, ch = c - rho * 18;
            const int kap = (rho & 32) + 8 * ((rho >> 2) & 3) + 4 * ((rho >> 4) & 1) + (rho & 3); wv[i] = (unsigned)kap * 9216u + (unsigned)ch * 16u; }
        const char* xb = (const char*)x + (size_t)(pm * 128 + p * 8) * 1152;
        const char* wb = (const char*)win + pn * 288;
        auto issueX = [&](int k, int s) {
#pragma unroll
            for (int i = 0; i < 8; ++i) GLDS(xb + (size_t)i * (16 * 1152) + k * 128 + vx, lds + s + (p + 2 * i) * 1024, 0); };
        auto issueW = [&](int k, int coloff, int s) {
            const char* b = wb + (size_t)k * (64 * 9216) + coloff;
#pragma unroll
            for (int i = 0; i < 9; ++i) GLDS(b + wv[i], lds + s + BOFF + (p + 2 * i) * 1024, 0); };
        auto ord1 = [&](int s) { return (GM_LAG == 0 || (s & 3) == phi) ? s : (s >= 4 ? s - 4 : (s == 0 ? 8 : s + 4)); };
        issueX(ord1(0), 0); issueW(ord1(0), FH * 2, 0); issueX(ord1(1), STG); issueW(ord1(1), FH * 2, STG);
        issueX(ord1(2), 2 * STG); issueW(ord1(2), FH * 2, 2 * STG);
        E = flag_ld(ws, WS_E);
        int so = 0;
        WAITV(35); PBAR();
#pragma nounroll
        for (int S = 1; S < 16; ++S) {
            WAITV(17); PBAR();
            const int t = S + 2, k = ord1(t >= 9 ? t - 9 : t);
            issueX(k, so); issueW(k, t >= 9 ? 0 : FH * 2, so); so = nextbuf(so);
        }
        WAITV(17); PBAR(); issueS(ord2(0), 0);
        WAITV(8); PBAR(); issueS(ord2(1), STG);
        STAMP(1);
    } else if (wid < 4) {
        E = flag_ld(ws, WS_E);
        if (wid >> 1) consumer_a<4>(lds, zp, late, wid & 1, 1, lane, wsp_ TRA);
        else consumer_a<5>(lds, zp, late, wid & 1, 0, lane, wsp_ TRA);
    } else {
        if (h == 0 && lane < 12) *(LAS unsigned*)(lds + L_MISC + lane * 4) = 0u;
        const int row0 = lane / 9, chk0 = lane - row0 * 9;
        const int u = (pn >> 3) * 16 + pm, nblk = u < 8 ? 3 : 2;
        u32x2 uc[18]; u32x4 xv[9], gv[9], wv[3][5];
        if (h == 0) {
            { const char* xb = (const char*)x + (size_t)pm * 128 * 1152 + pn * 72; unsigned off = row0 * 1152 + chk0 * 8; int ch = chk0;
#pragma unroll
              for (int i = 0; i < 9; ++i) { uc[i] = *(const u32x2*)(xb + off); uc[9 + i] = *(const u32x2*)(xb + 64 * 1152 + off); off += 7 * 1152 + 8; if (++ch == 9) { ch = 0; off += 1152 - 72; } } }
            { const char* xr = (const char*)x + (size_t)(pm * 128 + pn * 8 + (lane >> 3)) * 1152 + (lane & 7) * 16; const char* gr = (const char*)gw + (lane & 7) * 16;
#pragma unroll
              for (int i = 0; i < 9; ++i) { xv[i] = *(const u32x4*)(xr + i * 128); gv[i] = *(const u32x4*)(gr + i * 128); } }
        } else {
            unsigned wo_[5], hi_[5];
            { int row = row0, ch = chk0;
#pragma unroll
              for (int i = 0; i < 5; ++i) { const int rw = row < 32 ? row : 31; wo_[i] = (rw & 15) * 1152 + ch * 16; hi_[i] = rw >> 4; row += 7; if (++ch == 9) { ch = 0; ++row; } } }
#pragma unroll
            for (int b = 0; b < 3; ++b) {
                const int blk = (b < nblk) ? u + 32 * b : u, st = blk >> 2, jb = blk & 3;
                const int fA = st < 16 ? st * 144 + (jb < 2 ? 32 * jb : 16 + 32 * jb) : ((st - 16) * 8 + 2 * jb) * 144 + 64, dB = (st < 16 ? 16 : 144) * 1152;
                const char* wb_ = (const char*)wout + (size_t)fA * 1152 + qn * 144;
#pragma unroll
                for (int i = 0; i < 5; ++i) wv[b][i] = *(const u32x4*)(wb_ + (wo_[i] + hi_[i] * dB));
            }
        }
        E = flag_ld(ws, WS_E);
        if (lane < 24) *(LAS u32x4*)(lds + L_KT + (lane >> 3) * KT_SZ + (2 * h + ((lane >> 2) & 1)) * 1088 + (lane & 3) * 16) = (u32x4){0u, 0u, 0u, 0u};
        WAITL0();
        STAMP(10);
        __builtin_amdgcn_s_setprio(GM_HPRIO);
        {
            float kv[2][3];
#pragma nounroll
            for (int ei = 0; ei < 9; ++ei) {
                BAR();
                if (ei > 0) {
                    LAS unsigned char* kt = opq(lds + L_KT + (2 * h + ((ei - 1) >> 2)) * 1088 + 64 + ((ei - 1) & 3) * 256 + lane * 2);
#pragma unroll
                    for (int j = 0; j < 2; ++j)
#pragma unroll
                        for (int c = 0; c < 3; ++c) *(LAS u16*)(kt + c * KT_SZ + j * 128) = (u16)pk_bf16(kv[j][c], 0.f);
                }
#pragma unroll
                for (int j = 0; j < 2; ++j) {
                    const int jj = (ei & 3) * 2 + j, e = jj >= 4 ? 3 : jj >= 2 ? 2 : jj, lag = 256 * jj + 4 * lane + 2 * h + ((ei >> 2) & 1);
                    const int idx = e == 0 ? lag : 128 * e + ((lag + (1 << (e - 1)) - 1 + ((lag >> e) & 1)) >> e);
                    const LAS float* q = opq((const LAS float*)(lds + L_KD) + idx);
#pragma unroll
                    for (int c = 0; c < 3; ++c) kv[j][c] = q[c * 768];
                }
            }
        }
        if (h == 0) {
            LAS unsigned char* pzs = opq(lds + Z0 + lane * 8);
#pragma unroll
            for (int i = 0; i < 18; ++i) *(LAS u32x2*)(pzs + i * 512) = uc[i];
            CFENCE();
            { const LAS unsigned char* pt = opq(lds + Z0 + (8 * (4 * fq + (lane & 3)) + (fr >> 2)) * 72);
              const unsigned ub = WS_UT + (unsigned)(pn * 36 + (lane & 3)) * 4096u + (unsigned)(pm * 128 + 8 * (4 * fq + (fr >> 2))) * 2u;
#pragma unroll
              for (int cc = 0; cc < 9; ++cc) __builtin_amdgcn_raw_buffer_store_b128(__builtin_bit_cast(u32x4, cat8(tr_read(pt + cc * 8), tr_read(pt + 288 + cc * 8))), ws, ub + cc * 16384u, 0, 16); }
            float a = 0.f;
#pragma unroll
            for (int i = 0; i < 9; ++i)
#pragma unroll
                for (int j = 0; j < 4; ++j) { const unsigned xa = xv[i][j], ga = gv[i][j]; a = __builtin_amdgcn_fdot2_f32_bf16(__builtin_bit_cast(bf16x2, xa), __builtin_bit_cast(bf16x2, ga), a, false); }
#define DPPF(v, ctl) __int_as_float(__builtin_amdgcn_update_dpp(0, __float_as_int(v), ctl, 0xf, 0xf, true))
            a += DPPF(a, 0xb1); a += DPPF(a, 0x4e); a += DPPF(a, 0x141);
#undef DPPF
            const float gt = rbf(__builtin_amdgcn_rcpf(1.0f + __expf(-rbf(a))));
            if ((lane & 7) == 0) __builtin_amdgcn_raw_buffer_store_b32(__float_as_uint(gt), ws, WS_GATE + (unsigned)(pm * 128 + pn * 8 + (lane >> 3)) * 4u, 0, 16);
        } else {
            LAS unsigned char* pws = opq(lds + Z0 + 9216 + lane * 16);
            const LAS unsigned char* ptw = opq(lds + Z0 + 9216 + (4 * fq + (fr >> 2)) * 144 + (lane & 3) * 8);
#pragma unroll
            for (int b = 0; b < 3; ++b) {
                if (b < nblk) {
#pragma unroll
                    for (int i = 0; i < 5; ++i) if (i < 4 || lane < 32) *(LAS u32x4*)(pws + i * 1024) = wv[b][i];
                    CFENCE();
                    const unsigned ob = WS_WOF + (unsigned)qn * (18u * 18432u) + (unsigned)(u + 32 * b) * 4608u;
#pragma unroll
                    for (int nn = 0; nn < 5; ++nn) {
                        const u32x4 v = __builtin_bit_cast(u32x4, cat8(tr_read(ptw + nn * 32), tr_read(ptw + 2304 + nn * 32)));
                        if (nn < 4) __builtin_amdgcn_raw_buffer_store_b128(v, ws, ob + nn * 1024 + lane * 16, 0, 16);
                        else if (fr < 8) __builtin_amdgcn_raw_buffer_store_b128(v, ws, ob + 4096 + (fq * 8 + fr) * 16, 0, 16);
                    }
                    CFENCE();
                }
            }
        }
        nb = 9;
        mk_tag();
        WAITL0();
        STEPS_TO(10);
        STAMP(1);
        {
            const f32x4 sp = *(const LAS f32x4*)(lds + L_SP + (h * 64 + lane) * 16);
            const float dm = sp[0] - sp[2];
            const f32x2 mine = {(80.f * sp[0] + 64.f * sp[2]) * (1.0f / 144.f), sp[1] + sp[3] + (80.f * 64.f / 144.f) * dm * dm};
            __builtin_amdgcn_raw_buffer_store_b128((u32x4){T[0], __float_as_uint(mine.x), T[1], __float_as_uint(mine.y)}, ws, WS_ST + (unsigned)((pm * 16 + pn) * 128 + h * 64 + lane) * 16u, 0, 16);
            STEP(18);
            const unsigned so_ = WS_ST + (unsigned)(pm * 16 * 128 + h * 64 + lane) * 16u;
            const u64 t0 = __builtin_amdgcn_s_memrealtime();
            u32x4 st[16];
            for (;;) {
                CFENCE();
#pragma unroll
                for (int j = 0; j < 16; ++j) st[j] = __builtin_amdgcn_raw_buffer_load_b128(ws, so_ + j * 2048u, 0, 16);
                STEP(18);
                unsigned bad = 0;
#pragma unroll
                for (int j = 0; j < 16; ++j) bad |= (st[j][0] ^ T[0]) | (st[j][2] ^ T[1]);
                if (__builtin_amdgcn_ballot_w64(bad != 0u) == 0ull) break;
                if (__builtin_amdgcn_s_memrealtime() - t0 > 300000ull) break;
            }
            STAMP(2);
            STEP(18);
            float mean = 0.f, m2 = 0.f;
#pragma unroll
            for (int j = 0; j < 16; ++j) mean += __uint_as_float(st[j][1]);
            mean *= (1.0f / 16.f);
#pragma unroll
            for (int j = 0; j < 16; ++j) { const float d = __uint_as_float(st[j][1]) - mean; m2 += __uint_as_float(st[j][3]) + 144.f * d * d; }
            const float rstd = __builtin_amdgcn_rsqf(m2 * (1.0f / FH) + 1e-5f);
            *(LAS f32x2*)(lds + L_SP + (h * 64 + lane) * 16) = (f32x2){rstd, -mean * rstd};
            WAITL0();
            if (lane == 0) *(volatile LAS unsigned*)(lds + L_MISC + 4 + h * 4) = 1u;
            const volatile LAS unsigned* zd = (const volatile LAS unsigned*)(lds + L_MISC + 16 + h * 8 + (lane & 1) * 4);
            for (;;) {
                STEP(18);
                if (__builtin_amdgcn_ballot_w64(*zd == 0u) == 0ull) break;
                if (__builtin_amdgcn_s_memrealtime() - t0 > 800000ull) break;
            }
            CFENCE();
            const LAS unsigned char* ptz = opq(lds + Z0 + (h * 64 + 8 * fq + (fr >> 2)) * ZST + (lane & 3) * 8);
            const unsigned zo = WS_Z2F + (unsigned)((pn * 64 + pm * 4 + h * 2) * 9) * 1024u + lane * 16;
#pragma unroll
            for (int i = 0; i < 18; ++i) {
                const LAS unsigned char* pt = ptz + (i / 9) * 32 * ZST + (i % 9) * 32;
                __builtin_amdgcn_raw_buffer_store_b128(__builtin_bit_cast(u32x4, cat8(tr_read(pt), tr_read(pt + 4 * ZST))), ws, zo + i * 1024, 0, 16);
                if (i == 5 || i == 11) STEP(18);
            }
            STEP(18);
            flag_publish(ws, WS_FZ + (unsigned)(pn * 32 + pm * 2 + h) * 128u, T, lane);
            STAMP(3);
            STEPS_TO(18);
            __builtin_amdgcn_s_setprio(0);
        }
    }

    if (is_prod) {
        mk_tag();
        const char* lin = (const char*)wsp_ + p * 1024 + lane * 16;
        auto issueLA = [&](unsigned off, int s) {
#pragma unroll
            for (int i = 0; i < 8; ++i) GLDS(lin + off + i * 2048, lds + s + (p + 2 * i) * 1024, 16); };
        auto issueLB = [&](unsigned off, int s) {
#pragma unroll
            for (int i = 0; i < 9; ++i) GLDS(lin + off + i * 2048, lds + s + BOFF + (p + 2 * i) * 1024, 16); };
        auto ord3 = [&](int s) { return (GM_LAG == 0 || s >= 16) ? s : (((s & 3) == phi) ? s : ((s - GM_LAG) & 15)); };
        const unsigned zb = WS_Z2F + (unsigned)pn * (64u * 9216u);
        flag_wait(ws, WS_FZ + (unsigned)pn * 32u * 128u, 32, T, lane);
        STAMP(2);
        issueLB(zb + (unsigned)ord2(0) * 18432u, 0); issueLB(zb + (unsigned)ord2(1) * 18432u, STG);
        WAITV(9); PBAR();
        int so = 2 * STG;
        { const int k = ord2(2); issueS(k, so); issueLB(zb + (unsigned)k * 18432u, so); so = nextbuf(so); }
#pragma nounroll
        for (int S = 19; S < 48; ++S) {
            WAITV(17); PBAR();
            const int k = ord2(S - 16); issueS(k, so); issueLB(zb + (unsigned)k * 18432u, so); so = nextbuf(so);
        }
        const unsigned hb = WS_HF + (unsigned)qm * (18u * 16384u), ob = WS_WOF + (unsigned)qn * (18u * 18432u);
        WAITV(17); PBAR();
        flag_wait(ws, WS_FZ + (unsigned)(pn ^ 8) * 32u * 128u, 32, T, lane);
        issueLB(ob + (unsigned)ord3(0) * 18432u, 2 * STG);
        PBAR();
        issueLB(ob + (unsigned)ord3(1) * 18432u, 0);
        STAMP(3);
        flag_wait(ws, WS_FH + (unsigned)qm * 32u * 128u, 32, T, lane);
        STAMP(4);
        issueLA(hb + (unsigned)ord3(0) * 16384u, 2 * STG); issueLA(hb + (unsigned)ord3(1) * 16384u, 0);
        WAITV(8); PBAR();
        so = STG;
        { const int k = ord3(2); issueLA(hb + (unsigned)k * 16384u, so); issueLB(ob + (unsigned)k * 18432u, so); so = nextbuf(so); }
#pragma nounroll
        for (int S = 51; S < 66; ++S) {
            WAITV(17); PBAR();
            const int k = ord3(S - 48); issueLA(hb + (unsigned)k * 16384u, so); issueLB(ob + (unsigned)k * 18432u, so); so = nextbuf(so);
        }
        WAITV(17); PBAR();
        WAITV(0); PBAR();
        PBAR();
        STAMP(5); PDUMP();
        if (bid == 0 && p == 0 && lane == 0) __builtin_amdgcn_raw_buffer_store_b64(T, ws, WS_E, 0, 16);
        return;
    }

    if (wid < 4) {
        mk_tag();
        if (wid >> 1) consumer_b<4>(lds, ws, T, zp, bs, pm, pn, wid & 1, 1, lane, wsp_ TRA);
        else consumer_b<5>(lds, ws, T, zp, bs, pm, pn, wid & 1, 0, lane, wsp_ TRA);
    } else {
        __builtin_amdgcn_s_setprio(GM_HPRIO);
        STEPS_TO(19);
        u32x4 uu[3][2];
#pragma unroll
        for (int c = 0; c < 3; ++c)
#pragma unroll
            for (int j = 0; j < 2; ++j) uu[c][j] = __builtin_amdgcn_raw_buffer_load_b128(ws, WS_UT + (unsigned)(ch0 + (c < nch ? 16 * c : 0)) * 4096u + h * 2048 + j * 1024 + lane * 16, 0, 16);
        STEPS_TO(22);
#pragma unroll
        for (int c = 0; c < 3; ++c) {
            BAR(); ++nb;
#pragma unroll
            for (int j = 0; j < 2; ++j) *(LAS u32x4*)(lds + L_UT + c * UT_SZ + 256 + h * 2048 + j * 1024 + lane * 16) = uu[c][j];
            if (lane < 16) *(LAS u32x4*)(lds + L_UT + c * UT_SZ + h * 4352 + lane * 16) = (u32x4){0u, 0u, 0u, 0u};
        }
        WAITL0();
        BAR(); ++nb;
        STAMP(4);
    }

    if (wid < 4) nb = 50;
    for (;;) {
        if (nb > 45) STEPS_TO(50);
        unsigned tk = 0;
        if (lane == 0) tk = __hip_atomic_fetch_add((LAS unsigned*)(lds + L_MISC), 1u, __ATOMIC_RELAXED, __HIP_MEMORY_SCOPE_WORKGROUP);
        tk = __builtin_amdgcn_readfirstlane(tk);
        if (tk >= (unsigned)(4 * nch)) break;
        const int c = tk >> 2, tl = tk & 1;
        if (tk & 2) conv_task<0, 10>(lds, L_KT + c * KT_SZ, L_UT + c * UT_SZ, ytab(c), tl, lane, nb TRA);
        else conv_task<11, 15>(lds, L_KT + c * KT_SZ, L_UT + c * UT_SZ, ytab(c), tl, lane, nb TRA);
    }
    STEPS_TO(50);
    WAITL0();
    STAMP(7);

    if (wid < 4) {
        const int kq = wid >> 1, mh = wid & 1;
        f32x4 acc[5][2];
        struct F3 { s16x4 alo[4], ahi[4]; bf16x8 b[10]; } f0, f1;
        const LAS unsigned char* ph = lds + 2 * STG + mh * 8192 + kq * 2048 + lane * 16;
        const LAS unsigned char* pw = lds + 2 * STG + BOFF + kq * 9216 + lane * 16;
        const LAS unsigned char* pw4 = lds + 2 * STG + BOFF + kq * 9216 + 4096 + (fq * 8 + (fr & 7)) * 16;
        auto rd3 = [&](F3& f, int i, bool single) {
            const int j = i / 7, r = i % 7;
            if (r < 2) {
                if (!single) { const bf16x8 v = *(const LAS bf16x8*)(ph + r * 4096 + j * 1024); f.alo[j * 2 + r] = __builtin_shufflevector(v, v, 0, 1, 2, 3); f.ahi[j * 2 + r] = __builtin_shufflevector(v, v, 4, 5, 6, 7); }
                else { const LAS unsigned char* q = ph - lane * 8 + r * 4096 + j * 1024; f.alo[j * 2 + r] = *(const LAS s16x4*)q; f.ahi[j * 2 + r] = *(const LAS s16x4*)(q + 512); }
            } else if (r < 6) f.b[j * 5 + r - 2] = *(const LAS bf16x8*)(pw + j * 4608 + (r - 2) * 1024);
            else f.b[j * 5 + 4] = *(const LAS bf16x8*)(pw4 + j * 4608);
        };
        int so = 2 * STG;
        auto step3 = [&](const F3& c, F3& l, int mode  , bool first) {
            const int delta = so == 2 * STG ? -2 * STG : STG; so += delta;
#pragma unroll
            for (int i = 0; i < 20; ++i) {
                const int j = i / 10, n = (i % 10) >> 1, m = i & 1;
                acc[n][m] = MFMA16(c.b[j * 5 + n], cat8(c.alo[j * 2 + m], c.ahi[j * 2 + m]), (first && j == 0) ? ((f32x4){0.f, 0.f, 0.f, 0.f}) : acc[n][m]);
                if (mode < 2 && i < 7) { rd3(l, 2 * i, mode == 1); rd3(l, 2 * i + 1, mode == 1); }
                if (mode < 2) { if (i == 8) { ph += delta; asm volatile("" : "+v"(ph)); } if (i == 9) { pw += delta; asm volatile("" : "+v"(pw)); } if (i == 10) { pw4 += delta; asm volatile("" : "+v"(pw4)); } }
                SB0();
            }
        };
        __builtin_amdgcn_s_waitcnt(0xC07F);
        BAR();
#pragma unroll
        for (int i = 0; i < 14; ++i) rd3(f0, i, false);
        ph -= 2 * STG; pw -= 2 * STG; pw4 -= 2 * STG; so = 0;
        WAITL0(); BAR(); step3(f0, f1, 0, true);
#pragma nounroll
        for (int j = 0; j < 7; ++j) {
            WAITL0(); BAR(); step3(f1, f0, 0, false);
            WAITL0(); BAR(); step3(f0, f1, 0, false);
        }
        WAITL0(); BAR(); step3(f1, f0, 1, false);
        WAITL0(); BAR(); step3(f0, f1, 1, false);
        step3(f1, f0, 2, false);
        STAMP(8);
        LAS unsigned char* prw = opq(lds + L_R + wid * 5120 + lane * 16);
#pragma unroll
        for (int n = 0; n < 5; ++n) { const f32x4 x0 = acc[n][0], x1 = acc[n][1]; *(LAS f32x4*)(prw + n * 1024) = kq ? x0 : x1; acc[n][0] = kq ? x1 : x0; }
        WAITL0();
        BAR();
        const int m = mh * 2 + kq;
        const rsrc_t ors = __builtin_amdgcn_make_buffer_rsrc(out, 0, SEQ * DM * 2, 0x00020000);
        const float g = *(const LAS float*)(lds + L_MISC + 64 + (16 * m + fr) * 4), g1 = rbf(1.0f - g);
        const LAS unsigned char* prr = opq(lds + L_R + (wid ^ 2) * 5120 + lane * 16);
        const LAS unsigned char* pat = opq(lds + L_AT + (4 * fq + (fr >> 2)) * 128 + (16 * m + 4 * (lane & 3)) * 2);
#pragma unroll
        for (int n = 0; n < 5; ++n) {
            const f32x4 b = acc[n][0] + *(const LAS f32x4*)(prr + n * 1024);
            const u32x2 a = __builtin_bit_cast(u32x2, tr_read(pat + n * 2048));
            const u32x2 o = {pk_bf16(g * bflo(a[0]) + g1 * b[0], g * bfhi(a[0]) + g1 * b[1]), pk_bf16(g * bflo(a[1]) + g1 * b[2], g * bfhi(a[1]) + g1 * b[3])};
            if (n < 4 || fq < 2) __builtin_amdgcn_raw_buffer_store_b64(o, ors, (unsigned)((qm * 64 + 16 * m + fr) * 1152 + (qn * 72 + 16 * n + 4 * fq) * 2), 0, GM_OAUX);
        }
    } else {
        int d = pn * 36 + pm;
#pragma nounroll
        for (int c = 0; c < nch; ++c) {
            const float dsk = bf2f(Dsk[d]);
#pragma unroll
            for (int k = 0; k < 2; ++k) {
                BAR(); ++nb;
                const int l0 = h * 1024 + k * 512 + lane * 8;
                f32x4 cv = *(const LAS f32x4*)(lds + ytab(c) + 8192 + (l0 >= 16 ? (l0 >> 4) - 1 : 0) * 16);
                if ((lane & 1) || l0 == 0) cv = (f32x4){0.f, 0.f, 0.f, 0.f};
                const f32x4 y0 = *(const LAS f32x4*)(lds + ytab(c) + l0 * 4) + cv, y1 = *(const LAS f32x4*)(lds + ytab(c) + l0 * 4 + 16);
                const u32x4 uv = *(const LAS u32x4*)(lds + L_UT + c * UT_SZ + 256 + l0 * 2);
                u32x4 o;
#pragma unroll
                for (int j = 0; j < 4; ++j) {
                    const float ya = j < 2 ? y0[2 * j] : y1[2 * j - 4], yb = j < 2 ? y0[2 * j + 1] : y1[2 * j - 3];
                    const unsigned yp = pk_bf16(ya, yb), up = pk_bf16(bflo(uv[j]) * dsk, bfhi(uv[j]) * dsk);
                    o[j] = pk_bf16(bflo(yp) + bflo(up), bfhi(yp) + bfhi(up));
                }
                __builtin_amdgcn_raw_buffer_store_b128(o, ws, WS_AT + (unsigned)d * 4096u + l0 * 2, 0, 16);
            }
            d += 16;
        }
        STEPS_TO(58);
        flag_publish(ws, WS_FA + (unsigned)((pn >> 1) * 64 + (pn & 1) * 32 + pm * 2 + h) * 128u, T, lane);
        STAMP(5);
        STEPS_TO(62);
        if (h == 0) {
            const unsigned fo = WS_FA + (unsigned)(qn * 64 + lane) * 128u;
            const u64 t0 = __builtin_amdgcn_s_memrealtime();
            for (;;) {
                CFENCE();
                const u32x2 v = flag_ld(ws, fo);
                STEP(68); STEP(68);
                if (flag_all(v, T, true)) break;
                if (__builtin_amdgcn_s_memrealtime() - t0 > 300000ull) break;
            }
            ACQ();
            u32x4 av[9];
#pragma unroll
            for (int i = 0; i < 9; ++i) av[i] = __builtin_amdgcn_raw_buffer_load_b128(ws, WS_AT + (unsigned)(qn * 72 + i * 8 + (lane >> 3)) * 4096u + qm * 128 + (lane & 7) * 16, 0, 16);
            STEP(68); STEP(68);
            LAS unsigned char* pa_ = opq(lds + L_AT + lane * 16);
#pragma unroll
            for (int i = 0; i < 9; ++i) *(LAS u32x4*)(pa_ + i * 1024) = av[i];
        } else {
            const unsigned gv = __builtin_amdgcn_raw_buffer_load_b32(ws, WS_GATE + (unsigned)(qm * 64 + lane) * 4u, 0, 16);
            BAR(); BAR(); nb += 2;
            *(LAS unsigned*)(lds + L_MISC + 64 + lane * 4) = gv;
        }
        STEPS_TO(68);
        WAITL0();
        BAR();
    }
    STAMP(9); TRDUMP();
}
}

extern "C" void kernel_launch(void* const* d_in, const int* in_sizes, int n_in, void* d_out, int out_size, void* d_ws, size_t ws_size, hipStream_t stream) {
    (void)in_sizes; (void)n_in; (void)out_size; (void)ws_size;
    (void)hipFuncSetAttribute((const void*)gm::gm_kernel, hipFuncAttributeMaxDynamicSharedMemorySize, gm::LDS_BYTES);
    hipLaunchKernelGGL(gm::gm_kernel, dim3(256), dim3(512), gm::LDS_BYTES, stream, (const u16*)d_in[0], (const u16*)d_in[1], (const u16*)d_in[2], (const u16*)d_in[3],
                       (const u16*)d_in[4], (const u16*)d_in[5], (const u16*)d_in[6], (const u16*)d_in[8], (const u16*)d_in[9], (const u16*)d_in[10],
                       (const u16*)d_in[11], (const u16*)d_in[12], (u16*)d_out, (unsigned char*)d_ws);
}
```

```cpp
#define GM_WARM 0
#include <hip/hip_runtime.h>
#include <stdint.h>
#include <type_traits>

#define LAS __attribute__((address_space(3)))
typedef unsigned short u16;
typedef unsigned long long u64;
typedef short bf16x8 __attribute__((ext_vector_type(8)));
typedef short s16x4 __attribute__((ext_vector_type(4)));
typedef _Float16 f16x8 __attribute__((ext_vector_type(8)));
typedef _Float16 f16x2 __attribute__((ext_vector_type(2)));
typedef __bf16 bf16x2 __attribute__((ext_vector_type(2)));
typedef float f32x4 __attribute__((ext_vector_type(4)));
typedef float f32x2 __attribute__((ext_vector_type(2)));
typedef unsigned u32x4 __attribute__((ext_vector_type(4)));
typedef unsigned u32x2 __attribute__((ext_vector_type(2)));
typedef __amdgpu_buffer_rsrc_t rsrc_t;

namespace gm {
constexpr int SEQ = 2048, DM = 576, FH = 2304;
constexpr unsigned WS_E = 0, WS_FS = 4096, WS_FZ = WS_FS + 65536, WS_FH = WS_FZ + 65536, WS_FA = WS_FH + 131072;
constexpr unsigned WS_ST = 0x60000, WS_GATE = 0xE0000, WS_DBG = 0x1E00000, WS_Z2F = 0x100000, WS_HF = 0xA00000, WS_WOF = 0x1300000, WS_UT = 0x1600000, WS_AT = 0x1900000, WS_END = 0x1C00000, WS_TRC = 0x1C00000;
constexpr int STG = 34816, BOFF = 16384, Z0 = 3 * STG, ZST = 296, T0 = Z0 + 128 * ZST;
constexpr int KT_SZ = 4352, UT_SZ = 4608, Y_SZ = 10240;
constexpr int L_MISC = 163840 - 320;
constexpr int L_KT = L_MISC - 3 * KT_SZ, L_SP = T0, L_KD = Z0 + 3 * 8448, L_UT = Z0 + 3 * Y_SZ;
constexpr int L_AT = Z0, L_R = Z0 + 10240;
constexpr int LDS_BYTES = 163840;
static_assert(L_UT + 3 * UT_SZ <= L_KT && L_KD + 3 * 3072 <= T0 && L_SP + 2048 <= L_KT, "lds map");
#define GM_HPRIO 1
#define GM_OAUX 0
#define GM_PFL 13
#define GM_CODE 49792
#define GM_ORD 0
#define GM_WN 4
#define GM_LAG 4

__device__ __forceinline__ float bf2f(unsigned v) { return __uint_as_float(v << 16); }
__device__ __forceinline__ float bflo(unsigned w) { return __uint_as_float(w << 16); }
__device__ __forceinline__ float bfhi(unsigned w) { return __uint_as_float(w & 0xffff0000u); }
__device__ __forceinline__ unsigned pk_bf16(float a, float b) { f32x2 v = {a, b}; return __builtin_bit_cast(unsigned, __builtin_convertvector(v, bf16x2)); }
__device__ __forceinline__ unsigned pk_f16(float a, float b) { f32x2 v = {a, b}; return __builtin_bit_cast(unsigned, __builtin_convertvector(v, f16x2)); }
__device__ __forceinline__ float rbf(float a) { return bflo(pk_bf16(a, a)); }
__device__ __forceinline__ f32x2 gelu2(f32x2 x) {
    const f32x2 w = (x * x) * (-0.10294324f) + (-2.30220819f);
    const f32x2 a = w * x;
    f32x2 e; e.x = __builtin_amdgcn_exp2f(a.x); e.y = __builtin_amdgcn_exp2f(a.y);
    e = e + 1.0f;
    f32x2 r; r.x = __builtin_amdgcn_rcpf(e.x); r.y = __builtin_amdgcn_rcpf(e.y);
    return x * r;
}
__device__ __forceinline__ f32x4 gelu4(f32x4 v) { const f32x2 a = gelu2((f32x2){v[0], v[1]}), b = gelu2((f32x2){v[2], v[3]}); return (f32x4){a.x, a.y, b.x, b.y}; }
__device__ __forceinline__ float allred_q(float v) {
    unsigned u = __float_as_uint(v);
    auto r = __builtin_amdgcn_permlane16_swap(u, u, false, false);
    v = __uint_as_float(r[0]) + __uint_as_float(r[1]);
    u = __float_as_uint(v);
    auto s = __builtin_amdgcn_permlane32_swap(u, u, false, false);
    return __uint_as_float(s[0]) + __uint_as_float(s[1]);
}
#define ACQ() __builtin_amdgcn_fence(__ATOMIC_ACQUIRE, "wavefront")
#define TRP
#define TRA
#define TRDUMP() do { } while (0)
#define BAR() __builtin_amdgcn_s_barrier()
#define PBAR() BAR()
#define PDUMP() TRDUMP()
#define WAITV(n) asm volatile("s_waitcnt vmcnt(" #n ")" ::: "memory")
#define WAITL0() asm volatile("s_waitcnt lgkmcnt(0)" ::: "memory")
#define CFENCE() asm volatile("" ::: "memory")
#define SGB(mask, n, id) __builtin_amdgcn_sched_group_barrier(mask, n, id)
#define MFMA16(a, b, c) __builtin_amdgcn_mfma_f32_16x16x32_bf16(a, b, c, 0, 0, 0)
__device__ __forceinline__ s16x4 tr_read(const LAS unsigned char* p) { return __builtin_amdgcn_ds_read_tr16_b64_v4i16((LAS s16x4*)p); }
__device__ __forceinline__ bf16x8 cat8(s16x4 a, s16x4 b) { return __builtin_shufflevector(a, b, 0, 1, 2, 3, 4, 5, 6, 7); }
template <class P> __device__ __forceinline__ P opq(P p) { asm volatile("" : "+v"(p)); return p; }
__device__ __forceinline__ int nextbuf(int so) { return so == 2 * STG ? 0 : so + STG; }

#define STAMP(i) do { } while (0)

__device__ __forceinline__ u32x2 flag_ld(rsrc_t ws, unsigned off) { return __builtin_amdgcn_raw_buffer_load_b64(ws, off, 0, 16); }
__device__ __forceinline__ bool flag_all(u32x2 v, u32x2 T, bool active) { return __builtin_amdgcn_ballot_w64(active && (v[0] != T[0] || v[1] != T[1])) == 0ull; }
__device__ __forceinline__ void flag_publish(rsrc_t ws, unsigned off, u32x2 T, int lane) {
    WAITV(0);
    if (lane == 0) __builtin_amdgcn_raw_buffer_store_b64(T, ws, off, 0, 16);
}
__device__ __forceinline__ void flag_wait(rsrc_t ws, unsigned base, int n, u32x2 T, int lane) {
    const unsigned off = base + (unsigned)(lane < n ? lane : 0) * 128u;
    const u64 t0 = __builtin_amdgcn_s_memrealtime();
    for (;;) {
        CFENCE();
        const u32x2 v = flag_ld(ws, off);
        if (flag_all(v, T, true)) break;
        if (__builtin_amdgcn_s_memrealtime() - t0 > 300000ull) break;
        __builtin_amdgcn_s_sleep(1);
    }
    ACQ();
}

#define SB0() __builtin_amdgcn_sched_barrier(0)
template <int NT> struct Frag { bf16x8 a[4]; s16x4 lo[NT], hi[NT]; };
template <int NT, bool TR, int HALF>
__device__ __forceinline__ void frag_read(Frag<NT>& f, int i, const LAS unsigned char* pa, const LAS unsigned char* pb) {
    if (GM_ORD) { constexpr int NB = TR ? 2 * NT : NT; i = i == 0 ? 0 : i <= NB ? i + 3 : i - NB; }
    if (i < 4) f.a[i] = *(const LAS bf16x8*)(pa + i * 2048);
    else if (TR) { const int n = (i - 4) >> 1; if ((i - 4) & 1) f.hi[n] = tr_read(pb + HALF * 9216 + 4608 + n * 32); else f.lo[n] = tr_read(pb + HALF * 9216 + n * 32); }
    else { const int n = i - 4; const bf16x8 v = *(const LAS bf16x8*)(pb + HALF * 9216 + n * 1024);
           f.lo[n] = __builtin_shufflevector(v, v, 0, 1, 2, 3); f.hi[n] = __builtin_shufflevector(v, v, 4, 5, 6, 7); }
}
template <int NT, bool TR, int LHALF, int RPS, bool FIRST, bool UPD>
__device__ __forceinline__ void half_step(f32x4 (&acc)[NT][4], const Frag<NT>& c, Frag<NT>& l, const LAS unsigned char*& pa0, const LAS unsigned char*& pa1, const LAS unsigned char*& pb, int delta, const float* ini = nullptr) {
    constexpr int NRD = TR ? 4 + 2 * NT : 4 + NT, NM = 4 * NT, LAST = (NRD + RPS - 1) / RPS;
#pragma unroll
    for (int i = 0; i < NM; ++i) {
        const int n = GM_ORD ? i % NT : i >> 2, m = GM_ORD ? i / NT : i & 3;
        acc[n][m] = MFMA16(cat8(c.lo[n], c.hi[n]), c.a[m], FIRST ? (TR ? ((f32x4){0.f, 0.f, 0.f, 0.f}) : ((f32x4){ini[m], ini[m], ini[m], ini[m]})) : acc[n][m]);
        if (LHALF >= 0) {
#pragma unroll
            for (int r = 0; r < RPS; ++r) if (i * RPS + r < NRD) frag_read<NT, TR, (LHALF > 0 ? 1 : 0)>(l, i * RPS + r, LHALF > 0 ? pa1 : pa0, pb);
        }
        if (UPD) { if (i == LAST) { pa0 += delta; asm volatile("" : "+v"(pa0)); } if (i == LAST + 1) { pb += delta; asm volatile("" : "+v"(pb)); } if (i == LAST + 2) { pa1 += delta; asm volatile("" : "+v"(pa1)); } }
        SB0();
    }
}
template <int NT, bool TR, class HK>
__device__ __forceinline__ void gemm_phase(int nsteps, f32x4 (&acc)[NT][4], int& so, const LAS unsigned char* pa0, const LAS unsigned char* pa1, const LAS unsigned char* pb,
                                           const float* ini, const volatile LAS unsigned* rdy, HK hook TRP) {
    constexpr int NRD = TR ? 4 + 2 * NT : 4 + NT;
    Frag<NT> f0, f1;
    pa0 += so; pa1 += so; pb += so;
    asm volatile("" : "+v"(pa0), "+v"(pa1), "+v"(pb));
    __builtin_amdgcn_s_waitcnt(0xC07F);
    BAR();
#pragma unroll
    for (int i = 0; i < NRD; ++i) frag_read<NT, TR, 0>(f0, i, pa0, pb);
    SB0();
    int delta = so == 2 * STG ? -2 * STG : STG; so += delta;
    half_step<NT, TR, 1, 2, true, true>(acc, f0, f1, pa0, pa1, pb, delta, ini);
#pragma nounroll
    for (int s = 1; s < nsteps; ++s) {
        WAITL0();
        BAR();
        unsigned rv = 0;
        if (TR) { rv = *rdy; SB0(); }
        delta = so == 2 * STG ? -2 * STG : STG; so += delta;
        half_step<NT, TR, 0, 1, false, false>(acc, f1, f0, pa0, pa1, pb, 0);
        half_step<NT, TR, 1, 2, false, true>(acc, f0, f1, pa0, pa1, pb, delta);
        if (TR) hook(rv);
    }
    half_step<NT, TR, -1, 1, false, false>(acc, f1, f0, pa0, pa1, pb, 0);
}

template <int NT, class LT>
__device__ __forceinline__ void consumer_a(LAS unsigned char* lds, u32x2 (&zp)[5][4], LT late, int wm, int wn, int lane, unsigned char* wsp_ TRP) {
    const int fr = lane & 15, fq = lane >> 4;
    constexpr int N0 = (NT == 5) ? 0 : 5;
    f32x4 acc[NT][4];
    int so = 0;
    const int sw = (fr >> 1) & 7;
    const int a0 = (wm * 64 + fr) * 128 + ((fq ^ sw) << 4), a1 = (wm * 64 + fr) * 128 + (((4 + fq) ^ sw) << 4);
    const int bw = BOFF + (4 * fq + (fr >> 2)) * 288 + (lane & 3) * 8 + N0 * 32;
    const volatile LAS unsigned* rdy = (const volatile LAS unsigned*)(lds + L_MISC + 4 + wm * 4);
    bool done = false;
    auto normalise = [&]() {
        const LAS unsigned char* ps = opq(lds + L_SP + (wm * 64 + fr) * 16);
        LAS unsigned char* pz = opq(lds + Z0 + (wm * 64 + fr) * ZST + N0 * 32 + fq * 8);
#pragma unroll
        for (int m = 0; m < 4; ++m) {
            const f32x2 sr = *(const LAS f32x2*)(ps + m * 256);
#pragma unroll
            for (int n = 0; n < NT; ++n) {
                const f32x2 u = (f32x2){bflo(zp[n][m][0]), bfhi(zp[n][m][0])} * sr.x + sr.y, v = (f32x2){bflo(zp[n][m][1]), bfhi(zp[n][m][1])} * sr.x + sr.y;
                *(LAS u32x2*)(pz + m * 16 * ZST + n * 32) = (u32x2){pk_bf16(u.x, u.y), pk_bf16(v.x, v.y)};
            }
        }
        WAITL0();
        if (lane == 0) *(volatile LAS unsigned*)(lds + L_MISC + 16 + (wm * 2 + wn) * 4) = 1u;
        done = true;
    };
    auto hook = [&](unsigned rv) { if (__builtin_amdgcn_readfirstlane(rv) != 0u && !done) normalise(); };
#pragma unroll
    for (int n = 0; n < NT; ++n)
#pragma unroll
        for (int m = 0; m < 4; ++m) zp[n][m] = (u32x2){0u, 0u};
#pragma nounroll
    for (int it = 0; it < 2; ++it) {
        gemm_phase<NT, true>(9, acc, so, lds + a0, lds + a1, lds + bw, nullptr, rdy, hook TRA);
        STAMP(1 + 2 * it);
        late();
        if (it == 1 && !done) {
            const u64 t0 = __builtin_amdgcn_s_memrealtime();
            while (__builtin_amdgcn_readfirstlane(*rdy) == 0u && __builtin_amdgcn_s_memrealtime() - t0 < 400000ull) __builtin_amdgcn_s_sleep(1);
            normalise();
        }
#pragma unroll
        for (int m = 0; m < 4; ++m)
#pragma unroll
            for (int n = 0; n < NT; ++n) { acc[n][m] = gelu4(acc[n][m]); zp[n][m] = (u32x2){pk_bf16(acc[n][m][0], acc[n][m][1]), pk_bf16(acc[n][m][2], acc[n][m][3])}; }
        if (it == 0) {
            LAS unsigned char* ps = opq(lds + L_SP + (wm * 64 + fr) * 16 + wn * 8);
#pragma unroll
            for (int m = 0; m < 4; ++m) {
                f32x4 s4 = acc[0][m];
#pragma unroll
                for (int n = 1; n < NT; ++n) s4 += acc[n][m];
                const float mean = opq(allred_q((s4[0] + s4[1]) + (s4[2] + s4[3])) * (1.0f / (16 * NT)));
                f32x4 q4 = {0.f, 0.f, 0.f, 0.f};
#pragma unroll
                for (int n = 0; n < NT; ++n) { const f32x4 d = acc[n][m] - mean; q4 += d * d; }
                const float m2 = allred_q((q4[0] + q4[1]) + (q4[2] + q4[3]));
                if (fq == 0) *(LAS f32x2*)(ps + m * 256) = (f32x2){mean, m2};
            }
            STAMP(2);
        }
    }
    STAMP(4);
}

template <int NT>
__device__ __forceinline__ void consumer_b(LAS unsigned char* lds, rsrc_t ws, u32x2 T, const u32x2 (&z1)[5][4], const float (&bs_)[4], int pm, int pn, int wm, int wn, int lane, unsigned char* wsp_ TRP) {
    float bs[4] = {bf2f(__float_as_uint(opq(bs_[0]))), bf2f(__float_as_uint(opq(bs_[1]))), bf2f(__float_as_uint(opq(bs_[2]))), bf2f(__float_as_uint(opq(bs_[3])))};
    const int fr = lane & 15, fq = lane >> 4;
    constexpr int N0 = (NT == 5) ? 0 : 5;
    f32x4 acc[NT][4];
    int so = 0;
    const int sw = (fr >> 1) & 7;
    const int a0 = (wm * 64 + fr) * 128 + ((fq ^ sw) << 4), a1 = (wm * 64 + fr) * 128 + (((4 + fq) ^ sw) << 4);
    const int bz = BOFF + N0 * 1024 + lane * 16;
    gemm_phase<NT, false>(32, acc, so, lds + a0, lds + a1, lds + bz, bs, nullptr, [](unsigned) {} TRA);
    STAMP(5);
    __builtin_amdgcn_s_setprio(3);
    const int qm = 2 * pm + wm;
    const unsigned hb = WS_HF + (unsigned)qm * (18u * 16384u);
#pragma unroll
    for (int m = 0; m < 4; ++m) {
        u32x2 hp[NT];
#pragma unroll
        for (int n = 0; n < NT; ++n) {
            const f32x2 u = (f32x2){bflo(z1[n][m][0]), bfhi(z1[n][m][0])} * (f32x2){acc[n][m][0], acc[n][m][1]}, v = (f32x2){bflo(z1[n][m][1]), bfhi(z1[n][m][1])} * (f32x2){acc[n][m][2], acc[n][m][3]};
            hp[n] = (u32x2){pk_bf16(u.x, u.y), pk_bf16(v.x, v.y)};
        }
#pragma unroll
        for (int j = 0; j < 2; ++j)
            __builtin_amdgcn_raw_buffer_store_b128((u32x4){hp[2 * j][0], hp[2 * j][1], hp[2 * j + 1][0], hp[2 * j + 1][1]}, ws,
                                                   hb + (unsigned)pn * 16384u + (unsigned)(m * 4 + wn * 2 + j) * 1024u + lane * 16, 0, 16);
        if (NT == 5) __builtin_amdgcn_raw_buffer_store_b64(hp[NT - 1], ws, hb + (unsigned)(16 + (pn >> 3)) * 16384u + m * 4096u + (unsigned)(pn & 7) * 512u + lane * 8, 0, 16);
    }
    flag_publish(ws, WS_FH + (unsigned)(qm * 32 + pn * 2 + wn) * 128u, T, lane);
    __builtin_amdgcn_s_setprio(0);
    STAMP(6);
}

template <int TLO, int THI>
__device__ __forceinline__ void conv_task(LAS unsigned char* lds, int kt, int ut, int yt, int tl, int lane, int& nb TRP) {
#define CSL() do { if (nb < 50) { BAR(); ++nb; } } while (0)
    constexpr int NTL = THI - TLO + 1, ND = THI + 2;
    const int i = lane & 15, g = lane >> 4;
    const LAS unsigned char* pk = opq(lds + kt + (i & 3) * 1088 + ((i >> 2) + g) * 16);
    const LAS unsigned char* pu = opq(lds + ut + (128 + 16 * tl - 32 * g - 4 * (i >> 2) + 4 * (i & 3)) * 2 - 32);
    bf16x8 ub[ND];
#pragma unroll
    for (int d = 0; d < ND; ++d) { if (d == 0 || d == 8) CSL(); ub[d] = cat8(tr_read(pu + 256 * d + 32), tr_read(pu + 256 * d)); }
    f32x4 acc[NTL];
#pragma unroll
    for (int t = 0; t < NTL; ++t) acc[t] = (f32x4){0.f, 0.f, 0.f, 0.f};
#pragma unroll
    for (int s = -1; s <= THI; ++s) {
        if (TLO == 0 ? (s <= 3 || s == 5 || s == 7) : (s & 1) != 0 && s < 12) CSL();
        const bf16x8 kf = *(const LAS bf16x8*)(pk + 64 * (s + 1));
#pragma unroll
        for (int t = TLO; t <= THI; ++t) if (t >= s) acc[t - TLO] = MFMA16(kf, ub[t - s], acc[t - TLO]);
    }
    LAS float* y = opq((LAS float*)(lds + yt) + 32 * g + 16 * tl + i);
    LAS float* cy = opq((LAS float*)(lds + yt + 8192) + (2 * g + tl) * 4 + (i & 3));
#define DPPF(v, ctl) __int_as_float(__builtin_amdgcn_update_dpp(0, __float_as_int(v), ctl, 0xf, 0xf, true))
#pragma unroll
    for (int t = TLO; t <= THI; ++t) {
        if (((t - TLO) % 2) == 0) CSL();
        const f32x4 a = acc[t - TLO];
        y[128 * t] = (a[0] + DPPF(a[1], 0x111)) + (DPPF(a[2], 0x112) + DPPF(a[3], 0x113));
        const float cv = DPPF(a[1], 0x10f) + (DPPF(a[2], 0x10e) + DPPF(a[3], 0x10d));
        if (i < 4) cy[32 * t] = cv;
    }
#undef DPPF
#undef CSL
}
__device__ __forceinline__ int ytab(int c) { return Z0 + c * Y_SZ; }

__global__ __launch_bounds__(512, 2) void gm_kernel(const u16* __restrict__ x, const u16* __restrict__ logA, const u16* __restrict__ Aim, const u16* __restrict__ Cre,
                                                    const u16* __restrict__ Cim, const u16* __restrict__ logdt, const u16* __restrict__ Dsk, const u16* __restrict__ win,
                                                    const u16* __restrict__ wsp, const u16* __restrict__ bsp, const u16* __restrict__ wout, const u16* __restrict__ gw,
                                                    u16* __restrict__ out, unsigned char* wsp_) {
    extern __shared__ __attribute__((aligned(16))) unsigned char lds_[];
    LAS unsigned char* lds = (LAS unsigned char*)lds_;
    const int wid = __builtin_amdgcn_readfirstlane(threadIdx.x >> 6), lane = threadIdx.x & 63;
    const int bid = blockIdx.x, xg = bid & 7, rr = bid >> 3, bi = rr >> 2, bj = rr & 3;
    const int pm = (xg >> 2) * 8 + bi, pn = (xg & 3) * 4 + bj;
    const int qm = 2 * pm + (pn >> 3), qn = pn & 7;
    const int nch = pm < 4 ? 3 : 2;
    const rsrc_t ws = __builtin_amdgcn_make_buffer_rsrc(wsp_, 0, WS_END, 0x00020000);
    STAMP(0);
    u64 pc0; asm volatile("s_getpc_b64 %0" : "=s"(pc0));

    const int fr = lane & 15, fq = lane >> 4;
    const int h = wid - 6, p = wid - 4;
    const bool is_prod = (wid >> 1) == 2;
    const int ch0 = pn * 36 + pm;
    float bs[4];
    auto late = [&]() {
#pragma unroll
        for (int m = 0; m < 4; ++m) bs[m] = __uint_as_float((unsigned)bsp[pm * 128 + (wid & 1) * 64 + m * 16 + fr]);
    };
    const int phi = (-(bi + bj)) & 3;
    const unsigned rsw = (unsigned)(((lane & 7) ^ ((4 * p + (lane >> 4)) & 7)) * 16);
#define GLDS(src, dst, aux) __builtin_amdgcn_global_load_lds((const unsigned*)(src), (LAS unsigned*)(dst), 16, 0, aux)
    auto issueS = [&](int k, int s) {
        const char* sb = (const char*)wsp + (size_t)(pm * 128 + p * 8) * 4096 + k * 128; const unsigned vs = (unsigned)(lane >> 3) * 4096u + rsw;
#pragma unroll
        for (int i = 0; i < 8; ++i) GLDS(sb + (size_t)i * (16 * 4096) + vs, lds + s + (p + 2 * i) * 1024, 0); };
    auto ord2 = [&](int s) { return GM_LAG == 0 ? s : (((s & 3) == phi) ? s : ((s - GM_LAG) & 31)); };
    u32x2 E, T;
    auto mk_tag = [&]() { u64 z = (((u64)__builtin_amdgcn_readfirstlane(E[1]) << 32) | __builtin_amdgcn_readfirstlane(E[0])) + 0x9E3779B97F4A7C15ull;
        z = (z ^ (z >> 30)) * 0xBF58476D1CE4E5B9ull; z = (z ^ (z >> 27)) * 0x94D049BB133111EBull; z ^= z >> 31; T = (u32x2){(unsigned)z, (unsigned)(z >> 32)}; };
    u32x2 zp[5][4];
#define STEP(lim) do { if (nb < (lim)) { BAR(); ++nb; } } while (0)
#define STEPS_TO(lim) _Pragma("nounroll") while (nb < (lim)) { BAR(); ++nb; }
    int nb = 0;
    if (wid < nch) {
        const int kch = ch0 + 16 * wid, ki = kch * 64 + lane;
        const unsigned pA = ((const unsigned*)logA)[ki], pI = ((const unsigned*)Aim)[ki], pR = ((const unsigned*)Cre)[ki], pC = ((const unsigned*)Cim)[ki], pT = logdt[kch];
        LAS u32x2* tw = opq((LAS u32x2*)(lds + Z0 + wid * 8448) + lane);
        const LAS unsigned char* trd = opq(lds + Z0 + wid * 8448 + fr * 528 + fq * 16);
        const float dt = rbf(__expf(bf2f(pT)));
        const f32x2 ar = (f32x2){rbf(__expf(bflo(pA))), rbf(__expf(bfhi(pA)))} * dt * -1.44269504f, ai = (f32x2){bflo(pI), bfhi(pI)} * dt * 0.15915494f;
        const f32x2 cr = (f32x2){bflo(pR), bfhi(pR)} * dt, ci = (f32x2){bflo(pC), bfhi(pC)} * dt;
        float cm = fmaxf(fmaxf(fabsf(cr.x), fabsf(ci.x)), fmaxf(fabsf(cr.y), fabsf(ci.y)));
#define DPPF(v, ctl) __int_as_float(__builtin_amdgcn_update_dpp(0, __float_as_int(v), ctl, 0xf, 0xf, true))
        cm = fmaxf(cm, DPPF(cm, 0x121)); cm = fmaxf(cm, DPPF(cm, 0x122)); cm = fmaxf(cm, DPPF(cm, 0x124)); cm = fmaxf(cm, DPPF(cm, 0x128));
#undef DPPF
        { auto r = __builtin_amdgcn_permlane16_swap(__float_as_uint(cm), __float_as_uint(cm), false, false); cm = fmaxf(__uint_as_float(r[0]), __uint_as_float(r[1]));
          auto q = __builtin_amdgcn_permlane32_swap(__float_as_uint(cm), __float_as_uint(cm), false, false); cm = fmaxf(fmaxf(__uint_as_float(q[0]), __uint_as_float(q[1])), 1e-30f); }
        struct C2 { f32x2 x, y; };
        auto cexp = [&](float p_) { const f32x2 e = ar * p_, t = ai * p_;
            const f32x2 mg = {__builtin_amdgcn_exp2f(e.x), __builtin_amdgcn_exp2f(e.y)}; const float t0 = __builtin_amdgcn_fractf(t.x), t1 = __builtin_amdgcn_fractf(t.y);
            return C2{mg * (f32x2){__builtin_amdgcn_cosf(t0), __builtin_amdgcn_cosf(t1)}, mg * (f32x2){__builtin_amdgcn_sinf(t0), __builtin_amdgcn_sinf(t1)}}; };
        auto cmul = [](C2 a, C2 b) { return C2{__builtin_elementwise_fma(a.x, b.x, -(a.y * b.y)), __builtin_elementwise_fma(a.x, b.y, a.y * b.x)}; };
        auto put = [&](int row, C2 v, bool conj) { tw[row * 66] = conj ? (u32x2){pk_f16(v.x.x, -v.y.x), pk_f16(v.x.y, -v.y.y)} : (u32x2){pk_f16(v.x.x, v.y.x), pk_f16(v.x.y, v.y.y)}; };
        C2 wp[16], zr[8], vp[4], cv[17];
        wp[1] = cexp(8.f); zr[1] = cexp(1.f); vp[0] = cexp(128.f);
        tw[0] = (u32x2){pk_f16(1.f, 0.f), pk_f16(1.f, 0.f)};
#pragma unroll
        for (int j = 1; j < 16; ++j) { if (j > 1) wp[j] = cmul(wp[j >> 1], wp[j - (j >> 1)]); put(j, wp[j], true); }
        CFENCE();
        u32x4 fb[8];
#pragma unroll
        for (int ks = 0; ks < 8; ++ks) fb[ks] = *(const LAS u32x4*)(trd + ks * 64);
        CFENCE();
#pragma unroll
        for (int r = 2; r < 8; ++r) zr[r] = cmul(zr[r >> 1], zr[r - (r >> 1)]);
#pragma unroll
        for (int k = 1; k < 4; ++k) vp[k] = cmul(vp[k - 1], vp[k - 1]);
        const float rc = __builtin_amdgcn_rcpf(cm);
        cv[0] = C2{cr * rc, ci * rc};
#pragma unroll
        for (int k = 1; k < 8; ++k) { const int lg = k >= 4 ? 2 : k >= 2 ? 1 : 0; cv[k] = cmul(cv[k - (1 << lg)], vp[lg]); }
        LAS float* kb_ = (LAS float*)(lds + L_KD + wid * 3072);
        const float sc = 2.0f * cm;
        u32x4 fa[3][8];
#pragma unroll
        for (int tt = 0; tt < 3; ++tt) {
#pragma unroll
            for (int i = 0; i < (tt == 2 ? 9 : 16); ++i) {
                const int r = tt == 0 ? (i & 7) : tt == 1 ? (i < 8 ? 2 * (i & 3) : 4 * (i & 1)) : 0;
                const int hh = tt == 0 ? (i >> 3) : tt == 1 ? (i < 8 ? 2 + (i >> 2) : 4 + ((i - 8) >> 1)) : 8 + i;
                if (tt == 2) cv[hh] = cmul(cv[hh - 8], vp[3]);
                put(i, r == 0 ? cv[hh] : cmul(cv[hh], zr[r]), false);
            }
            CFENCE();
#pragma unroll
            for (int ks = 0; ks < 8; ++ks) fa[tt][ks] = *(const LAS u32x4*)(trd + ks * 64);
            CFENCE();
            SB0();
        }
        f32x4 kd[3] = {{0.f, 0.f, 0.f, 0.f}, {0.f, 0.f, 0.f, 0.f}, {0.f, 0.f, 0.f, 0.f}};
#pragma unroll
        for (int ks = 0; ks < 8; ++ks)
#pragma unroll
            for (int tt = 0; tt < 3; ++tt) kd[tt] = __builtin_amdgcn_mfma_f32_16x16x32_f16(__builtin_bit_cast(f16x8, fa[tt][ks]), __builtin_bit_cast(f16x8, fb[ks]), kd[tt], 0, 0, 0);
#pragma unroll
        for (int tt = 0; tt < 3; ++tt) {
            LAS float* kw = opq(kb_ + (tt == 0 ? 128 * (fq >> 1) + 4 * (fq & 1) + 8 * fr : tt == 1 ? 256 + 64 * fq + (fq < 2 ? 4 : 2) * fr : 512 + 64 * fq + fr));
            LAS float* kwb = tt == 1 ? opq(kw + (fq < 2 ? 2 : 32)) : kw + (tt == 0 ? 2 : 32);
            kw[0] = kd[tt][0] * sc; kw[tt == 2 ? 16 : 1] = kd[tt][1] * sc; kwb[0] = kd[tt][2] * sc; kwb[tt == 2 ? 16 : 1] = kd[tt][3] * sc;
        }
        WAITL0();
    }
    if (wid == 3) {
        unsigned w_ = 0;
        if (lane < GM_PFL) w_ = *(const __attribute__((address_space(1))) unsigned*)((pc0 & ~127ull) + (unsigned)min((rr * GM_PFL + lane) * 128, GM_CODE));
        asm volatile("" :: "v"(w_));
    }
#define IC(n) std::integral_constant<int, n>{}
    if (wid < 6) STAMP(10);
    if (is_prod) {
        const unsigned vx = (unsigned)(lane >> 3) * 1152u + rsw;
        unsigned wv[9];
#pragma unroll
        for (int i = 0; i < 9; ++i) { const int c = (p + 2 * i) * 64 + lane, rho = c / 18, ch = c - rho * 18;
            const int kap = (rho & 32) + 8 * ((rho >> 2) & 3) + 4 * ((rho >> 4) & 1) + (rho & 3); wv[i] = (unsigned)kap * 9216u + (unsigned)ch * 16u; }
        const char* xb = (const char*)x + (size_t)(pm * 128 + p * 8) * 1152;
        const char* wb = (const char*)win + pn * 288;
        auto issueX = [&](int k, int s) {
#pragma unroll
            for (int i = 0; i < 8; ++i) GLDS(xb + (size_t)i * (16 * 1152) + k * 128 + vx, lds + s + (p + 2 * i) * 1024, 0); };
        auto issueW = [&](int k, int coloff, int s) {
            const char* b = wb + (size_t)k * (64 * 9216) + coloff;
#pragma unroll
            for (int i = 0; i < 9; ++i) GLDS(b + wv[i], lds + s + BOFF + (p + 2 * i) * 1024, 0); };
        auto ord1 = [&](int s) { return (GM_LAG == 0 || (s & 3) == phi) ? s : (s >= 4 ? s - 4 : (s == 0 ? 8 : s + 4)); };
        issueX(ord1(0), 0); issueW(ord1(0), FH * 2, 0); issueX(ord1(1), STG); issueW(ord1(1), FH * 2, STG);
        issueX(ord1(2), 2 * STG); issueW(ord1(2), FH * 2, 2 * STG);
        E = flag_ld(ws, WS_E);
        int so = 0;
        WAITV(35); PBAR();
#pragma nounroll
        for (int S = 1; S < 16; ++S) {
            if (S >= 8 && S <= 10) WAITV(9); else WAITV(17);
            PBAR();
            const int t = S + 2, k = ord1(t < 9 ? t : t < 12 ? t - 3 : t - 12);
            if (t < 9 || t >= 12) issueX(k, so);
            issueW(k, t >= 9 ? 0 : FH * 2, so); so = nextbuf(so);
        }
        WAITV(17); PBAR(); issueS(ord2(0), 0);
        WAITV(8); PBAR(); issueS(ord2(1), STG);
        STAMP(1);
    } else if (wid < 4) {
        E = flag_ld(ws, WS_E);
        if (wid >> 1) consumer_a<4>(lds, zp, late, wid & 1, 1, lane, wsp_ TRA);
        else consumer_a<5>(lds, zp, late, wid & 1, 0, lane, wsp_ TRA);
    } else {
        if (h == 0 && lane < 12) *(LAS unsigned*)(lds + L_MISC + lane * 4) = 0u;
        const int row0 = lane / 9, chk0 = lane - row0 * 9;
        const int u = (pn >> 3) * 16 + pm, nblk = u < 8 ? 3 : 2;
        u32x2 uc[18]; u32x4 xv[9], gv[9], wv[3][5];
        if (h == 0) {
            { const char* xb = (const char*)x + (size_t)pm * 128 * 1152 + pn * 72; unsigned off = row0 * 1152 + chk0 * 8; int ch = chk0;
#pragma unroll
              for (int i = 0; i < 9; ++i) { uc[i] = *(const u32x2*)(xb + off); uc[9 + i] = *(const u32x2*)(xb + 64 * 1152 + off); off += 7 * 1152 + 8; if (++ch == 9) { ch = 0; off += 1152 - 72; } } }
            { const char* xr = (const char*)x + (size_t)(pm * 128 + pn * 8 + (lane >> 3)) * 1152 + (lane & 7) * 16; const char* gr = (const char*)gw + (lane & 7) * 16;
#pragma unroll
              for (int i = 0; i < 9; ++i) { xv[i] = *(const u32x4*)(xr + i * 128); gv[i] = *(const u32x4*)(gr + i * 128); } }
        } else {
            unsigned wo_[5], hi_[5];
            { int row = row0, ch = chk0;
#pragma unroll
              for (int i = 0; i < 5; ++i) { const int rw = row < 32 ? row : 31; wo_[i] = (rw & 15) * 1152 + ch * 16; hi_[i] = rw >> 4; row += 7; if (++ch == 9) { ch = 0; ++row; } } }
#pragma unroll
            for (int b = 0; b < 3; ++b) {
                const int blk = (b < nblk) ? u + 32 * b : u, st = blk >> 2, jb = blk & 3;
                const int fA = st < 16 ? st * 144 + (jb < 2 ? 32 * jb : 16 + 32 * jb) : ((st - 16) * 8 + 2 * jb) * 144 + 64, dB = (st < 16 ? 16 : 144) * 1152;
                const char* wb_ = (const char*)wout + (size_t)fA * 1152 + qn * 144;
#pragma unroll
                for (int i = 0; i < 5; ++i) wv[b][i] = *(const u32x4*)(wb_ + (wo_[i] + hi_[i] * dB));
            }
        }
        E = flag_ld(ws, WS_E);
        if (lane < 24) *(LAS u32x4*)(lds + L_KT + (lane >> 3) * KT_SZ + (2 * h + ((lane >> 2) & 1)) * 1088 + (lane & 3) * 16) = (u32x4){0u, 0u, 0u, 0u};
        WAITL0();
        STAMP(10);
        __builtin_amdgcn_s_setprio(GM_HPRIO);
        {
            float kv[2][3];
#pragma nounroll
            for (int ei = 0; ei < 9; ++ei) {
                BAR();
                if (ei > 0) {
                    LAS unsigned char* kt = opq(lds + L_KT + (2 * h + ((ei - 1) >> 2)) * 1088 + 64 + ((ei - 1) & 3) * 256 + lane * 2);
#pragma unroll
                    for (int j = 0; j < 2; ++j)
#pragma unroll
                        for (int c = 0; c < 3; ++c) *(LAS u16*)(kt + c * KT_SZ + j * 128) = (u16)pk_bf16(kv[j][c], 0.f);
                }
#pragma unroll
                for (int j = 0; j < 2; ++j) {
                    const int jj = (ei & 3) * 2 + j, e = jj >= 4 ? 3 : jj >= 2 ? 2 : jj, lag = 256 * jj + 4 * lane + 2 * h + ((ei >> 2) & 1);
                    const int idx = e == 0 ? lag : 128 * e + ((lag + (1 << (e - 1)) - 1 + ((lag >> e) & 1)) >> e);
                    const LAS float* q = opq((const LAS float*)(lds + L_KD) + idx);
#pragma unroll
                    for (int c = 0; c < 3; ++c) kv[j][c] = q[c * 768];
                }
            }
        }
        if (h == 0) {
            LAS unsigned char* pzs = opq(lds + Z0 + lane * 8);
#pragma unroll
            for (int i = 0; i < 18; ++i) *(LAS u32x2*)(pzs + i * 512) = uc[i];
            CFENCE();
            { const LAS unsigned char* pt = opq(lds + Z0 + (8 * (4 * fq + (lane & 3)) + (fr >> 2)) * 72);
              const unsigned ub = WS_UT + (unsigned)(pn * 36 + (lane & 3)) * 4096u + (unsigned)(pm * 128 + 8 * (4 * fq + (fr >> 2))) * 2u;
#pragma unroll
              for (int cc = 0; cc < 9; ++cc) __builtin_amdgcn_raw_buffer_store_b128(__builtin_bit_cast(u32x4, cat8(tr_read(pt + cc * 8), tr_read(pt + 288 + cc * 8))), ws, ub + cc * 16384u, 0, 16); }
            float a = 0.f;
#pragma unroll
            for (int i = 0; i < 9; ++i)
#pragma unroll
                for (int j = 0; j < 4; ++j) { const unsigned xa = xv[i][j], ga = gv[i][j]; a = __builtin_amdgcn_fdot2_f32_bf16(__builtin_bit_cast(bf16x2, xa), __builtin_bit_cast(bf16x2, ga), a, false); }
#define DPPF(v, ctl) __int_as_float(__builtin_amdgcn_update_dpp(0, __float_as_int(v), ctl, 0xf, 0xf, true))
            a += DPPF(a, 0xb1); a += DPPF(a, 0x4e); a += DPPF(a, 0x141);
#undef DPPF
            const float gt = rbf(__builtin_amdgcn_rcpf(1.0f + __expf(-rbf(a))));
            if ((lane & 7) == 0) __builtin_amdgcn_raw_buffer_store_b32(__float_as_uint(gt), ws, WS_GATE + (unsigned)(pm * 128 + pn * 8 + (lane >> 3)) * 4u, 0, 16);
        } else {
            LAS unsigned char* pws = opq(lds + Z0 + 9216 + lane * 16);
            const LAS unsigned char* ptw = opq(lds + Z0 + 9216 + (4 * fq + (fr >> 2)) * 144 + (lane & 3) * 8);
#pragma unroll
            for (int b = 0; b < 3; ++b) {
                if (b < nblk) {
#pragma unroll
                    for (int i = 0; i < 5; ++i) if (i < 4 || lane < 32) *(LAS u32x4*)(pws + i * 1024) = wv[b][i];
                    CFENCE();
                    const unsigned ob = WS_WOF + (unsigned)qn * (18u * 18432u) + (unsigned)(u + 32 * b) * 4608u;
#pragma unroll
                    for (int nn = 0; nn < 5; ++nn) {
                        const u32x4 v = __builtin_bit_cast(u32x4, cat8(tr_read(ptw + nn * 32), tr_read(ptw + 2304 + nn * 32)));
                        if (nn < 4) __builtin_amdgcn_raw_buffer_store_b128(v, ws, ob + nn * 1024 + lane * 16, 0, 16);
                        else if (fr < 8) __builtin_amdgcn_raw_buffer_store_b128(v, ws, ob + 4096 + (fq * 8 + fr) * 16, 0, 16);
                    }
                    CFENCE();
                }
            }
        }
        nb = 9;
        mk_tag();
        WAITL0();
        STEPS_TO(10);
        STAMP(1);
        {
            const f32x4 sp = *(const LAS f32x4*)(lds + L_SP + (h * 64 + lane) * 16);
            const float dm = sp[0] - sp[2];
            const f32x2 mine = {(80.f * sp[0] + 64.f * sp[2]) * (1.0f / 144.f), sp[1] + sp[3] + (80.f * 64.f / 144.f) * dm * dm};
            __builtin_amdgcn_raw_buffer_store_b128((u32x4){T[0], __float_as_uint(mine.x), T[1], __float_as_uint(mine.y)}, ws, WS_ST + (unsigned)((pm * 16 + pn) * 128 + h * 64 + lane) * 16u, 0, 16);
            STEP(18);
            const unsigned so_ = WS_ST + (unsigned)(pm * 16 * 128 + h * 64 + lane) * 16u;
            const u64 t0 = __builtin_amdgcn_s_memrealtime();
            u32x4 st[16];
            for (;;) {
                CFENCE();
#pragma unroll
                for (int j = 0; j < 16; ++j) st[j] = __builtin_amdgcn_raw_buffer_load_b128(ws, so_ + j * 2048u, 0, 16);
                STEP(18);
                unsigned bad = 0;
#pragma unroll
                for (int j = 0; j < 16; ++j) bad |= (st[j][0] ^ T[0]) | (st[j][2] ^ T[1]);
                if (__builtin_amdgcn_ballot_w64(bad != 0u) == 0ull) break;
                if (__builtin_amdgcn_s_memrealtime() - t0 > 300000ull) break;
            }
            STAMP(2);
            STEP(18);
            float mean = 0.f, m2 = 0.f;
#pragma unroll
            for (int j = 0; j < 16; ++j) mean += __uint_as_float(st[j][1]);
            mean *= (1.0f / 16.f);
#pragma unroll
            for (int j = 0; j < 16; ++j) { const float d = __uint_as_float(st[j][1]) - mean; m2 += __uint_as_float(st[j][3]) + 144.f * d * d; }
            const float rstd = __builtin_amdgcn_rsqf(m2 * (1.0f / FH) + 1e-5f);
            *(LAS f32x2*)(lds + L_SP + (h * 64 + lane) * 16) = (f32x2){rstd, -mean * rstd};
            WAITL0();
            if (lane == 0) *(volatile LAS unsigned*)(lds + L_MISC + 4 + h * 4) = 1u;
            const volatile LAS unsigned* zd = (const volatile LAS unsigned*)(lds + L_MISC + 16 + h * 8 + (lane & 1) * 4);
            for (;;) {
                STEP(18);
                if (__builtin_amdgcn_ballot_w64(*zd == 0u) == 0ull) break;
                if (__builtin_amdgcn_s_memrealtime() - t0 > 800000ull) break;
            }
            CFENCE();
            const LAS unsigned char* ptz = opq(lds + Z0 + (h * 64 + 8 * fq + (fr >> 2)) * ZST + (lane & 3) * 8);
            const unsigned zo = WS_Z2F + (unsigned)((pn * 64 + pm * 4 + h * 2) * 9) * 1024u + lane * 16;
#pragma unroll
            for (int i = 0; i < 18; ++i) {
                const LAS unsigned char* pt = ptz + (i / 9) * 32 * ZST + (i % 9) * 32;
                __builtin_amdgcn_raw_buffer_store_b128(__builtin_bit_cast(u32x4, cat8(tr_read(pt), tr_read(pt + 4 * ZST))), ws, zo + i * 1024, 0, 16);
                if (i == 5 || i == 11) STEP(18);
            }
            STEP(18);
            flag_publish(ws, WS_FZ + (unsigned)(pn * 32 + pm * 2 + h) * 128u, T, lane);
            STAMP(3);
            STEPS_TO(18);
            __builtin_amdgcn_s_setprio(0);
        }
    }

    if (is_prod) {
        mk_tag();
        const char* lin = (const char*)wsp_ + p * 1024 + lane * 16;
        auto issueLA = [&](unsigned off, int s) {
#pragma unroll
            for (int i = 0; i < 8; ++i) GLDS(lin + off + i * 2048, lds + s + (p + 2 * i) * 1024, 16); };
        auto issueLB = [&](unsigned off, int s) {
#pragma unroll
            for (int i = 0; i < 9; ++i) GLDS(lin + off + i * 2048, lds + s + BOFF + (p + 2 * i) * 1024, 16); };
        auto ord3 = [&](int s) { return (GM_LAG == 0 || s >= 16) ? s : (((s & 3) == phi) ? s : ((s - GM_LAG) & 15)); };
        u32x2 pv = {0u, 0u}; unsigned rdy = 0; bool pend = false;
        const char* fp = (const char*)wsp_ + WS_FZ + ((unsigned)pn * 32u + (lane & 31)) * 128u;
        auto poll_issue = [&]() { asm volatile("global_load_dwordx2 %0, %1, off sc1" : "+v"(pv) : "v"(fp) : "memory"); };
        auto poll_take = [&]() { asm volatile("" : "+v"(pv)); rdy |= (unsigned)__builtin_amdgcn_ballot_w64(pv[0] == T[0] && pv[1] == T[1]); };
        auto need = [&](unsigned bits) {
            if (pend) { poll_take(); pend = false; }
            if ((rdy & bits) != bits) {
                const u64 t0 = __builtin_amdgcn_s_memrealtime();
                do { poll_issue(); WAITV(0); poll_take(); } while ((rdy & bits) != bits && __builtin_amdgcn_s_memrealtime() - t0 < 300000ull);
            }
            if (rdy != ~0u) { poll_issue(); pend = true; }
        };
        const unsigned zb = WS_Z2F + (unsigned)pn * (64u * 9216u);
        unsigned wm_ = 0;
        if (GM_WARM & 1) {
            const char* wr = (const char*)wsp + (size_t)(pm * 128 + bj * 32 + p * 16 + (lane >> 2)) * 4096 + (lane & 3) * 64;
#pragma unroll
            for (int i = 0; i < GM_WN; ++i) asm volatile("global_load_dword %0, %1, off offset:%2" : "+v"(wm_) : "v"(wr), "n"(256 * i) : "memory");
        }
        need((1u << ord2(0)) | (1u << ord2(1)));
        STAMP(2);
        issueLB(zb + (unsigned)ord2(0) * 18432u, 0); issueLB(zb + (unsigned)ord2(1) * 18432u, STG);
        int so = 2 * STG;
        WAITV(9);
#pragma nounroll
        for (int S = 18; S < 48; ++S) {
            PBAR();
            const int k = ord2(S - 16); need(1u << k); issueS(k, so); issueLB(zb + (unsigned)k * 18432u, so); so = nextbuf(so);
            WAITV(17);
        }
        const unsigned hb = WS_HF + (unsigned)qm * (18u * 16384u), ob = WS_WOF + (unsigned)qn * (18u * 18432u);
        PBAR();
        fp = (const char*)wsp_ + WS_FZ + ((unsigned)(pn ^ 8) * 32u + (lane & 31)) * 128u; rdy = 0; pend = false;
        need(~0u);
        issueLB(ob + (unsigned)ord3(0) * 18432u, 2 * STG);
        PBAR();
        issueLB(ob + (unsigned)ord3(1) * 18432u, 0);
        if (GM_WARM & 2) {
            const char* wr = (const char*)wsp_ + ob + (unsigned)bi * 41472u + p * 20736u + lane * 64;
#pragma unroll
            for (int i = 0; i < 6; ++i) { const char* q_ = wr + (i < 5 ? i * 4096 : 20736 - 4096); asm volatile("global_load_dword %0, %1, off sc1" : "+v"(wm_) : "v"(q_) : "memory"); }
        }
        STAMP(3);
        fp = (const char*)wsp_ + WS_FH + ((unsigned)qm * 32u + (lane & 31)) * 128u; rdy = 0;
        need(~0u);
        STAMP(4);
        issueLA(hb + (unsigned)ord3(0) * 16384u, 2 * STG); issueLA(hb + (unsigned)ord3(1) * 16384u, 0);
        so = STG;
        WAITV(8);
#pragma nounroll
        for (int S = 50; S < 66; ++S) {
            PBAR();
            const int k = ord3(S - 48); issueLA(hb + (unsigned)k * 16384u, so); issueLB(ob + (unsigned)k * 18432u, so); so = nextbuf(so);
            WAITV(17);
        }
        PBAR();
        WAITV(0); PBAR();
        PBAR();
        asm volatile("" :: "v"(wm_));
        STAMP(5); PDUMP();
        if (bid == 0 && p == 0 && lane == 0) __builtin_amdgcn_raw_buffer_store_b64(T, ws, WS_E, 0, 16);
        return;
    }

    if (wid < 4) {
        mk_tag();
        if (wid >> 1) consumer_b<4>(lds, ws, T, zp, bs, pm, pn, wid & 1, 1, lane, wsp_ TRA);
        else consumer_b<5>(lds, ws, T, zp, bs, pm, pn, wid & 1, 0, lane, wsp_ TRA);
    } else {
        __builtin_amdgcn_s_setprio(GM_HPRIO);
        {   const unsigned fo = WS_FZ + (unsigned)(pn * 32 + (lane & 31)) * 128u;
            const u64 t0 = __builtin_amdgcn_s_memrealtime();
            for (;;) {
                STEP(44);
                CFENCE();
                const u32x2 v = flag_ld(ws, fo);
                STEP(44);
                if (flag_all(v, T, true)) break;
                if (__builtin_amdgcn_s_memrealtime() - t0 > 300000ull) break;
            } }
        u32x4 uu[3][2];
#pragma unroll
        for (int c = 0; c < 3; ++c)
#pragma unroll
            for (int j = 0; j < 2; ++j) uu[c][j] = __builtin_amdgcn_raw_buffer_load_b128(ws, WS_UT + (unsigned)(ch0 + (c < nch ? 16 * c : 0)) * 4096u + h * 2048 + j * 1024 + lane * 16, 0, 16);
#pragma unroll
        for (int c = 0; c < 3; ++c) if (lane < 16) *(LAS u32x4*)(lds + L_UT + c * UT_SZ + h * 4352 + lane * 16) = (u32x4){0u, 0u, 0u, 0u};
        BAR(); BAR(); BAR(); nb += 3;
        LAS unsigned char* pu_ = opq(lds + L_UT + 256 + h * 2048 + lane * 16);
#pragma unroll
        for (int c = 0; c < 3; ++c)
#pragma unroll
            for (int j = 0; j < 2; ++j) *(LAS u32x4*)(pu_ + c * UT_SZ + j * 1024) = uu[c][j];
        WAITL0();
        if (lane == 0) *(volatile LAS unsigned*)(lds + L_MISC + 32 + h * 4) = 1u;
        {   const volatile LAS unsigned* uw = (const volatile LAS unsigned*)(lds + L_MISC + 32 + (lane & 1) * 4);
            const u64 t0 = __builtin_amdgcn_s_memrealtime();
            do { STEP(50); } while (__builtin_amdgcn_ballot_w64(*uw == 0u) != 0ull && __builtin_amdgcn_s_memrealtime() - t0 < 800000ull);
            CFENCE(); }
        STAMP(4);
    }

    if (wid < 4) nb = 50;
    for (;;) {
        if (nb > 45) STEPS_TO(50);
        unsigned tk = 0;
        if (lane == 0) tk = __hip_atomic_fetch_add((LAS unsigned*)(lds + L_MISC), 1u, __ATOMIC_RELAXED, __HIP_MEMORY_SCOPE_WORKGROUP);
        tk = __builtin_amdgcn_readfirstlane(tk);
        if (tk >= (unsigned)(4 * nch)) break;
        const int c = tk >> 2, tl = tk & 1;
        if (tk & 2) conv_task<0, 10>(lds, L_KT + c * KT_SZ, L_UT + c * UT_SZ, ytab(c), tl, lane, nb TRA);
        else conv_task<11, 15>(lds, L_KT + c * KT_SZ, L_UT + c * UT_SZ, ytab(c), tl, lane, nb TRA);
    }
    STEPS_TO(50);
    WAITL0();
    STAMP(7);

    if (wid < 4) {
        const int kq = wid >> 1, mh = wid & 1;
        f32x4 acc[5][2];
        struct F3 { s16x4 alo[4], ahi[4]; bf16x8 b[10]; } f0, f1;
        const LAS unsigned char* ph = lds + 2 * STG + mh * 8192 + kq * 2048 + lane * 16;
        const LAS unsigned char* pw = lds + 2 * STG + BOFF + kq * 9216 + lane * 16;
        const LAS unsigned char* pw4 = lds + 2 * STG + BOFF + kq * 9216 + 4096 + (fq * 8 + (fr & 7)) * 16;
        auto rd3 = [&](F3& f, int i, bool single) {
            const int j = i / 7, r = i % 7;
            if (r < 2) {
                if (!single) { const bf16x8 v = *(const LAS bf16x8*)(ph + r * 4096 + j * 1024); f.alo[j * 2 + r] = __builtin_shufflevector(v, v, 0, 1, 2, 3); f.ahi[j * 2 + r] = __builtin_shufflevector(v, v, 4, 5, 6, 7); }
                else { const LAS unsigned char* q = ph - lane * 8 + r * 4096 + j * 1024; f.alo[j * 2 + r] = *(const LAS s16x4*)q; f.ahi[j * 2 + r] = *(const LAS s16x4*)(q + 512); }
            } else if (r < 6) f.b[j * 5 + r - 2] = *(const LAS bf16x8*)(pw + j * 4608 + (r - 2) * 1024);
            else f.b[j * 5 + 4] = *(const LAS bf16x8*)(pw4 + j * 4608);
        };
        int so = 2 * STG;
        auto step3 = [&](const F3& c, F3& l, int mode  , bool first) {
            const int delta = so == 2 * STG ? -2 * STG : STG; so += delta;
#pragma unroll
            for (int i = 0; i < 20; ++i) {
                const int j = i / 10, n = (i % 10) >> 1, m = i & 1;
                acc[n][m] = MFMA16(c.b[j * 5 + n], cat8(c.alo[j * 2 + m], c.ahi[j * 2 + m]), (first && j == 0) ? ((f32x4){0.f, 0.f, 0.f, 0.f}) : acc[n][m]);
                if (mode < 2 && i < 7) { rd3(l, 2 * i, mode == 1); rd3(l, 2 * i + 1, mode == 1); }
                if (mode < 2) { if (i == 8) { ph += delta; asm volatile("" : "+v"(ph)); } if (i == 9) { pw += delta; asm volatile("" : "+v"(pw)); } if (i == 10) { pw4 += delta; asm volatile("" : "+v"(pw4)); } }
                SB0();
            }
        };
        __builtin_amdgcn_s_waitcnt(0xC07F);
        BAR();
#pragma unroll
        for (int i = 0; i < 14; ++i) rd3(f0, i, false);
        ph -= 2 * STG; pw -= 2 * STG; pw4 -= 2 * STG; so = 0;
        WAITL0(); BAR(); step3(f0, f1, 0, true);
#pragma nounroll
        for (int j = 0; j < 7; ++j) {
            WAITL0(); BAR(); step3(f1, f0, 0, false);
            WAITL0(); BAR(); step3(f0, f1, 0, false);
        }
        WAITL0(); BAR(); step3(f1, f0, 1, false);
        WAITL0(); BAR(); step3(f0, f1, 1, false);
        step3(f1, f0, 2, false);
        STAMP(8);
        LAS unsigned char* prw = opq(lds + L_R + wid * 5120 + lane * 16);
#pragma unroll
        for (int n = 0; n < 5; ++n) { const f32x4 x0 = acc[n][0], x1 = acc[n][1]; *(LAS f32x4*)(prw + n * 1024) = kq ? x0 : x1; acc[n][0] = kq ? x1 : x0; }
        WAITL0();
        BAR();
        const int m = mh * 2 + kq;
        const rsrc_t ors = __builtin_amdgcn_make_buffer_rsrc(out, 0, SEQ * DM * 2, 0x00020000);
        const float g = *(const LAS float*)(lds + L_MISC + 64 + (16 * m + fr) * 4), g1 = rbf(1.0f - g);
        const LAS unsigned char* prr = opq(lds + L_R + (wid ^ 2) * 5120 + lane * 16);
        const LAS unsigned char* pat = opq(lds + L_AT + (4 * fq + (fr >> 2)) * 128 + (16 * m + 4 * (lane & 3)) * 2);
#pragma unroll
        for (int n = 0; n < 5; ++n) {
            const f32x4 b = acc[n][0] + *(const LAS f32x4*)(prr + n * 1024);
            const u32x2 a = __builtin_bit_cast(u32x2, tr_read(pat + n * 2048));
            const u32x2 o = {pk_bf16(g * bflo(a[0]) + g1 * b[0], g * bfhi(a[0]) + g1 * b[1]), pk_bf16(g * bflo(a[1]) + g1 * b[2], g * bfhi(a[1]) + g1 * b[3])};
            if (n < 4 || fq < 2) __builtin_amdgcn_raw_buffer_store_b64(o, ors, (unsigned)((qm * 64 + 16 * m + fr) * 1152 + (qn * 72 + 16 * n + 4 * fq) * 2), 0, GM_OAUX);
        }
    } else {
        int d = pn * 36 + pm;
#pragma nounroll
        for (int c = 0; c < nch; ++c) {
            const float dsk = bf2f(Dsk[d]);
#pragma unroll
            for (int k = 0; k < 2; ++k) {
                BAR(); ++nb;
                const int l0 = h * 1024 + k * 512 + lane * 8;
                f32x4 cv = *(const LAS f32x4*)(lds + ytab(c) + 8192 + (l0 >= 16 ? (l0 >> 4) - 1 : 0) * 16);
                if ((lane & 1) || l0 == 0) cv = (f32x4){0.f, 0.f, 0.f, 0.f};
                const f32x4 y0 = *(const LAS f32x4*)(lds + ytab(c) + l0 * 4) + cv, y1 = *(const LAS f32x4*)(lds + ytab(c) + l0 * 4 + 16);
                const u32x4 uv = *(const LAS u32x4*)(lds + L_UT + c * UT_SZ + 256 + l0 * 2);
                u32x4 o;
#pragma unroll
                for (int j = 0; j < 4; ++j) {
                    const float ya = j < 2 ? y0[2 * j] : y1[2 * j - 4], yb = j < 2 ? y0[2 * j + 1] : y1[2 * j - 3];
                    const unsigned yp = pk_bf16(ya, yb), up = pk_bf16(bflo(uv[j]) * dsk, bfhi(uv[j]) * dsk);
                    o[j] = pk_bf16(bflo(yp) + bflo(up), bfhi(yp) + bfhi(up));
                }
                __builtin_amdgcn_raw_buffer_store_b128(o, ws, WS_AT + (unsigned)d * 4096u + l0 * 2, 0, 16);
            }
            d += 16;
        }
        STEPS_TO(58);
        flag_publish(ws, WS_FA + (unsigned)((pn >> 1) * 64 + (pn & 1) * 32 + pm * 2 + h) * 128u, T, lane);
        STAMP(5);
        STEPS_TO(62);
        if (h == 0) {
            const unsigned fo = WS_FA + (unsigned)(qn * 64 + lane) * 128u;
            const u64 t0 = __builtin_amdgcn_s_memrealtime();
            for (;;) {
                CFENCE();
                const u32x2 v = flag_ld(ws, fo);
                STEP(68); STEP(68);
                if (flag_all(v, T, true)) break;
                if (__builtin_amdgcn_s_memrealtime() - t0 > 300000ull) break;
            }
            ACQ();
            u32x4 av[9];
#pragma unroll
            for (int i = 0; i < 9; ++i) av[i] = __builtin_amdgcn_raw_buffer_load_b128(ws, WS_AT + (unsigned)(qn * 72 + i * 8 + (lane >> 3)) * 4096u + qm * 128 + (lane & 7) * 16, 0, 16);
            STEP(68); STEP(68);
            LAS unsigned char* pa_ = opq(lds + L_AT + lane * 16);
#pragma unroll
            for (int i = 0; i < 9; ++i) *(LAS u32x4*)(pa_ + i * 1024) = av[i];
        } else {
            STEPS_TO(65);
            const unsigned gv = __builtin_amdgcn_raw_buffer_load_b32(ws, WS_GATE + (unsigned)(qm * 64 + lane) * 4u, 0, 16);
            BAR(); BAR(); nb += 2;
            *(LAS unsigned*)(lds + L_MISC + 64 + lane * 4) = gv;
        }
        STEPS_TO(68);
        WAITL0();
        BAR();
    }
    STAMP(9); TRDUMP();
}
}

extern "C" void kernel_launch(void* const* d_in, const int* in_sizes, int n_in, void* d_out, int out_size, void* d_ws, size_t ws_size, hipStream_t stream) {
    (void)in_sizes; (void)n_in; (void)out_size; (void)ws_size;
    (void)hipFuncSetAttribute((const void*)gm::gm_kernel, hipFuncAttributeMaxDynamicSharedMemorySize, gm::LDS_BYTES);
    hipLaunchKernelGGL(gm::gm_kernel, dim3(256), dim3(512), gm::LDS_BYTES, stream, (const u16*)d_in[0], (const u16*)d_in[1], (const u16*)d_in[2], (const u16*)d_in[3],
                       (const u16*)d_in[4], (const u16*)d_in[5], (const u16*)d_in[6], (const u16*)d_in[8], (const u16*)d_in[9], (const u16*)d_in[10],
                       (const u16*)d_in[11], (const u16*)d_in[12], (u16*)d_out, (unsigned char*)d_ws);
}
```

```cpp
#include <hip/hip_runtime.h>
#include <stdint.h>
#include <type_traits>

#define LAS __attribute__((address_space(3)))
typedef unsigned short u16;
typedef unsigned long long u64;
typedef short bf16x8 __attribute__((ext_vector_type(8)));
typedef short s16x4 __attribute__((ext_vector_type(4)));
typedef _Float16 f16x8 __attribute__((ext_vector_type(8)));
typedef _Float16 f16x2 __attribute__((ext_vector_type(2)));
typedef __bf16 bf16x2 __attribute__((ext_vector_type(2)));
typedef float f32x4 __attribute__((ext_vector_type(4)));
typedef float f32x2 __attribute__((ext_vector_type(2)));
typedef unsigned u32x4 __attribute__((ext_vector_type(4)));
typedef unsigned u32x2 __attribute__((ext_vector_type(2)));
typedef __amdgpu_buffer_rsrc_t rsrc_t;

namespace gm {
constexpr int SEQ = 2048, DM = 576, FH = 2304;
constexpr unsigned WS_E = 0, WS_FS = 4096, WS_FZ = WS_FS + 65536, WS_FH = WS_FZ + 65536, WS_FA = WS_FH + 131072;
constexpr unsigned WS_ST = 0x60000, WS_GATE = 0xE0000, WS_DBG = 0x1E00000, WS_Z2F = 0x100000, WS_HF = 0xA00000, WS_WOF = 0x1300000, WS_UT = 0x1600000, WS_AT = 0x1900000, WS_END = 0x1C00000, WS_TRC = 0x1C00000;
constexpr int STG = 34816, BOFF = 16384, Z0 = 3 * STG, ZST = 296, T0 = Z0 + 128 * ZST;
constexpr int KT_SZ = 4352, UT_SZ = 4608, Y_SZ = 10240;
constexpr int L_MISC = 163840 - 320;
constexpr int L_KT = L_MISC - 3 * KT_SZ, L_SP = T0, L_KD = Z0 + 3 * 8448, L_UT = Z0 + 3 * Y_SZ;
constexpr int L_AT = Z0, L_R = Z0 + 10240;
constexpr int LDS_BYTES = 163840;
static_assert(L_UT + 3 * UT_SZ <= L_KT && L_KD + 3 * 3072 <= T0 && L_SP + 2048 <= L_KT, "lds map");
#define GM_HPRIO 1
#define GM_OAUX 0
#define GM_PFL 13
#define GM_CODE 49792
#define GM_ORD 0
#define GM_PW 3
#define GM_LAG 4

__device__ __forceinline__ float bf2f(unsigned v) { return __uint_as_float(v << 16); }
__device__ __forceinline__ float bflo(unsigned w) { return __uint_as_float(w << 16); }
__device__ __forceinline__ float bfhi(unsigned w) { return __uint_as_float(w & 0xffff0000u); }
__device__ __forceinline__ unsigned pk_bf16(float a, float b) { f32x2 v = {a, b}; return __builtin_bit_cast(unsigned, __builtin_convertvector(v, bf16x2)); }
__device__ __forceinline__ unsigned pk_f16(float a, float b) { f32x2 v = {a, b}; return __builtin_bit_cast(unsigned, __builtin_convertvector(v, f16x2)); }
__device__ __forceinline__ float rbf(float a) { return bflo(pk_bf16(a, a)); }
__device__ __forceinline__ f32x2 gelu2(f32x2 x) {
    const f32x2 w = (x * x) * (-0.10294324f) + (-2.30220819f);
    const f32x2 a = w * x;
    f32x2 e; e.x = __builtin_amdgcn_exp2f(a.x); e.y = __builtin_amdgcn_exp2f(a.y);
    e = e + 1.0f;
    f32x2 r; r.x = __builtin_amdgcn_rcpf(e.x); r.y = __builtin_amdgcn_rcpf(e.y);
    return x * r;
}
__device__ __forceinline__ f32x4 gelu4(f32x4 v) { const f32x2 a = gelu2((f32x2){v[0], v[1]}), b = gelu2((f32x2){v[2], v[3]}); return (f32x4){a.x, a.y, b.x, b.y}; }
__device__ __forceinline__ float allred_q(float v) {
    unsigned u = __float_as_uint(v);
    auto r = __builtin_amdgcn_permlane16_swap(u, u, false, false);
    v = __uint_as_float(r[0]) + __uint_as_float(r[1]);
    u = __float_as_uint(v);
    auto s = __builtin_amdgcn_permlane32_swap(u, u, false, false);
    return __uint_as_float(s[0]) + __uint_as_float(s[1]);
}
#define ACQ() __builtin_amdgcn_fence(__ATOMIC_ACQUIRE, "wavefront")
#define TRP
#define TRA
#define TRDUMP() do { } while (0)
#define BAR() __builtin_amdgcn_s_barrier()
#define PBAR() BAR()
#define PDUMP() TRDUMP()
#define WAITV(n) asm volatile("s_waitcnt vmcnt(" #n ")" ::: "memory")
#define WAITL0() asm volatile("s_waitcnt lgkmcnt(0)" ::: "memory")
#define CFENCE() asm volatile("" ::: "memory")
#define SGB(mask, n, id) __builtin_amdgcn_sched_group_barrier(mask, n, id)
#define MFMA16(a, b, c) __builtin_amdgcn_mfma_f32_16x16x32_bf16(a, b, c, 0, 0, 0)
__device__ __forceinline__ s16x4 tr_read(const LAS unsigned char* p) { return __builtin_amdgcn_ds_read_tr16_b64_v4i16((LAS s16x4*)p); }
__device__ __forceinline__ bf16x8 cat8(s16x4 a, s16x4 b) { return __builtin_shufflevector(a, b, 0, 1, 2, 3, 4, 5, 6, 7); }
template <class P> __device__ __forceinline__ P opq(P p) { asm volatile("" : "+v"(p)); return p; }
__device__ __forceinline__ int nextbuf(int so) { return so == 2 * STG ? 0 : so + STG; }

#define STAMP(i) do { } while (0)

__device__ __forceinline__ u32x2 flag_ld(rsrc_t ws, unsigned off) { return __builtin_amdgcn_raw_buffer_load_b64(ws, off, 0, 16); }
__device__ __forceinline__ bool flag_all(u32x2 v, u32x2 T, bool active) { return __builtin_amdgcn_ballot_w64(active && (v[0] != T[0] || v[1] != T[1])) == 0ull; }
__device__ __forceinline__ void flag_publish(rsrc_t ws, unsigned off, u32x2 T, int lane) {
    WAITV(0);
    if (lane == 0) __builtin_amdgcn_raw_buffer_store_b64(T, ws, off, 0, 16);
}
__device__ __forceinline__ void flag_wait(rsrc_t ws, unsigned base, int n, u32x2 T, int lane) {
    const unsigned off = base + (unsigned)(lane < n ? lane : 0) * 128u;
    const u64 t0 = __builtin_amdgcn_s_memrealtime();
    for (;;) {
        CFENCE();
        const u32x2 v = flag_ld(ws, off);
        if (flag_all(v, T, true)) break;
        if (__builtin_amdgcn_s_memrealtime() - t0 > 300000ull) break;
        __builtin_amdgcn_s_sleep(1);
    }
    ACQ();
}

#define SB0() __builtin_amdgcn_sched_barrier(0)
template <int NT> struct Frag { bf16x8 a[4]; s16x4 lo[NT], hi[NT]; };
template <int NT, bool TR, int HALF>
__device__ __forceinline__ void frag_read(Frag<NT>& f, int i, const LAS unsigned char* pa, const LAS unsigned char* pb) {
    if (GM_ORD) { constexpr int NB = TR ? 2 * NT : NT; i = i == 0 ? 0 : i <= NB ? i + 3 : i - NB; }
    if (i < 4) f.a[i] = *(const LAS bf16x8*)(pa + i * 2048);
    else if (TR) { const int n = (i - 4) >> 1; if ((i - 4) & 1) f.hi[n] = tr_read(pb + HALF * 9216 + 4608 + n * 32); else f.lo[n] = tr_read(pb + HALF * 9216 + n * 32); }
    else { const int n = i - 4; const bf16x8 v = *(const LAS bf16x8*)(pb + HALF * 9216 + n * 1024);
           f.lo[n] = __builtin_shufflevector(v, v, 0, 1, 2, 3); f.hi[n] = __builtin_shufflevector(v, v, 4, 5, 6, 7); }
}
template <int NT, bool TR, int LHALF, int RPS, bool FIRST, bool UPD>
__device__ __forceinline__ void half_step(f32x4 (&acc)[NT][4], const Frag<NT>& c, Frag<NT>& l, const LAS unsigned char*& pa0, const LAS unsigned char*& pa1, const LAS unsigned char*& pb, int delta, const float* ini = nullptr) {
    constexpr int NRD = TR ? 4 + 2 * NT : 4 + NT, NM = 4 * NT, LAST = (NRD + RPS - 1) / RPS;
#pragma unroll
    for (int i = 0; i < NM; ++i) {
        const int n = GM_ORD ? i % NT : i >> 2, m = GM_ORD ? i / NT : i & 3;
        acc[n][m] = MFMA16(cat8(c.lo[n], c.hi[n]), c.a[m], FIRST ? (TR ? ((f32x4){0.f, 0.f, 0.f, 0.f}) : ((f32x4){ini[m], ini[m], ini[m], ini[m]})) : acc[n][m]);
        if (LHALF >= 0) {
#pragma unroll
            for (int r = 0; r < RPS; ++r) if (i * RPS + r < NRD) frag_read<NT, TR, (LHALF > 0 ? 1 : 0)>(l, i * RPS + r, LHALF > 0 ? pa1 : pa0, pb);
        }
        if (UPD) { if (i == LAST) { pa0 += delta; asm volatile("" : "+v"(pa0)); } if (i == LAST + 1) { pb += delta; asm volatile("" : "+v"(pb)); } if (i == LAST + 2) { pa1 += delta; asm volatile("" : "+v"(pa1)); } }
        SB0();
    }
}
template <int NT, bool TR, class HK>
__device__ __forceinline__ void gemm_phase(int nsteps, f32x4 (&acc)[NT][4], int& so, const LAS unsigned char* pa0, const LAS unsigned char* pa1, const LAS unsigned char* pb,
                                           const float* ini, const volatile LAS unsigned* rdy, HK hook TRP) {
    constexpr int NRD = TR ? 4 + 2 * NT : 4 + NT;
    Frag<NT> f0, f1;
    pa0 += so; pa1 += so; pb += so;
    asm volatile("" : "+v"(pa0), "+v"(pa1), "+v"(pb));
    __builtin_amdgcn_s_waitcnt(0xC07F);
    BAR();
#pragma unroll
    for (int i = 0; i < NRD; ++i) frag_read<NT, TR, 0>(f0, i, pa0, pb);
    SB0();
    int delta = so == 2 * STG ? -2 * STG : STG; so += delta;
    half_step<NT, TR, 1, 2, true, true>(acc, f0, f1, pa0, pa1, pb, delta, ini);
#pragma nounroll
    for (int s = 1; s < nsteps; ++s) {
        WAITL0();
        BAR();
        unsigned rv = 0;
        if (TR) { rv = *rdy; SB0(); }
        delta = so == 2 * STG ? -2 * STG : STG; so += delta;
        half_step<NT, TR, 0, 1, false, false>(acc, f1, f0, pa0, pa1, pb, 0);
        half_step<NT, TR, 1, 2, false, true>(acc, f0, f1, pa0, pa1, pb, delta);
        if (TR) hook(rv);
    }
    half_step<NT, TR, -1, 1, false, false>(acc, f1, f0, pa0, pa1, pb, 0);
}

template <int NT, class LT>
__device__ __forceinline__ void consumer_a(LAS unsigned char* lds, u32x2 (&zp)[5][4], LT late, int wm, int wn, int lane, unsigned char* wsp_ TRP) {
    const int fr = lane & 15, fq = lane >> 4;
    constexpr int N0 = (NT == 5) ? 0 : 5;
    f32x4 acc[NT][4];
    int so = 0;
    const int sw = (fr >> 1) & 7;
    const int a0 = (wm * 64 + fr) * 128 + ((fq ^ sw) << 4), a1 = (wm * 64 + fr) * 128 + (((4 + fq) ^ sw) << 4);
    const int bw = BOFF + (4 * fq + (fr >> 2)) * 288 + (lane & 3) * 8 + N0 * 32;
    const volatile LAS unsigned* rdy = (const volatile LAS unsigned*)(lds + L_MISC + 4 + wm * 4);
    bool done = false;
    auto normalise = [&]() {
        const LAS unsigned char* ps = opq(lds + L_SP + (wm * 64 + fr) * 16);
        LAS unsigned char* pz = opq(lds + Z0 + (wm * 64 + fr) * ZST + N0 * 32 + fq * 8);
#pragma unroll
        for (int m = 0; m < 4; ++m) {
            const f32x2 sr = *(const LAS f32x2*)(ps + m * 256);
#pragma unroll
            for (int n = 0; n < NT; ++n) {
                const f32x2 u = (f32x2){bflo(zp[n][m][0]), bfhi(zp[n][m][0])} * sr.x + sr.y, v = (f32x2){bflo(zp[n][m][1]), bfhi(zp[n][m][1])} * sr.x + sr.y;
                *(LAS u32x2*)(pz + m * 16 * ZST + n * 32) = (u32x2){pk_bf16(u.x, u.y), pk_bf16(v.x, v.y)};
            }
        }
        WAITL0();
        if (lane == 0) *(volatile LAS unsigned*)(lds + L_MISC + 16 + (wm * 2 + wn) * 4) = 1u;
        done = true;
    };
    auto hook = [&](unsigned rv) { if (__builtin_amdgcn_readfirstlane(rv) != 0u && !done) normalise(); };
#pragma unroll
    for (int n = 0; n < NT; ++n)
#pragma unroll
        for (int m = 0; m < 4; ++m) zp[n][m] = (u32x2){0u, 0u};
#pragma nounroll
    for (int it = 0; it < 2; ++it) {
        gemm_phase<NT, true>(9, acc, so, lds + a0, lds + a1, lds + bw, nullptr, rdy, hook TRA);
        STAMP(1 + 2 * it);
        late();
        if (it == 1 && !done) {
            const u64 t0 = __builtin_amdgcn_s_memrealtime();
            while (__builtin_amdgcn_readfirstlane(*rdy) == 0u && __builtin_amdgcn_s_memrealtime() - t0 < 400000ull) __builtin_amdgcn_s_sleep(1);
            normalise();
        }
#pragma unroll
        for (int m = 0; m < 4; ++m)
#pragma unroll
            for (int n = 0; n < NT; ++n) { acc[n][m] = gelu4(acc[n][m]); zp[n][m] = (u32x2){pk_bf16(acc[n][m][0], acc[n][m][1]), pk_bf16(acc[n][m][2], acc[n][m][3])}; }
        if (it == 0) {
            LAS unsigned char* ps = opq(lds + L_SP + (wm * 64 + fr) * 16 + wn * 8);
#pragma unroll
            for (int m = 0; m < 4; ++m) {
                f32x4 s4 = acc[0][m];
#pragma unroll
                for (int n = 1; n < NT; ++n) s4 += acc[n][m];
                const float mean = opq(allred_q((s4[0] + s4[1]) + (s4[2] + s4[3])) * (1.0f / (16 * NT)));
                f32x4 q4 = {0.f, 0.f, 0.f, 0.f};
#pragma unroll
                for (int n = 0; n < NT; ++n) { const f32x4 d = acc[n][m] - mean; q4 += d * d; }
                const float m2 = allred_q((q4[0] + q4[1]) + (q4[2] + q4[3]));
                if (fq == 0) *(LAS f32x2*)(ps + m * 256) = (f32x2){mean, m2};
            }
            STAMP(2);
        }
    }
    STAMP(4);
}

template <int NT>
__device__ __forceinline__ void consumer_b(LAS unsigned char* lds, rsrc_t ws, u32x2 T, const u32x2 (&z1)[5][4], const float (&bs_)[4], int pm, int pn, int wm, int wn, int lane, unsigned char* wsp_ TRP) {
    float bs[4] = {bf2f(__float_as_uint(opq(bs_[0]))), bf2f(__float_as_uint(opq(bs_[1]))), bf2f(__float_as_uint(opq(bs_[2]))), bf2f(__float_as_uint(opq(bs_[3])))};
    const int fr = lane & 15, fq = lane >> 4;
    constexpr int N0 = (NT == 5) ? 0 : 5;
    f32x4 acc[NT][4];
    int so = 0;
    const int sw = (fr >> 1) & 7;
    const int a0 = (wm * 64 + fr) * 128 + ((fq ^ sw) << 4), a1 = (wm * 64 + fr) * 128 + (((4 + fq) ^ sw) << 4);
    const int bz = BOFF + N0 * 1024 + lane * 16;
    gemm_phase<NT, false>(32, acc, so, lds + a0, lds + a1, lds + bz, bs, nullptr, [](unsigned) {} TRA);
    STAMP(5);
    __builtin_amdgcn_s_setprio(3);
    const int qm = 2 * pm + wm;
    const unsigned hb = WS_HF + (unsigned)qm * (18u * 16384u);
#pragma unroll
    for (int m = 0; m < 4; ++m) {
        u32x2 hp[NT];
#pragma unroll
        for (int n = 0; n < NT; ++n) {
            const f32x2 u = (f32x2){bflo(z1[n][m][0]), bfhi(z1[n][m][0])} * (f32x2){acc[n][m][0], acc[n][m][1]}, v = (f32x2){bflo(z1[n][m][1]), bfhi(z1[n][m][1])} * (f32x2){acc[n][m][2], acc[n][m][3]};
            hp[n] = (u32x2){pk_bf16(u.x, u.y), pk_bf16(v.x, v.y)};
        }
#pragma unroll
        for (int j = 0; j < 2; ++j)
            __builtin_amdgcn_raw_buffer_store_b128((u32x4){hp[2 * j][0], hp[2 * j][1], hp[2 * j + 1][0], hp[2 * j + 1][1]}, ws,
                                                   hb + (unsigned)pn * 16384u + (unsigned)(m * 4 + wn * 2 + j) * 1024u + lane * 16, 0, 16);
        if (NT == 5) __builtin_amdgcn_raw_buffer_store_b64(hp[NT - 1], ws, hb + (unsigned)(16 + (pn >> 3)) * 16384u + m * 4096u + (unsigned)(pn & 7) * 512u + lane * 8, 0, 16);
    }
    flag_publish(ws, WS_FH + (unsigned)(qm * 32 + pn * 2 + wn) * 128u, T, lane);
    __builtin_amdgcn_s_setprio(0);
    STAMP(6);
}

template <int TLO, int THI>
__device__ __forceinline__ void conv_task(LAS unsigned char* lds, int kt, int ut, int yt, int tl, int lane, int& nb TRP) {
#define CSL() do { if (nb < 50) { BAR(); ++nb; } } while (0)
    constexpr int NTL = THI - TLO + 1, ND = THI + 2;
    const int i = lane & 15, g = lane >> 4;
    const LAS unsigned char* pk = opq(lds + kt + (i & 3) * 1088 + ((i >> 2) + g) * 16);
    const LAS unsigned char* pu = opq(lds + ut + (128 + 16 * tl - 32 * g - 4 * (i >> 2) + 4 * (i & 3)) * 2 - 32);
    bf16x8 ub[ND];
#pragma unroll
    for (int d = 0; d < ND; ++d) { if (d == 0 || d == 8) CSL(); ub[d] = cat8(tr_read(pu + 256 * d + 32), tr_read(pu + 256 * d)); }
    f32x4 acc[NTL];
#pragma unroll
    for (int t = 0; t < NTL; ++t) acc[t] = (f32x4){0.f, 0.f, 0.f, 0.f};
#pragma unroll
    for (int s = -1; s <= THI; ++s) {
        if (TLO == 0 ? (s <= 3 || s == 5 || s == 7) : (s & 1) != 0 && s < 12) CSL();
        const bf16x8 kf = *(const LAS bf16x8*)(pk + 64 * (s + 1));
#pragma unroll
        for (int t = TLO; t <= THI; ++t) if (t >= s) acc[t - TLO] = MFMA16(kf, ub[t - s], acc[t - TLO]);
    }
    LAS float* y = opq((LAS float*)(lds + yt) + 32 * g + 16 * tl + i);
    LAS float* cy = opq((LAS float*)(lds + yt + 8192) + (2 * g + tl) * 4 + (i & 3));
#define DPPF(v, ctl) __int_as_float(__builtin_amdgcn_update_dpp(0, __float_as_int(v), ctl, 0xf, 0xf, true))
#pragma unroll
    for (int t = TLO; t <= THI; ++t) {
        if (((t - TLO) % 2) == 0) CSL();
        const f32x4 a = acc[t - TLO];
        y[128 * t] = (a[0] + DPPF(a[1], 0x111)) + (DPPF(a[2], 0x112) + DPPF(a[3], 0x113));
        const float cv = DPPF(a[1], 0x10f) + (DPPF(a[2], 0x10e) + DPPF(a[3], 0x10d));
        if (i < 4) cy[32 * t] = cv;
    }
#undef DPPF
#undef CSL
}
__device__ __forceinline__ int ytab(int c) { return Z0 + c * Y_SZ; }

__global__ __launch_bounds__(512, 2) void gm_kernel(const u16* __restrict__ x, const u16* __restrict__ logA, const u16* __restrict__ Aim, const u16* __restrict__ Cre,
                                                    const u16* __restrict__ Cim, const u16* __restrict__ logdt, const u16* __restrict__ Dsk, const u16* __restrict__ win,
                                                    const u16* __restrict__ wsp, const u16* __restrict__ bsp, const u16* __restrict__ wout, const u16* __restrict__ gw,
                                                    u16* __restrict__ out, unsigned char* wsp_) {
    extern __shared__ __attribute__((aligned(16))) unsigned char lds_[];
    LAS unsigned char* lds = (LAS unsigned char*)lds_;
    const int wid = __builtin_amdgcn_readfirstlane(threadIdx.x >> 6), lane = threadIdx.x & 63;
    const int bid = blockIdx.x, xg = bid & 7, rr = bid >> 3, bi = rr >> 2, bj = rr & 3;
    const int pm = (xg >> 2) * 8 + bi, pn = (xg & 3) * 4 + bj;
    const int qm = 2 * pm + (pn >> 3), qn = pn & 7;
    const int nch = pm < 4 ? 3 : 2;
    const rsrc_t ws = __builtin_amdgcn_make_buffer_rsrc(wsp_, 0, WS_END, 0x00020000);
    STAMP(0);
    u64 pc0; asm volatile("s_getpc_b64 %0" : "=s"(pc0));

    const int fr = lane & 15, fq = lane >> 4;
    const int h = wid - 6, p = wid - 4;
    const bool is_prod = (wid >> 1) == 2;
    const int ch0 = pn * 36 + pm;
    float bs[4];
    auto late = [&]() {
#pragma unroll
        for (int m = 0; m < 4; ++m) bs[m] = __uint_as_float((unsigned)bsp[pm * 128 + (wid & 1) * 64 + m * 16 + fr]);
    };
    const int phi = (-(bi + bj)) & 3;
    const unsigned rsw = (unsigned)(((lane & 7) ^ ((4 * p + (lane >> 4)) & 7)) * 16);
#define GLDS(src, dst, aux) __builtin_amdgcn_global_load_lds((const unsigned*)(src), (LAS unsigned*)(dst), 16, 0, aux)
    auto issueS = [&](int k, int s) {
        const char* sb = (const char*)wsp + (size_t)(pm * 128 + p * 8) * 4096 + k * 128; const unsigned vs = (unsigned)(lane >> 3) * 4096u + rsw;
#pragma unroll
        for (int i = 0; i < 8; ++i) GLDS(sb + (size_t)i * (16 * 4096) + vs, lds + s + (p + 2 * i) * 1024, 0); };
    auto ord2 = [&](int s) { return GM_LAG == 0 ? s : (((s & 3) == phi) ? s : ((s - GM_LAG) & 31)); };
    u32x2 E, T;
    auto mk_tag = [&]() { u64 z = (((u64)__builtin_amdgcn_readfirstlane(E[1]) << 32) | __builtin_amdgcn_readfirstlane(E[0])) + 0x9E3779B97F4A7C15ull;
        z = (z ^ (z >> 30)) * 0xBF58476D1CE4E5B9ull; z = (z ^ (z >> 27)) * 0x94D049BB133111EBull; z ^= z >> 31; T = (u32x2){(unsigned)z, (unsigned)(z >> 32)}; };
    u32x2 zp[5][4];
#define STEP(lim) do { if (nb < (lim)) { BAR(); ++nb; } } while (0)
#define STEPS_TO(lim) _Pragma("nounroll") while (nb < (lim)) { BAR(); ++nb; }
    int nb = 0;
    if (wid < nch) {
        const int kch = ch0 + 16 * wid, ki = kch * 64 + lane;
        const unsigned pA = ((const unsigned*)logA)[ki], pI = ((const unsigned*)Aim)[ki], pR = ((const unsigned*)Cre)[ki], pC = ((const unsigned*)Cim)[ki], pT = logdt[kch];
        LAS u32x2* tw = opq((LAS u32x2*)(lds + Z0 + wid * 8448) + lane);
        const LAS unsigned char* trd = opq(lds + Z0 + wid * 8448 + fr * 528 + fq * 16);
        const float dt = rbf(__expf(bf2f(pT)));
        const f32x2 ar = (f32x2){rbf(__expf(bflo(pA))), rbf(__expf(bfhi(pA)))} * dt * -1.44269504f, ai = (f32x2){bflo(pI), bfhi(pI)} * dt * 0.15915494f;
        const f32x2 cr = (f32x2){bflo(pR), bfhi(pR)} * dt, ci = (f32x2){bflo(pC), bfhi(pC)} * dt;
        float cm = fmaxf(fmaxf(fabsf(cr.x), fabsf(ci.x)), fmaxf(fabsf(cr.y), fabsf(ci.y)));
#define DPPF(v, ctl) __int_as_float(__builtin_amdgcn_update_dpp(0, __float_as_int(v), ctl, 0xf, 0xf, true))
        cm = fmaxf(cm, DPPF(cm, 0x121)); cm = fmaxf(cm, DPPF(cm, 0x122)); cm = fmaxf(cm, DPPF(cm, 0x124)); cm = fmaxf(cm, DPPF(cm, 0x128));
#undef DPPF
        { auto r = __builtin_amdgcn_permlane16_swap(__float_as_uint(cm), __float_as_uint(cm), false, false); cm = fmaxf(__uint_as_float(r[0]), __uint_as_float(r[1]));
          auto q = __builtin_amdgcn_permlane32_swap(__float_as_uint(cm), __float_as_uint(cm), false, false); cm = fmaxf(fmaxf(__uint_as_float(q[0]), __uint_as_float(q[1])), 1e-30f); }
        struct C2 { f32x2 x, y; };
        auto cexp = [&](float p_) { const f32x2 e = ar * p_, t = ai * p_;
            const f32x2 mg = {__builtin_amdgcn_exp2f(e.x), __builtin_amdgcn_exp2f(e.y)}; const float t0 = __builtin_amdgcn_fractf(t.x), t1 = __builtin_amdgcn_fractf(t.y);
            return C2{mg * (f32x2){__builtin_amdgcn_cosf(t0), __builtin_amdgcn_cosf(t1)}, mg * (f32x2){__builtin_amdgcn_sinf(t0), __builtin_amdgcn_sinf(t1)}}; };
        auto cmul = [](C2 a, C2 b) { return C2{__builtin_elementwise_fma(a.x, b.x, -(a.y * b.y)), __builtin_elementwise_fma(a.x, b.y, a.y * b.x)}; };
        auto put = [&](int row, C2 v, bool conj) { tw[row * 66] = conj ? (u32x2){pk_f16(v.x.x, -v.y.x), pk_f16(v.x.y, -v.y.y)} : (u32x2){pk_f16(v.x.x, v.y.x), pk_f16(v.x.y, v.y.y)}; };
        C2 wp[16], zr[8], vp[4], cv[17];
        wp[1] = cexp(8.f); zr[1] = cexp(1.f); vp[0] = cexp(128.f);
        tw[0] = (u32x2){pk_f16(1.f, 0.f), pk_f16(1.f, 0.f)};
#pragma unroll
        for (int j = 1; j < 16; ++j) { if (j > 1) wp[j] = cmul(wp[j >> 1], wp[j - (j >> 1)]); put(j, wp[j], true); }
        CFENCE();
        u32x4 fb[8];
#pragma unroll
        for (int ks = 0; ks < 8; ++ks) fb[ks] = *(const LAS u32x4*)(trd + ks * 64);
        CFENCE();
#pragma unroll
        for (int r = 2; r < 8; ++r) zr[r] = cmul(zr[r >> 1], zr[r - (r >> 1)]);
#pragma unroll
        for (int k = 1; k < 4; ++k) vp[k] = cmul(vp[k - 1], vp[k - 1]);
        const float rc = __builtin_amdgcn_rcpf(cm);
        cv[0] = C2{cr * rc, ci * rc};
#pragma unroll
        for (int k = 1; k < 8; ++k) { const int lg = k >= 4 ? 2 : k >= 2 ? 1 : 0; cv[k] = cmul(cv[k - (1 << lg)], vp[lg]); }
        LAS float* kb_ = (LAS float*)(lds + L_KD + wid * 3072);
        const float sc = 2.0f * cm;
        u32x4 fa[3][8];
#pragma unroll
        for (int tt = 0; tt < 3; ++tt) {
#pragma unroll
            for (int i = 0; i < (tt == 2 ? 9 : 16); ++i) {
                const int r = tt == 0 ? (i & 7) : tt == 1 ? (i < 8 ? 2 * (i & 3) : 4 * (i & 1)) : 0;
                const int hh = tt == 0 ? (i >> 3) : tt == 1 ? (i < 8 ? 2 + (i >> 2) : 4 + ((i - 8) >> 1)) : 8 + i;
                if (tt == 2) cv[hh] = cmul(cv[hh - 8], vp[3]);
                put(i, r == 0 ? cv[hh] : cmul(cv[hh], zr[r]), false);
            }
            CFENCE();
#pragma unroll
            for (int ks = 0; ks < 8; ++ks) fa[tt][ks] = *(const LAS u32x4*)(trd + ks * 64);
            CFENCE();
            SB0();
        }
        f32x4 kd[3] = {{0.f, 0.f, 0.f, 0.f}, {0.f, 0.f, 0.f, 0.f}, {0.f, 0.f, 0.f, 0.f}};
#pragma unroll
        for (int ks = 0; ks < 8; ++ks)
#pragma unroll
            for (int tt = 0; tt < 3; ++tt) kd[tt] = __builtin_amdgcn_mfma_f32_16x16x32_f16(__builtin_bit_cast(f16x8, fa[tt][ks]), __builtin_bit_cast(f16x8, fb[ks]), kd[tt], 0, 0, 0);
#pragma unroll
        for (int tt = 0; tt < 3; ++tt) {
            LAS float* kw = opq(kb_ + (tt == 0 ? 128 * (fq >> 1) + 4 * (fq & 1) + 8 * fr : tt == 1 ? 256 + 64 * fq + (fq < 2 ? 4 : 2) * fr : 512 + 64 * fq + fr));
            LAS float* kwb = tt == 1 ? opq(kw + (fq < 2 ? 2 : 32)) : kw + (tt == 0 ? 2 : 32);
            kw[0] = kd[tt][0] * sc; kw[tt == 2 ? 16 : 1] = kd[tt][1] * sc; kwb[0] = kd[tt][2] * sc; kwb[tt == 2 ? 16 : 1] = kd[tt][3] * sc;
        }
        WAITL0();
    }
    if (wid == 3) {
        unsigned w_ = 0;
        if (lane < GM_PFL) w_ = *(const __attribute__((address_space(1))) unsigned*)((pc0 & ~127ull) + (unsigned)min((rr * GM_PFL + lane) * 128, GM_CODE));
        asm volatile("" :: "v"(w_));
    }
#define IC(n) std::integral_constant<int, n>{}
    if (wid < 6) STAMP(10);
    if (is_prod) {
        const unsigned vx = (unsigned)(lane >> 3) * 1152u + rsw;
        unsigned wv[9];
#pragma unroll
        for (int i = 0; i < 9; ++i) { const int c = (p + 2 * i) * 64 + lane, rho = c / 18, ch = c - rho * 18;
            const int kap = (rho & 32) + 8 * ((rho >> 2) & 3) + 4 * ((rho >> 4) & 1) + (rho & 3); wv[i] = (unsigned)kap * 9216u + (unsigned)ch * 16u; }
        const char* xb = (const char*)x + (size_t)(pm * 128 + p * 8) * 1152;
        const char* wb = (const char*)win + pn * 288;
        auto issueX = [&](int k, int s) {
#pragma unroll
            for (int i = 0; i < 8; ++i) GLDS(xb + (size_t)i * (16 * 1152) + k * 128 + vx, lds + s + (p + 2 * i) * 1024, 0); };
        auto issueW = [&](int k, int coloff, int s) {
            const char* b = wb + (size_t)k * (64 * 9216) + coloff;
#pragma unroll
            for (int i = 0; i < 9; ++i) GLDS(b + wv[i], lds + s + BOFF + (p + 2 * i) * 1024, 0); };
        auto ord1 = [&](int s) { return (GM_LAG == 0 || (s & 3) == phi) ? s : (s >= 4 ? s - 4 : (s == 0 ? 8 : s + 4)); };
        issueX(ord1(0), 0); issueW(ord1(0), FH * 2, 0); issueX(ord1(1), STG); issueW(ord1(1), FH * 2, STG);
        issueX(ord1(2), 2 * STG); issueW(ord1(2), FH * 2, 2 * STG);
        E = flag_ld(ws, WS_E);
        unsigned wm_ = 0;
        auto touch = [&](const void* base, unsigned off, unsigned last) { const char* q_ = (const char*)base + min(off + lane * 64u, last); asm volatile("global_load_dword %0, %1, off" : "+v"(wm_) : "v"(q_) : "memory"); };
        if (GM_PW & 1) {
#pragma unroll
            for (int i = 0; i < 2; ++i) touch(x, bid * 9216u + p * 4608u + i * 4096u, bid * 9216u + p * 4608u + 4544u);
#pragma unroll
            for (int i = 0; i < 3; ++i) touch(win, bid * 20736u + p * 10368u + i * 4096u, bid * 20736u + p * 10368u + 10304u);
        }
        int so = 0;
        WAITV(35); PBAR();
#pragma nounroll
        for (int S = 1; S < 16; ++S) {
            if (S >= 8 && S <= 10) WAITV(9); else WAITV(17);
            PBAR();
            const int t = S + 2, k = ord1(t < 9 ? t : t < 12 ? t - 3 : t - 12);
            if (t < 9 || t >= 12) issueX(k, so);
            issueW(k, t >= 9 ? 0 : FH * 2, so); so = nextbuf(so);
            if ((GM_PW & 2) && S == 8) {
#pragma unroll
                for (int i = 0; i < 4; ++i) touch(wsp, bid * 32768u + p * 16384u + i * 4096u, 0x7fffffffu);
            }
        }
        WAITV(17); PBAR(); issueS(ord2(0), 0);
        WAITV(8); PBAR(); issueS(ord2(1), STG);
        asm volatile("" :: "v"(wm_));
        STAMP(1);
    } else if (wid < 4) {
        E = flag_ld(ws, WS_E);
        if (wid >> 1) consumer_a<4>(lds, zp, late, wid & 1, 1, lane, wsp_ TRA);
        else consumer_a<5>(lds, zp, late, wid & 1, 0, lane, wsp_ TRA);
    } else {
        if (h == 0 && lane < 12) *(LAS unsigned*)(lds + L_MISC + lane * 4) = 0u;
        const int row0 = lane / 9, chk0 = lane - row0 * 9;
        const int u = (pn >> 3) * 16 + pm, nblk = u < 8 ? 3 : 2;
        u32x2 uc[18]; u32x4 xv[9], gv[9], wv[3][5];
        if (h == 0) {
            { const char* xb = (const char*)x + (size_t)pm * 128 * 1152 + pn * 72; unsigned off = row0 * 1152 + chk0 * 8; int ch = chk0;
#pragma unroll
              for (int i = 0; i < 9; ++i) { uc[i] = *(const u32x2*)(xb + off); uc[9 + i] = *(const u32x2*)(xb + 64 * 1152 + off); off += 7 * 1152 + 8; if (++ch == 9) { ch = 0; off += 1152 - 72; } } }
            { const char* xr = (const char*)x + (size_t)(pm * 128 + pn * 8 + (lane >> 3)) * 1152 + (lane & 7) * 16; const char* gr = (const char*)gw + (lane & 7) * 16;
#pragma unroll
              for (int i = 0; i < 9; ++i) { xv[i] = *(const u32x4*)(xr + i * 128); gv[i] = *(const u32x4*)(gr + i * 128); } }
        } else {
            unsigned wo_[5], hi_[5];
            { int row = row0, ch = chk0;
#pragma unroll
              for (int i = 0; i < 5; ++i) { const int rw = row < 32 ? row : 31; wo_[i] = (rw & 15) * 1152 + ch * 16; hi_[i] = rw >> 4; row += 7; if (++ch == 9) { ch = 0; ++row; } } }
#pragma unroll
            for (int b = 0; b < 3; ++b) {
                const int blk = (b < nblk) ? u + 32 * b : u, st = blk >> 2, jb = blk & 3;
                const int fA = st < 16 ? st * 144 + (jb < 2 ? 32 * jb : 16 + 32 * jb) : ((st - 16) * 8 + 2 * jb) * 144 + 64, dB = (st < 16 ? 16 : 144) * 1152;
                const char* wb_ = (const char*)wout + (size_t)fA * 1152 + qn * 144;
#pragma unroll
                for (int i = 0; i < 5; ++i) wv[b][i] = *(const u32x4*)(wb_ + (wo_[i] + hi_[i] * dB));
            }
        }
        E = flag_ld(ws, WS_E);
        if (lane < 24) *(LAS u32x4*)(lds + L_KT + (lane >> 3) * KT_SZ + (2 * h + ((lane >> 2) & 1)) * 1088 + (lane & 3) * 16) = (u32x4){0u, 0u, 0u, 0u};
        WAITL0();
        STAMP(10);
        __builtin_amdgcn_s_setprio(GM_HPRIO);
        {
            float kv[2][3];
#pragma nounroll
            for (int ei = 0; ei < 9; ++ei) {
                BAR();
                if (ei > 0) {
                    LAS unsigned char* kt = opq(lds + L_KT + (2 * h + ((ei - 1) >> 2)) * 1088 + 64 + ((ei - 1) & 3) * 256 + lane * 2);
#pragma unroll
                    for (int j = 0; j < 2; ++j)
#pragma unroll
                        for (int c = 0; c < 3; ++c) *(LAS u16*)(kt + c * KT_SZ + j * 128) = (u16)pk_bf16(kv[j][c], 0.f);
                }
#pragma unroll
                for (int j = 0; j < 2; ++j) {
                    const int jj = (ei & 3) * 2 + j, e = jj >= 4 ? 3 : jj >= 2 ? 2 : jj, lag = 256 * jj + 4 * lane + 2 * h + ((ei >> 2) & 1);
                    const int idx = e == 0 ? lag : 128 * e + ((lag + (1 << (e - 1)) - 1 + ((lag >> e) & 1)) >> e);
                    const LAS float* q = opq((const LAS float*)(lds + L_KD) + idx);
#pragma unroll
                    for (int c = 0; c < 3; ++c) kv[j][c] = q[c * 768];
                }
            }
        }
        if (h == 0) {
            LAS unsigned char* pzs = opq(lds + Z0 + lane * 8);
#pragma unroll
            for (int i = 0; i < 18; ++i) *(LAS u32x2*)(pzs + i * 512) = uc[i];
            CFENCE();
            { const LAS unsigned char* pt = opq(lds + Z0 + (8 * (4 * fq + (lane & 3)) + (fr >> 2)) * 72);
              const unsigned ub = WS_UT + (unsigned)(pn * 36 + (lane & 3)) * 4096u + (unsigned)(pm * 128 + 8 * (4 * fq + (fr >> 2))) * 2u;
#pragma unroll
              for (int cc = 0; cc < 9; ++cc) __builtin_amdgcn_raw_buffer_store_b128(__builtin_bit_cast(u32x4, cat8(tr_read(pt + cc * 8), tr_read(pt + 288 + cc * 8))), ws, ub + cc * 16384u, 0, 16); }
            float a = 0.f;
#pragma unroll
            for (int i = 0; i < 9; ++i)
#pragma unroll
                for (int j = 0; j < 4; ++j) { const unsigned xa = xv[i][j], ga = gv[i][j]; a = __builtin_amdgcn_fdot2_f32_bf16(__builtin_bit_cast(bf16x2, xa), __builtin_bit_cast(bf16x2, ga), a, false); }
#define DPPF(v, ctl) __int_as_float(__builtin_amdgcn_update_dpp(0, __float_as_int(v), ctl, 0xf, 0xf, true))
            a += DPPF(a, 0xb1); a += DPPF(a, 0x4e); a += DPPF(a, 0x141);
#undef DPPF
            const float gt = rbf(__builtin_amdgcn_rcpf(1.0f + __expf(-rbf(a))));
            if ((lane & 7) == 0) __builtin_amdgcn_raw_buffer_store_b32(__float_as_uint(gt), ws, WS_GATE + (unsigned)(pm * 128 + pn * 8 + (lane >> 3)) * 4u, 0, 16);
        } else {
            LAS unsigned char* pws = opq(lds + Z0 + 9216 + lane * 16);
            const LAS unsigned char* ptw = opq(lds + Z0 + 9216 + (4 * fq + (fr >> 2)) * 144 + (lane & 3) * 8);
#pragma unroll
            for (int b = 0; b < 3; ++b) {
                if (b < nblk) {
#pragma unroll
                    for (int i = 0; i < 5; ++i) if (i < 4 || lane < 32) *(LAS u32x4*)(pws + i * 1024) = wv[b][i];
                    CFENCE();
                    const unsigned ob = WS_WOF + (unsigned)qn * (18u * 18432u) + (unsigned)(u + 32 * b) * 4608u;
#pragma unroll
                    for (int nn = 0; nn < 5; ++nn) {
                        const u32x4 v = __builtin_bit_cast(u32x4, cat8(tr_read(ptw + nn * 32), tr_read(ptw + 2304 + nn * 32)));
                        if (nn < 4) __builtin_amdgcn_raw_buffer_store_b128(v, ws, ob + nn * 1024 + lane * 16, 0, 16);
                        else if (fr < 8) __builtin_amdgcn_raw_buffer_store_b128(v, ws, ob + 4096 + (fq * 8 + fr) * 16, 0, 16);
                    }
                    CFENCE();
                }
            }
        }
        nb = 9;
        mk_tag();
        WAITL0();
        STEPS_TO(10);
        STAMP(1);
        {
            const f32x4 sp = *(const LAS f32x4*)(lds + L_SP + (h * 64 + lane) * 16);
            const float dm = sp[0] - sp[2];
            const f32x2 mine = {(80.f * sp[0] + 64.f * sp[2]) * (1.0f / 144.f), sp[1] + sp[3] + (80.f * 64.f / 144.f) * dm * dm};
            __builtin_amdgcn_raw_buffer_store_b128((u32x4){T[0], __float_as_uint(mine.x), T[1], __float_as_uint(mine.y)}, ws, WS_ST + (unsigned)((pm * 16 + pn) * 128 + h * 64 + lane) * 16u, 0, 16);
            STEP(18);
            const unsigned so_ = WS_ST + (unsigned)(pm * 16 * 128 + h * 64 + lane) * 16u;
            const u64 t0 = __builtin_amdgcn_s_memrealtime();
            u32x4 st[16];
            for (;;) {
                CFENCE();
#pragma unroll
                for (int j = 0; j < 16; ++j) st[j] = __builtin_amdgcn_raw_buffer_load_b128(ws, so_ + j * 2048u, 0, 16);
                STEP(18);
                unsigned bad = 0;
#pragma unroll
                for (int j = 0; j < 16; ++j) bad |= (st[j][0] ^ T[0]) | (st[j][2] ^ T[1]);
                if (__builtin_amdgcn_ballot_w64(bad != 0u) == 0ull) break;
                if (__builtin_amdgcn_s_memrealtime() - t0 > 300000ull) break;
            }
            STAMP(2);
            STEP(18);
            float mean = 0.f, m2 = 0.f;
#pragma unroll
            for (int j = 0; j < 16; ++j) mean += __uint_as_float(st[j][1]);
            mean *= (1.0f / 16.f);
#pragma unroll
            for (int j = 0; j < 16; ++j) { const float d = __uint_as_float(st[j][1]) - mean; m2 += __uint_as_float(st[j][3]) + 144.f * d * d; }
            const float rstd = __builtin_amdgcn_rsqf(m2 * (1.0f / FH) + 1e-5f);
            *(LAS f32x2*)(lds + L_SP + (h * 64 + lane) * 16) = (f32x2){rstd, -mean * rstd};
            WAITL0();
            if (lane == 0) *(volatile LAS unsigned*)(lds + L_MISC + 4 + h * 4) = 1u;
            const volatile LAS unsigned* zd = (const volatile LAS unsigned*)(lds + L_MISC + 16 + h * 8 + (lane & 1) * 4);
            for (;;) {
                STEP(18);
                if (__builtin_amdgcn_ballot_w64(*zd == 0u) == 0ull) break;
                if (__builtin_amdgcn_s_memrealtime() - t0 > 800000ull) break;
            }
            CFENCE();
            const LAS unsigned char* ptz = opq(lds + Z0 + (h * 64 + 8 * fq + (fr >> 2)) * ZST + (lane & 3) * 8);
            const unsigned zo = WS_Z2F + (unsigned)((pn * 64 + pm * 4 + h * 2) * 9) * 1024u + lane * 16;
#pragma unroll
            for (int i = 0; i < 18; ++i) {
                const LAS unsigned char* pt = ptz + (i / 9) * 32 * ZST + (i % 9) * 32;
                __builtin_amdgcn_raw_buffer_store_b128(__builtin_bit_cast(u32x4, cat8(tr_read(pt), tr_read(pt + 4 * ZST))), ws, zo + i * 1024, 0, 16);
                if (i == 5 || i == 11) STEP(18);
            }
            STEP(18);
            flag_publish(ws, WS_FZ + (unsigned)(pn * 32 + pm * 2 + h) * 128u, T, lane);
            STAMP(3);
            STEPS_TO(18);
            __builtin_amdgcn_s_setprio(0);
        }
    }

    if (is_prod) {
        mk_tag();
        const char* lin = (const char*)wsp_ + p * 1024 + lane * 16;
        auto issueLA = [&](unsigned off, int s) {
#pragma unroll
            for (int i = 0; i < 8; ++i) GLDS(lin + off + i * 2048, lds + s + (p + 2 * i) * 1024, 16); };
        auto issueLB = [&](unsigned off, int s) {
#pragma unroll
            for (int i = 0; i < 9; ++i) GLDS(lin + off + i * 2048, lds + s + BOFF + (p + 2 * i) * 1024, 16); };
        auto ord3 = [&](int s) { return (GM_LAG == 0 || s >= 16) ? s : (((s & 3) == phi) ? s : ((s - GM_LAG) & 15)); };
        u32x2 pv = {0u, 0u}; unsigned rdy = 0; bool pend = false;
        const char* fp = (const char*)wsp_ + WS_FZ + ((unsigned)pn * 32u + (lane & 31)) * 128u;
        auto poll_issue = [&]() { asm volatile("global_load_dwordx2 %0, %1, off sc1" : "+v"(pv) : "v"(fp) : "memory"); };
        auto poll_take = [&]() { asm volatile("" : "+v"(pv)); rdy |= (unsigned)__builtin_amdgcn_ballot_w64(pv[0] == T[0] && pv[1] == T[1]); };
        auto need = [&](unsigned bits) {
            if (pend) { poll_take(); pend = false; }
            if ((rdy & bits) != bits) {
                const u64 t0 = __builtin_amdgcn_s_memrealtime();
                do { poll_issue(); WAITV(0); poll_take(); } while ((rdy & bits) != bits && __builtin_amdgcn_s_memrealtime() - t0 < 300000ull);
            }
            if (rdy != ~0u) { poll_issue(); pend = true; }
        };
        const unsigned zb = WS_Z2F + (unsigned)pn * (64u * 9216u);
        need((1u << ord2(0)) | (1u << ord2(1)));
        STAMP(2);
        issueLB(zb + (unsigned)ord2(0) * 18432u, 0); issueLB(zb + (unsigned)ord2(1) * 18432u, STG);
        int so = 2 * STG;
        WAITV(9);
#pragma nounroll
        for (int S = 18; S < 48; ++S) {
            PBAR();
            const int k = ord2(S - 16); need(1u << k); issueS(k, so); issueLB(zb + (unsigned)k * 18432u, so); so = nextbuf(so);
            WAITV(17);
        }
        const unsigned hb = WS_HF + (unsigned)qm * (18u * 16384u), ob = WS_WOF + (unsigned)qn * (18u * 18432u);
        PBAR();
        fp = (const char*)wsp_ + WS_FZ + ((unsigned)(pn ^ 8) * 32u + (lane & 31)) * 128u; rdy = 0; pend = false;
        need(~0u);
        issueLB(ob + (unsigned)ord3(0) * 18432u, 2 * STG);
        PBAR();
        issueLB(ob + (unsigned)ord3(1) * 18432u, 0);
        STAMP(3);
        fp = (const char*)wsp_ + WS_FH + ((unsigned)qm * 32u + (lane & 31)) * 128u; rdy = 0;
        need(~0u);
        STAMP(4);
        issueLA(hb + (unsigned)ord3(0) * 16384u, 2 * STG); issueLA(hb + (unsigned)ord3(1) * 16384u, 0);
        so = STG;
        WAITV(8);
#pragma nounroll
        for (int S = 50; S < 66; ++S) {
            PBAR();
            const int k = ord3(S - 48); issueLA(hb + (unsigned)k * 16384u, so); issueLB(ob + (unsigned)k * 18432u, so); so = nextbuf(so);
            WAITV(17);
        }
        PBAR();
        WAITV(0); PBAR();
        PBAR();
        STAMP(5); PDUMP();
        if (bid == 0 && p == 0 && lane == 0) __builtin_amdgcn_raw_buffer_store_b64(T, ws, WS_E, 0, 16);
        return;
    }

    if (wid < 4) {
        mk_tag();
        if (wid >> 1) consumer_b<4>(lds, ws, T, zp, bs, pm, pn, wid & 1, 1, lane, wsp_ TRA);
        else consumer_b<5>(lds, ws, T, zp, bs, pm, pn, wid & 1, 0, lane, wsp_ TRA);
    } else {
        __builtin_amdgcn_s_setprio(GM_HPRIO);
        {   const unsigned fo = WS_FZ + (unsigned)(pn * 32 + (lane & 31)) * 128u;
            const u64 t0 = __builtin_amdgcn_s_memrealtime();
            for (;;) {
                STEP(44);
                CFENCE();
                const u32x2 v = flag_ld(ws, fo);
                STEP(44);
                if (flag_all(v, T, true)) break;
                if (__builtin_amdgcn_s_memrealtime() - t0 > 300000ull) break;
            } }
        u32x4 uu[3][2];
#pragma unroll
        for (int c = 0; c < 3; ++c)
#pragma unroll
            for (int j = 0; j < 2; ++j) uu[c][j] = __builtin_amdgcn_raw_buffer_load_b128(ws, WS_UT + (unsigned)(ch0 + (c < nch ? 16 * c : 0)) * 4096u + h * 2048 + j * 1024 + lane * 16, 0, 16);
#pragma unroll
        for (int c = 0; c < 3; ++c) if (lane < 16) *(LAS u32x4*)(lds + L_UT + c * UT_SZ + h * 4352 + lane * 16) = (u32x4){0u, 0u, 0u, 0u};
        BAR(); BAR(); BAR(); nb += 3;
        LAS unsigned char* pu_ = opq(lds + L_UT + 256 + h * 2048 + lane * 16);
#pragma unroll
        for (int c = 0; c < 3; ++c)
#pragma unroll
            for (int j = 0; j < 2; ++j) *(LAS u32x4*)(pu_ + c * UT_SZ + j * 1024) = uu[c][j];
        WAITL0();
        if (lane == 0) *(volatile LAS unsigned*)(lds + L_MISC + 32 + h * 4) = 1u;
        {   const volatile LAS unsigned* uw = (const volatile LAS unsigned*)(lds + L_MISC + 32 + (lane & 1) * 4);
            const u64 t0 = __builtin_amdgcn_s_memrealtime();
            do { STEP(50); } while (__builtin_amdgcn_ballot_w64(*uw == 0u) != 0ull && __builtin_amdgcn_s_memrealtime() - t0 < 800000ull);
            CFENCE(); }
        STAMP(4);
    }

    if (wid < 4) nb = 50;
    for (;;) {
        if (nb > 45) STEPS_TO(50);
        unsigned tk = 0;
        if (lane == 0) tk = __hip_atomic_fetch_add((LAS unsigned*)(lds + L_MISC), 1u, __ATOMIC_RELAXED, __HIP_MEMORY_SCOPE_WORKGROUP);
        tk = __builtin_amdgcn_readfirstlane(tk);
        if (tk >= (unsigned)(4 * nch)) break;
        const int c = tk >> 2, tl = tk & 1;
        if (tk & 2) conv_task<0, 10>(lds, L_KT + c * KT_SZ, L_UT + c * UT_SZ, ytab(c), tl, lane, nb TRA);
        else conv_task<11, 15>(lds, L_KT + c * KT_SZ, L_UT + c * UT_SZ, ytab(c), tl, lane, nb TRA);
    }
    STEPS_TO(50);
    WAITL0();
    STAMP(7);

    if (wid < 4) {
        const int kq = wid >> 1, mh = wid & 1;
        f32x4 acc[5][2];
        struct F3 { s16x4 alo[4], ahi[4]; bf16x8 b[10]; } f0, f1;
        const LAS unsigned char* ph = lds + 2 * STG + mh * 8192 + kq * 2048 + lane * 16;
        const LAS unsigned char* pw = lds + 2 * STG + BOFF + kq * 9216 + lane * 16;
        const LAS unsigned char* pw4 = lds + 2 * STG + BOFF + kq * 9216 + 4096 + (fq * 8 + (fr & 7)) * 16;
        auto rd3 = [&](F3& f, int i, bool single) {
            const int j = i / 7, r = i % 7;
            if (r < 2) {
                if (!single) { const bf16x8 v = *(const LAS bf16x8*)(ph + r * 4096 + j * 1024); f.alo[j * 2 + r] = __builtin_shufflevector(v, v, 0, 1, 2, 3); f.ahi[j * 2 + r] = __builtin_shufflevector(v, v, 4, 5, 6, 7); }
                else { const LAS unsigned char* q = ph - lane * 8 + r * 4096 + j * 1024; f.alo[j * 2 + r] = *(const LAS s16x4*)q; f.ahi[j * 2 + r] = *(const LAS s16x4*)(q + 512); }
            } else if (r < 6) f.b[j * 5 + r - 2] = *(const LAS bf16x8*)(pw + j * 4608 + (r - 2) * 1024);
            else f.b[j * 5 + 4] = *(const LAS bf16x8*)(pw4 + j * 4608);
        };
        int so = 2 * STG;
        auto step3 = [&](const F3& c, F3& l, int mode  , bool first) {
            const int delta = so == 2 * STG ? -2 * STG : STG; so += delta;
#pragma unroll
            for (int i = 0; i < 20; ++i) {
                const int j = i / 10, n = (i % 10) >> 1, m = i & 1;
                acc[n][m] = MFMA16(c.b[j * 5 + n], cat8(c.alo[j * 2 + m], c.ahi[j * 2 + m]), (first && j == 0) ? ((f32x4){0.f, 0.f, 0.f, 0.f}) : acc[n][m]);
                if (mode < 2 && i < 7) { rd3(l, 2 * i, mode == 1); rd3(l, 2 * i + 1, mode == 1); }
                if (mode < 2) { if (i == 8) { ph += delta; asm volatile("" : "+v"(ph)); } if (i == 9) { pw += delta; asm volatile("" : "+v"(pw)); } if (i == 10) { pw4 += delta; asm volatile("" : "+v"(pw4)); } }
                SB0();
            }
        };
        __builtin_amdgcn_s_waitcnt(0xC07F);
        BAR();
#pragma unroll
        for (int i = 0; i < 14; ++i) rd3(f0, i, false);
        ph -= 2 * STG; pw -= 2 * STG; pw4 -= 2 * STG; so = 0;
        WAITL0(); BAR(); step3(f0, f1, 0, true);
#pragma nounroll
        for (int j = 0; j < 7; ++j) {
            WAITL0(); BAR(); step3(f1, f0, 0, false);
            WAITL0(); BAR(); step3(f0, f1, 0, false);
        }
        WAITL0(); BAR(); step3(f1, f0, 1, false);
        WAITL0(); BAR(); step3(f0, f1, 1, false);
        step3(f1, f0, 2, false);
        STAMP(8);
        LAS unsigned char* prw = opq(lds + L_R + wid * 5120 + lane * 16);
#pragma unroll
        for (int n = 0; n < 5; ++n) { const f32x4 x0 = acc[n][0], x1 = acc[n][1]; *(LAS f32x4*)(prw + n * 1024) = kq ? x0 : x1; acc[n][0] = kq ? x1 : x0; }
        WAITL0();
        BAR();
        const int m = mh * 2 + kq;
        const rsrc_t ors = __builtin_amdgcn_make_buffer_rsrc(out, 0, SEQ * DM * 2, 0x00020000);
        const float g = *(const LAS float*)(lds + L_MISC + 64 + (16 * m + fr) * 4), g1 = rbf(1.0f - g);
        const LAS unsigned char* prr = opq(lds + L_R + (wid ^ 2) * 5120 + lane * 16);
        const LAS unsigned char* pat = opq(lds + L_AT + (4 * fq + (fr >> 2)) * 128 + (16 * m + 4 * (lane & 3)) * 2);
#pragma unroll
        for (int n = 0; n < 5; ++n) {
            const f32x4 b = acc[n][0] + *(const LAS f32x4*)(prr + n * 1024);
            const u32x2 a = __builtin_bit_cast(u32x2, tr_read(pat + n * 2048));
            const u32x2 o = {pk_bf16(g * bflo(a[0]) + g1 * b[0], g * bfhi(a[0]) + g1 * b[1]), pk_bf16(g * bflo(a[1]) + g1 * b[2], g * bfhi(a[1]) + g1 * b[3])};
            if (n < 4 || fq < 2) __builtin_amdgcn_raw_buffer_store_b64(o, ors, (unsigned)((qm * 64 + 16 * m + fr) * 1152 + (qn * 72 + 16 * n + 4 * fq) * 2), 0, GM_OAUX);
        }
    } else {
        int d = pn * 36 + pm;
#pragma nounroll
        for (int c = 0; c < nch; ++c) {
            const float dsk = bf2f(Dsk[d]);
#pragma unroll
            for (int k = 0; k < 2; ++k) {
                BAR(); ++nb;
                const int l0 = h * 1024 + k * 512 + lane * 8;
                f32x4 cv = *(const LAS f32x4*)(lds + ytab(c) + 8192 + (l0 >= 16 ? (l0 >> 4) - 1 : 0) * 16);
                if ((lane & 1) || l0 == 0) cv = (f32x4){0.f, 0.f, 0.f, 0.f};
                const f32x4 y0 = *(const LAS f32x4*)(lds + ytab(c) + l0 * 4) + cv, y1 = *(const LAS f32x4*)(lds + ytab(c) + l0 * 4 + 16);
                const u32x4 uv = *(const LAS u32x4*)(lds + L_UT + c * UT_SZ + 256 + l0 * 2);
                u32x4 o;
#pragma unroll
                for (int j = 0; j < 4; ++j) {
                    const float ya = j < 2 ? y0[2 * j] : y1[2 * j - 4], yb = j < 2 ? y0[2 * j + 1] : y1[2 * j - 3];
                    const unsigned yp = pk_bf16(ya, yb), up = pk_bf16(bflo(uv[j]) * dsk, bfhi(uv[j]) * dsk);
                    o[j] = pk_bf16(bflo(yp) + bflo(up), bfhi(yp) + bfhi(up));
                }
                __builtin_amdgcn_raw_buffer_store_b128(o, ws, WS_AT + (unsigned)d * 4096u + l0 * 2, 0, 16);
            }
            d += 16;
        }
        STEPS_TO(58);
        flag_publish(ws, WS_FA + (unsigned)((pn >> 1) * 64 + (pn & 1) * 32 + pm * 2 + h) * 128u, T, lane);
        STAMP(5);
        STEPS_TO(62);
        if (h == 0) {
            const unsigned fo = WS_FA + (unsigned)(qn * 64 + lane) * 128u;
            const u64 t0 = __builtin_amdgcn_s_memrealtime();
            for (;;) {
                CFENCE();
                const u32x2 v = flag_ld(ws, fo);
                STEP(68); STEP(68);
                if (flag_all(v, T, true)) break;
                if (__builtin_amdgcn_s_memrealtime() - t0 > 300000ull) break;
            }
            ACQ();
            u32x4 av[9];
#pragma unroll
            for (int i = 0; i < 9; ++i) av[i] = __builtin_amdgcn_raw_buffer_load_b128(ws, WS_AT + (unsigned)(qn * 72 + i * 8 + (lane >> 3)) * 4096u + qm * 128 + (lane & 7) * 16, 0, 16);
            STEP(68); STEP(68);
            LAS unsigned char* pa_ = opq(lds + L_AT + lane * 16);
#pragma unroll
            for (int i = 0; i < 9; ++i) *(LAS u32x4*)(pa_ + i * 1024) = av[i];
        } else {
            STEPS_TO(65);
            const unsigned gv = __builtin_amdgcn_raw_buffer_load_b32(ws, WS_GATE + (unsigned)(qm * 64 + lane) * 4u, 0, 16);
            BAR(); BAR(); nb += 2;
            *(LAS unsigned*)(lds + L_MISC + 64 + lane * 4) = gv;
        }
        STEPS_TO(68);
        WAITL0();
        BAR();
    }
    STAMP(9); TRDUMP();
}
}

extern "C" void kernel_launch(void* const* d_in, const int* in_sizes, int n_in, void* d_out, int out_size, void* d_ws, size_t ws_size, hipStream_t stream) {
    (void)in_sizes; (void)n_in; (void)out_size; (void)ws_size;
    (void)hipFuncSetAttribute((const void*)gm::gm_kernel, hipFuncAttributeMaxDynamicSharedMemorySize, gm::LDS_BYTES);
    hipLaunchKernelGGL(gm::gm_kernel, dim3(256), dim3(512), gm::LDS_BYTES, stream, (const u16*)d_in[0], (const u16*)d_in[1], (const u16*)d_in[2], (const u16*)d_in[3],
                       (const u16*)d_in[4], (const u16*)d_in[5], (const u16*)d_in[6], (const u16*)d_in[8], (const u16*)d_in[9], (const u16*)d_in[10],
                       (const u16*)d_in[11], (const u16*)d_in[12], (u16*)d_out, (unsigned char*)d_ws);
}
```

```cpp
#define GM_DA 0
#define GM_DB 0
#include <hip/hip_runtime.h>
#include <stdint.h>
#include <type_traits>

#define LAS __attribute__((address_space(3)))
typedef unsigned short u16;
typedef unsigned long long u64;
typedef short bf16x8 __attribute__((ext_vector_type(8)));
typedef short s16x4 __attribute__((ext_vector_type(4)));
typedef _Float16 f16x8 __attribute__((ext_vector_type(8)));
typedef _Float16 f16x2 __attribute__((ext_vector_type(2)));
typedef __bf16 bf16x2 __attribute__((ext_vector_type(2)));
typedef float f32x4 __attribute__((ext_vector_type(4)));
typedef float f32x2 __attribute__((ext_vector_type(2)));
typedef unsigned u32x4 __attribute__((ext_vector_type(4)));
typedef unsigned u32x2 __attribute__((ext_vector_type(2)));
typedef __amdgpu_buffer_rsrc_t rsrc_t;

namespace gm {
constexpr int SEQ = 2048, DM = 576, FH = 2304;
constexpr unsigned WS_E = 0, WS_FS = 4096, WS_FZ = WS_FS + 65536, WS_FH = WS_FZ + 65536, WS_FA = WS_FH + 131072;
constexpr unsigned WS_ST = 0x60000, WS_GATE = 0xE0000, WS_DBG = 0x1E00000, WS_Z2F = 0x100000, WS_HF = 0xA00000, WS_WOF = 0x1300000, WS_UT = 0x1600000, WS_AT = 0x1900000, WS_END = 0x1C00000, WS_TRC = 0x1C00000;
constexpr int STG = 34816, BOFF = 16384, Z0 = 3 * STG, ZST = 296, T0 = Z0 + 128 * ZST;
constexpr int KT_SZ = 4352, UT_SZ = 4608, Y_SZ = 10240;
constexpr int L_MISC = 163840 - 320;
constexpr int L_KT = L_MISC - 3 * KT_SZ, L_SP = T0, L_KD = Z0 + 3 * 8448, L_UT = Z0 + 3 * Y_SZ;
constexpr int L_AT = Z0, L_R = Z0 + 10240;
constexpr int LDS_BYTES = 163840;
static_assert(L_UT + 3 * UT_SZ <= L_KT && L_KD + 3 * 3072 <= T0 && L_SP + 2048 <= L_KT, "lds map");
#define GM_HPRIO 1
#define GM_OAUX 0
#define GM_PFL 13
#define GM_CODE 49792
#define GM_ORD 0
#define GM_PW 3
#define GM_LAG 4

__device__ __forceinline__ float bf2f(unsigned v) { return __uint_as_float(v << 16); }
__device__ __forceinline__ float bflo(unsigned w) { return __uint_as_float(w << 16); }
__device__ __forceinline__ float bfhi(unsigned w) { return __uint_as_float(w & 0xffff0000u); }
__device__ __forceinline__ unsigned pk_bf16(float a, float b) { f32x2 v = {a, b}; return __builtin_bit_cast(unsigned, __builtin_convertvector(v, bf16x2)); }
__device__ __forceinline__ unsigned pk_f16(float a, float b) { f32x2 v = {a, b}; return __builtin_bit_cast(unsigned, __builtin_convertvector(v, f16x2)); }
__device__ __forceinline__ float rbf(float a) { return bflo(pk_bf16(a, a)); }
__device__ __forceinline__ f32x2 gelu2(f32x2 x) {
    const f32x2 w = (x * x) * (-0.10294324f) + (-2.30220819f);
    const f32x2 a = w * x;
    f32x2 e; e.x = __builtin_amdgcn_exp2f(a.x); e.y = __builtin_amdgcn_exp2f(a.y);
    e = e + 1.0f;
    f32x2 r; r.x = __builtin_amdgcn_rcpf(e.x); r.y = __builtin_amdgcn_rcpf(e.y);
    return x * r;
}
__device__ __forceinline__ f32x4 gelu4(f32x4 v) { const f32x2 a = gelu2((f32x2){v[0], v[1]}), b = gelu2((f32x2){v[2], v[3]}); return (f32x4){a.x, a.y, b.x, b.y}; }
__device__ __forceinline__ float allred_q(float v) {
    unsigned u = __float_as_uint(v);
    auto r = __builtin_amdgcn_permlane16_swap(u, u, false, false);
    v = __uint_as_float(r[0]) + __uint_as_float(r[1]);
    u = __float_as_uint(v);
    auto s = __builtin_amdgcn_permlane32_swap(u, u, false, false);
    return __uint_as_float(s[0]) + __uint_as_float(s[1]);
}
#define ACQ() __builtin_amdgcn_fence(__ATOMIC_ACQUIRE, "wavefront")
#define TRP
#define TRA
#define TRDUMP() do { } while (0)
#define BAR() __builtin_amdgcn_s_barrier()
#define PBAR() BAR()
#define PDUMP() TRDUMP()
#define WAITV(n) asm volatile("s_waitcnt vmcnt(" #n ")" ::: "memory")
#define WAITL0() asm volatile("s_waitcnt lgkmcnt(0)" ::: "memory")
#define CFENCE() asm volatile("" ::: "memory")
#define SGB(mask, n, id) __builtin_amdgcn_sched_group_barrier(mask, n, id)
#define MFMA16(a, b, c) __builtin_amdgcn_mfma_f32_16x16x32_bf16(a, b, c, 0, 0, 0)
__device__ __forceinline__ s16x4 tr_read(const LAS unsigned char* p) { return __builtin_amdgcn_ds_read_tr16_b64_v4i16((LAS s16x4*)p); }
__device__ __forceinline__ bf16x8 cat8(s16x4 a, s16x4 b) { return __builtin_shufflevector(a, b, 0, 1, 2, 3, 4, 5, 6, 7); }
template <class P> __device__ __forceinline__ P opq(P p) { asm volatile("" : "+v"(p)); return p; }
__device__ __forceinline__ int nextbuf(int so) { return so == 2 * STG ? 0 : so + STG; }

#define STAMP(i) do { } while (0)

__device__ __forceinline__ u32x2 flag_ld(rsrc_t ws, unsigned off) { return __builtin_amdgcn_raw_buffer_load_b64(ws, off, 0, 16); }
__device__ __forceinline__ bool flag_all(u32x2 v, u32x2 T, bool active) { return __builtin_amdgcn_ballot_w64(active && (v[0] != T[0] || v[1] != T[1])) == 0ull; }
__device__ __forceinline__ void flag_publish(rsrc_t ws, unsigned off, u32x2 T, int lane) {
    WAITV(0);
    if (lane == 0) __builtin_amdgcn_raw_buffer_store_b64(T, ws, off, 0, 16);
}
__device__ __forceinline__ void flag_wait(rsrc_t ws, unsigned base, int n, u32x2 T, int lane) {
    const unsigned off = base + (unsigned)(lane < n ? lane : 0) * 128u;
    const u64 t0 = __builtin_amdgcn_s_memrealtime();
    for (;;) {
        CFENCE();
        const u32x2 v = flag_ld(ws, off);
        if (flag_all(v, T, true)) break;
        if (__builtin_amdgcn_s_memrealtime() - t0 > 300000ull) break;
        __builtin_amdgcn_s_sleep(1);
    }
    ACQ();
}

#define SB0() __builtin_amdgcn_sched_barrier(0)
template <int NT> struct Frag { bf16x8 a[4]; s16x4 lo[NT], hi[NT]; };
template <int NT, bool TR, int HALF>
__device__ __forceinline__ void frag_read(Frag<NT>& f, int i, const LAS unsigned char* pa, const LAS unsigned char* pb) {
    if (GM_ORD) { constexpr int NB = TR ? 2 * NT : NT; i = i == 0 ? 0 : i <= NB ? i + 3 : i - NB; }
    if (i < 4) f.a[i] = *(const LAS bf16x8*)(pa + i * 2048);
    else if (TR) { const int n = (i - 4) >> 1; if ((i - 4) & 1) f.hi[n] = tr_read(pb + HALF * 9216 + 4608 + n * 32); else f.lo[n] = tr_read(pb + HALF * 9216 + n * 32); }
    else { const int n = i - 4; const bf16x8 v = *(const LAS bf16x8*)(pb + HALF * 9216 + n * 1024);
           f.lo[n] = __builtin_shufflevector(v, v, 0, 1, 2, 3); f.hi[n] = __builtin_shufflevector(v, v, 4, 5, 6, 7); }
}
template <int NT, bool TR, int LHALF, int RPS, bool FIRST, bool UPD>
__device__ __forceinline__ void half_step(f32x4 (&acc)[NT][4], const Frag<NT>& c, Frag<NT>& l, const LAS unsigned char*& pa0, const LAS unsigned char*& pa1, const LAS unsigned char*& pb, int delta, const float* ini = nullptr) {
    constexpr int NRD = TR ? 4 + 2 * NT : 4 + NT, NM = 4 * NT, LAST = (NRD + RPS - 1) / RPS;
#pragma unroll
    for (int i = 0; i < NM; ++i) {
        const int n = GM_ORD ? i % NT : i >> 2, m = GM_ORD ? i / NT : i & 3;
        acc[n][m] = MFMA16(cat8(c.lo[n], c.hi[n]), c.a[m], FIRST ? (TR ? ((f32x4){0.f, 0.f, 0.f, 0.f}) : ((f32x4){ini[m], ini[m], ini[m], ini[m]})) : acc[n][m]);
        if (LHALF >= 0) {
#pragma unroll
            for (int r = 0; r < RPS; ++r) if (i * RPS + r < NRD) frag_read<NT, TR, (LHALF > 0 ? 1 : 0)>(l, i * RPS + r, LHALF > 0 ? pa1 : pa0, pb);
        }
        if (UPD) { if (i == LAST) { pa0 += delta; asm volatile("" : "+v"(pa0)); } if (i == LAST + 1) { pb += delta; asm volatile("" : "+v"(pb)); } if (i == LAST + 2) { pa1 += delta; asm volatile("" : "+v"(pa1)); } }
        SB0();
    }
}
template <int NT, bool TR, class HK>
__device__ __forceinline__ void gemm_phase(int nsteps, f32x4 (&acc)[NT][4], int& so, const LAS unsigned char* pa0, const LAS unsigned char* pa1, const LAS unsigned char* pb,
                                           const float* ini, const volatile LAS unsigned* rdy, HK hook TRP) {
    constexpr int NRD = TR ? 4 + 2 * NT : 4 + NT;
    Frag<NT> f0, f1;
    pa0 += so; pa1 += so; pb += so;
    asm volatile("" : "+v"(pa0), "+v"(pa1), "+v"(pb));
    __builtin_amdgcn_s_waitcnt(0xC07F);
    BAR();
#pragma unroll
    for (int i = 0; i < NRD; ++i) frag_read<NT, TR, 0>(f0, i, pa0, pb);
    SB0();
    int delta = so == 2 * STG ? -2 * STG : STG; so += delta;
    half_step<NT, TR, 1, 2, true, true>(acc, f0, f1, pa0, pa1, pb, delta, ini);
#pragma nounroll
    for (int s = 1; s < nsteps; ++s) {
        WAITL0();
        BAR();
        unsigned rv = 0;
        if (TR) { rv = *rdy; SB0(); }
        delta = so == 2 * STG ? -2 * STG : STG; so += delta;
        half_step<NT, TR, 0, 1, false, false>(acc, f1, f0, pa0, pa1, pb, 0);
        half_step<NT, TR, 1, 2, false, true>(acc, f0, f1, pa0, pa1, pb, delta);
        if (TR) hook(rv);
    }
    half_step<NT, TR, -1, 1, false, false>(acc, f1, f0, pa0, pa1, pb, 0);
}

template <int NT, class LT>
__device__ __forceinline__ void consumer_a(LAS unsigned char* lds, u32x2 (&zp)[5][4], LT late, int wm, int wn, int lane, unsigned char* wsp_ TRP) {
    const int fr = lane & 15, fq = lane >> 4;
    constexpr int N0 = (NT == 5) ? 0 : 5;
    f32x4 acc[NT][4];
    int so = 0;
    const int sw = (fr >> 1) & 7;
    const int a0 = (wm * 64 + fr) * 128 + ((fq ^ sw) << 4), a1 = (wm * 64 + fr) * 128 + (((4 + fq) ^ sw) << 4);
    const int bw = BOFF + (4 * fq + (fr >> 2)) * 288 + (lane & 3) * 8 + N0 * 32;
    const volatile LAS unsigned* rdy = (const volatile LAS unsigned*)(lds + L_MISC + 4 + wm * 4);
    bool done = false;
    auto normalise = [&]() {
        const LAS unsigned char* ps = opq(lds + L_SP + (wm * 64 + fr) * 16);
        LAS unsigned char* pz = opq(lds + Z0 + (wm * 64 + fr) * ZST + N0 * 32 + fq * 8);
#pragma unroll
        for (int m = 0; m < 4; ++m) {
            const f32x2 sr = *(const LAS f32x2*)(ps + m * 256);
#pragma unroll
            for (int n = 0; n < NT; ++n) {
                const f32x2 u = (f32x2){bflo(zp[n][m][0]), bfhi(zp[n][m][0])} * sr.x + sr.y, v = (f32x2){bflo(zp[n][m][1]), bfhi(zp[n][m][1])} * sr.x + sr.y;
                *(LAS u32x2*)(pz + m * 16 * ZST + n * 32) = (u32x2){pk_bf16(u.x, u.y), pk_bf16(v.x, v.y)};
            }
        }
        WAITL0();
        if (lane == 0) *(volatile LAS unsigned*)(lds + L_MISC + 16 + (wm * 2 + wn) * 4) = 1u;
        done = true;
    };
    auto hook = [&](unsigned rv) { if (__builtin_amdgcn_readfirstlane(rv) != 0u && !done) normalise(); };
#pragma unroll
    for (int n = 0; n < NT; ++n)
#pragma unroll
        for (int m = 0; m < 4; ++m) zp[n][m] = (u32x2){0u, 0u};
#pragma nounroll
    for (int it = 0; it < 2; ++it) {
        gemm_phase<NT, true>(9, acc, so, lds + a0, lds + a1, lds + bw, nullptr, rdy, hook TRA);
        STAMP(1 + 2 * it);
        late();
        if (it == 1 && !done) {
            const u64 t0 = __builtin_amdgcn_s_memrealtime();
            while (__builtin_amdgcn_readfirstlane(*rdy) == 0u && __builtin_amdgcn_s_memrealtime() - t0 < 400000ull) __builtin_amdgcn_s_sleep(1);
            normalise();
        }
#pragma unroll
        for (int m = 0; m < 4; ++m)
#pragma unroll
            for (int n = 0; n < NT; ++n) { acc[n][m] = gelu4(acc[n][m]); zp[n][m] = (u32x2){pk_bf16(acc[n][m][0], acc[n][m][1]), pk_bf16(acc[n][m][2], acc[n][m][3])}; }
        if (it == 0) {
            LAS unsigned char* ps = opq(lds + L_SP + (wm * 64 + fr) * 16 + wn * 8);
#pragma unroll
            for (int m = 0; m < 4; ++m) {
                f32x4 s4 = acc[0][m];
#pragma unroll
                for (int n = 1; n < NT; ++n) s4 += acc[n][m];
                const float mean = opq(allred_q((s4[0] + s4[1]) + (s4[2] + s4[3])) * (1.0f / (16 * NT)));
                f32x4 q4 = {0.f, 0.f, 0.f, 0.f};
#pragma unroll
                for (int n = 0; n < NT; ++n) { const f32x4 d = acc[n][m] - mean; q4 += d * d; }
                const float m2 = allred_q((q4[0] + q4[1]) + (q4[2] + q4[3]));
                if (fq == 0) *(LAS f32x2*)(ps + m * 256) = (f32x2){mean, m2};
            }
            STAMP(2);
        }
    }
    STAMP(4);
}

template <int NT>
__device__ __forceinline__ void consumer_b(LAS unsigned char* lds, rsrc_t ws, u32x2 T, const u32x2 (&z1)[5][4], const float (&bs_)[4], int pm, int pn, int wm, int wn, int lane, unsigned char* wsp_ TRP) {
    float bs[4] = {bf2f(__float_as_uint(opq(bs_[0]))), bf2f(__float_as_uint(opq(bs_[1]))), bf2f(__float_as_uint(opq(bs_[2]))), bf2f(__float_as_uint(opq(bs_[3])))};
    const int fr = lane & 15, fq = lane >> 4;
    constexpr int N0 = (NT == 5) ? 0 : 5;
    f32x4 acc[NT][4];
    int so = 0;
    const int sw = (fr >> 1) & 7;
    const int a0 = (wm * 64 + fr) * 128 + ((fq ^ sw) << 4), a1 = (wm * 64 + fr) * 128 + (((4 + fq) ^ sw) << 4);
    const int bz = BOFF + N0 * 1024 + lane * 16;
    gemm_phase<NT, false>(32, acc, so, lds + a0, lds + a1, lds + bz, bs, nullptr, [](unsigned) {} TRA);
    STAMP(5);
    __builtin_amdgcn_s_setprio(3);
    const int qm = 2 * pm + wm;
    const unsigned hb = WS_HF + (unsigned)qm * (18u * 16384u);
#pragma unroll
    for (int m = 0; m < 4; ++m) {
        u32x2 hp[NT];
#pragma unroll
        for (int n = 0; n < NT; ++n) {
            const f32x2 u = (f32x2){bflo(z1[n][m][0]), bfhi(z1[n][m][0])} * (f32x2){acc[n][m][0], acc[n][m][1]}, v = (f32x2){bflo(z1[n][m][1]), bfhi(z1[n][m][1])} * (f32x2){acc[n][m][2], acc[n][m][3]};
            hp[n] = (u32x2){pk_bf16(u.x, u.y), pk_bf16(v.x, v.y)};
        }
#pragma unroll
        for (int j = 0; j < 2; ++j)
            __builtin_amdgcn_raw_buffer_store_b128((u32x4){hp[2 * j][0], hp[2 * j][1], hp[2 * j + 1][0], hp[2 * j + 1][1]}, ws,
                                                   hb + (unsigned)pn * 16384u + (unsigned)(m * 4 + wn * 2 + j) * 1024u + lane * 16, 0, 16);
        if (NT == 5) __builtin_amdgcn_raw_buffer_store_b64(hp[NT - 1], ws, hb + (unsigned)(16 + (pn >> 3)) * 16384u + m * 4096u + (unsigned)(pn & 7) * 512u + lane * 8, 0, 16);
    }
    flag_publish(ws, WS_FH + (unsigned)(qm * 32 + pn * 2 + wn) * 128u, T, lane);
    __builtin_amdgcn_s_setprio(0);
    STAMP(6);
}

template <int TLO, int THI>
__device__ __forceinline__ void conv_task(LAS unsigned char* lds, int kt, int ut, int yt, int tl, int lane, int& nb TRP) {
#define CSL() do { if (nb < 50) { BAR(); ++nb; } } while (0)
    constexpr int NTL = THI - TLO + 1, ND = THI + 2;
    const int i = lane & 15, g = lane >> 4;
    const LAS unsigned char* pk = opq(lds + kt + (i & 3) * 1088 + ((i >> 2) + g) * 16);
    const LAS unsigned char* pu = opq(lds + ut + (128 + 16 * tl - 32 * g - 4 * (i >> 2) + 4 * (i & 3)) * 2 - 32);
    bf16x8 ub[ND];
#pragma unroll
    for (int d = 0; d < ND; ++d) { if (d == 0 || d == 8) CSL(); ub[d] = cat8(tr_read(pu + 256 * d + 32), tr_read(pu + 256 * d)); }
    f32x4 acc[NTL];
#pragma unroll
    for (int t = 0; t < NTL; ++t) acc[t] = (f32x4){0.f, 0.f, 0.f, 0.f};
#pragma unroll
    for (int s = -1; s <= THI; ++s) {
        if (TLO == 0 ? (s <= 3 || s == 5 || s == 7) : (s & 1) != 0 && s < 12) CSL();
        const bf16x8 kf = *(const LAS bf16x8*)(pk + 64 * (s + 1));
#pragma unroll
        for (int t = TLO; t <= THI; ++t) if (t >= s) acc[t - TLO] = MFMA16(kf, ub[t - s], acc[t - TLO]);
    }
    LAS float* y = opq((LAS float*)(lds + yt) + 32 * g + 16 * tl + i);
    LAS float* cy = opq((LAS float*)(lds + yt + 8192) + (2 * g + tl) * 4 + (i & 3));
#define DPPF(v, ctl) __int_as_float(__builtin_amdgcn_update_dpp(0, __float_as_int(v), ctl, 0xf, 0xf, true))
#pragma unroll
    for (int t = TLO; t <= THI; ++t) {
        if (((t - TLO) % 2) == 0) CSL();
        const f32x4 a = acc[t - TLO];
        y[128 * t] = (a[0] + DPPF(a[1], 0x111)) + (DPPF(a[2], 0x112) + DPPF(a[3], 0x113));
        const float cv = DPPF(a[1], 0x10f) + (DPPF(a[2], 0x10e) + DPPF(a[3], 0x10d));
        if (i < 4) cy[32 * t] = cv;
    }
#undef DPPF
#undef CSL
}
__device__ __forceinline__ int ytab(int c) { return Z0 + c * Y_SZ; }

__global__ __launch_bounds__(512, 2) void gm_kernel(const u16* __restrict__ x, const u16* __restrict__ logA, const u16* __restrict__ Aim, const u16* __restrict__ Cre,
                                                    const u16* __restrict__ Cim, const u16* __restrict__ logdt, const u16* __restrict__ Dsk, const u16* __restrict__ win,
                                                    const u16* __restrict__ wsp, const u16* __restrict__ bsp, const u16* __restrict__ wout, const u16* __restrict__ gw,
                                                    u16* __restrict__ out, unsigned char* wsp_) {
    extern __shared__ __attribute__((aligned(16))) unsigned char lds_[];
    LAS unsigned char* lds = (LAS unsigned char*)lds_;
    const int wid = __builtin_amdgcn_readfirstlane(threadIdx.x >> 6), lane = threadIdx.x & 63;
    const int bid = blockIdx.x, xg = bid & 7, rr = bid >> 3, bi = rr >> 2, bj = rr & 3;
    const int pm = (xg >> 2) * 8 + bi, pn = (xg & 3) * 4 + bj;
    const int qm = 2 * pm + (pn >> 3), qn = pn & 7;
    const int nch = pm < 4 ? 3 : 2;
    const rsrc_t ws = __builtin_amdgcn_make_buffer_rsrc(wsp_, 0, WS_END, 0x00020000);
    STAMP(0);
    const u64 dg0 = __builtin_amdgcn_s_memrealtime(); u64 dgA = dg0, dgB = dg0;
#define MARK(i) do { if (GM_DA == (i)) dgA = __builtin_amdgcn_s_memrealtime(); if (GM_DB == (i)) dgB = __builtin_amdgcn_s_memrealtime(); } while (0)
    u64 pc0; asm volatile("s_getpc_b64 %0" : "=s"(pc0));

    const int fr = lane & 15, fq = lane >> 4;
    const int h = wid - 6, p = wid - 4;
    const bool is_prod = (wid >> 1) == 2;
    const int ch0 = pn * 36 + pm;
    float bs[4];
    auto late = [&]() {
#pragma unroll
        for (int m = 0; m < 4; ++m) bs[m] = __uint_as_float((unsigned)bsp[pm * 128 + (wid & 1) * 64 + m * 16 + fr]);
    };
    const int phi = (-(bi + bj)) & 3;
    const unsigned rsw = (unsigned)(((lane & 7) ^ ((4 * p + (lane >> 4)) & 7)) * 16);
#define GLDS(src, dst, aux) __builtin_amdgcn_global_load_lds((const unsigned*)(src), (LAS unsigned*)(dst), 16, 0, aux)
    auto issueS = [&](int k, int s) {
        const char* sb = (const char*)wsp + (size_t)(pm * 128 + p * 8) * 4096 + k * 128; const unsigned vs = (unsigned)(lane >> 3) * 4096u + rsw;
#pragma unroll
        for (int i = 0; i < 8; ++i) GLDS(sb + (size_t)i * (16 * 4096) + vs, lds + s + (p + 2 * i) * 1024, 0); };
    auto ord2 = [&](int s) { return GM_LAG == 0 ? s : (((s & 3) == phi) ? s : ((s - GM_LAG) & 31)); };
    u32x2 E, T;
    auto mk_tag = [&]() { u64 z = (((u64)__builtin_amdgcn_readfirstlane(E[1]) << 32) | __builtin_amdgcn_readfirstlane(E[0])) + 0x9E3779B97F4A7C15ull;
        z = (z ^ (z >> 30)) * 0xBF58476D1CE4E5B9ull; z = (z ^ (z >> 27)) * 0x94D049BB133111EBull; z ^= z >> 31; T = (u32x2){(unsigned)z, (unsigned)(z >> 32)}; };
    u32x2 zp[5][4];
#define STEP(lim) do { if (nb < (lim)) { BAR(); ++nb; } } while (0)
#define STEPS_TO(lim) _Pragma("nounroll") while (nb < (lim)) { BAR(); ++nb; }
    int nb = 0;
    if (wid < nch) {
        const int kch = ch0 + 16 * wid, ki = kch * 64 + lane;
        const unsigned pA = ((const unsigned*)logA)[ki], pI = ((const unsigned*)Aim)[ki], pR = ((const unsigned*)Cre)[ki], pC = ((const unsigned*)Cim)[ki], pT = logdt[kch];
        LAS u32x2* tw = opq((LAS u32x2*)(lds + Z0 + wid * 8448) + lane);
        const LAS unsigned char* trd = opq(lds + Z0 + wid * 8448 + fr * 528 + fq * 16);
        const float dt = rbf(__expf(bf2f(pT)));
        const f32x2 ar = (f32x2){rbf(__expf(bflo(pA))), rbf(__expf(bfhi(pA)))} * dt * -1.44269504f, ai = (f32x2){bflo(pI), bfhi(pI)} * dt * 0.15915494f;
        const f32x2 cr = (f32x2){bflo(pR), bfhi(pR)} * dt, ci = (f32x2){bflo(pC), bfhi(pC)} * dt;
        float cm = fmaxf(fmaxf(fabsf(cr.x), fabsf(ci.x)), fmaxf(fabsf(cr.y), fabsf(ci.y)));
#define DPPF(v, ctl) __int_as_float(__builtin_amdgcn_update_dpp(0, __float_as_int(v), ctl, 0xf, 0xf, true))
        cm = fmaxf(cm, DPPF(cm, 0x121)); cm = fmaxf(cm, DPPF(cm, 0x122)); cm = fmaxf(cm, DPPF(cm, 0x124)); cm = fmaxf(cm, DPPF(cm, 0x128));
#undef DPPF
        { auto r = __builtin_amdgcn_permlane16_swap(__float_as_uint(cm), __float_as_uint(cm), false, false); cm = fmaxf(__uint_as_float(r[0]), __uint_as_float(r[1]));
          auto q = __builtin_amdgcn_permlane32_swap(__float_as_uint(cm), __float_as_uint(cm), false, false); cm = fmaxf(fmaxf(__uint_as_float(q[0]), __uint_as_float(q[1])), 1e-30f); }
        struct C2 { f32x2 x, y; };
        auto cexp = [&](float p_) { const f32x2 e = ar * p_, t = ai * p_;
            const f32x2 mg = {__builtin_amdgcn_exp2f(e.x), __builtin_amdgcn_exp2f(e.y)}; const float t0 = __builtin_amdgcn_fractf(t.x), t1 = __builtin_amdgcn_fractf(t.y);
            return C2{mg * (f32x2){__builtin_amdgcn_cosf(t0), __builtin_amdgcn_cosf(t1)}, mg * (f32x2){__builtin_amdgcn_sinf(t0), __builtin_amdgcn_sinf(t1)}}; };
        auto cmul = [](C2 a, C2 b) { return C2{__builtin_elementwise_fma(a.x, b.x, -(a.y * b.y)), __builtin_elementwise_fma(a.x, b.y, a.y * b.x)}; };
        auto put = [&](int row, C2 v, bool conj) { tw[row * 66] = conj ? (u32x2){pk_f16(v.x.x, -v.y.x), pk_f16(v.x.y, -v.y.y)} : (u32x2){pk_f16(v.x.x, v.y.x), pk_f16(v.x.y, v.y.y)}; };
        C2 wp[16], zr[8], vp[4], cv[17];
        wp[1] = cexp(8.f); zr[1] = cexp(1.f); vp[0] = cexp(128.f);
        tw[0] = (u32x2){pk_f16(1.f, 0.f), pk_f16(1.f, 0.f)};
#pragma unroll
        for (int j = 1; j < 16; ++j) { if (j > 1) wp[j] = cmul(wp[j >> 1], wp[j - (j >> 1)]); put(j, wp[j], true); }
        CFENCE();
        u32x4 fb[8];
#pragma unroll
        for (int ks = 0; ks < 8; ++ks) fb[ks] = *(const LAS u32x4*)(trd + ks * 64);
        CFENCE();
#pragma unroll
        for (int r = 2; r < 8; ++r) zr[r] = cmul(zr[r >> 1], zr[r - (r >> 1)]);
#pragma unroll
        for (int k = 1; k < 4; ++k) vp[k] = cmul(vp[k - 1], vp[k - 1]);
        const float rc = __builtin_amdgcn_rcpf(cm);
        cv[0] = C2{cr * rc, ci * rc};
#pragma unroll
        for (int k = 1; k < 8; ++k) { const int lg = k >= 4 ? 2 : k >= 2 ? 1 : 0; cv[k] = cmul(cv[k - (1 << lg)], vp[lg]); }
        LAS float* kb_ = (LAS float*)(lds + L_KD + wid * 3072);
        const float sc = 2.0f * cm;
        u32x4 fa[3][8];
#pragma unroll
        for (int tt = 0; tt < 3; ++tt) {
#pragma unroll
            for (int i = 0; i < (tt == 2 ? 9 : 16); ++i) {
                const int r = tt == 0 ? (i & 7) : tt == 1 ? (i < 8 ? 2 * (i & 3) : 4 * (i & 1)) : 0;
                const int hh = tt == 0 ? (i >> 3) : tt == 1 ? (i < 8 ? 2 + (i >> 2) : 4 + ((i - 8) >> 1)) : 8 + i;
                if (tt == 2) cv[hh] = cmul(cv[hh - 8], vp[3]);
                put(i, r == 0 ? cv[hh] : cmul(cv[hh], zr[r]), false);
            }
            CFENCE();
#pragma unroll
            for (int ks = 0; ks < 8; ++ks) fa[tt][ks] = *(const LAS u32x4*)(trd + ks * 64);
            CFENCE();
            SB0();
        }
        f32x4 kd[3] = {{0.f, 0.f, 0.f, 0.f}, {0.f, 0.f, 0.f, 0.f}, {0.f, 0.f, 0.f, 0.f}};
#pragma unroll
        for (int ks = 0; ks < 8; ++ks)
#pragma unroll
            for (int tt = 0; tt < 3; ++tt) kd[tt] = __builtin_amdgcn_mfma_f32_16x16x32_f16(__builtin_bit_cast(f16x8, fa[tt][ks]), __builtin_bit_cast(f16x8, fb[ks]), kd[tt], 0, 0, 0);
#pragma unroll
        for (int tt = 0; tt < 3; ++tt) {
            LAS float* kw = opq(kb_ + (tt == 0 ? 128 * (fq >> 1) + 4 * (fq & 1) + 8 * fr : tt == 1 ? 256 + 64 * fq + (fq < 2 ? 4 : 2) * fr : 512 + 64 * fq + fr));
            LAS float* kwb = tt == 1 ? opq(kw + (fq < 2 ? 2 : 32)) : kw + (tt == 0 ? 2 : 32);
            kw[0] = kd[tt][0] * sc; kw[tt == 2 ? 16 : 1] = kd[tt][1] * sc; kwb[0] = kd[tt][2] * sc; kwb[tt == 2 ? 16 : 1] = kd[tt][3] * sc;
        }
        WAITL0();
    }
    if (wid == 3) {
        unsigned w_ = 0;
        if (lane < GM_PFL) w_ = *(const __attribute__((address_space(1))) unsigned*)((pc0 & ~127ull) + (unsigned)min((rr * GM_PFL + lane) * 128, GM_CODE));
        asm volatile("" :: "v"(w_));
    }
#define IC(n) std::integral_constant<int, n>{}
    if (wid < 6) STAMP(10);
    if (is_prod) {
        const unsigned vx = (unsigned)(lane >> 3) * 1152u + rsw;
        unsigned wv[9];
#pragma unroll
        for (int i = 0; i < 9; ++i) { const int c = (p + 2 * i) * 64 + lane, rho = c / 18, ch = c - rho * 18;
            const int kap = (rho & 32) + 8 * ((rho >> 2) & 3) + 4 * ((rho >> 4) & 1) + (rho & 3); wv[i] = (unsigned)kap * 9216u + (unsigned)ch * 16u; }
        const char* xb = (const char*)x + (size_t)(pm * 128 + p * 8) * 1152;
        const char* wb = (const char*)win + pn * 288;
        auto issueX = [&](int k, int s) {
#pragma unroll
            for (int i = 0; i < 8; ++i) GLDS(xb + (size_t)i * (16 * 1152) + k * 128 + vx, lds + s + (p + 2 * i) * 1024, 0); };
        auto issueW = [&](int k, int coloff, int s) {
            const char* b = wb + (size_t)k * (64 * 9216) + coloff;
#pragma unroll
            for (int i = 0; i < 9; ++i) GLDS(b + wv[i], lds + s + BOFF + (p + 2 * i) * 1024, 0); };
        auto ord1 = [&](int s) { return (GM_LAG == 0 || (s & 3) == phi) ? s : (s >= 4 ? s - 4 : (s == 0 ? 8 : s + 4)); };
        issueX(ord1(0), 0); issueW(ord1(0), FH * 2, 0); issueX(ord1(1), STG); issueW(ord1(1), FH * 2, STG);
        issueX(ord1(2), 2 * STG); issueW(ord1(2), FH * 2, 2 * STG);
        E = flag_ld(ws, WS_E);
        unsigned wm_ = 0;
        auto touch = [&](const void* base, unsigned off, unsigned last) { const char* q_ = (const char*)base + min(off + lane * 64u, last); asm volatile("global_load_dword %0, %1, off" : "+v"(wm_) : "v"(q_) : "memory"); };
        if (GM_PW & 1) {
#pragma unroll
            for (int i = 0; i < 2; ++i) touch(x, bid * 9216u + p * 4608u + i * 4096u, bid * 9216u + p * 4608u + 4544u);
#pragma unroll
            for (int i = 0; i < 3; ++i) touch(win, bid * 20736u + p * 10368u + i * 4096u, bid * 20736u + p * 10368u + 10304u);
        }
        int so = 0;
        WAITV(35); PBAR();
#pragma nounroll
        for (int S = 1; S < 16; ++S) {
            if (S >= 8 && S <= 10) WAITV(9); else WAITV(17);
            PBAR(); if (S == 9) MARK(2);
            const int t = S + 2, k = ord1(t < 9 ? t : t < 12 ? t - 3 : t - 12);
            if (t < 9 || t >= 12) issueX(k, so);
            issueW(k, t >= 9 ? 0 : FH * 2, so); so = nextbuf(so);
            if ((GM_PW & 2) && S == 8) {
#pragma unroll
                for (int i = 0; i < 4; ++i) touch(wsp, bid * 32768u + p * 16384u + i * 4096u, 0x7fffffffu);
            }
        }
        WAITV(17); PBAR(); issueS(ord2(0), 0);
        WAITV(8); PBAR(); issueS(ord2(1), STG);
        asm volatile("" :: "v"(wm_));
        STAMP(1);
    } else if (wid < 4) {
        E = flag_ld(ws, WS_E);
        if (wid >> 1) consumer_a<4>(lds, zp, late, wid & 1, 1, lane, wsp_ TRA);
        else consumer_a<5>(lds, zp, late, wid & 1, 0, lane, wsp_ TRA);
    } else {
        if (h == 0 && lane < 12) *(LAS unsigned*)(lds + L_MISC + lane * 4) = 0u;
        const int row0 = lane / 9, chk0 = lane - row0 * 9;
        const int u = (pn >> 3) * 16 + pm, nblk = u < 8 ? 3 : 2;
        u32x2 uc[18]; u32x4 xv[9], gv[9], wv[3][5];
        if (h == 0) {
            { const char* xb = (const char*)x + (size_t)pm * 128 * 1152 + pn * 72; unsigned off = row0 * 1152 + chk0 * 8; int ch = chk0;
#pragma unroll
              for (int i = 0; i < 9; ++i) { uc[i] = *(const u32x2*)(xb + off); uc[9 + i] = *(const u32x2*)(xb + 64 * 1152 + off); off += 7 * 1152 + 8; if (++ch == 9) { ch = 0; off += 1152 - 72; } } }
            { const char* xr = (const char*)x + (size_t)(pm * 128 + pn * 8 + (lane >> 3)) * 1152 + (lane & 7) * 16; const char* gr = (const char*)gw + (lane & 7) * 16;
#pragma unroll
              for (int i = 0; i < 9; ++i) { xv[i] = *(const u32x4*)(xr + i * 128); gv[i] = *(const u32x4*)(gr + i * 128); } }
        } else {
            unsigned wo_[5], hi_[5];
            { int row = row0, ch = chk0;
#pragma unroll
              for (int i = 0; i < 5; ++i) { const int rw = row < 32 ? row : 31; wo_[i] = (rw & 15) * 1152 + ch * 16; hi_[i] = rw >> 4; row += 7; if (++ch == 9) { ch = 0; ++row; } } }
#pragma unroll
            for (int b = 0; b < 3; ++b) {
                const int blk = (b < nblk) ? u + 32 * b : u, st = blk >> 2, jb = blk & 3;
                const int fA = st < 16 ? st * 144 + (jb < 2 ? 32 * jb : 16 + 32 * jb) : ((st - 16) * 8 + 2 * jb) * 144 + 64, dB = (st < 16 ? 16 : 144) * 1152;
                const char* wb_ = (const char*)wout + (size_t)fA * 1152 + qn * 144;
#pragma unroll
                for (int i = 0; i < 5; ++i) wv[b][i] = *(const u32x4*)(wb_ + (wo_[i] + hi_[i] * dB));
            }
        }
        E = flag_ld(ws, WS_E);
        if (lane < 24) *(LAS u32x4*)(lds + L_KT + (lane >> 3) * KT_SZ + (2 * h + ((lane >> 2) & 1)) * 1088 + (lane & 3) * 16) = (u32x4){0u, 0u, 0u, 0u};
        WAITL0();
        STAMP(10);
        __builtin_amdgcn_s_setprio(GM_HPRIO);
        {
            float kv[2][3];
#pragma nounroll
            for (int ei = 0; ei < 9; ++ei) {
                BAR();
                if (ei > 0) {
                    LAS unsigned char* kt = opq(lds + L_KT + (2 * h + ((ei - 1) >> 2)) * 1088 + 64 + ((ei - 1) & 3) * 256 + lane * 2);
#pragma unroll
                    for (int j = 0; j < 2; ++j)
#pragma unroll
                        for (int c = 0; c < 3; ++c) *(LAS u16*)(kt + c * KT_SZ + j * 128) = (u16)pk_bf16(kv[j][c], 0.f);
                }
#pragma unroll
                for (int j = 0; j < 2; ++j) {
                    const int jj = (ei & 3) * 2 + j, e = jj >= 4 ? 3 : jj >= 2 ? 2 : jj, lag = 256 * jj + 4 * lane + 2 * h + ((ei >> 2) & 1);
                    const int idx = e == 0 ? lag : 128 * e + ((lag + (1 << (e - 1)) - 1 + ((lag >> e) & 1)) >> e);
                    const LAS float* q = opq((const LAS float*)(lds + L_KD) + idx);
#pragma unroll
                    for (int c = 0; c < 3; ++c) kv[j][c] = q[c * 768];
                }
            }
        }
        if (h == 0) {
            LAS unsigned char* pzs = opq(lds + Z0 + lane * 8);
#pragma unroll
            for (int i = 0; i < 18; ++i) *(LAS u32x2*)(pzs + i * 512) = uc[i];
            CFENCE();
            { const LAS unsigned char* pt = opq(lds + Z0 + (8 * (4 * fq + (lane & 3)) + (fr >> 2)) * 72);
              const unsigned ub = WS_UT + (unsigned)(pn * 36 + (lane & 3)) * 4096u + (unsigned)(pm * 128 + 8 * (4 * fq + (fr >> 2))) * 2u;
#pragma unroll
              for (int cc = 0; cc < 9; ++cc) __builtin_amdgcn_raw_buffer_store_b128(__builtin_bit_cast(u32x4, cat8(tr_read(pt + cc * 8), tr_read(pt + 288 + cc * 8))), ws, ub + cc * 16384u, 0, 16); }
            float a = 0.f;
#pragma unroll
            for (int i = 0; i < 9; ++i)
#pragma unroll
                for (int j = 0; j < 4; ++j) { const unsigned xa = xv[i][j], ga = gv[i][j]; a = __builtin_amdgcn_fdot2_f32_bf16(__builtin_bit_cast(bf16x2, xa), __builtin_bit_cast(bf16x2, ga), a, false); }
#define DPPF(v, ctl) __int_as_float(__builtin_amdgcn_update_dpp(0, __float_as_int(v), ctl, 0xf, 0xf, true))
            a += DPPF(a, 0xb1); a += DPPF(a, 0x4e); a += DPPF(a, 0x141);
#undef DPPF
            const float gt = rbf(__builtin_amdgcn_rcpf(1.0f + __expf(-rbf(a))));
            if ((lane & 7) == 0) __builtin_amdgcn_raw_buffer_store_b32(__float_as_uint(gt), ws, WS_GATE + (unsigned)(pm * 128 + pn * 8 + (lane >> 3)) * 4u, 0, 16);
        } else {
            LAS unsigned char* pws = opq(lds + Z0 + 9216 + lane * 16);
            const LAS unsigned char* ptw = opq(lds + Z0 + 9216 + (4 * fq + (fr >> 2)) * 144 + (lane & 3) * 8);
#pragma unroll
            for (int b = 0; b < 3; ++b) {
                if (b < nblk) {
#pragma unroll
                    for (int i = 0; i < 5; ++i) if (i < 4 || lane < 32) *(LAS u32x4*)(pws + i * 1024) = wv[b][i];
                    CFENCE();
                    const unsigned ob = WS_WOF + (unsigned)qn * (18u * 18432u) + (unsigned)(u + 32 * b) * 4608u;
#pragma unroll
                    for (int nn = 0; nn < 5; ++nn) {
                        const u32x4 v = __builtin_bit_cast(u32x4, cat8(tr_read(ptw + nn * 32), tr_read(ptw + 2304 + nn * 32)));
                        if (nn < 4) __builtin_amdgcn_raw_buffer_store_b128(v, ws, ob + nn * 1024 + lane * 16, 0, 16);
                        else if (fr < 8) __builtin_amdgcn_raw_buffer_store_b128(v, ws, ob + 4096 + (fq * 8 + fr) * 16, 0, 16);
                    }
                    CFENCE();
                }
            }
        }
        nb = 9;
        mk_tag();
        WAITL0();
        STEPS_TO(10);
        STAMP(1);
        {
            const f32x4 sp = *(const LAS f32x4*)(lds + L_SP + (h * 64 + lane) * 16);
            const float dm = sp[0] - sp[2];
            const f32x2 mine = {(80.f * sp[0] + 64.f * sp[2]) * (1.0f / 144.f), sp[1] + sp[3] + (80.f * 64.f / 144.f) * dm * dm};
            __builtin_amdgcn_raw_buffer_store_b128((u32x4){T[0], __float_as_uint(mine.x), T[1], __float_as_uint(mine.y)}, ws, WS_ST + (unsigned)((pm * 16 + pn) * 128 + h * 64 + lane) * 16u, 0, 16);
            STEP(18);
            const unsigned so_ = WS_ST + (unsigned)(pm * 16 * 128 + h * 64 + lane) * 16u;
            const u64 t0 = __builtin_amdgcn_s_memrealtime();
            u32x4 st[16];
            for (;;) {
                CFENCE();
#pragma unroll
                for (int j = 0; j < 16; ++j) st[j] = __builtin_amdgcn_raw_buffer_load_b128(ws, so_ + j * 2048u, 0, 16);
                STEP(18);
                unsigned bad = 0;
#pragma unroll
                for (int j = 0; j < 16; ++j) bad |= (st[j][0] ^ T[0]) | (st[j][2] ^ T[1]);
                if (__builtin_amdgcn_ballot_w64(bad != 0u) == 0ull) break;
                if (__builtin_amdgcn_s_memrealtime() - t0 > 300000ull) break;
            }
            STAMP(2);
            STEP(18);
            float mean = 0.f, m2 = 0.f;
#pragma unroll
            for (int j = 0; j < 16; ++j) mean += __uint_as_float(st[j][1]);
            mean *= (1.0f / 16.f);
#pragma unroll
            for (int j = 0; j < 16; ++j) { const float d = __uint_as_float(st[j][1]) - mean; m2 += __uint_as_float(st[j][3]) + 144.f * d * d; }
            const float rstd = __builtin_amdgcn_rsqf(m2 * (1.0f / FH) + 1e-5f);
            *(LAS f32x2*)(lds + L_SP + (h * 64 + lane) * 16) = (f32x2){rstd, -mean * rstd};
            WAITL0();
            if (lane == 0) *(volatile LAS unsigned*)(lds + L_MISC + 4 + h * 4) = 1u;
            const volatile LAS unsigned* zd = (const volatile LAS unsigned*)(lds + L_MISC + 16 + h * 8 + (lane & 1) * 4);
            for (;;) {
                STEP(18);
                if (__builtin_amdgcn_ballot_w64(*zd == 0u) == 0ull) break;
                if (__builtin_amdgcn_s_memrealtime() - t0 > 800000ull) break;
            }
            CFENCE();
            const LAS unsigned char* ptz = opq(lds + Z0 + (h * 64 + 8 * fq + (fr >> 2)) * ZST + (lane & 3) * 8);
            const unsigned zo = WS_Z2F + (unsigned)((pn * 64 + pm * 4 + h * 2) * 9) * 1024u + lane * 16;
#pragma unroll
            for (int i = 0; i < 18; ++i) {
                const LAS unsigned char* pt = ptz + (i / 9) * 32 * ZST + (i % 9) * 32;
                __builtin_amdgcn_raw_buffer_store_b128(__builtin_bit_cast(u32x4, cat8(tr_read(pt), tr_read(pt + 4 * ZST))), ws, zo + i * 1024, 0, 16);
                if (i == 5 || i == 11) STEP(18);
            }
            STEP(18);
            flag_publish(ws, WS_FZ + (unsigned)(pn * 32 + pm * 2 + h) * 128u, T, lane);
            STAMP(3);
            STEPS_TO(18);
            __builtin_amdgcn_s_setprio(0);
        }
    }

    if (is_prod) {
        mk_tag();
        const char* lin = (const char*)wsp_ + p * 1024 + lane * 16;
        auto issueLA = [&](unsigned off, int s) {
#pragma unroll
            for (int i = 0; i < 8; ++i) GLDS(lin + off + i * 2048, lds + s + (p + 2 * i) * 1024, 16); };
        auto issueLB = [&](unsigned off, int s) {
#pragma unroll
            for (int i = 0; i < 9; ++i) GLDS(lin + off + i * 2048, lds + s + BOFF + (p + 2 * i) * 1024, 16); };
        auto ord3 = [&](int s) { return (GM_LAG == 0 || s >= 16) ? s : (((s & 3) == phi) ? s : ((s - GM_LAG) & 15)); };
        u32x2 pv = {0u, 0u}; unsigned rdy = 0; bool pend = false;
        const char* fp = (const char*)wsp_ + WS_FZ + ((unsigned)pn * 32u + (lane & 31)) * 128u;
        auto poll_issue = [&]() { asm volatile("global_load_dwordx2 %0, %1, off sc1" : "+v"(pv) : "v"(fp) : "memory"); };
        auto poll_take = [&]() { asm volatile("" : "+v"(pv)); rdy |= (unsigned)__builtin_amdgcn_ballot_w64(pv[0] == T[0] && pv[1] == T[1]); };
        auto need = [&](unsigned bits) {
            if (pend) { poll_take(); pend = false; }
            if ((rdy & bits) != bits) {
                const u64 t0 = __builtin_amdgcn_s_memrealtime();
                do { poll_issue(); WAITV(0); poll_take(); } while ((rdy & bits) != bits && __builtin_amdgcn_s_memrealtime() - t0 < 300000ull);
            }
            if (rdy != ~0u) { poll_issue(); pend = true; }
        };
        const unsigned zb = WS_Z2F + (unsigned)pn * (64u * 9216u);
        need((1u << ord2(0)) | (1u << ord2(1)));
        STAMP(2);
        issueLB(zb + (unsigned)ord2(0) * 18432u, 0); issueLB(zb + (unsigned)ord2(1) * 18432u, STG);
        int so = 2 * STG;
        WAITV(9);
#pragma nounroll
        for (int S = 18; S < 48; ++S) {
            PBAR(); if (S == 18) MARK(3);
            const int k = ord2(S - 16); need(1u << k); issueS(k, so); issueLB(zb + (unsigned)k * 18432u, so); so = nextbuf(so);
            WAITV(17);
        }
        const unsigned hb = WS_HF + (unsigned)qm * (18u * 16384u), ob = WS_WOF + (unsigned)qn * (18u * 18432u);
        PBAR();
        fp = (const char*)wsp_ + WS_FZ + ((unsigned)(pn ^ 8) * 32u + (lane & 31)) * 128u; rdy = 0; pend = false;
        need(~0u);
        issueLB(ob + (unsigned)ord3(0) * 18432u, 2 * STG);
        PBAR(); MARK(4);
        issueLB(ob + (unsigned)ord3(1) * 18432u, 0);
        STAMP(3);
        fp = (const char*)wsp_ + WS_FH + ((unsigned)qm * 32u + (lane & 31)) * 128u; rdy = 0;
        need(~0u);
        STAMP(4);
        issueLA(hb + (unsigned)ord3(0) * 16384u, 2 * STG); issueLA(hb + (unsigned)ord3(1) * 16384u, 0);
        so = STG;
        WAITV(8);
#pragma nounroll
        for (int S = 50; S < 66; ++S) {
            PBAR(); if (S == 50) MARK(5);
            const int k = ord3(S - 48); issueLA(hb + (unsigned)k * 16384u, so); issueLB(ob + (unsigned)k * 18432u, so); so = nextbuf(so);
            WAITV(17);
        }
        PBAR();
        WAITV(0); PBAR();
        PBAR(); MARK(6);
        { const u64 now = __builtin_amdgcn_s_memrealtime(); const u64 until = now + (dgB - dgA) + 1000ull;
          while (__builtin_amdgcn_s_memrealtime() < until) __builtin_amdgcn_s_sleep(1); }
        STAMP(5); PDUMP();
        if (bid == 0 && p == 0 && lane == 0) __builtin_amdgcn_raw_buffer_store_b64(T, ws, WS_E, 0, 16);
        return;
    }

    if (wid < 4) {
        mk_tag();
        if (wid >> 1) consumer_b<4>(lds, ws, T, zp, bs, pm, pn, wid & 1, 1, lane, wsp_ TRA);
        else consumer_b<5>(lds, ws, T, zp, bs, pm, pn, wid & 1, 0, lane, wsp_ TRA);
    } else {
        __builtin_amdgcn_s_setprio(GM_HPRIO);
        {   const unsigned fo = WS_FZ + (unsigned)(pn * 32 + (lane & 31)) * 128u;
            const u64 t0 = __builtin_amdgcn_s_memrealtime();
            for (;;) {
                STEP(44);
                CFENCE();
                const u32x2 v = flag_ld(ws, fo);
                STEP(44);
                if (flag_all(v, T, true)) break;
                if (__builtin_amdgcn_s_memrealtime() - t0 > 300000ull) break;
            } }
        u32x4 uu[3][2];
#pragma unroll
        for (int c = 0; c < 3; ++c)
#pragma unroll
            for (int j = 0; j < 2; ++j) uu[c][j] = __builtin_amdgcn_raw_buffer_load_b128(ws, WS_UT + (unsigned)(ch0 + (c < nch ? 16 * c : 0)) * 4096u + h * 2048 + j * 1024 + lane * 16, 0, 16);
#pragma unroll
        for (int c = 0; c < 3; ++c) if (lane < 16) *(LAS u32x4*)(lds + L_UT + c * UT_SZ + h * 4352 + lane * 16) = (u32x4){0u, 0u, 0u, 0u};
        BAR(); BAR(); BAR(); nb += 3;
        LAS unsigned char* pu_ = opq(lds + L_UT + 256 + h * 2048 + lane * 16);
#pragma unroll
        for (int c = 0; c < 3; ++c)
#pragma unroll
            for (int j = 0; j < 2; ++j) *(LAS u32x4*)(pu_ + c * UT_SZ + j * 1024) = uu[c][j];
        WAITL0();
        if (lane == 0) *(volatile LAS unsigned*)(lds + L_MISC + 32 + h * 4) = 1u;
        {   const volatile LAS unsigned* uw = (const volatile LAS unsigned*)(lds + L_MISC + 32 + (lane & 1) * 4);
            const u64 t0 = __builtin_amdgcn_s_memrealtime();
            do { STEP(50); } while (__builtin_amdgcn_ballot_w64(*uw == 0u) != 0ull && __builtin_amdgcn_s_memrealtime() - t0 < 800000ull);
            CFENCE(); }
        STAMP(4);
    }

    if (wid < 4) nb = 50;
    for (;;) {
        if (nb > 45) STEPS_TO(50);
        unsigned tk = 0;
        if (lane == 0) tk = __hip_atomic_fetch_add((LAS unsigned*)(lds + L_MISC), 1u, __ATOMIC_RELAXED, __HIP_MEMORY_SCOPE_WORKGROUP);
        tk = __builtin_amdgcn_readfirstlane(tk);
        if (tk >= (unsigned)(4 * nch)) break;
        const int c = tk >> 2, tl = tk & 1;
        if (tk & 2) conv_task<0, 10>(lds, L_KT + c * KT_SZ, L_UT + c * UT_SZ, ytab(c), tl, lane, nb TRA);
        else conv_task<11, 15>(lds, L_KT + c * KT_SZ, L_UT + c * UT_SZ, ytab(c), tl, lane, nb TRA);
    }
    STEPS_TO(50);
    WAITL0();
    STAMP(7);

    if (wid < 4) {
        const int kq = wid >> 1, mh = wid & 1;
        f32x4 acc[5][2];
        struct F3 { s16x4 alo[4], ahi[4]; bf16x8 b[10]; } f0, f1;
        const LAS unsigned char* ph = lds + 2 * STG + mh * 8192 + kq * 2048 + lane * 16;
        const LAS unsigned char* pw = lds + 2 * STG + BOFF + kq * 9216 + lane * 16;
        const LAS unsigned char* pw4 = lds + 2 * STG + BOFF + kq * 9216 + 4096 + (fq * 8 + (fr & 7)) * 16;
        auto rd3 = [&](F3& f, int i, bool single) {
            const int j = i / 7, r = i % 7;
            if (r < 2) {
                if (!single) { const bf16x8 v = *(const LAS bf16x8*)(ph + r * 4096 + j * 1024); f.alo[j * 2 + r] = __builtin_shufflevector(v, v, 0, 1, 2, 3); f.ahi[j * 2 + r] = __builtin_shufflevector(v, v, 4, 5, 6, 7); }
                else { const LAS unsigned char* q = ph - lane * 8 + r * 4096 + j * 1024; f.alo[j * 2 + r] = *(const LAS s16x4*)q; f.ahi[j * 2 + r] = *(const LAS s16x4*)(q + 512); }
            } else if (r < 6) f.b[j * 5 + r - 2] = *(const LAS bf16x8*)(pw + j * 4608 + (r - 2) * 1024);
            else f.b[j * 5 + 4] = *(const LAS bf16x8*)(pw4 + j * 4608);
        };
        int so = 2 * STG;
        auto step3 = [&](const F3& c, F3& l, int mode  , bool first) {
            const int delta = so == 2 * STG ? -2 * STG : STG; so += delta;
#pragma unroll
            for (int i = 0; i < 20; ++i) {
                const int j = i / 10, n = (i % 10) >> 1, m = i & 1;
                acc[n][m] = MFMA16(c.b[j * 5 + n], cat8(c.alo[j * 2 + m], c.ahi[j * 2 + m]), (first && j == 0) ? ((f32x4){0.f, 0.f, 0.f, 0.f}) : acc[n][m]);
                if (mode < 2 && i < 7) { rd3(l, 2 * i, mode == 1); rd3(l, 2 * i + 1, mode == 1); }
                if (mode < 2) { if (i == 8) { ph += delta; asm volatile("" : "+v"(ph)); } if (i == 9) { pw += delta; asm volatile("" : "+v"(pw)); } if (i == 10) { pw4 += delta; asm volatile("" : "+v"(pw4)); } }
                SB0();
            }
        };
        __builtin_amdgcn_s_waitcnt(0xC07F);
        BAR();
#pragma unroll
        for (int i = 0; i < 14; ++i) rd3(f0, i, false);
        ph -= 2 * STG; pw -= 2 * STG; pw4 -= 2 * STG; so = 0;
        WAITL0(); BAR(); step3(f0, f1, 0, true);
#pragma nounroll
        for (int j = 0; j < 7; ++j) {
            WAITL0(); BAR(); step3(f1, f0, 0, false);
            WAITL0(); BAR(); step3(f0, f1, 0, false);
        }
        WAITL0(); BAR(); step3(f1, f0, 1, false);
        WAITL0(); BAR(); step3(f0, f1, 1, false);
        step3(f1, f0, 2, false);
        STAMP(8);
        LAS unsigned char* prw = opq(lds + L_R + wid * 5120 + lane * 16);
#pragma unroll
        for (int n = 0; n < 5; ++n) { const f32x4 x0 = acc[n][0], x1 = acc[n][1]; *(LAS f32x4*)(prw + n * 1024) = kq ? x0 : x1; acc[n][0] = kq ? x1 : x0; }
        WAITL0();
        BAR();
        const int m = mh * 2 + kq;
        const rsrc_t ors = __builtin_amdgcn_make_buffer_rsrc(out, 0, SEQ * DM * 2, 0x00020000);
        const float g = *(const LAS float*)(lds + L_MISC + 64 + (16 * m + fr) * 4), g1 = rbf(1.0f - g);
        const LAS unsigned char* prr = opq(lds + L_R + (wid ^ 2) * 5120 + lane * 16);
        const LAS unsigned char* pat = opq(lds + L_AT + (4 * fq + (fr >> 2)) * 128 + (16 * m + 4 * (lane & 3)) * 2);
#pragma unroll
        for (int n = 0; n < 5; ++n) {
            const f32x4 b = acc[n][0] + *(const LAS f32x4*)(prr + n * 1024);
            const u32x2 a = __builtin_bit_cast(u32x2, tr_read(pat + n * 2048));
            const u32x2 o = {pk_bf16(g * bflo(a[0]) + g1 * b[0], g * bfhi(a[0]) + g1 * b[1]), pk_bf16(g * bflo(a[1]) + g1 * b[2], g * bfhi(a[1]) + g1 * b[3])};
            if (n < 4 || fq < 2) __builtin_amdgcn_raw_buffer_store_b64(o, ors, (unsigned)((qm * 64 + 16 * m + fr) * 1152 + (qn * 72 + 16 * n + 4 * fq) * 2), 0, GM_OAUX);
        }
    } else {
        int d = pn * 36 + pm;
#pragma nounroll
        for (int c = 0; c < nch; ++c) {
            const float dsk = bf2f(Dsk[d]);
#pragma unroll
            for (int k = 0; k < 2; ++k) {
                BAR(); ++nb;
                const int l0 = h * 1024 + k * 512 + lane * 8;
                f32x4 cv = *(const LAS f32x4*)(lds + ytab(c) + 8192 + (l0 >= 16 ? (l0 >> 4) - 1 : 0) * 16);
                if ((lane & 1) || l0 == 0) cv = (f32x4){0.f, 0.f, 0.f, 0.f};
                const f32x4 y0 = *(const LAS f32x4*)(lds + ytab(c) + l0 * 4) + cv, y1 = *(const LAS f32x4*)(lds + ytab(c) + l0 * 4 + 16);
                const u32x4 uv = *(const LAS u32x4*)(lds + L_UT + c * UT_SZ + 256 + l0 * 2);
                u32x4 o;
#pragma unroll
                for (int j = 0; j < 4; ++j) {
                    const float ya = j < 2 ? y0[2 * j] : y1[2 * j - 4], yb = j < 2 ? y0[2 * j + 1] : y1[2 * j - 3];
                    const unsigned yp = pk_bf16(ya, yb), up = pk_bf16(bflo(uv[j]) * dsk, bfhi(uv[j]) * dsk);
                    o[j] = pk_bf16(bflo(yp) + bflo(up), bfhi(yp) + bfhi(up));
                }
                __builtin_amdgcn_raw_buffer_store_b128(o, ws, WS_AT + (unsigned)d * 4096u + l0 * 2, 0, 16);
            }
            d += 16;
        }
        STEPS_TO(58);
        flag_publish(ws, WS_FA + (unsigned)((pn >> 1) * 64 + (pn & 1) * 32 + pm * 2 + h) * 128u, T, lane);
        STAMP(5);
        STEPS_TO(62);
        if (h == 0) {
            const unsigned fo = WS_FA + (unsigned)(qn * 64 + lane) * 128u;
            const u64 t0 = __builtin_amdgcn_s_memrealtime();
            for (;;) {
                CFENCE();
                const u32x2 v = flag_ld(ws, fo);
                STEP(68); STEP(68);
                if (flag_all(v, T, true)) break;
                if (__builtin_amdgcn_s_memrealtime() - t0 > 300000ull) break;
            }
            ACQ();
            u32x4 av[9];
#pragma unroll
            for (int i = 0; i < 9; ++i) av[i] = __builtin_amdgcn_raw_buffer_load_b128(ws, WS_AT + (unsigned)(qn * 72 + i * 8 + (lane >> 3)) * 4096u + qm * 128 + (lane & 7) * 16, 0, 16);
            STEP(68); STEP(68);
            LAS unsigned char* pa_ = opq(lds + L_AT + lane * 16);
#pragma unroll
            for (int i = 0; i < 9; ++i) *(LAS u32x4*)(pa_ + i * 1024) = av[i];
        } else {
            STEPS_TO(65);
            const unsigned gv = __builtin_amdgcn_raw_buffer_load_b32(ws, WS_GATE + (unsigned)(qm * 64 + lane) * 4u, 0, 16);
            BAR(); BAR(); nb += 2;
            *(LAS unsigned*)(lds + L_MISC + 64 + lane * 4) = gv;
        }
        STEPS_TO(68);
        WAITL0();
        BAR();
    }
    STAMP(9); TRDUMP();
}
}

extern "C" void kernel_launch(void* const* d_in, const int* in_sizes, int n_in, void* d_out, int out_size, void* d_ws, size_t ws_size, hipStream_t stream) {
    (void)in_sizes; (void)n_in; (void)out_size; (void)ws_size;
    (void)hipFuncSetAttribute((const void*)gm::gm_kernel, hipFuncAttributeMaxDynamicSharedMemorySize, gm::LDS_BYTES);
    hipLaunchKernelGGL(gm::gm_kernel, dim3(256), dim3(512), gm::LDS_BYTES, stream, (const u16*)d_in[0], (const u16*)d_in[1], (const u16*)d_in[2], (const u16*)d_in[3],
                       (const u16*)d_in[4], (const u16*)d_in[5], (const u16*)d_in[6], (const u16*)d_in[8], (const u16*)d_in[9], (const u16*)d_in[10],
                       (const u16*)d_in[11], (const u16*)d_in[12], (u16*)d_out, (unsigned char*)d_ws);
}
```

```cpp
#define GM_PW 3
#include <hip/hip_runtime.h>
#include <stdint.h>
#include <type_traits>

#define LAS __attribute__((address_space(3)))
typedef unsigned short u16;
typedef unsigned long long u64;
typedef short bf16x8 __attribute__((ext_vector_type(8)));
typedef short s16x4 __attribute__((ext_vector_type(4)));
typedef _Float16 f16x8 __attribute__((ext_vector_type(8)));
typedef _Float16 f16x2 __attribute__((ext_vector_type(2)));
typedef __bf16 bf16x2 __attribute__((ext_vector_type(2)));
typedef float f32x4 __attribute__((ext_vector_type(4)));
typedef float f32x2 __attribute__((ext_vector_type(2)));
typedef unsigned u32x4 __attribute__((ext_vector_type(4)));
typedef unsigned u32x2 __attribute__((ext_vector_type(2)));
typedef __amdgpu_buffer_rsrc_t rsrc_t;

namespace gm {
constexpr int SEQ = 2048, DM = 576, FH = 2304;
constexpr unsigned WS_E = 0, WS_FS = 4096, WS_FZ = WS_FS + 65536, WS_FH = WS_FZ + 65536, WS_FA = WS_FH + 131072;
constexpr unsigned WS_ST = 0x60000, WS_GATE = 0xE0000, WS_DBG = 0x1E00000, WS_Z2F = 0x100000, WS_HF = 0xA00000, WS_WOF = 0x1300000, WS_UT = 0x1600000, WS_AT = 0x1900000, WS_END = 0x1C00000, WS_TRC = 0x1C00000;
constexpr int STG = 34816, BOFF = 16384, Z0 = 3 * STG, ZST = 296, T0 = Z0 + 128 * ZST;
constexpr int KT_SZ = 4352, UT_SZ = 4608, Y_SZ = 10240;
constexpr int L_MISC = 163840 - 320;
constexpr int L_KT = L_MISC - 3 * KT_SZ, L_SP = T0, L_KD = Z0 + 3 * 8448, L_UT = Z0 + 3 * Y_SZ;
constexpr int L_AT = Z0, L_R = Z0 + 10240;
constexpr int LDS_BYTES = 163840;
static_assert(L_UT + 3 * UT_SZ <= L_KT && L_KD + 3 * 3072 <= T0 && L_SP + 2048 <= L_KT, "lds map");
#define GM_HPRIO 1
#define GM_OAUX 0
#define GM_PFL 13
#define GM_CODE 49792
#define GM_ORD 0
#define GM_LAG 4

__device__ __forceinline__ float bf2f(unsigned v) { return __uint_as_float(v << 16); }
__device__ __forceinline__ float bflo(unsigned w) { return __uint_as_float(w << 16); }
__device__ __forceinline__ float bfhi(unsigned w) { return __uint_as_float(w & 0xffff0000u); }
__device__ __forceinline__ unsigned pk_bf16(float a, float b) { f32x2 v = {a, b}; return __builtin_bit_cast(unsigned, __builtin_convertvector(v, bf16x2)); }
__device__ __forceinline__ unsigned pk_f16(float a, float b) { f32x2 v = {a, b}; return __builtin_bit_cast(unsigned, __builtin_convertvector(v, f16x2)); }
__device__ __forceinline__ float rbf(float a) { return bflo(pk_bf16(a, a)); }
__device__ __forceinline__ f32x2 gelu2(f32x2 x) {
    const f32x2 w = (x * x) * (-0.10294324f) + (-2.30220819f);
    const f32x2 a = w * x;
    f32x2 e; e.x = __builtin_amdgcn_exp2f(a.x); e.y = __builtin_amdgcn_exp2f(a.y);
    e = e + 1.0f;
    f32x2 r; r.x = __builtin_amdgcn_rcpf(e.x); r.y = __builtin_amdgcn_rcpf(e.y);
    return x * r;
}
__device__ __forceinline__ f32x4 gelu4(f32x4 v) { const f32x2 a = gelu2((f32x2){v[0], v[1]}), b = gelu2((f32x2){v[2], v[3]}); return (f32x4){a.x, a.y, b.x, b.y}; }
__device__ __forceinline__ float allred_q(float v) {
    unsigned u = __float_as_uint(v);
    auto r = __builtin_amdgcn_permlane16_swap(u, u, false, false);
    v = __uint_as_float(r[0]) + __uint_as_float(r[1]);
    u = __float_as_uint(v);
    auto s = __builtin_amdgcn_permlane32_swap(u, u, false, false);
    return __uint_as_float(s[0]) + __uint_as_float(s[1]);
}
#define ACQ() __builtin_amdgcn_fence(__ATOMIC_ACQUIRE, "wavefront")
#define TRP
#define TRA
#define TRDUMP() do { } while (0)
#define BAR() __builtin_amdgcn_s_barrier()
#define PBAR() BAR()
#define PDUMP() TRDUMP()
#define WAITV(n) asm volatile("s_waitcnt vmcnt(" #n ")" ::: "memory")
#define WAITL0() asm volatile("s_waitcnt lgkmcnt(0)" ::: "memory")
#define CFENCE() asm volatile("" ::: "memory")
#define SGB(mask, n, id) __builtin_amdgcn_sched_group_barrier(mask, n, id)
#define MFMA16(a, b, c) __builtin_amdgcn_mfma_f32_16x16x32_bf16(a, b, c, 0, 0, 0)
__device__ __forceinline__ s16x4 tr_read(const LAS unsigned char* p) { return __builtin_amdgcn_ds_read_tr16_b64_v4i16((LAS s16x4*)p); }
__device__ __forceinline__ bf16x8 cat8(s16x4 a, s16x4 b) { return __builtin_shufflevector(a, b, 0, 1, 2, 3, 4, 5, 6, 7); }
template <class P> __device__ __forceinline__ P opq(P p) { asm volatile("" : "+v"(p)); return p; }
__device__ __forceinline__ int nextbuf(int so) { return so == 2 * STG ? 0 : so + STG; }

#define STAMP(i) do { } while (0)

__device__ __forceinline__ u32x2 flag_ld(rsrc_t ws, unsigned off) { return __builtin_amdgcn_raw_buffer_load_b64(ws, off, 0, 16); }
__device__ __forceinline__ bool flag_all(u32x2 v, u32x2 T, bool active) { return __builtin_amdgcn_ballot_w64(active && (v[0] != T[0] || v[1] != T[1])) == 0ull; }
__device__ __forceinline__ void flag_publish(rsrc_t ws, unsigned off, u32x2 T, int lane) {
    WAITV(0);
    if (lane == 0) __builtin_amdgcn_raw_buffer_store_b64(T, ws, off, 0, 16);
}
__device__ __forceinline__ void flag_wait(rsrc_t ws, unsigned base, int n, u32x2 T, int lane) {
    const unsigned off = base + (unsigned)(lane < n ? lane : 0) * 128u;
    const u64 t0 = __builtin_amdgcn_s_memrealtime();
    for (;;) {
        CFENCE();
        const u32x2 v = flag_ld(ws, off);
        if (flag_all(v, T, true)) break;
        if (__builtin_amdgcn_s_memrealtime() - t0 > 300000ull) break;
        __builtin_amdgcn_s_sleep(1);
    }
    ACQ();
}

#define SB0() __builtin_amdgcn_sched_barrier(0)
template <int NT> struct Frag { bf16x8 a[4]; s16x4 lo[NT], hi[NT]; };
template <int NT, bool TR, int HALF>
__device__ __forceinline__ void frag_read(Frag<NT>& f, int i, const LAS unsigned char* pa, const LAS unsigned char* pb) {
    if (GM_ORD) { constexpr int NB = TR ? 2 * NT : NT; i = i == 0 ? 0 : i <= NB ? i + 3 : i - NB; }
    if (i < 4) f.a[i] = *(const LAS bf16x8*)(pa + i * 2048);
    else if (TR) { const int n = (i - 4) >> 1; if ((i - 4) & 1) f.hi[n] = tr_read(pb + HALF * 9216 + 4608 + n * 32); else f.lo[n] = tr_read(pb + HALF * 9216 + n * 32); }
    else { const int n = i - 4; const bf16x8 v = *(const LAS bf16x8*)(pb + HALF * 9216 + n * 1024);
           f.lo[n] = __builtin_shufflevector(v, v, 0, 1, 2, 3); f.hi[n] = __builtin_shufflevector(v, v, 4, 5, 6, 7); }
}
template <int NT, bool TR, int LHALF, int RPS, bool FIRST, bool UPD>
__device__ __forceinline__ void half_step(f32x4 (&acc)[NT][4], const Frag<NT>& c, Frag<NT>& l, const LAS unsigned char*& pa0, const LAS unsigned char*& pa1, const LAS unsigned char*& pb, int delta, const float* ini = nullptr) {
    constexpr int NRD = TR ? 4 + 2 * NT : 4 + NT, NM = 4 * NT, LAST = (NRD + RPS - 1) / RPS;
#pragma unroll
    for (int i = 0; i < NM; ++i) {
        const int n = GM_ORD ? i % NT : i >> 2, m = GM_ORD ? i / NT : i & 3;
        acc[n][m] = MFMA16(cat8(c.lo[n], c.hi[n]), c.a[m], FIRST ? (TR ? ((f32x4){0.f, 0.f, 0.f, 0.f}) : ((f32x4){ini[m], ini[m], ini[m], ini[m]})) : acc[n][m]);
        if (LHALF >= 0) {
#pragma unroll
            for (int r = 0; r < RPS; ++r) if (i * RPS + r < NRD) frag_read<NT, TR, (LHALF > 0 ? 1 : 0)>(l, i * RPS + r, LHALF > 0 ? pa1 : pa0, pb);
        }
        if (UPD) { if (i == LAST) { pa0 += delta; asm volatile("" : "+v"(pa0)); } if (i == LAST + 1) { pb += delta; asm volatile("" : "+v"(pb)); } if (i == LAST + 2) { pa1 += delta; asm volatile("" : "+v"(pa1)); } }
        SB0();
    }
}
template <int NT, bool TR, class HK>
__device__ __forceinline__ void gemm_phase(int nsteps, f32x4 (&acc)[NT][4], int& so, const LAS unsigned char* pa0, const LAS unsigned char* pa1, const LAS unsigned char* pb,
                                           const float* ini, const volatile LAS unsigned* rdy, HK hook TRP) {
    constexpr int NRD = TR ? 4 + 2 * NT : 4 + NT;
    Frag<NT> f0, f1;
    pa0 += so; pa1 += so; pb += so;
    asm volatile("" : "+v"(pa0), "+v"(pa1), "+v"(pb));
    __builtin_amdgcn_s_waitcnt(0xC07F);
    BAR();
#pragma unroll
    for (int i = 0; i < NRD; ++i) frag_read<NT, TR, 0>(f0, i, pa0, pb);
    SB0();
    int delta = so == 2 * STG ? -2 * STG : STG; so += delta;
    half_step<NT, TR, 1, 2, true, true>(acc, f0, f1, pa0, pa1, pb, delta, ini);
#pragma nounroll
    for (int s = 1; s < nsteps; ++s) {
        WAITL0();
        BAR();
        unsigned rv = 0;
        if (TR) { rv = *rdy; SB0(); }
        delta = so == 2 * STG ? -2 * STG : STG; so += delta;
        half_step<NT, TR, 0, 1, false, false>(acc, f1, f0, pa0, pa1, pb, 0);
        half_step<NT, TR, 1, 2, false, true>(acc, f0, f1, pa0, pa1, pb, delta);
        if (TR) hook(rv);
    }
    half_step<NT, TR, -1, 1, false, false>(acc, f1, f0, pa0, pa1, pb, 0);
}

template <int NT, class LT>
__device__ __forceinline__ void consumer_a(LAS unsigned char* lds, u32x2 (&zp)[5][4], LT late, int wm, int wn, int lane, unsigned char* wsp_ TRP) {
    const int fr = lane & 15, fq = lane >> 4;
    constexpr int N0 = (NT == 5) ? 0 : 5;
    f32x4 acc[NT][4];
    int so = 0;
    const int sw = (fr >> 1) & 7;
    const int a0 = (wm * 64 + fr) * 128 + ((fq ^ sw) << 4), a1 = (wm * 64 + fr) * 128 + (((4 + fq) ^ sw) << 4);
    const int bw = BOFF + (4 * fq + (fr >> 2)) * 288 + (lane & 3) * 8 + N0 * 32;
    const volatile LAS unsigned* rdy = (const volatile LAS unsigned*)(lds + L_MISC + 4 + wm * 4);
    bool done = false;
    auto normalise = [&]() {
        const LAS unsigned char* ps = opq(lds + L_SP + (wm * 64 + fr) * 16);
        LAS unsigned char* pz = opq(lds + Z0 + (wm * 64 + fr) * ZST + N0 * 32 + fq * 8);
#pragma unroll
        for (int m = 0; m < 4; ++m) {
            const f32x2 sr = *(const LAS f32x2*)(ps + m * 256);
#pragma unroll
            for (int n = 0; n < NT; ++n) {
                const f32x2 u = (f32x2){bflo(zp[n][m][0]), bfhi(zp[n][m][0])} * sr.x + sr.y, v = (f32x2){bflo(zp[n][m][1]), bfhi(zp[n][m][1])} * sr.x + sr.y;
                *(LAS u32x2*)(pz + m * 16 * ZST + n * 32) = (u32x2){pk_bf16(u.x, u.y), pk_bf16(v.x, v.y)};
            }
        }
        WAITL0();
        if (lane == 0) *(volatile LAS unsigned*)(lds + L_MISC + 16 + (wm * 2 + wn) * 4) = 1u;
        done = true;
    };
    auto hook = [&](unsigned rv) { if (__builtin_amdgcn_readfirstlane(rv) != 0u && !done) normalise(); };
#pragma unroll
    for (int n = 0; n < NT; ++n)
#pragma unroll
        for (int m = 0; m < 4; ++m) zp[n][m] = (u32x2){0u, 0u};
#pragma nounroll
    for (int it = 0; it < 2; ++it) {
        gemm_phase<NT, true>(9, acc, so, lds + a0, lds + a1, lds + bw, nullptr, rdy, hook TRA);
        STAMP(1 + 2 * it);
        late();
        if (it == 1 && !done) {
            const u64 t0 = __builtin_amdgcn_s_memrealtime();
            while (__builtin_amdgcn_readfirstlane(*rdy) == 0u && __builtin_amdgcn_s_memrealtime() - t0 < 400000ull) __builtin_amdgcn_s_sleep(1);
            normalise();
        }
#pragma unroll
        for (int m = 0; m < 4; ++m)
#pragma unroll
            for (int n = 0; n < NT; ++n) { acc[n][m] = gelu4(acc[n][m]); zp[n][m] = (u32x2){pk_bf16(acc[n][m][0], acc[n][m][1]), pk_bf16(acc[n][m][2], acc[n][m][3])}; }
        if (it == 0) {
            LAS unsigned char* ps = opq(lds + L_SP + (wm * 64 + fr) * 16 + wn * 8);
#pragma unroll
            for (int m = 0; m < 4; ++m) {
                f32x4 s4 = acc[0][m];
#pragma unroll
                for (int n = 1; n < NT; ++n) s4 += acc[n][m];
                const float mean = opq(allred_q((s4[0] + s4[1]) + (s4[2] + s4[3])) * (1.0f / (16 * NT)));
                f32x4 q4 = {0.f, 0.f, 0.f, 0.f};
#pragma unroll
                for (int n = 0; n < NT; ++n) { const f32x4 d = acc[n][m] - mean; q4 += d * d; }
                const float m2 = allred_q((q4[0] + q4[1]) + (q4[2] + q4[3]));
                if (fq == 0) *(LAS f32x2*)(ps + m * 256) = (f32x2){mean, m2};
            }
            STAMP(2);
        }
    }
    STAMP(4);
}

template <int NT>
__device__ __forceinline__ void consumer_b(LAS unsigned char* lds, rsrc_t ws, u32x2 T, const u32x2 (&z1)[5][4], const float (&bs_)[4], int pm, int pn, int wm, int wn, int lane, unsigned char* wsp_ TRP) {
    float bs[4] = {bf2f(__float_as_uint(opq(bs_[0]))), bf2f(__float_as_uint(opq(bs_[1]))), bf2f(__float_as_uint(opq(bs_[2]))), bf2f(__float_as_uint(opq(bs_[3])))};
    const int fr = lane & 15, fq = lane >> 4;
    constexpr int N0 = (NT == 5) ? 0 : 5;
    f32x4 acc[NT][4];
    int so = 0;
    const int sw = (fr >> 1) & 7;
    const int a0 = (wm * 64 + fr) * 128 + ((fq ^ sw) << 4), a1 = (wm * 64 + fr) * 128 + (((4 + fq) ^ sw) << 4);
    const int bz = BOFF + N0 * 1024 + lane * 16;
    gemm_phase<NT, false>(32, acc, so, lds + a0, lds + a1, lds + bz, bs, nullptr, [](unsigned) {} TRA);
    STAMP(5);
    __builtin_amdgcn_s_setprio(3);
    const int qm = 2 * pm + wm;
    const unsigned hb = WS_HF + (unsigned)qm * (18u * 16384u);
#pragma unroll
    for (int m = 0; m < 4; ++m) {
        u32x2 hp[NT];
#pragma unroll
        for (int n = 0; n < NT; ++n) {
            const f32x2 u = (f32x2){bflo(z1[n][m][0]), bfhi(z1[n][m][0])} * (f32x2){acc[n][m][0], acc[n][m][1]}, v = (f32x2){bflo(z1[n][m][1]), bfhi(z1[n][m][1])} * (f32x2){acc[n][m][2], acc[n][m][3]};
            hp[n] = (u32x2){pk_bf16(u.x, u.y), pk_bf16(v.x, v.y)};
        }
#pragma unroll
        for (int j = 0; j < 2; ++j)
            __builtin_amdgcn_raw_buffer_store_b128((u32x4){hp[2 * j][0], hp[2 * j][1], hp[2 * j + 1][0], hp[2 * j + 1][1]}, ws,
                                                   hb + (unsigned)pn * 16384u + (unsigned)(m * 4 + wn * 2 + j) * 1024u + lane * 16, 0, 16);
        if (NT == 5) __builtin_amdgcn_raw_buffer_store_b64(hp[NT - 1], ws, hb + (unsigned)(16 + (pn >> 3)) * 16384u + m * 4096u + (unsigned)(pn & 7) * 512u + lane * 8, 0, 16);
    }
    flag_publish(ws, WS_FH + (unsigned)(qm * 32 + pn * 2 + wn) * 128u, T, lane);
    __builtin_amdgcn_s_setprio(0);
    STAMP(6);
}

template <int TLO, int THI>
__device__ __forceinline__ void conv_task(LAS unsigned char* lds, int kt, int ut, int yt, int tl, int lane, int& nb TRP) {
#define CSL() do { if (nb < 50) { BAR(); ++nb; } } while (0)
    constexpr int NTL = THI - TLO + 1, ND = THI + 2;
    const int i = lane & 15, g = lane >> 4;
    const LAS unsigned char* pk = opq(lds + kt + (i & 3) * 1088 + ((i >> 2) + g) * 16);
    const LAS unsigned char* pu = opq(lds + ut + (128 + 16 * tl - 32 * g - 4 * (i >> 2) + 4 * (i & 3)) * 2 - 32);
    bf16x8 ub[ND];
#pragma unroll
    for (int d = 0; d < ND; ++d) { if (d == 0 || d == 8) CSL(); ub[d] = cat8(tr_read(pu + 256 * d + 32), tr_read(pu + 256 * d)); }
    f32x4 acc[NTL];
#pragma unroll
    for (int t = 0; t < NTL; ++t) acc[t] = (f32x4){0.f, 0.f, 0.f, 0.f};
#pragma unroll
    for (int s = -1; s <= THI; ++s) {
        if (TLO == 0 ? (s <= 3 || s == 5 || s == 7) : (s & 1) != 0 && s < 12) CSL();
        const bf16x8 kf = *(const LAS bf16x8*)(pk + 64 * (s + 1));
#pragma unroll
        for (int t = TLO; t <= THI; ++t) if (t >= s) acc[t - TLO] = MFMA16(kf, ub[t - s], acc[t - TLO]);
    }
    LAS float* y = opq((LAS float*)(lds + yt) + 32 * g + 16 * tl + i);
    LAS float* cy = opq((LAS float*)(lds + yt + 8192) + (2 * g + tl) * 4 + (i & 3));
#define DPPF(v, ctl) __int_as_float(__builtin_amdgcn_update_dpp(0, __float_as_int(v), ctl, 0xf, 0xf, true))
#pragma unroll
    for (int t = TLO; t <= THI; ++t) {
        if (((t - TLO) % 2) == 0) CSL();
        const f32x4 a = acc[t - TLO];
        y[128 * t] = (a[0] + DPPF(a[1], 0x111)) + (DPPF(a[2], 0x112) + DPPF(a[3], 0x113));
        const float cv = DPPF(a[1], 0x10f) + (DPPF(a[2], 0x10e) + DPPF(a[3], 0x10d));
        if (i < 4) cy[32 * t] = cv;
    }
#undef DPPF
#undef CSL
}
__device__ __forceinline__ int ytab(int c) { return Z0 + c * Y_SZ; }

__global__ __launch_bounds__(512, 2) void gm_kernel(const u16* __restrict__ x, const u16* __restrict__ logA, const u16* __restrict__ Aim, const u16* __restrict__ Cre,
                                                    const u16* __restrict__ Cim, const u16* __restrict__ logdt, const u16* __restrict__ Dsk, const u16* __restrict__ win,
                                                    const u16* __restrict__ wsp, const u16* __restrict__ bsp, const u16* __restrict__ wout, const u16* __restrict__ gw,
                                                    u16* __restrict__ out, unsigned char* wsp_) {
    extern __shared__ __attribute__((aligned(16))) unsigned char lds_[];
    LAS unsigned char* lds = (LAS unsigned char*)lds_;
    const int wid = __builtin_amdgcn_readfirstlane(threadIdx.x >> 6), lane = threadIdx.x & 63;
    const int bid = blockIdx.x, xg = bid & 7, rr = bid >> 3, bi = rr >> 2, bj = rr & 3;
    const int pm = (xg >> 2) * 8 + bi, pn = (xg & 3) * 4 + bj;
    const int qm = 2 * pm + (pn >> 3), qn = pn & 7;
    const int nch = pm < 4 ? 3 : 2;
    const rsrc_t ws = __builtin_amdgcn_make_buffer_rsrc(wsp_, 0, WS_END, 0x00020000);
    STAMP(0);
    u64 pc0; asm volatile("s_getpc_b64 %0" : "=s"(pc0));

    const int fr = lane & 15, fq = lane >> 4;
    const int h = wid - 6, p = wid - 4;
    const bool is_prod = (wid >> 1) == 2;
    const int ch0 = pn * 36 + pm;
    float bs[4];
    auto late = [&]() {
#pragma unroll
        for (int m = 0; m < 4; ++m) bs[m] = __uint_as_float((unsigned)bsp[pm * 128 + (wid & 1) * 64 + m * 16 + fr]);
    };
    const int phi = (-(bi + bj)) & 3;
    const unsigned rsw = (unsigned)(((lane & 7) ^ ((4 * p + (lane >> 4)) & 7)) * 16);
#define GLDS(src, dst, aux) __builtin_amdgcn_global_load_lds((const unsigned*)(src), (LAS unsigned*)(dst), 16, 0, aux)
    auto issueS = [&](int k, int s) {
        const char* sb = (const char*)wsp + (size_t)(pm * 128 + p * 8) * 4096 + k * 128; const unsigned vs = (unsigned)(lane >> 3) * 4096u + rsw;
#pragma unroll
        for (int i = 0; i < 8; ++i) GLDS(sb + (size_t)i * (16 * 4096) + vs, lds + s + (p + 2 * i) * 1024, 0); };
    auto ord2 = [&](int s) { return GM_LAG == 0 ? s : (((s & 3) == phi) ? s : ((s - GM_LAG) & 31)); };
    u32x2 E, T;
    auto mk_tag = [&]() { u64 z = (((u64)__builtin_amdgcn_readfirstlane(E[1]) << 32) | __builtin_amdgcn_readfirstlane(E[0])) + 0x9E3779B97F4A7C15ull;
        z = (z ^ (z >> 30)) * 0xBF58476D1CE4E5B9ull; z = (z ^ (z >> 27)) * 0x94D049BB133111EBull; z ^= z >> 31; T = (u32x2){(unsigned)z, (unsigned)(z >> 32)}; };
    u32x2 zp[5][4];
#define STEP(lim) do { if (nb < (lim)) { BAR(); ++nb; } } while (0)
#define STEPS_TO(lim) _Pragma("nounroll") while (nb < (lim)) { BAR(); ++nb; }
    int nb = 0;
    if (wid < nch) {
        const int kch = ch0 + 16 * wid, ki = kch * 64 + lane;
        const unsigned pA = ((const unsigned*)logA)[ki], pI = ((const unsigned*)Aim)[ki], pR = ((const unsigned*)Cre)[ki], pC = ((const unsigned*)Cim)[ki], pT = logdt[kch];
        LAS u32x2* tw = opq((LAS u32x2*)(lds + Z0 + wid * 8448) + lane);
        const LAS unsigned char* trd = opq(lds + Z0 + wid * 8448 + fr * 528 + fq * 16);
        const float dt = rbf(__expf(bf2f(pT)));
        const f32x2 ar = (f32x2){rbf(__expf(bflo(pA))), rbf(__expf(bfhi(pA)))} * dt * -1.44269504f, ai = (f32x2){bflo(pI), bfhi(pI)} * dt * 0.15915494f;
        const f32x2 cr = (f32x2){bflo(pR), bfhi(pR)} * dt, ci = (f32x2){bflo(pC), bfhi(pC)} * dt;
        float cm = fmaxf(fmaxf(fabsf(cr.x), fabsf(ci.x)), fmaxf(fabsf(cr.y), fabsf(ci.y)));
#define DPPF(v, ctl) __int_as_float(__builtin_amdgcn_update_dpp(0, __float_as_int(v), ctl, 0xf, 0xf, true))
        cm = fmaxf(cm, DPPF(cm, 0x121)); cm = fmaxf(cm, DPPF(cm, 0x122)); cm = fmaxf(cm, DPPF(cm, 0x124)); cm = fmaxf(cm, DPPF(cm, 0x128));
#undef DPPF
        { auto r = __builtin_amdgcn_permlane16_swap(__float_as_uint(cm), __float_as_uint(cm), false, false); cm = fmaxf(__uint_as_float(r[0]), __uint_as_float(r[1]));
          auto q = __builtin_amdgcn_permlane32_swap(__float_as_uint(cm), __float_as_uint(cm), false, false); cm = fmaxf(fmaxf(__uint_as_float(q[0]), __uint_as_float(q[1])), 1e-30f); }
        struct C2 { f32x2 x, y; };
        auto cexp = [&](float p_) { const f32x2 e = ar * p_, t = ai * p_;
            const f32x2 mg = {__builtin_amdgcn_exp2f(e.x), __builtin_amdgcn_exp2f(e.y)}; const float t0 = __builtin_amdgcn_fractf(t.x), t1 = __builtin_amdgcn_fractf(t.y);
            return C2{mg * (f32x2){__builtin_amdgcn_cosf(t0), __builtin_amdgcn_cosf(t1)}, mg * (f32x2){__builtin_amdgcn_sinf(t0), __builtin_amdgcn_sinf(t1)}}; };
        auto cmul = [](C2 a, C2 b) { return C2{__builtin_elementwise_fma(a.x, b.x, -(a.y * b.y)), __builtin_elementwise_fma(a.x, b.y, a.y * b.x)}; };
        auto put = [&](int row, C2 v, bool conj) { tw[row * 66] = conj ? (u32x2){pk_f16(v.x.x, -v.y.x), pk_f16(v.x.y, -v.y.y)} : (u32x2){pk_f16(v.x.x, v.y.x), pk_f16(v.x.y, v.y.y)}; };
        C2 wp[16], zr[8], vp[4], cv[17];
        wp[1] = cexp(8.f); zr[1] = cexp(1.f); vp[0] = cexp(128.f);
        tw[0] = (u32x2){pk_f16(1.f, 0.f), pk_f16(1.f, 0.f)};
#pragma unroll
        for (int j = 1; j < 16; ++j) { if (j > 1) wp[j] = cmul(wp[j >> 1], wp[j - (j >> 1)]); put(j, wp[j], true); }
        CFENCE();
        u32x4 fb[8];
#pragma unroll
        for (int ks = 0; ks < 8; ++ks) fb[ks] = *(const LAS u32x4*)(trd + ks * 64);
        CFENCE();
#pragma unroll
        for (int r = 2; r < 8; ++r) zr[r] = cmul(zr[r >> 1], zr[r - (r >> 1)]);
#pragma unroll
        for (int k = 1; k < 4; ++k) vp[k] = cmul(vp[k - 1], vp[k - 1]);
        const float rc = __builtin_amdgcn_rcpf(cm);
        cv[0] = C2{cr * rc, ci * rc};
#pragma unroll
        for (int k = 1; k < 8; ++k) { const int lg = k >= 4 ? 2 : k >= 2 ? 1 : 0; cv[k] = cmul(cv[k - (1 << lg)], vp[lg]); }
        LAS float* kb_ = (LAS float*)(lds + L_KD + wid * 3072);
        const float sc = 2.0f * cm;
        u32x4 fa[3][8];
#pragma unroll
        for (int tt = 0; tt < 3; ++tt) {
#pragma unroll
            for (int i = 0; i < (tt == 2 ? 9 : 16); ++i) {
                const int r = tt == 0 ? (i & 7) : tt == 1 ? (i < 8 ? 2 * (i & 3) : 4 * (i & 1)) : 0;
                const int hh = tt == 0 ? (i >> 3) : tt == 1 ? (i < 8 ? 2 + (i >> 2) : 4 + ((i - 8) >> 1)) : 8 + i;
                if (tt == 2) cv[hh] = cmul(cv[hh - 8], vp[3]);
                put(i, r == 0 ? cv[hh] : cmul(cv[hh], zr[r]), false);
            }
            CFENCE();
#pragma unroll
            for (int ks = 0; ks < 8; ++ks) fa[tt][ks] = *(const LAS u32x4*)(trd + ks * 64);
            CFENCE();
            SB0();
        }
        f32x4 kd[3] = {{0.f, 0.f, 0.f, 0.f}, {0.f, 0.f, 0.f, 0.f}, {0.f, 0.f, 0.f, 0.f}};
#pragma unroll
        for (int ks = 0; ks < 8; ++ks)
#pragma unroll
            for (int tt = 0; tt < 3; ++tt) kd[tt] = __builtin_amdgcn_mfma_f32_16x16x32_f16(__builtin_bit_cast(f16x8, fa[tt][ks]), __builtin_bit_cast(f16x8, fb[ks]), kd[tt], 0, 0, 0);
#pragma unroll
        for (int tt = 0; tt < 3; ++tt) {
            LAS float* kw = opq(kb_ + (tt == 0 ? 128 * (fq >> 1) + 4 * (fq & 1) + 8 * fr : tt == 1 ? 256 + 64 * fq + (fq < 2 ? 4 : 2) * fr : 512 + 64 * fq + fr));
            LAS float* kwb = tt == 1 ? opq(kw + (fq < 2 ? 2 : 32)) : kw + (tt == 0 ? 2 : 32);
            kw[0] = kd[tt][0] * sc; kw[tt == 2 ? 16 : 1] = kd[tt][1] * sc; kwb[0] = kd[tt][2] * sc; kwb[tt == 2 ? 16 : 1] = kd[tt][3] * sc;
        }
        WAITL0();
    }
    if (wid == 3) {
        unsigned w_ = 0;
        if (lane < GM_PFL) w_ = *(const __attribute__((address_space(1))) unsigned*)((pc0 & ~127ull) + (unsigned)min((rr * GM_PFL + lane) * 128, GM_CODE));
        asm volatile("" :: "v"(w_));
    }
#define IC(n) std::integral_constant<int, n>{}
    if (wid < 6) STAMP(10);
    if (is_prod) {
        const unsigned vx = (unsigned)(lane >> 3) * 1152u + rsw;
        unsigned wv[9];
#pragma unroll
        for (int i = 0; i < 9; ++i) { const int c = (p + 2 * i) * 64 + lane, rho = c / 18, ch = c - rho * 18;
            const int kap = (rho & 32) + 8 * ((rho >> 2) & 3) + 4 * ((rho >> 4) & 1) + (rho & 3); wv[i] = (unsigned)kap * 9216u + (unsigned)ch * 16u; }
        const char* xb = (const char*)x + (size_t)(pm * 128 + p * 8) * 1152;
        const char* wb = (const char*)win + pn * 288;
        auto issueX = [&](int k, int s) {
#pragma unroll
            for (int i = 0; i < 8; ++i) GLDS(xb + (size_t)i * (16 * 1152) + k * 128 + vx, lds + s + (p + 2 * i) * 1024, 0); };
        auto issueW = [&](int k, int coloff, int s) {
            const char* b = wb + (size_t)k * (64 * 9216) + coloff;
#pragma unroll
            for (int i = 0; i < 9; ++i) GLDS(b + wv[i], lds + s + BOFF + (p + 2 * i) * 1024, 0); };
        auto ord1 = [&](int s) { return (GM_LAG == 0 || (s & 3) == phi) ? s : (s >= 4 ? s - 4 : (s == 0 ? 8 : s + 4)); };
        issueX(ord1(0), 0); issueW(ord1(0), FH * 2, 0); issueX(ord1(1), STG); issueW(ord1(1), FH * 2, STG);
        issueX(ord1(2), 2 * STG); issueW(ord1(2), FH * 2, 2 * STG);
        E = flag_ld(ws, WS_E);
        unsigned wm_ = 0;
        auto touch = [&](const void* base, unsigned off, unsigned last) { const char* q_ = (const char*)base + min(off + lane * 64u, last); asm volatile("global_load_dword %0, %1, off" : "+v"(wm_) : "v"(q_) : "memory"); };
        if (GM_PW & 1) {
#pragma unroll
            for (int i = 0; i < 2; ++i) touch(x, bid * 9216u + p * 4608u + i * 4096u, bid * 9216u + p * 4608u + 4544u);
#pragma unroll
            for (int i = 0; i < 3; ++i) touch(win, bid * 20736u + p * 10368u + i * 4096u, bid * 20736u + p * 10368u + 10304u);
        }
        if ((GM_PW & 4) && p == 0) {
            const char* q_ = lane < 14 ? (const char*)wsp_ + lane * 0x200000u : lane < 16 ? (const char*)out + (lane - 14) * 0x200000u : lane < 18 ? (const char*)wsp + (size_t)pm * 0x80000u + (lane - 16) * 0x7ffc0u
                           : lane == 18 ? (const char*)bsp : (const char*)Dsk;
            asm volatile("global_load_dword %0, %1, off sc1" : "+v"(wm_) : "v"(q_) : "memory");
        }
        int so = 0;
        WAITV(35); PBAR();
#pragma nounroll
        for (int S = 1; S < 16; ++S) {
            if (S >= 8 && S <= 10) WAITV(9); else WAITV(17);
            PBAR();
            const int t = S + 2, k = ord1(t < 9 ? t : t < 12 ? t - 3 : t - 12);
            if (t < 9 || t >= 12) issueX(k, so);
            issueW(k, t >= 9 ? 0 : FH * 2, so); so = nextbuf(so);
            if ((GM_PW & 2) && S == 8) {
#pragma unroll
                for (int i = 0; i < 4; ++i) touch(wsp, bid * 32768u + p * 16384u + i * 4096u, 0x7fffffffu);
            }
        }
        WAITV(17); PBAR(); issueS(ord2(0), 0);
        WAITV(8); PBAR(); issueS(ord2(1), STG);
        asm volatile("" :: "v"(wm_));
        STAMP(1);
    } else if (wid < 4) {
        E = flag_ld(ws, WS_E);
        if (wid >> 1) consumer_a<4>(lds, zp, late, wid & 1, 1, lane, wsp_ TRA);
        else consumer_a<5>(lds, zp, late, wid & 1, 0, lane, wsp_ TRA);
    } else {
        if (h == 0 && lane < 12) *(LAS unsigned*)(lds + L_MISC + lane * 4) = 0u;
        const int row0 = lane / 9, chk0 = lane - row0 * 9;
        const int u = (pn >> 3) * 16 + pm, nblk = u < 8 ? 3 : 2;
        u32x2 uc[18]; u32x4 xv[9], gv[9], wv[3][5];
        if (h == 0) {
            { const char* xb = (const char*)x + (size_t)pm * 128 * 1152 + pn * 72; unsigned off = row0 * 1152 + chk0 * 8; int ch = chk0;
#pragma unroll
              for (int i = 0; i < 9; ++i) { uc[i] = *(const u32x2*)(xb + off); uc[9 + i] = *(const u32x2*)(xb + 64 * 1152 + off); off += 7 * 1152 + 8; if (++ch == 9) { ch = 0; off += 1152 - 72; } } }
            { const char* xr = (const char*)x + (size_t)(pm * 128 + pn * 8 + (lane >> 3)) * 1152 + (lane & 7) * 16; const char* gr = (const char*)gw + (lane & 7) * 16;
#pragma unroll
              for (int i = 0; i < 9; ++i) { xv[i] = *(const u32x4*)(xr + i * 128); gv[i] = *(const u32x4*)(gr + i * 128); } }
        } else {
            unsigned wo_[5], hi_[5];
            { int row = row0, ch = chk0;
#pragma unroll
              for (int i = 0; i < 5; ++i) { const int rw = row < 32 ? row : 31; wo_[i] = (rw & 15) * 1152 + ch * 16; hi_[i] = rw >> 4; row += 7; if (++ch == 9) { ch = 0; ++row; } } }
#pragma unroll
            for (int b = 0; b < 3; ++b) {
                const int blk = (b < nblk) ? u + 32 * b : u, st = blk >> 2, jb = blk & 3;
                const int fA = st < 16 ? st * 144 + (jb < 2 ? 32 * jb : 16 + 32 * jb) : ((st - 16) * 8 + 2 * jb) * 144 + 64, dB = (st < 16 ? 16 : 144) * 1152;
                const char* wb_ = (const char*)wout + (size_t)fA * 1152 + qn * 144;
#pragma unroll
                for (int i = 0; i < 5; ++i) wv[b][i] = *(const u32x4*)(wb_ + (wo_[i] + hi_[i] * dB));
            }
        }
        E = flag_ld(ws, WS_E);
        if (lane < 24) *(LAS u32x4*)(lds + L_KT + (lane >> 3) * KT_SZ + (2 * h + ((lane >> 2) & 1)) * 1088 + (lane & 3) * 16) = (u32x4){0u, 0u, 0u, 0u};
        WAITL0();
        STAMP(10);
        __builtin_amdgcn_s_setprio(GM_HPRIO);
        {
            float kv[2][3];
#pragma nounroll
            for (int ei = 0; ei < 9; ++ei) {
                BAR();
                if (ei > 0) {
                    LAS unsigned char* kt = opq(lds + L_KT + (2 * h + ((ei - 1) >> 2)) * 1088 + 64 + ((ei - 1) & 3) * 256 + lane * 2);
#pragma unroll
                    for (int j = 0; j < 2; ++j)
#pragma unroll
                        for (int c = 0; c < 3; ++c) *(LAS u16*)(kt + c * KT_SZ + j * 128) = (u16)pk_bf16(kv[j][c], 0.f);
                }
#pragma unroll
                for (int j = 0; j < 2; ++j) {
                    const int jj = (ei & 3) * 2 + j, e = jj >= 4 ? 3 : jj >= 2 ? 2 : jj, lag = 256 * jj + 4 * lane + 2 * h + ((ei >> 2) & 1);
                    const int idx = e == 0 ? lag : 128 * e + ((lag + (1 << (e - 1)) - 1 + ((lag >> e) & 1)) >> e);
                    const LAS float* q = opq((const LAS float*)(lds + L_KD) + idx);
#pragma unroll
                    for (int c = 0; c < 3; ++c) kv[j][c] = q[c * 768];
                }
            }
        }
        if (h == 0) {
            LAS unsigned char* pzs = opq(lds + Z0 + lane * 8);
#pragma unroll
            for (int i = 0; i < 18; ++i) *(LAS u32x2*)(pzs + i * 512) = uc[i];
            CFENCE();
            { const LAS unsigned char* pt = opq(lds + Z0 + (8 * (4 * fq + (lane & 3)) + (fr >> 2)) * 72);
              const unsigned ub = WS_UT + (unsigned)(pn * 36 + (lane & 3)) * 4096u + (unsigned)(pm * 128 + 8 * (4 * fq + (fr >> 2))) * 2u;
#pragma unroll
              for (int cc = 0; cc < 9; ++cc) __builtin_amdgcn_raw_buffer_store_b128(__builtin_bit_cast(u32x4, cat8(tr_read(pt + cc * 8), tr_read(pt + 288 + cc * 8))), ws, ub + cc * 16384u, 0, 16); }
            float a = 0.f;
#pragma unroll
            for (int i = 0; i < 9; ++i)
#pragma unroll
                for (int j = 0; j < 4; ++j) { const unsigned xa = xv[i][j], ga = gv[i][j]; a = __builtin_amdgcn_fdot2_f32_bf16(__builtin_bit_cast(bf16x2, xa), __builtin_bit_cast(bf16x2, ga), a, false); }
#define DPPF(v, ctl) __int_as_float(__builtin_amdgcn_update_dpp(0, __float_as_int(v), ctl, 0xf, 0xf, true))
            a += DPPF(a, 0xb1); a += DPPF(a, 0x4e); a += DPPF(a, 0x141);
#undef DPPF
            const float gt = rbf(__builtin_amdgcn_rcpf(1.0f + __expf(-rbf(a))));
            if ((lane & 7) == 0) __builtin_amdgcn_raw_buffer_store_b32(__float_as_uint(gt), ws, WS_GATE + (unsigned)(pm * 128 + pn * 8 + (lane >> 3)) * 4u, 0, 16);
        } else {
            LAS unsigned char* pws = opq(lds + Z0 + 9216 + lane * 16);
            const LAS unsigned char* ptw = opq(lds + Z0 + 9216 + (4 * fq + (fr >> 2)) * 144 + (lane & 3) * 8);
#pragma unroll
            for (int b = 0; b < 3; ++b) {
                if (b < nblk) {
#pragma unroll
                    for (int i = 0; i < 5; ++i) if (i < 4 || lane < 32) *(LAS u32x4*)(pws + i * 1024) = wv[b][i];
                    CFENCE();
                    const unsigned ob = WS_WOF + (unsigned)qn * (18u * 18432u) + (unsigned)(u + 32 * b) * 4608u;
#pragma unroll
                    for (int nn = 0; nn < 5; ++nn) {
                        const u32x4 v = __builtin_bit_cast(u32x4, cat8(tr_read(ptw + nn * 32), tr_read(ptw + 2304 + nn * 32)));
                        if (nn < 4) __builtin_amdgcn_raw_buffer_store_b128(v, ws, ob + nn * 1024 + lane * 16, 0, 16);
                        else if (fr < 8) __builtin_amdgcn_raw_buffer_store_b128(v, ws, ob + 4096 + (fq * 8 + fr) * 16, 0, 16);
                    }
                    CFENCE();
                }
            }
        }
        nb = 9;
        mk_tag();
        WAITL0();
        STEPS_TO(10);
        STAMP(1);
        {
            const f32x4 sp = *(const LAS f32x4*)(lds + L_SP + (h * 64 + lane) * 16);
            const float dm = sp[0] - sp[2];
            const f32x2 mine = {(80.f * sp[0] + 64.f * sp[2]) * (1.0f / 144.f), sp[1] + sp[3] + (80.f * 64.f / 144.f) * dm * dm};
            __builtin_amdgcn_raw_buffer_store_b128((u32x4){T[0], __float_as_uint(mine.x), T[1], __float_as_uint(mine.y)}, ws, WS_ST + (unsigned)((pm * 16 + pn) * 128 + h * 64 + lane) * 16u, 0, 16);
            STEP(18);
            const unsigned so_ = WS_ST + (unsigned)(pm * 16 * 128 + h * 64 + lane) * 16u;
            const u64 t0 = __builtin_amdgcn_s_memrealtime();
            u32x4 st[16];
            for (;;) {
                CFENCE();
#pragma unroll
                for (int j = 0; j < 16; ++j) st[j] = __builtin_amdgcn_raw_buffer_load_b128(ws, so_ + j * 2048u, 0, 16);
                STEP(18);
                unsigned bad = 0;
#pragma unroll
                for (int j = 0; j < 16; ++j) bad |= (st[j][0] ^ T[0]) | (st[j][2] ^ T[1]);
                if (__builtin_amdgcn_ballot_w64(bad != 0u) == 0ull) break;
                if (__builtin_amdgcn_s_memrealtime() - t0 > 300000ull) break;
            }
            STAMP(2);
            STEP(18);
            float mean = 0.f, m2 = 0.f;
#pragma unroll
            for (int j = 0; j < 16; ++j) mean += __uint_as_float(st[j][1]);
            mean *= (1.0f / 16.f);
#pragma unroll
            for (int j = 0; j < 16; ++j) { const float d = __uint_as_float(st[j][1]) - mean; m2 += __uint_as_float(st[j][3]) + 144.f * d * d; }
            const float rstd = __builtin_amdgcn_rsqf(m2 * (1.0f / FH) + 1e-5f);
            *(LAS f32x2*)(lds + L_SP + (h * 64 + lane) * 16) = (f32x2){rstd, -mean * rstd};
            WAITL0();
            if (lane == 0) *(volatile LAS unsigned*)(lds + L_MISC + 4 + h * 4) = 1u;
            const volatile LAS unsigned* zd = (const volatile LAS unsigned*)(lds + L_MISC + 16 + h * 8 + (lane & 1) * 4);
            for (;;) {
                STEP(18);
                if (__builtin_amdgcn_ballot_w64(*zd == 0u) == 0ull) break;
                if (__builtin_amdgcn_s_memrealtime() - t0 > 800000ull) break;
            }
            CFENCE();
            const LAS unsigned char* ptz = opq(lds + Z0 + (h * 64 + 8 * fq + (fr >> 2)) * ZST + (lane & 3) * 8);
            const unsigned zo = WS_Z2F + (unsigned)((pn * 64 + pm * 4 + h * 2) * 9) * 1024u + lane * 16;
#pragma unroll
            for (int i = 0; i < 18; ++i) {
                const LAS unsigned char* pt = ptz + (i / 9) * 32 * ZST + (i % 9) * 32;
                __builtin_amdgcn_raw_buffer_store_b128(__builtin_bit_cast(u32x4, cat8(tr_read(pt), tr_read(pt + 4 * ZST))), ws, zo + i * 1024, 0, 16);
                if (i == 5 || i == 11) STEP(18);
            }
            STEP(18);
            flag_publish(ws, WS_FZ + (unsigned)(pn * 32 + pm * 2 + h) * 128u, T, lane);
            STAMP(3);
            STEPS_TO(18);
            __builtin_amdgcn_s_setprio(0);
        }
    }

    if (is_prod) {
        mk_tag();
        const char* lin = (const char*)wsp_ + p * 1024 + lane * 16;
        auto issueLA = [&](unsigned off, int s) {
#pragma unroll
            for (int i = 0; i < 8; ++i) GLDS(lin + off + i * 2048, lds + s + (p + 2 * i) * 1024, 16); };
        auto issueLB = [&](unsigned off, int s) {
#pragma unroll
            for (int i = 0; i < 9; ++i) GLDS(lin + off + i * 2048, lds + s + BOFF + (p + 2 * i) * 1024, 16); };
        auto ord3 = [&](int s) { return (GM_LAG == 0 || s >= 16) ? s : (((s & 3) == phi) ? s : ((s - GM_LAG) & 15)); };
        u32x2 pv = {0u, 0u}; unsigned rdy = 0; bool pend = false;
        const char* fp = (const char*)wsp_ + WS_FZ + ((unsigned)pn * 32u + (lane & 31)) * 128u;
        auto poll_issue = [&]() { asm volatile("global_load_dwordx2 %0, %1, off sc1" : "+v"(pv) : "v"(fp) : "memory"); };
        auto poll_take = [&]() { asm volatile("" : "+v"(pv)); rdy |= (unsigned)__builtin_amdgcn_ballot_w64(pv[0] == T[0] && pv[1] == T[1]); };
        auto need = [&](unsigned bits) {
            if (pend) { poll_take(); pend = false; }
            if ((rdy & bits) != bits) {
                const u64 t0 = __builtin_amdgcn_s_memrealtime();
                do { poll_issue(); WAITV(0); poll_take(); } while ((rdy & bits) != bits && __builtin_amdgcn_s_memrealtime() - t0 < 300000ull);
            }
            if (rdy != ~0u) { poll_issue(); pend = true; }
        };
        const unsigned zb = WS_Z2F + (unsigned)pn * (64u * 9216u);
        need((1u << ord2(0)) | (1u << ord2(1)));
        STAMP(2);
        issueLB(zb + (unsigned)ord2(0) * 18432u, 0); issueLB(zb + (unsigned)ord2(1) * 18432u, STG);
        int so = 2 * STG;
        WAITV(9);
#pragma nounroll
        for (int S = 18; S < 48; ++S) {
            PBAR();
            const int k = ord2(S - 16); need(1u << k); issueS(k, so); issueLB(zb + (unsigned)k * 18432u, so); so = nextbuf(so);
            WAITV(17);
        }
        const unsigned hb = WS_HF + (unsigned)qm * (18u * 16384u), ob = WS_WOF + (unsigned)qn * (18u * 18432u);
        PBAR();
        fp = (const char*)wsp_ + WS_FZ + ((unsigned)(pn ^ 8) * 32u + (lane & 31)) * 128u; rdy = 0; pend = false;
        need(~0u);
        issueLB(ob + (unsigned)ord3(0) * 18432u, 2 * STG);
        PBAR();
        issueLB(ob + (unsigned)ord3(1) * 18432u, 0);
        STAMP(3);
        fp = (const char*)wsp_ + WS_FH + ((unsigned)qm * 32u + (lane & 31)) * 128u; rdy = 0;
        need(~0u);
        STAMP(4);
        issueLA(hb + (unsigned)ord3(0) * 16384u, 2 * STG); issueLA(hb + (unsigned)ord3(1) * 16384u, 0);
        so = STG;
        WAITV(8);
#pragma nounroll
        for (int S = 50; S < 66; ++S) {
            PBAR();
            const int k = ord3(S - 48); issueLA(hb + (unsigned)k * 16384u, so); issueLB(ob + (unsigned)k * 18432u, so); so = nextbuf(so);
            WAITV(17);
        }
        PBAR();
        WAITV(0); PBAR();
        PBAR();
        STAMP(5); PDUMP();
        if (bid == 0 && p == 0 && lane == 0) __builtin_amdgcn_raw_buffer_store_b64(T, ws, WS_E, 0, 16);
        return;
    }

    if (wid < 4) {
        mk_tag();
        if (wid >> 1) consumer_b<4>(lds, ws, T, zp, bs, pm, pn, wid & 1, 1, lane, wsp_ TRA);
        else consumer_b<5>(lds, ws, T, zp, bs, pm, pn, wid & 1, 0, lane, wsp_ TRA);
    } else {
        __builtin_amdgcn_s_setprio(GM_HPRIO);
        {   const unsigned fo = WS_FZ + (unsigned)(pn * 32 + (lane & 31)) * 128u;
            const u64 t0 = __builtin_amdgcn_s_memrealtime();
            for (;;) {
                STEP(44);
                CFENCE();
                const u32x2 v = flag_ld(ws, fo);
                STEP(44);
                if (flag_all(v, T, true)) break;
                if (__builtin_amdgcn_s_memrealtime() - t0 > 300000ull) break;
            } }
        u32x4 uu[3][2];
#pragma unroll
        for (int c = 0; c < 3; ++c)
#pragma unroll
            for (int j = 0; j < 2; ++j) uu[c][j] = __builtin_amdgcn_raw_buffer_load_b128(ws, WS_UT + (unsigned)(ch0 + (c < nch ? 16 * c : 0)) * 4096u + h * 2048 + j * 1024 + lane * 16, 0, 16);
#pragma unroll
        for (int c = 0; c < 3; ++c) if (lane < 16) *(LAS u32x4*)(lds + L_UT + c * UT_SZ + h * 4352 + lane * 16) = (u32x4){0u, 0u, 0u, 0u};
        BAR(); BAR(); BAR(); nb += 3;
        LAS unsigned char* pu_ = opq(lds + L_UT + 256 + h * 2048 + lane * 16);
#pragma unroll
        for (int c = 0; c < 3; ++c)
#pragma unroll
            for (int j = 0; j < 2; ++j) *(LAS u32x4*)(pu_ + c * UT_SZ + j * 1024) = uu[c][j];
        WAITL0();
        if (lane == 0) *(volatile LAS unsigned*)(lds + L_MISC + 32 + h * 4) = 1u;
        {   const volatile LAS unsigned* uw = (const volatile LAS unsigned*)(lds + L_MISC + 32 + (lane & 1) * 4);
            const u64 t0 = __builtin_amdgcn_s_memrealtime();
            do { STEP(50); } while (__builtin_amdgcn_ballot_w64(*uw == 0u) != 0ull && __builtin_amdgcn_s_memrealtime() - t0 < 800000ull);
            CFENCE(); }
        STAMP(4);
    }

    if (wid < 4) nb = 50;
    for (;;) {
        if (nb > 45) STEPS_TO(50);
        unsigned tk = 0;
        if (lane == 0) tk = __hip_atomic_fetch_add((LAS unsigned*)(lds + L_MISC), 1u, __ATOMIC_RELAXED, __HIP_MEMORY_SCOPE_WORKGROUP);
        tk = __builtin_amdgcn_readfirstlane(tk);
        if (tk >= (unsigned)(4 * nch)) break;
        const int c = tk >> 2, tl = tk & 1;
        if (tk & 2) conv_task<0, 10>(lds, L_KT + c * KT_SZ, L_UT + c * UT_SZ, ytab(c), tl, lane, nb TRA);
        else conv_task<11, 15>(lds, L_KT + c * KT_SZ, L_UT + c * UT_SZ, ytab(c), tl, lane, nb TRA);
    }
    STEPS_TO(50);
    WAITL0();
    STAMP(7);

    if (wid < 4) {
        const int kq = wid >> 1, mh = wid & 1;
        f32x4 acc[5][2];
        struct F3 { s16x4 alo[4], ahi[4]; bf16x8 b[10]; } f0, f1;
        const LAS unsigned char* ph = lds + 2 * STG + mh * 8192 + kq * 2048 + lane * 16;
        const LAS unsigned char* pw = lds + 2 * STG + BOFF + kq * 9216 + lane * 16;
        const LAS unsigned char* pw4 = lds + 2 * STG + BOFF + kq * 9216 + 4096 + (fq * 8 + (fr & 7)) * 16;
        auto rd3 = [&](F3& f, int i, bool single) {
            const int j = i / 7, r = i % 7;
            if (r < 2) {
                if (!single) { const bf16x8 v = *(const LAS bf16x8*)(ph + r * 4096 + j * 1024); f.alo[j * 2 + r] = __builtin_shufflevector(v, v, 0, 1, 2, 3); f.ahi[j * 2 + r] = __builtin_shufflevector(v, v, 4, 5, 6, 7); }
                else { const LAS unsigned char* q = ph - lane * 8 + r * 4096 + j * 1024; f.alo[j * 2 + r] = *(const LAS s16x4*)q; f.ahi[j * 2 + r] = *(const LAS s16x4*)(q + 512); }
            } else if (r < 6) f.b[j * 5 + r - 2] = *(const LAS bf16x8*)(pw + j * 4608 + (r - 2) * 1024);
            else f.b[j * 5 + 4] = *(const LAS bf16x8*)(pw4 + j * 4608);
        };
        int so = 2 * STG;
        auto step3 = [&](const F3& c, F3& l, int mode  , bool first) {
            const int delta = so == 2 * STG ? -2 * STG : STG; so += delta;
#pragma unroll
            for (int i = 0; i < 20; ++i) {
                const int j = i / 10, n = (i % 10) >> 1, m = i & 1;
                acc[n][m] = MFMA16(c.b[j * 5 + n], cat8(c.alo[j * 2 + m], c.ahi[j * 2 + m]), (first && j == 0) ? ((f32x4){0.f, 0.f, 0.f, 0.f}) : acc[n][m]);
                if (mode < 2 && i < 7) { rd3(l, 2 * i, mode == 1); rd3(l, 2 * i + 1, mode == 1); }
                if (mode < 2) { if (i == 8) { ph += delta; asm volatile("" : "+v"(ph)); } if (i == 9) { pw += delta; asm volatile("" : "+v"(pw)); } if (i == 10) { pw4 += delta; asm volatile("" : "+v"(pw4)); } }
                SB0();
            }
        };
        __builtin_amdgcn_s_waitcnt(0xC07F);
        BAR();
#pragma unroll
        for (int i = 0; i < 14; ++i) rd3(f0, i, false);
        ph -= 2 * STG; pw -= 2 * STG; pw4 -= 2 * STG; so = 0;
        WAITL0(); BAR(); step3(f0, f1, 0, true);
#pragma nounroll
        for (int j = 0; j < 7; ++j) {
            WAITL0(); BAR(); step3(f1, f0, 0, false);
            WAITL0(); BAR(); step3(f0, f1, 0, false);
        }
        WAITL0(); BAR(); step3(f1, f0, 1, false);
        WAITL0(); BAR(); step3(f0, f1, 1, false);
        step3(f1, f0, 2, false);
        STAMP(8);
        LAS unsigned char* prw = opq(lds + L_R + wid * 5120 + lane * 16);
#pragma unroll
        for (int n = 0; n < 5; ++n) { const f32x4 x0 = acc[n][0], x1 = acc[n][1]; *(LAS f32x4*)(prw + n * 1024) = kq ? x0 : x1; acc[n][0] = kq ? x1 : x0; }
        WAITL0();
        BAR();
        const int m = mh * 2 + kq;
        const rsrc_t ors = __builtin_amdgcn_make_buffer_rsrc(out, 0, SEQ * DM * 2, 0x00020000);
        const float g = *(const LAS float*)(lds + L_MISC + 64 + (16 * m + fr) * 4), g1 = rbf(1.0f - g);
        const LAS unsigned char* prr = opq(lds + L_R + (wid ^ 2) * 5120 + lane * 16);
        const LAS unsigned char* pat = opq(lds + L_AT + (4 * fq + (fr >> 2)) * 128 + (16 * m + 4 * (lane & 3)) * 2);
#pragma unroll
        for (int n = 0; n < 5; ++n) {
            const f32x4 b = acc[n][0] + *(const LAS f32x4*)(prr + n * 1024);
            const u32x2 a = __builtin_bit_cast(u32x2, tr_read(pat + n * 2048));
            const u32x2 o = {pk_bf16(g * bflo(a[0]) + g1 * b[0], g * bfhi(a[0]) + g1 * b[1]), pk_bf16(g * bflo(a[1]) + g1 * b[2], g * bfhi(a[1]) + g1 * b[3])};
            if (n < 4 || fq < 2) __builtin_amdgcn_raw_buffer_store_b64(o, ors, (unsigned)((qm * 64 + 16 * m + fr) * 1152 + (qn * 72 + 16 * n + 4 * fq) * 2), 0, GM_OAUX);
        }
    } else {
        int d = pn * 36 + pm;
#pragma nounroll
        for (int c = 0; c < nch; ++c) {
            const float dsk = bf2f(Dsk[d]);
#pragma unroll
            for (int k = 0; k < 2; ++k) {
                BAR(); ++nb;
                const int l0 = h * 1024 + k * 512 + lane * 8;
                f32x4 cv = *(const LAS f32x4*)(lds + ytab(c) + 8192 + (l0 >= 16 ? (l0 >> 4) - 1 : 0) * 16);
                if ((lane & 1) || l0 == 0) cv = (f32x4){0.f, 0.f, 0.f, 0.f};
                const f32x4 y0 = *(const LAS f32x4*)(lds + ytab(c) + l0 * 4) + cv, y1 = *(const LAS f32x4*)(lds + ytab(c) + l0 * 4 + 16);
                const u32x4 uv = *(const LAS u32x4*)(lds + L_UT + c * UT_SZ + 256 + l0 * 2);
                u32x4 o;
#pragma unroll
                for (int j = 0; j < 4; ++j) {
                    const float ya = j < 2 ? y0[2 * j] : y1[2 * j - 4], yb = j < 2 ? y0[2 * j + 1] : y1[2 * j - 3];
                    const unsigned yp = pk_bf16(ya, yb), up = pk_bf16(bflo(uv[j]) * dsk, bfhi(uv[j]) * dsk);
                    o[j] = pk_bf16(bflo(yp) + bflo(up), bfhi(yp) + bfhi(up));
                }
                __builtin_amdgcn_raw_buffer_store_b128(o, ws, WS_AT + (unsigned)d * 4096u + l0 * 2, 0, 16);
            }
            d += 16;
        }
        STEPS_TO(58);
        flag_publish(ws, WS_FA + (unsigned)((pn >> 1) * 64 + (pn & 1) * 32 + pm * 2 + h) * 128u, T, lane);
        STAMP(5);
        STEPS_TO(62);
        if (h == 0) {
            const unsigned fo = WS_FA + (unsigned)(qn * 64 + lane) * 128u;
            const u64 t0 = __builtin_amdgcn_s_memrealtime();
            for (;;) {
                CFENCE();
                const u32x2 v = flag_ld(ws, fo);
                STEP(68); STEP(68);
                if (flag_all(v, T, true)) break;
                if (__builtin_amdgcn_s_memrealtime() - t0 > 300000ull) break;
            }
            ACQ();
            u32x4 av[9];
#pragma unroll
            for (int i = 0; i < 9; ++i) av[i] = __builtin_amdgcn_raw_buffer_load_b128(ws, WS_AT + (unsigned)(qn * 72 + i * 8 + (lane >> 3)) * 4096u + qm * 128 + (lane & 7) * 16, 0, 16);
            STEP(68); STEP(68);
            LAS unsigned char* pa_ = opq(lds + L_AT + lane * 16);
#pragma unroll
            for (int i = 0; i < 9; ++i) *(LAS u32x4*)(pa_ + i * 1024) = av[i];
        } else {
            STEPS_TO(65);
            const unsigned gv = __builtin_amdgcn_raw_buffer_load_b32(ws, WS_GATE + (unsigned)(qm * 64 + lane) * 4u, 0, 16);
            BAR(); BAR(); nb += 2;
            *(LAS unsigned*)(lds + L_MISC + 64 + lane * 4) = gv;
        }
        STEPS_TO(68);
        WAITL0();
        BAR();
    }
    STAMP(9); TRDUMP();
}
}

extern "C" void kernel_launch(void* const* d_in, const int* in_sizes, int n_in, void* d_out, int out_size, void* d_ws, size_t ws_size, hipStream_t stream) {
    (void)in_sizes; (void)n_in; (void)out_size; (void)ws_size;
    (void)hipFuncSetAttribute((const void*)gm::gm_kernel, hipFuncAttributeMaxDynamicSharedMemorySize, gm::LDS_BYTES);
    hipLaunchKernelGGL(gm::gm_kernel, dim3(256), dim3(512), gm::LDS_BYTES, stream, (const u16*)d_in[0], (const u16*)d_in[1], (const u16*)d_in[2], (const u16*)d_in[3],
                       (const u16*)d_in[4], (const u16*)d_in[5], (const u16*)d_in[6], (const u16*)d_in[8], (const u16*)d_in[9], (const u16*)d_in[10],
                       (const u16*)d_in[11], (const u16*)d_in[12], (u16*)d_out, (unsigned char*)d_ws);
}
```

```cpp
#include <hip/hip_runtime.h>
#include <stdint.h>
#include <type_traits>

#define LAS __attribute__((address_space(3)))
typedef unsigned short u16;
typedef unsigned long long u64;
typedef short bf16x8 __attribute__((ext_vector_type(8)));
typedef short s16x4 __attribute__((ext_vector_type(4)));
typedef _Float16 f16x8 __attribute__((ext_vector_type(8)));
typedef _Float16 f16x2 __attribute__((ext_vector_type(2)));
typedef __bf16 bf16x2 __attribute__((ext_vector_type(2)));
typedef float f32x4 __attribute__((ext_vector_type(4)));
typedef float f32x2 __attribute__((ext_vector_type(2)));
typedef unsigned u32x4 __attribute__((ext_vector_type(4)));
typedef unsigned u32x2 __attribute__((ext_vector_type(2)));
typedef __amdgpu_buffer_rsrc_t rsrc_t;

namespace gm {
constexpr int SEQ = 2048, DM = 576, FH = 2304;
constexpr unsigned WS_E = 0, WS_FS = 4096, WS_FZ = WS_FS + 65536, WS_FH = WS_FZ + 65536, WS_FA = WS_FH + 131072;
constexpr unsigned WS_ST = 0x60000, WS_GATE = 0xE0000, WS_DBG = 0x1E00000, WS_Z2F = 0x100000, WS_HF = 0xA00000, WS_WOF = 0x1300000, WS_UT = 0x1600000, WS_AT = 0x1900000, WS_END = 0x1C00000, WS_TRC = 0x1C00000;
constexpr int STG = 34816, BOFF = 16384, Z0 = 3 * STG, ZST = 296, T0 = Z0 + 128 * ZST;
constexpr int KT_SZ = 4352, UT_SZ = 4608, Y_SZ = 10240;
constexpr int L_MISC = 163840 - 320;
constexpr int L_KT = L_MISC - 3 * KT_SZ, L_SP = T0, L_KD = Z0 + 3 * 8448, L_UT = Z0 + 3 * Y_SZ;
constexpr int L_AT = Z0, L_R = Z0 + 10240;
constexpr int LDS_BYTES = 163840;
static_assert(L_UT + 3 * UT_SZ <= L_KT && L_KD + 3 * 3072 <= T0 && L_SP + 2048 <= L_KT, "lds map");
#define GM_HPRIO 1
#define GM_OAUX 0
#define GM_PFW(w) ((w) == 3)
#define GM_PFL 13
#define GM_CODE 50176
#define GM_ORD 0
#define GM_PW 7
#define GM_LAG 4

__device__ __forceinline__ float bf2f(unsigned v) { return __uint_as_float(v << 16); }
__device__ __forceinline__ float bflo(unsigned w) { return __uint_as_float(w << 16); }
__device__ __forceinline__ float bfhi(unsigned w) { return __uint_as_float(w & 0xffff0000u); }
__device__ __forceinline__ unsigned pk_bf16(float a, float b) { f32x2 v = {a, b}; return __builtin_bit_cast(unsigned, __builtin_convertvector(v, bf16x2)); }
__device__ __forceinline__ unsigned pk_f16(float a, float b) { f32x2 v = {a, b}; return __builtin_bit_cast(unsigned, __builtin_convertvector(v, f16x2)); }
__device__ __forceinline__ float rbf(float a) { return bflo(pk_bf16(a, a)); }
__device__ __forceinline__ f32x2 gelu2(f32x2 x) {
    const f32x2 w = (x * x) * (-0.10294324f) + (-2.30220819f);
    const f32x2 a = w * x;
    f32x2 e; e.x = __builtin_amdgcn_exp2f(a.x); e.y = __builtin_amdgcn_exp2f(a.y);
    e = e + 1.0f;
    f32x2 r; r.x = __builtin_amdgcn_rcpf(e.x); r.y = __builtin_amdgcn_rcpf(e.y);
    return x * r;
}
__device__ __forceinline__ f32x4 gelu4(f32x4 v) { const f32x2 a = gelu2((f32x2){v[0], v[1]}), b = gelu2((f32x2){v[2], v[3]}); return (f32x4){a.x, a.y, b.x, b.y}; }
__device__ __forceinline__ float allred_q(float v) {
    unsigned u = __float_as_uint(v);
    auto r = __builtin_amdgcn_permlane16_swap(u, u, false, false);
    v = __uint_as_float(r[0]) + __uint_as_float(r[1]);
    u = __float_as_uint(v);
    auto s = __builtin_amdgcn_permlane32_swap(u, u, false, false);
    return __uint_as_float(s[0]) + __uint_as_float(s[1]);
}
#define ACQ() __builtin_amdgcn_fence(__ATOMIC_ACQUIRE, "wavefront")
#define TRP
#define TRA
#define TRDUMP() do { } while (0)
#define BAR() __builtin_amdgcn_s_barrier()
#define PBAR() BAR()
#define PDUMP() TRDUMP()
#define WAITV(n) asm volatile("s_waitcnt vmcnt(" #n ")" ::: "memory")
#define WAITL0() asm volatile("s_waitcnt lgkmcnt(0)" ::: "memory")
#define CFENCE() asm volatile("" ::: "memory")
#define SGB(mask, n, id) __builtin_amdgcn_sched_group_barrier(mask, n, id)
#define MFMA16(a, b, c) __builtin_amdgcn_mfma_f32_16x16x32_bf16(a, b, c, 0, 0, 0)
__device__ __forceinline__ s16x4 tr_read(const LAS unsigned char* p) { return __builtin_amdgcn_ds_read_tr16_b64_v4i16((LAS s16x4*)p); }
__device__ __forceinline__ bf16x8 cat8(s16x4 a, s16x4 b) { return __builtin_shufflevector(a, b, 0, 1, 2, 3, 4, 5, 6, 7); }
template <class P> __device__ __forceinline__ P opq(P p) { asm volatile("" : "+v"(p)); return p; }
__device__ __forceinline__ int nextbuf(int so) { return so == 2 * STG ? 0 : so + STG; }

#define STAMP(i) do { } while (0)

__device__ __forceinline__ u32x2 flag_ld(rsrc_t ws, unsigned off) { return __builtin_amdgcn_raw_buffer_load_b64(ws, off, 0, 16); }
__device__ __forceinline__ bool flag_all(u32x2 v, u32x2 T, bool active) { return __builtin_amdgcn_ballot_w64(active && (v[0] != T[0] || v[1] != T[1])) == 0ull; }
__device__ __forceinline__ void flag_publish(rsrc_t ws, unsigned off, u32x2 T, int lane) {
    WAITV(0);
    if (lane == 0) __builtin_amdgcn_raw_buffer_store_b64(T, ws, off, 0, 16);
}
__device__ __forceinline__ void flag_wait(rsrc_t ws, unsigned base, int n, u32x2 T, int lane) {
    const unsigned off = base + (unsigned)(lane < n ? lane : 0) * 128u;
    const u64 t0 = __builtin_amdgcn_s_memrealtime();
    for (;;) {
        CFENCE();
        const u32x2 v = flag_ld(ws, off);
        if (flag_all(v, T, true)) break;
        if (__builtin_amdgcn_s_memrealtime() - t0 > 300000ull) break;
        __builtin_amdgcn_s_sleep(1);
    }
    ACQ();
}

#define SB0() __builtin_amdgcn_sched_barrier(0)
template <int NT> struct Frag { bf16x8 a[4]; s16x4 lo[NT], hi[NT]; };
template <int NT, bool TR, int HALF>
__device__ __forceinline__ void frag_read(Frag<NT>& f, int i, const LAS unsigned char* pa, const LAS unsigned char* pb) {
    if (GM_ORD) { constexpr int NB = TR ? 2 * NT : NT; i = i == 0 ? 0 : i <= NB ? i + 3 : i - NB; }
    if (i < 4) f.a[i] = *(const LAS bf16x8*)(pa + i * 2048);
    else if (TR) { const int n = (i - 4) >> 1; if ((i - 4) & 1) f.hi[n] = tr_read(pb + HALF * 9216 + 4608 + n * 32); else f.lo[n] = tr_read(pb + HALF * 9216 + n * 32); }
    else { const int n = i - 4; const bf16x8 v = *(const LAS bf16x8*)(pb + HALF * 9216 + n * 1024);
           f.lo[n] = __builtin_shufflevector(v, v, 0, 1, 2, 3); f.hi[n] = __builtin_shufflevector(v, v, 4, 5, 6, 7); }
}
template <int NT, bool TR, int LHALF, int RPS, bool FIRST, bool UPD>
__device__ __forceinline__ void half_step(f32x4 (&acc)[NT][4], const Frag<NT>& c, Frag<NT>& l, const LAS unsigned char*& pa0, const LAS unsigned char*& pa1, const LAS unsigned char*& pb, int delta, const float* ini = nullptr) {
    constexpr int NRD = TR ? 4 + 2 * NT : 4 + NT, NM = 4 * NT, LAST = (NRD + RPS - 1) / RPS;
#pragma unroll
    for (int i = 0; i < NM; ++i) {
        const int n = GM_ORD ? i % NT : i >> 2, m = GM_ORD ? i / NT : i & 3;
        acc[n][m] = MFMA16(cat8(c.lo[n], c.hi[n]), c.a[m], FIRST ? (TR ? ((f32x4){0.f, 0.f, 0.f, 0.f}) : ((f32x4){ini[m], ini[m], ini[m], ini[m]})) : acc[n][m]);
        if (LHALF >= 0) {
#pragma unroll
            for (int r = 0; r < RPS; ++r) if (i * RPS + r < NRD) frag_read<NT, TR, (LHALF > 0 ? 1 : 0)>(l, i * RPS + r, LHALF > 0 ? pa1 : pa0, pb);
        }
        if (UPD) { if (i == LAST) { pa0 += delta; asm volatile("" : "+v"(pa0)); } if (i == LAST + 1) { pb += delta; asm volatile("" : "+v"(pb)); } if (i == LAST + 2) { pa1 += delta; asm volatile("" : "+v"(pa1)); } }
        SB0();
    }
}
template <int NT, bool TR, class HK>
__device__ __forceinline__ void gemm_phase(int nsteps, f32x4 (&acc)[NT][4], int& so, const LAS unsigned char* pa0, const LAS unsigned char* pa1, const LAS unsigned char* pb,
                                           const float* ini, const volatile LAS unsigned* rdy, HK hook TRP) {
    constexpr int NRD = TR ? 4 + 2 * NT : 4 + NT;
    Frag<NT> f0, f1;
    pa0 += so; pa1 += so; pb += so;
    asm volatile("" : "+v"(pa0), "+v"(pa1), "+v"(pb));
    __builtin_amdgcn_s_waitcnt(0xC07F);
    BAR();
#pragma unroll
    for (int i = 0; i < NRD; ++i) frag_read<NT, TR, 0>(f0, i, pa0, pb);
    SB0();
    int delta = so == 2 * STG ? -2 * STG : STG; so += delta;
    half_step<NT, TR, 1, 2, true, true>(acc, f0, f1, pa0, pa1, pb, delta, ini);
#pragma nounroll
    for (int s = 1; s < nsteps; ++s) {
        WAITL0();
        BAR();
        unsigned rv = 0;
        if (TR) { rv = *rdy; SB0(); }
        delta = so == 2 * STG ? -2 * STG : STG; so += delta;
        half_step<NT, TR, 0, 1, false, false>(acc, f1, f0, pa0, pa1, pb, 0);
        half_step<NT, TR, 1, 2, false, true>(acc, f0, f1, pa0, pa1, pb, delta);
        if (TR) hook(rv);
    }
    half_step<NT, TR, -1, 1, false, false>(acc, f1, f0, pa0, pa1, pb, 0);
}

template <int NT, class LT>
__device__ __forceinline__ void consumer_a(LAS unsigned char* lds, u32x2 (&zp)[5][4], LT late, int wm, int wn, int lane, unsigned char* wsp_ TRP) {
    const int fr = lane & 15, fq = lane >> 4;
    constexpr int N0 = (NT == 5) ? 0 : 5;
    f32x4 acc[NT][4];
    int so = 0;
    const int sw = (fr >> 1) & 7;
    const int a0 = (wm * 64 + fr) * 128 + ((fq ^ sw) << 4), a1 = (wm * 64 + fr) * 128 + (((4 + fq) ^ sw) << 4);
    const int bw = BOFF + (4 * fq + (fr >> 2)) * 288 + (lane & 3) * 8 + N0 * 32;
    const volatile LAS unsigned* rdy = (const volatile LAS unsigned*)(lds + L_MISC + 4 + wm * 4);
    bool done = false;
    auto normalise = [&]() {
        const LAS unsigned char* ps = opq(lds + L_SP + (wm * 64 + fr) * 16);
        LAS unsigned char* pz = opq(lds + Z0 + (wm * 64 + fr) * ZST + N0 * 32 + fq * 8);
#pragma unroll
        for (int m = 0; m < 4; ++m) {
            const f32x2 sr = *(const LAS f32x2*)(ps + m * 256);
#pragma unroll
            for (int n = 0; n < NT; ++n) {
                const f32x2 u = (f32x2){bflo(zp[n][m][0]), bfhi(zp[n][m][0])} * sr.x + sr.y, v = (f32x2){bflo(zp[n][m][1]), bfhi(zp[n][m][1])} * sr.x + sr.y;
                *(LAS u32x2*)(pz + m * 16 * ZST + n * 32) = (u32x2){pk_bf16(u.x, u.y), pk_bf16(v.x, v.y)};
            }
        }
        WAITL0();
        if (lane == 0) *(volatile LAS unsigned*)(lds + L_MISC + 16 + (wm * 2 + wn) * 4) = 1u;
        done = true;
    };
    auto hook = [&](unsigned rv) { if (__builtin_amdgcn_readfirstlane(rv) != 0u && !done) normalise(); };
#pragma unroll
    for (int n = 0; n < NT; ++n)
#pragma unroll
        for (int m = 0; m < 4; ++m) zp[n][m] = (u32x2){0u, 0u};
#pragma nounroll
    for (int it = 0; it < 2; ++it) {
        gemm_phase<NT, true>(9, acc, so, lds + a0, lds + a1, lds + bw, nullptr, rdy, hook TRA);
        STAMP(1 + 2 * it);
        if (it == 1) late();
        if (it == 1 && !done) {
            const u64 t0 = __builtin_amdgcn_s_memrealtime();
            while (__builtin_amdgcn_readfirstlane(*rdy) == 0u && __builtin_amdgcn_s_memrealtime() - t0 < 400000ull) __builtin_amdgcn_s_sleep(1);
            normalise();
        }
#pragma unroll
        for (int m = 0; m < 4; ++m)
#pragma unroll
            for (int n = 0; n < NT; ++n) { acc[n][m] = gelu4(acc[n][m]); zp[n][m] = (u32x2){pk_bf16(acc[n][m][0], acc[n][m][1]), pk_bf16(acc[n][m][2], acc[n][m][3])}; }
        if (it == 0) {
            LAS unsigned char* ps = opq(lds + L_SP + (wm * 64 + fr) * 16 + wn * 8);
#pragma unroll
            for (int m = 0; m < 4; ++m) {
                f32x4 s4 = acc[0][m];
#pragma unroll
                for (int n = 1; n < NT; ++n) s4 += acc[n][m];
                const float mean = opq(allred_q((s4[0] + s4[1]) + (s4[2] + s4[3])) * (1.0f / (16 * NT)));
                f32x4 q4 = {0.f, 0.f, 0.f, 0.f};
#pragma unroll
                for (int n = 0; n < NT; ++n) { const f32x4 d = acc[n][m] - mean; q4 += d * d; }
                const float m2 = allred_q((q4[0] + q4[1]) + (q4[2] + q4[3]));
                if (fq == 0) *(LAS f32x2*)(ps + m * 256) = (f32x2){mean, m2};
            }
            STAMP(2);
        }
    }
    STAMP(4);
}

template <int NT>
__device__ __forceinline__ void consumer_b(LAS unsigned char* lds, rsrc_t ws, u32x2 T, const u32x2 (&z1)[5][4], const float (&bs_)[4], int pm, int pn, int wm, int wn, int lane, unsigned char* wsp_ TRP) {
    float bs[4] = {bf2f(__float_as_uint(opq(bs_[0]))), bf2f(__float_as_uint(opq(bs_[1]))), bf2f(__float_as_uint(opq(bs_[2]))), bf2f(__float_as_uint(opq(bs_[3])))};
    const int fr = lane & 15, fq = lane >> 4;
    constexpr int N0 = (NT == 5) ? 0 : 5;
    f32x4 acc[NT][4];
    int so = 0;
    const int sw = (fr >> 1) & 7;
    const int a0 = (wm * 64 + fr) * 128 + ((fq ^ sw) << 4), a1 = (wm * 64 + fr) * 128 + (((4 + fq) ^ sw) << 4);
    const int bz = BOFF + N0 * 1024 + lane * 16;
    gemm_phase<NT, false>(32, acc, so, lds + a0, lds + a1, lds + bz, bs, nullptr, [](unsigned) {} TRA);
    STAMP(5);
    __builtin_amdgcn_s_setprio(3);
    const int qm = 2 * pm + wm;
    const unsigned hb = WS_HF + (unsigned)qm * (18u * 16384u);
#pragma unroll
    for (int m = 0; m < 4; ++m) {
        u32x2 hp[NT];
#pragma unroll
        for (int n = 0; n < NT; ++n) {
            const f32x2 u = (f32x2){bflo(z1[n][m][0]), bfhi(z1[n][m][0])} * (f32x2){acc[n][m][0], acc[n][m][1]}, v = (f32x2){bflo(z1[n][m][1]), bfhi(z1[n][m][1])} * (f32x2){acc[n][m][2], acc[n][m][3]};
            hp[n] = (u32x2){pk_bf16(u.x, u.y), pk_bf16(v.x, v.y)};
        }
#pragma unroll
        for (int j = 0; j < 2; ++j)
            __builtin_amdgcn_raw_buffer_store_b128((u32x4){hp[2 * j][0], hp[2 * j][1], hp[2 * j + 1][0], hp[2 * j + 1][1]}, ws,
                                                   hb + (unsigned)pn * 16384u + (unsigned)(m * 4 + wn * 2 + j) * 1024u + lane * 16, 0, 16);
        if (NT == 5) __builtin_amdgcn_raw_buffer_store_b64(hp[NT - 1], ws, hb + (unsigned)(16 + (pn >> 3)) * 16384u + m * 4096u + (unsigned)(pn & 7) * 512u + lane * 8, 0, 16);
    }
    flag_publish(ws, WS_FH + (unsigned)(qm * 32 + pn * 2 + wn) * 128u, T, lane);
    __builtin_amdgcn_s_setprio(0);
    STAMP(6);
}

template <int TLO, int THI>
__device__ __forceinline__ void conv_task(LAS unsigned char* lds, int kt, int ut, int yt, int tl, int lane, int& nb TRP) {
#define CSL() do { if (nb < 50) { BAR(); ++nb; } } while (0)
    constexpr int NTL = THI - TLO + 1, ND = THI + 2;
    const int i = lane & 15, g = lane >> 4;
    const LAS unsigned char* pk = opq(lds + kt + (i & 3) * 1088 + ((i >> 2) + g) * 16);
    const LAS unsigned char* pu = opq(lds + ut + (128 + 16 * tl - 32 * g - 4 * (i >> 2) + 4 * (i & 3)) * 2 - 32);
    bf16x8 ub[ND];
#pragma unroll
    for (int d = 0; d < ND; ++d) { if (d == 0 || d == 8) CSL(); ub[d] = cat8(tr_read(pu + 256 * d + 32), tr_read(pu + 256 * d)); }
    f32x4 acc[NTL];
#pragma unroll
    for (int t = 0; t < NTL; ++t) acc[t] = (f32x4){0.f, 0.f, 0.f, 0.f};
#pragma unroll
    for (int s = -1; s <= THI; ++s) {
        if (TLO == 0 ? (s <= 3 || s == 5 || s == 7) : (s & 1) != 0 && s < 12) CSL();
        const bf16x8 kf = *(const LAS bf16x8*)(pk + 64 * (s + 1));
#pragma unroll
        for (int t = TLO; t <= THI; ++t) if (t >= s) acc[t - TLO] = MFMA16(kf, ub[t - s], acc[t - TLO]);
    }
    LAS float* y = opq((LAS float*)(lds + yt) + 32 * g + 16 * tl + i);
    LAS float* cy = opq((LAS float*)(lds + yt + 8192) + (2 * g + tl) * 4 + (i & 3));
#define DPPF(v, ctl) __int_as_float(__builtin_amdgcn_update_dpp(0, __float_as_int(v), ctl, 0xf, 0xf, true))
#pragma unroll
    for (int t = TLO; t <= THI; ++t) {
        if (((t - TLO) % 2) == 0) CSL();
        const f32x4 a = acc[t - TLO];
        y[128 * t] = (a[0] + DPPF(a[1], 0x111)) + (DPPF(a[2], 0x112) + DPPF(a[3], 0x113));
        const float cv = DPPF(a[1], 0x10f) + (DPPF(a[2], 0x10e) + DPPF(a[3], 0x10d));
        if (i < 4) cy[32 * t] = cv;
    }
#undef DPPF
#undef CSL
}
__device__ __forceinline__ int ytab(int c) { return Z0 + c * Y_SZ; }

__global__ __launch_bounds__(512, 2) void gm_kernel(const u16* __restrict__ x, const u16* __restrict__ logA, const u16* __restrict__ Aim, const u16* __restrict__ Cre,
                                                    const u16* __restrict__ Cim, const u16* __restrict__ logdt, const u16* __restrict__ Dsk, const u16* __restrict__ win,
                                                    const u16* __restrict__ wsp, const u16* __restrict__ bsp, const u16* __restrict__ wout, const u16* __restrict__ gw,
                                                    u16* __restrict__ out, unsigned char* wsp_) {
    extern __shared__ __attribute__((aligned(16))) unsigned char lds_[];
    LAS unsigned char* lds = (LAS unsigned char*)lds_;
    const int wid = __builtin_amdgcn_readfirstlane(threadIdx.x >> 6), lane = threadIdx.x & 63;
    const int bid = blockIdx.x, xg = bid & 7, rr = bid >> 3, bi = rr >> 2, bj = rr & 3;
    const int pm = (xg >> 2) * 8 + bi, pn = (xg & 3) * 4 + bj;
    const int qm = 2 * pm + (pn >> 3), qn = pn & 7;
    const int nch = pm < 4 ? 3 : 2;
    const rsrc_t ws = __builtin_amdgcn_make_buffer_rsrc(wsp_, 0, WS_END, 0x00020000);
    STAMP(0);
    unsigned pfw = 0;
    if (GM_PFW(wid)) {
        u64 pc0; asm volatile("s_getpc_b64 %0" : "=s"(pc0));
        const u64 q_ = (pc0 & ~127ull) + (unsigned)min((rr * GM_PFL + (lane >> 1)) * 128 + (lane & 1) * 64, GM_CODE);
        if (lane < 2 * GM_PFL) asm volatile("global_load_dword %0, %1, off" : "+v"(pfw) : "v"(q_) : "memory");
    }

    const int fr = lane & 15, fq = lane >> 4;
    const int h = wid - 6, p = wid - 4;
    const bool is_prod = (wid >> 1) == 2;
    const int ch0 = pn * 36 + pm;
    float bs[4];
    auto late = [&]() {
#pragma unroll
        for (int m = 0; m < 4; ++m) bs[m] = __uint_as_float((unsigned)bsp[pm * 128 + (wid & 1) * 64 + m * 16 + fr]);
    };
    const int phi = (-(bi + bj)) & 3;
    const unsigned rsw = (unsigned)(((lane & 7) ^ ((4 * p + (lane >> 4)) & 7)) * 16);
#define GLDS(src, dst, aux) __builtin_amdgcn_global_load_lds((const unsigned*)(src), (LAS unsigned*)(dst), 16, 0, aux)
    auto issueS = [&](int k, int s) {
        const char* sb = (const char*)wsp + (size_t)(pm * 128 + p * 8) * 4096 + k * 128; const unsigned vs = (unsigned)(lane >> 3) * 4096u + rsw;
#pragma unroll
        for (int i = 0; i < 8; ++i) GLDS(sb + (size_t)i * (16 * 4096) + vs, lds + s + (p + 2 * i) * 1024, 0); };
    auto ord2 = [&](int s) { return GM_LAG == 0 ? s : (((s & 3) == phi) ? s : ((s - GM_LAG) & 31)); };
    u32x2 E, T;
    auto mk_tag = [&]() { u64 z = (((u64)__builtin_amdgcn_readfirstlane(E[1]) << 32) | __builtin_amdgcn_readfirstlane(E[0])) + 0x9E3779B97F4A7C15ull;
        z = (z ^ (z >> 30)) * 0xBF58476D1CE4E5B9ull; z = (z ^ (z >> 27)) * 0x94D049BB133111EBull; z ^= z >> 31; T = (u32x2){(unsigned)z, (unsigned)(z >> 32)}; };
    u32x2 zp[5][4];
#define STEP(lim) do { if (nb < (lim)) { BAR(); ++nb; } } while (0)
#define STEPS_TO(lim) _Pragma("nounroll") while (nb < (lim)) { BAR(); ++nb; }
    int nb = 0;
    if (wid < nch) {
        const int kch = ch0 + 16 * wid, ki = kch * 64 + lane;
        const unsigned pA = ((const unsigned*)logA)[ki], pI = ((const unsigned*)Aim)[ki], pR = ((const unsigned*)Cre)[ki], pC = ((const unsigned*)Cim)[ki], pT = logdt[kch];
        LAS u32x2* tw = opq((LAS u32x2*)(lds + Z0 + wid * 8448) + lane);
        const LAS unsigned char* trd = opq(lds + Z0 + wid * 8448 + fr * 528 + fq * 16);
        const float dt = rbf(__expf(bf2f(pT)));
        const f32x2 ar = (f32x2){rbf(__expf(bflo(pA))), rbf(__expf(bfhi(pA)))} * dt * -1.44269504f, ai = (f32x2){bflo(pI), bfhi(pI)} * dt * 0.15915494f;
        const f32x2 cr = (f32x2){bflo(pR), bfhi(pR)} * dt, ci = (f32x2){bflo(pC), bfhi(pC)} * dt;
        float cm = fmaxf(fmaxf(fabsf(cr.x), fabsf(ci.x)), fmaxf(fabsf(cr.y), fabsf(ci.y)));
#define DPPF(v, ctl) __int_as_float(__builtin_amdgcn_update_dpp(0, __float_as_int(v), ctl, 0xf, 0xf, true))
        cm = fmaxf(cm, DPPF(cm, 0x121)); cm = fmaxf(cm, DPPF(cm, 0x122)); cm = fmaxf(cm, DPPF(cm, 0x124)); cm = fmaxf(cm, DPPF(cm, 0x128));
#undef DPPF
        { auto r = __builtin_amdgcn_permlane16_swap(__float_as_uint(cm), __float_as_uint(cm), false, false); cm = fmaxf(__uint_as_float(r[0]), __uint_as_float(r[1]));
          auto q = __builtin_amdgcn_permlane32_swap(__float_as_uint(cm), __float_as_uint(cm), false, false); cm = fmaxf(fmaxf(__uint_as_float(q[0]), __uint_as_float(q[1])), 1e-30f); }
        struct C2 { f32x2 x, y; };
        auto cexp = [&](float p_) { const f32x2 e = ar * p_, t = ai * p_;
            const f32x2 mg = {__builtin_amdgcn_exp2f(e.x), __builtin_amdgcn_exp2f(e.y)}; const float t0 = __builtin_amdgcn_fractf(t.x), t1 = __builtin_amdgcn_fractf(t.y);
            return C2{mg * (f32x2){__builtin_amdgcn_cosf(t0), __builtin_amdgcn_cosf(t1)}, mg * (f32x2){__builtin_amdgcn_sinf(t0), __builtin_amdgcn_sinf(t1)}}; };
        auto cmul = [](C2 a, C2 b) { return C2{__builtin_elementwise_fma(a.x, b.x, -(a.y * b.y)), __builtin_elementwise_fma(a.x, b.y, a.y * b.x)}; };
        auto put = [&](int row, C2 v, bool conj) { tw[row * 66] = conj ? (u32x2){pk_f16(v.x.x, -v.y.x), pk_f16(v.x.y, -v.y.y)} : (u32x2){pk_f16(v.x.x, v.y.x), pk_f16(v.x.y, v.y.y)}; };
        C2 wp[16], zr[8], vp[4], cv[17];
        wp[1] = cexp(8.f); zr[1] = cexp(1.f); vp[0] = cexp(128.f);
        tw[0] = (u32x2){pk_f16(1.f, 0.f), pk_f16(1.f, 0.f)};
#pragma unroll
        for (int j = 1; j < 16; ++j) { if (j > 1) wp[j] = cmul(wp[j >> 1], wp[j - (j >> 1)]); put(j, wp[j], true); }
        CFENCE();
        u32x4 fb[8];
#pragma unroll
        for (int ks = 0; ks < 8; ++ks) fb[ks] = *(const LAS u32x4*)(trd + ks * 64);
        CFENCE();
#pragma unroll
        for (int r = 2; r < 8; ++r) zr[r] = cmul(zr[r >> 1], zr[r - (r >> 1)]);
#pragma unroll
        for (int k = 1; k < 4; ++k) vp[k] = cmul(vp[k - 1], vp[k - 1]);
        const float rc = __builtin_amdgcn_rcpf(cm);
        cv[0] = C2{cr * rc, ci * rc};
#pragma unroll
        for (int k = 1; k < 8; ++k) { const int lg = k >= 4 ? 2 : k >= 2 ? 1 : 0; cv[k] = cmul(cv[k - (1 << lg)], vp[lg]); }
        LAS float* kb_ = (LAS float*)(lds + L_KD + wid * 3072);
        const float sc = 2.0f * cm;
        u32x4 fa[3][8];
#pragma unroll
        for (int tt = 0; tt < 3; ++tt) {
#pragma unroll
            for (int i = 0; i < (tt == 2 ? 9 : 16); ++i) {
                const int r = tt == 0 ? (i & 7) : tt == 1 ? (i < 8 ? 2 * (i & 3) : 4 * (i & 1)) : 0;
                const int hh = tt == 0 ? (i >> 3) : tt == 1 ? (i < 8 ? 2 + (i >> 2) : 4 + ((i - 8) >> 1)) : 8 + i;
                if (tt == 2) cv[hh] = cmul(cv[hh - 8], vp[3]);
                put(i, r == 0 ? cv[hh] : cmul(cv[hh], zr[r]), false);
            }
            CFENCE();
#pragma unroll
            for (int ks = 0; ks < 8; ++ks) fa[tt][ks] = *(const LAS u32x4*)(trd + ks * 64);
            CFENCE();
            SB0();
        }
        f32x4 kd[3] = {{0.f, 0.f, 0.f, 0.f}, {0.f, 0.f, 0.f, 0.f}, {0.f, 0.f, 0.f, 0.f}};
#pragma unroll
        for (int ks = 0; ks < 8; ++ks)
#pragma unroll
            for (int tt = 0; tt < 3; ++tt) kd[tt] = __builtin_amdgcn_mfma_f32_16x16x32_f16(__builtin_bit_cast(f16x8, fa[tt][ks]), __builtin_bit_cast(f16x8, fb[ks]), kd[tt], 0, 0, 0);
#pragma unroll
        for (int tt = 0; tt < 3; ++tt) {
            LAS float* kw = opq(kb_ + (tt == 0 ? 128 * (fq >> 1) + 4 * (fq & 1) + 8 * fr : tt == 1 ? 256 + 64 * fq + (fq < 2 ? 4 : 2) * fr : 512 + 64 * fq + fr));
            LAS float* kwb = tt == 1 ? opq(kw + (fq < 2 ? 2 : 32)) : kw + (tt == 0 ? 2 : 32);
            kw[0] = kd[tt][0] * sc; kw[tt == 2 ? 16 : 1] = kd[tt][1] * sc; kwb[0] = kd[tt][2] * sc; kwb[tt == 2 ? 16 : 1] = kd[tt][3] * sc;
        }
        WAITL0();
    }
#define IC(n) std::integral_constant<int, n>{}
    if (GM_PFW(wid)) { WAITV(0); asm volatile("" :: "v"(pfw)); }
    if (wid < 6) STAMP(10);
    if (is_prod) {
        const unsigned vx = (unsigned)(lane >> 3) * 1152u + rsw;
        unsigned wv[9];
#pragma unroll
        for (int i = 0; i < 9; ++i) { const int c = (p + 2 * i) * 64 + lane, rho = c / 18, ch = c - rho * 18;
            const int kap = (rho & 32) + 8 * ((rho >> 2) & 3) + 4 * ((rho >> 4) & 1) + (rho & 3); wv[i] = (unsigned)kap * 9216u + (unsigned)ch * 16u; }
        const char* xb = (const char*)x + (size_t)(pm * 128 + p * 8) * 1152;
        const char* wb = (const char*)win + pn * 288;
        auto issueX = [&](int k, int s) {
#pragma unroll
            for (int i = 0; i < 8; ++i) GLDS(xb + (size_t)i * (16 * 1152) + k * 128 + vx, lds + s + (p + 2 * i) * 1024, 0); };
        auto issueW = [&](int k, int coloff, int s) {
            const char* b = wb + (size_t)k * (64 * 9216) + coloff;
#pragma unroll
            for (int i = 0; i < 9; ++i) GLDS(b + wv[i], lds + s + BOFF + (p + 2 * i) * 1024, 0); };
        auto ord1 = [&](int s) { return (GM_LAG == 0 || (s & 3) == phi) ? s : (s >= 4 ? s - 4 : (s == 0 ? 8 : s + 4)); };
        issueX(ord1(0), 0); issueW(ord1(0), FH * 2, 0); issueX(ord1(1), STG); issueW(ord1(1), FH * 2, STG);
        issueX(ord1(2), 2 * STG); issueW(ord1(2), FH * 2, 2 * STG);
        E = flag_ld(ws, WS_E);
        unsigned wm_ = 0;
        auto touch = [&](const void* base, unsigned off, unsigned last) { const char* q_ = (const char*)base + min(off + lane * 64u, last); asm volatile("global_load_dword %0, %1, off" : "+v"(wm_) : "v"(q_) : "memory"); };
        if (GM_PW & 1) {
#pragma unroll
            for (int i = 0; i < 2; ++i) touch(x, bid * 9216u + p * 4608u + i * 4096u, bid * 9216u + p * 4608u + 4544u);
#pragma unroll
            for (int i = 0; i < 3; ++i) touch(win, bid * 20736u + p * 10368u + i * 4096u, bid * 20736u + p * 10368u + 10304u);
        }
        int so = 0;
        WAITV(35); PBAR();
#pragma nounroll
        for (int S = 1; S < 16; ++S) {
            if (S >= 8 && S <= 10) WAITV(9); else WAITV(17);
            PBAR();
            const int t = S + 2, k = ord1(t < 9 ? t : t < 12 ? t - 3 : t - 12);
            if (t < 9 || t >= 12) issueX(k, so);
            issueW(k, t >= 9 ? 0 : FH * 2, so); so = nextbuf(so);
            if ((GM_PW & 2) && S == 8) {
#pragma unroll
                for (int i = 0; i < 4; ++i) touch(wsp, bid * 32768u + p * 16384u + i * 4096u, 0x7fffffffu);
        if ((GM_PW & 4) && p == 0) {
            const char* q_ = lane < 14 ? (const char*)wsp_ + lane * 0x200000u : lane < 16 ? (const char*)out + (lane - 14) * 0x200000u : lane < 18 ? (const char*)wsp + (size_t)pm * 0x80000u + (lane - 16) * 0x7ffc0u
                           : lane == 18 ? (const char*)bsp : (const char*)Dsk;
            asm volatile("global_load_dword %0, %1, off sc1" : "+v"(wm_) : "v"(q_) : "memory");
        }
            }
        }
        WAITV(17); PBAR(); issueS(2 * pm, 0);
        WAITV(8); PBAR(); issueS(2 * pm + 1, STG);
        asm volatile("" :: "v"(wm_));
        STAMP(1);
    } else if (wid < 4) {
        E = flag_ld(ws, WS_E);
        if (wid >> 1) consumer_a<4>(lds, zp, late, wid & 1, 1, lane, wsp_ TRA);
        else consumer_a<5>(lds, zp, late, wid & 1, 0, lane, wsp_ TRA);
    } else {
        if (h == 0 && lane < 12) *(LAS unsigned*)(lds + L_MISC + lane * 4) = 0u;
        const int row0 = lane / 9, chk0 = lane - row0 * 9;
        const int u = (pn >> 3) * 16 + pm, nblk = u < 8 ? 3 : 2;
        u32x2 uc[18]; u32x4 xv[9], gv[9], wv[3][5];
        if (h == 0) {
            { const char* xb = (const char*)x + (size_t)pm * 128 * 1152 + pn * 72; unsigned off = row0 * 1152 + chk0 * 8; int ch = chk0;
#pragma unroll
              for (int i = 0; i < 9; ++i) { uc[i] = *(const u32x2*)(xb + off); uc[9 + i] = *(const u32x2*)(xb + 64 * 1152 + off); off += 7 * 1152 + 8; if (++ch == 9) { ch = 0; off += 1152 - 72; } } }
            { const char* xr = (const char*)x + (size_t)(pm * 128 + pn * 8 + (lane >> 3)) * 1152 + (lane & 7) * 16; const char* gr = (const char*)gw + (lane & 7) * 16;
#pragma unroll
              for (int i = 0; i < 9; ++i) { xv[i] = *(const u32x4*)(xr + i * 128); gv[i] = *(const u32x4*)(gr + i * 128); } }
        } else {
            unsigned wo_[5], hi_[5];
            { int row = row0, ch = chk0;
#pragma unroll
              for (int i = 0; i < 5; ++i) { const int rw = row < 32 ? row : 31; wo_[i] = (rw & 15) * 1152 + ch * 16; hi_[i] = rw >> 4; row += 7; if (++ch == 9) { ch = 0; ++row; } } }
#pragma unroll
            for (int b = 0; b < 3; ++b) {
                const int blk = (b < nblk) ? u + 32 * b : u, st = blk >> 2, jb = blk & 3;
                const int fA = st < 16 ? st * 144 + (jb < 2 ? 32 * jb : 16 + 32 * jb) : ((st - 16) * 8 + 2 * jb) * 144 + 64, dB = (st < 16 ? 16 : 144) * 1152;
                const char* wb_ = (const char*)wout + (size_t)fA * 1152 + qn * 144;
#pragma unroll
                for (int i = 0; i < 5; ++i) wv[b][i] = *(const u32x4*)(wb_ + (wo_[i] + hi_[i] * dB));
            }
        }
        E = flag_ld(ws, WS_E);
        if (lane < 24) *(LAS u32x4*)(lds + L_KT + (lane >> 3) * KT_SZ + (2 * h + ((lane >> 2) & 1)) * 1088 + (lane & 3) * 16) = (u32x4){0u, 0u, 0u, 0u};
        WAITL0();
        STAMP(10);
        __builtin_amdgcn_s_setprio(GM_HPRIO);
        {
            float kv[2][3];
#pragma nounroll
            for (int ei = 0; ei < 9; ++ei) {
                BAR();
                if (ei > 0) {
                    LAS unsigned char* kt = opq(lds + L_KT + (2 * h + ((ei - 1) >> 2)) * 1088 + 64 + ((ei - 1) & 3) * 256 + lane * 2);
#pragma unroll
                    for (int j = 0; j < 2; ++j)
#pragma unroll
                        for (int c = 0; c < 3; ++c) *(LAS u16*)(kt + c * KT_SZ + j * 128) = (u16)pk_bf16(kv[j][c], 0.f);
                }
#pragma unroll
                for (int j = 0; j < 2; ++j) {
                    const int jj = (ei & 3) * 2 + j, e = jj >= 4 ? 3 : jj >= 2 ? 2 : jj, lag = 256 * jj + 4 * lane + 2 * h + ((ei >> 2) & 1);
                    const int idx = e == 0 ? lag : 128 * e + ((lag + (1 << (e - 1)) - 1 + ((lag >> e) & 1)) >> e);
                    const LAS float* q = opq((const LAS float*)(lds + L_KD) + idx);
#pragma unroll
                    for (int c = 0; c < 3; ++c) kv[j][c] = q[c * 768];
                }
            }
        }
        if (h == 0) {
            LAS unsigned char* pzs = opq(lds + Z0 + lane * 8);
#pragma unroll
            for (int i = 0; i < 18; ++i) *(LAS u32x2*)(pzs + i * 512) = uc[i];
            CFENCE();
            { const LAS unsigned char* pt = opq(lds + Z0 + (8 * (4 * fq + (lane & 3)) + (fr >> 2)) * 72);
              const unsigned ub = WS_UT + (unsigned)(pn * 36 + (lane & 3)) * 4096u + (unsigned)(pm * 128 + 8 * (4 * fq + (fr >> 2))) * 2u;
#pragma unroll
              for (int cc = 0; cc < 9; ++cc) __builtin_amdgcn_raw_buffer_store_b128(__builtin_bit_cast(u32x4, cat8(tr_read(pt + cc * 8), tr_read(pt + 288 + cc * 8))), ws, ub + cc * 16384u, 0, 16); }
            float a = 0.f;
#pragma unroll
            for (int i = 0; i < 9; ++i)
#pragma unroll
                for (int j = 0; j < 4; ++j) { const unsigned xa = xv[i][j], ga = gv[i][j]; a = __builtin_amdgcn_fdot2_f32_bf16(__builtin_bit_cast(bf16x2, xa), __builtin_bit_cast(bf16x2, ga), a, false); }
#define DPPF(v, ctl) __int_as_float(__builtin_amdgcn_update_dpp(0, __float_as_int(v), ctl, 0xf, 0xf, true))
            a += DPPF(a, 0xb1); a += DPPF(a, 0x4e); a += DPPF(a, 0x141);
#undef DPPF
            const float gt = rbf(__builtin_amdgcn_rcpf(1.0f + __expf(-rbf(a))));
            if ((lane & 7) == 0) __builtin_amdgcn_raw_buffer_store_b32(__float_as_uint(gt), ws, WS_GATE + (unsigned)(pm * 128 + pn * 8 + (lane >> 3)) * 4u, 0, 16);
        } else {
            LAS unsigned char* pws = opq(lds + Z0 + 9216 + lane * 16);
            const LAS unsigned char* ptw = opq(lds + Z0 + 9216 + (4 * fq + (fr >> 2)) * 144 + (lane & 3) * 8);
#pragma unroll
            for (int b = 0; b < 3; ++b) {
                if (b < nblk) {
#pragma unroll
                    for (int i = 0; i < 5; ++i) if (i < 4 || lane < 32) *(LAS u32x4*)(pws + i * 1024) = wv[b][i];
                    CFENCE();
                    const unsigned ob = WS_WOF + (unsigned)qn * (18u * 18432u) + (unsigned)(u + 32 * b) * 4608u;
#pragma unroll
                    for (int nn = 0; nn < 5; ++nn) {
                        const u32x4 v = __builtin_bit_cast(u32x4, cat8(tr_read(ptw + nn * 32), tr_read(ptw + 2304 + nn * 32)));
                        if (nn < 4) __builtin_amdgcn_raw_buffer_store_b128(v, ws, ob + nn * 1024 + lane * 16, 0, 16);
                        else if (fr < 8) __builtin_amdgcn_raw_buffer_store_b128(v, ws, ob + 4096 + (fq * 8 + fr) * 16, 0, 16);
                    }
                    CFENCE();
                }
            }
        }
        nb = 9;
        mk_tag();
        WAITL0();
        STEPS_TO(10);
        STAMP(1);
        {
            const f32x4 sp = *(const LAS f32x4*)(lds + L_SP + (h * 64 + lane) * 16);
            const float dm = sp[0] - sp[2];
            const f32x2 mine = {(80.f * sp[0] + 64.f * sp[2]) * (1.0f / 144.f), sp[1] + sp[3] + (80.f * 64.f / 144.f) * dm * dm};
            __builtin_amdgcn_raw_buffer_store_b128((u32x4){T[0], __float_as_uint(mine.x), T[1], __float_as_uint(mine.y)}, ws, WS_ST + (unsigned)((pm * 16 + pn) * 128 + h * 64 + lane) * 16u, 0, 16);
            STEP(18);
            const unsigned so_ = WS_ST + (unsigned)(pm * 16 * 128 + h * 64 + lane) * 16u;
            const u64 t0 = __builtin_amdgcn_s_memrealtime();
            u32x4 st[16];
            for (;;) {
                CFENCE();
#pragma unroll
                for (int j = 0; j < 16; ++j) st[j] = __builtin_amdgcn_raw_buffer_load_b128(ws, so_ + j * 2048u, 0, 16);
                STEP(18);
                unsigned bad = 0;
#pragma unroll
                for (int j = 0; j < 16; ++j) bad |= (st[j][0] ^ T[0]) | (st[j][2] ^ T[1]);
                if (__builtin_amdgcn_ballot_w64(bad != 0u) == 0ull) break;
                if (__builtin_amdgcn_s_memrealtime() - t0 > 300000ull) break;
            }
            STAMP(2);
            STEP(18);
            float mean = 0.f, m2 = 0.f;
#pragma unroll
            for (int j = 0; j < 16; ++j) mean += __uint_as_float(st[j][1]);
            mean *= (1.0f / 16.f);
#pragma unroll
            for (int j = 0; j < 16; ++j) { const float d = __uint_as_float(st[j][1]) - mean; m2 += __uint_as_float(st[j][3]) + 144.f * d * d; }
            const float rstd = __builtin_amdgcn_rsqf(m2 * (1.0f / FH) + 1e-5f);
            *(LAS f32x2*)(lds + L_SP + (h * 64 + lane) * 16) = (f32x2){rstd, -mean * rstd};
            WAITL0();
            if (lane == 0) *(volatile LAS unsigned*)(lds + L_MISC + 4 + h * 4) = 1u;
            const volatile LAS unsigned* zd = (const volatile LAS unsigned*)(lds + L_MISC + 16 + h * 8 + (lane & 1) * 4);
            for (;;) {
                STEP(18);
                if (__builtin_amdgcn_ballot_w64(*zd == 0u) == 0ull) break;
                if (__builtin_amdgcn_s_memrealtime() - t0 > 800000ull) break;
            }
            CFENCE();
            const LAS unsigned char* ptz = opq(lds + Z0 + (h * 64 + 8 * fq + (fr >> 2)) * ZST + (lane & 3) * 8);
            const unsigned zo = WS_Z2F + (unsigned)((pn * 64 + pm * 4 + h * 2) * 9) * 1024u + lane * 16;
            u32x4 zf[18];
#pragma unroll
            for (int i = 0; i < 18; ++i) {
                const LAS unsigned char* pt = ptz + (i / 9) * 32 * ZST + (i % 9) * 32;
                zf[i] = __builtin_bit_cast(u32x4, cat8(tr_read(pt), tr_read(pt + 4 * ZST)));
                __builtin_amdgcn_raw_buffer_store_b128(zf[i], ws, zo + i * 1024, 0, 16);
                if (i == 5 || i == 11) STEP(18);
            }
            STEP(18);
            flag_publish(ws, WS_FZ + (unsigned)(pn * 32 + pm * 2 + h) * 128u, T, lane);
            STAMP(3);
            STEPS_TO(18);
            LAS unsigned char* pr_ = opq(lds + h * STG + BOFF + lane * 16);
#pragma unroll
            for (int i = 0; i < 18; ++i) *(LAS u32x4*)(pr_ + i * 1024) = zf[i];
            WAITL0();
        }
    }

    if (is_prod) {
        mk_tag();
        const char* lin = (const char*)wsp_ + p * 1024 + lane * 16;
        auto issueLA = [&](unsigned off, int s) {
#pragma unroll
            for (int i = 0; i < 8; ++i) GLDS(lin + off + i * 2048, lds + s + (p + 2 * i) * 1024, 16); };
        auto issueLB = [&](unsigned off, int s) {
#pragma unroll
            for (int i = 0; i < 9; ++i) GLDS(lin + off + i * 2048, lds + s + BOFF + (p + 2 * i) * 1024, 16); };
        auto ord3 = [&](int s) { return (GM_LAG == 0 || s >= 16) ? s : (((s & 3) == phi) ? s : ((s - GM_LAG) & 15)); };
        u32x2 pv = {0u, 0u}; unsigned rdy = 0; bool pend = false;
        const char* fp = (const char*)wsp_ + WS_FZ + ((unsigned)pn * 32u + (lane & 31)) * 128u;
        auto poll_issue = [&]() { asm volatile("global_load_dwordx2 %0, %1, off sc1" : "+v"(pv) : "v"(fp) : "memory"); };
        auto poll_take = [&]() { asm volatile("" : "+v"(pv)); rdy |= (unsigned)__builtin_amdgcn_ballot_w64(pv[0] == T[0] && pv[1] == T[1]); };
        auto need = [&](unsigned bits) {
            if (pend) { poll_take(); pend = false; }
            if ((rdy & bits) != bits) {
                const u64 t0 = __builtin_amdgcn_s_memrealtime();
                do { poll_issue(); WAITV(0); poll_take(); } while ((rdy & bits) != bits && __builtin_amdgcn_s_memrealtime() - t0 < 300000ull);
            }
            if (rdy != ~0u) { poll_issue(); pend = true; }
        };
        const unsigned zb = WS_Z2F + (unsigned)pn * (64u * 9216u);
        int so = 2 * STG;
        const int r0 = (ord2(0) >> 1) == pm ? ord2(1) : ord2(0), r1 = (ord2(1) >> 1) == pm ? ord2(0) : ord2(1);
        poll_issue(); pend = true; WAITV(0);
        STAMP(2);
#pragma nounroll
        for (int S = 18; S < 48; ++S) {
            PBAR();
            int k = ord2(S - 16); if ((k >> 1) == pm) k = (k & 1) ? r1 : r0;
            need(1u << k); issueS(k, so); issueLB(zb + (unsigned)k * 18432u, so); so = nextbuf(so);
            WAITV(17);
        }
        const unsigned hb = WS_HF + (unsigned)qm * (18u * 16384u), ob = WS_WOF + (unsigned)qn * (18u * 18432u);
        PBAR();
        fp = (const char*)wsp_ + WS_FZ + ((unsigned)(pn ^ 8) * 32u + (lane & 31)) * 128u; WAITV(0); rdy = 0; pend = false;
        need(~0u);
        issueLB(ob + (unsigned)ord3(0) * 18432u, 2 * STG);
        PBAR();
        issueLB(ob + (unsigned)ord3(1) * 18432u, 0);
        STAMP(3);
        fp = (const char*)wsp_ + WS_FH + ((unsigned)qm * 32u + (lane & 31)) * 128u; rdy = 0;
        need(~0u);
        STAMP(4);
        issueLA(hb + (unsigned)ord3(0) * 16384u, 2 * STG); issueLA(hb + (unsigned)ord3(1) * 16384u, 0);
        so = STG;
        WAITV(8);
#pragma nounroll
        for (int S = 50; S < 66; ++S) {
            PBAR();
            const int k = ord3(S - 48); issueLA(hb + (unsigned)k * 16384u, so); issueLB(ob + (unsigned)k * 18432u, so); so = nextbuf(so);
            WAITV(17);
        }
        PBAR();
        WAITV(0); PBAR();
        PBAR();
        STAMP(5); PDUMP();
        if (bid == 0 && p == 0 && lane == 0) __builtin_amdgcn_raw_buffer_store_b64(T, ws, WS_E, 0, 16);
        return;
    }

    if (wid < 4) {
        mk_tag();
        if (wid >> 1) consumer_b<4>(lds, ws, T, zp, bs, pm, pn, wid & 1, 1, lane, wsp_ TRA);
        else consumer_b<5>(lds, ws, T, zp, bs, pm, pn, wid & 1, 0, lane, wsp_ TRA);
    } else {
        __builtin_amdgcn_s_setprio(GM_HPRIO);
        {   const unsigned fo = WS_FZ + (unsigned)(pn * 32 + (lane & 31)) * 128u;
            const u64 t0 = __builtin_amdgcn_s_memrealtime();
            for (;;) {
                STEP(44);
                CFENCE();
                const u32x2 v = flag_ld(ws, fo);
                STEP(44);
                if (flag_all(v, T, true)) break;
                if (__builtin_amdgcn_s_memrealtime() - t0 > 300000ull) break;
            } }
        u32x4 uu[3][2];
#pragma unroll
        for (int c = 0; c < 3; ++c)
#pragma unroll
            for (int j = 0; j < 2; ++j) uu[c][j] = __builtin_amdgcn_raw_buffer_load_b128(ws, WS_UT + (unsigned)(ch0 + (c < nch ? 16 * c : 0)) * 4096u + h * 2048 + j * 1024 + lane * 16, 0, 16);
#pragma unroll
        for (int c = 0; c < 3; ++c) if (lane < 16) *(LAS u32x4*)(lds + L_UT + c * UT_SZ + h * 4352 + lane * 16) = (u32x4){0u, 0u, 0u, 0u};
        BAR(); BAR(); BAR(); nb += 3;
        LAS unsigned char* pu_ = opq(lds + L_UT + 256 + h * 2048 + lane * 16);
#pragma unroll
        for (int c = 0; c < 3; ++c)
#pragma unroll
            for (int j = 0; j < 2; ++j) *(LAS u32x4*)(pu_ + c * UT_SZ + j * 1024) = uu[c][j];
        WAITL0();
        if (lane == 0) *(volatile LAS unsigned*)(lds + L_MISC + 32 + h * 4) = 1u;
        {   const volatile LAS unsigned* uw = (const volatile LAS unsigned*)(lds + L_MISC + 32 + (lane & 1) * 4);
            const u64 t0 = __builtin_amdgcn_s_memrealtime();
            do { STEP(50); } while (__builtin_amdgcn_ballot_w64(*uw == 0u) != 0ull && __builtin_amdgcn_s_memrealtime() - t0 < 800000ull);
            CFENCE(); }
        STAMP(4);
    }

    if (wid < 4) nb = 50;
    for (;;) {
        if (nb > 45) STEPS_TO(50);
        unsigned tk = 0;
        if (lane == 0) tk = __hip_atomic_fetch_add((LAS unsigned*)(lds + L_MISC), 1u, __ATOMIC_RELAXED, __HIP_MEMORY_SCOPE_WORKGROUP);
        tk = __builtin_amdgcn_readfirstlane(tk);
        if (tk >= (unsigned)(4 * nch)) break;
        const int c = tk >> 2, tl = tk & 1;
        if (tk & 2) conv_task<0, 10>(lds, L_KT + c * KT_SZ, L_UT + c * UT_SZ, ytab(c), tl, lane, nb TRA);
        else conv_task<11, 15>(lds, L_KT + c * KT_SZ, L_UT + c * UT_SZ, ytab(c), tl, lane, nb TRA);
    }
    STEPS_TO(50);
    WAITL0();
    STAMP(7);

    if (wid < 4) {
        const int kq = wid >> 1, mh = wid & 1;
        f32x4 acc[5][2];
        struct F3 { s16x4 alo[4], ahi[4]; bf16x8 b[10]; } f0, f1;
        const LAS unsigned char* ph = lds + 2 * STG + mh * 8192 + kq * 2048 + lane * 16;
        const LAS unsigned char* pw = lds + 2 * STG + BOFF + kq * 9216 + lane * 16;
        const LAS unsigned char* pw4 = lds + 2 * STG + BOFF + kq * 9216 + 4096 + (fq * 8 + (fr & 7)) * 16;
        auto rd3 = [&](F3& f, int i, bool single) {
            const int j = i / 7, r = i % 7;
            if (r < 2) {
                if (!single) { const bf16x8 v = *(const LAS bf16x8*)(ph + r * 4096 + j * 1024); f.alo[j * 2 + r] = __builtin_shufflevector(v, v, 0, 1, 2, 3); f.ahi[j * 2 + r] = __builtin_shufflevector(v, v, 4, 5, 6, 7); }
                else { const LAS unsigned char* q = ph - lane * 8 + r * 4096 + j * 1024; f.alo[j * 2 + r] = *(const LAS s16x4*)q; f.ahi[j * 2 + r] = *(const LAS s16x4*)(q + 512); }
            } else if (r < 6) f.b[j * 5 + r - 2] = *(const LAS bf16x8*)(pw + j * 4608 + (r - 2) * 1024);
            else f.b[j * 5 + 4] = *(const LAS bf16x8*)(pw4 + j * 4608);
        };
        int so = 2 * STG;
        auto step3 = [&](const F3& c, F3& l, int mode  , bool first) {
            const int delta = so == 2 * STG ? -2 * STG : STG; so += delta;
#pragma unroll
            for (int i = 0; i < 20; ++i) {
                const int j = i / 10, n = (i % 10) >> 1, m = i & 1;
                acc[n][m] = MFMA16(c.b[j * 5 + n], cat8(c.alo[j * 2 + m], c.ahi[j * 2 + m]), (first && j == 0) ? ((f32x4){0.f, 0.f, 0.f, 0.f}) : acc[n][m]);
                if (mode < 2 && i < 7) { rd3(l, 2 * i, mode == 1); rd3(l, 2 * i + 1, mode == 1); }
                if (mode < 2) { if (i == 8) { ph += delta; asm volatile("" : "+v"(ph)); } if (i == 9) { pw += delta; asm volatile("" : "+v"(pw)); } if (i == 10) { pw4 += delta; asm volatile("" : "+v"(pw4)); } }
                SB0();
            }
        };
        __builtin_amdgcn_s_waitcnt(0xC07F);
        BAR();
#pragma unroll
        for (int i = 0; i < 14; ++i) rd3(f0, i, false);
        ph -= 2 * STG; pw -= 2 * STG; pw4 -= 2 * STG; so = 0;
        WAITL0(); BAR(); step3(f0, f1, 0, true);
#pragma nounroll
        for (int j = 0; j < 7; ++j) {
            WAITL0(); BAR(); step3(f1, f0, 0, false);
            WAITL0(); BAR(); step3(f0, f1, 0, false);
        }
        WAITL0(); BAR(); step3(f1, f0, 1, false);
        WAITL0(); BAR(); step3(f0, f1, 1, false);
        step3(f1, f0, 2, false);
        STAMP(8);
        LAS unsigned char* prw = opq(lds + L_R + wid * 5120 + lane * 16);
#pragma unroll
        for (int n = 0; n < 5; ++n) { const f32x4 x0 = acc[n][0], x1 = acc[n][1]; *(LAS f32x4*)(prw + n * 1024) = kq ? x0 : x1; acc[n][0] = kq ? x1 : x0; }
        WAITL0();
        BAR();
        const int m = mh * 2 + kq;
        const rsrc_t ors = __builtin_amdgcn_make_buffer_rsrc(out, 0, SEQ * DM * 2, 0x00020000);
        const float g = *(const LAS float*)(lds + L_MISC + 64 + (16 * m + fr) * 4), g1 = rbf(1.0f - g);
        const LAS unsigned char* prr = opq(lds + L_R + (wid ^ 2) * 5120 + lane * 16);
        const LAS unsigned char* pat = opq(lds + L_AT + (4 * fq + (fr >> 2)) * 128 + (16 * m + 4 * (lane & 3)) * 2);
#pragma unroll
        for (int n = 0; n < 5; ++n) {
            const f32x4 b = acc[n][0] + *(const LAS f32x4*)(prr + n * 1024);
            const u32x2 a = __builtin_bit_cast(u32x2, tr_read(pat + n * 2048));
            const u32x2 o = {pk_bf16(g * bflo(a[0]) + g1 * b[0], g * bfhi(a[0]) + g1 * b[1]), pk_bf16(g * bflo(a[1]) + g1 * b[2], g * bfhi(a[1]) + g1 * b[3])};
            if (n < 4 || fq < 2) __builtin_amdgcn_raw_buffer_store_b64(o, ors, (unsigned)((qm * 64 + 16 * m + fr) * 1152 + (qn * 72 + 16 * n + 4 * fq) * 2), 0, GM_OAUX);
        }
    } else {
        int d = pn * 36 + pm;
#pragma nounroll
        for (int c = 0; c < nch; ++c) {
            const float dsk = bf2f(Dsk[d]);
#pragma unroll
            for (int k = 0; k < 2; ++k) {
                BAR(); ++nb;
                const int l0 = h * 1024 + k * 512 + lane * 8;
                f32x4 cv = *(const LAS f32x4*)(lds + ytab(c) + 8192 + (l0 >= 16 ? (l0 >> 4) - 1 : 0) * 16);
                if ((lane & 1) || l0 == 0) cv = (f32x4){0.f, 0.f, 0.f, 0.f};
                const f32x4 y0 = *(const LAS f32x4*)(lds + ytab(c) + l0 * 4) + cv, y1 = *(const LAS f32x4*)(lds + ytab(c) + l0 * 4 + 16);
                const u32x4 uv = *(const LAS u32x4*)(lds + L_UT + c * UT_SZ + 256 + l0 * 2);
                u32x4 o;
#pragma unroll
                for (int j = 0; j < 4; ++j) {
                    const float ya = j < 2 ? y0[2 * j] : y1[2 * j - 4], yb = j < 2 ? y0[2 * j + 1] : y1[2 * j - 3];
                    const unsigned yp = pk_bf16(ya, yb), up = pk_bf16(bflo(uv[j]) * dsk, bfhi(uv[j]) * dsk);
                    o[j] = pk_bf16(bflo(yp) + bflo(up), bfhi(yp) + bfhi(up));
                }
                __builtin_amdgcn_raw_buffer_store_b128(o, ws, WS_AT + (unsigned)d * 4096u + l0 * 2, 0, 16);
            }
            d += 16;
        }
        STEPS_TO(58);
        flag_publish(ws, WS_FA + (unsigned)((pn >> 1) * 64 + (pn & 1) * 32 + pm * 2 + h) * 128u, T, lane);
        STAMP(5);
        STEPS_TO(62);
        if (h == 0) {
            const unsigned fo = WS_FA + (unsigned)(qn * 64 + lane) * 128u;
            const u64 t0 = __builtin_amdgcn_s_memrealtime();
            for (;;) {
                CFENCE();
                const u32x2 v = flag_ld(ws, fo);
                STEP(68); STEP(68);
                if (flag_all(v, T, true)) break;
                if (__builtin_amdgcn_s_memrealtime() - t0 > 300000ull) break;
            }
            ACQ();
            u32x4 av[9];
#pragma unroll
            for (int i = 0; i < 9; ++i) av[i] = __builtin_amdgcn_raw_buffer_load_b128(ws, WS_AT + (unsigned)(qn * 72 + i * 8 + (lane >> 3)) * 4096u + qm * 128 + (lane & 7) * 16, 0, 16);
            STEP(68); STEP(68);
            LAS unsigned char* pa_ = opq(lds + L_AT + lane * 16);
#pragma unroll
            for (int i = 0; i < 9; ++i) *(LAS u32x4*)(pa_ + i * 1024) = av[i];
        } else {
            STEPS_TO(65);
            const unsigned gv = __builtin_amdgcn_raw_buffer_load_b32(ws, WS_GATE + (unsigned)(qm * 64 + lane) * 4u, 0, 16);
            BAR(); BAR(); nb += 2;
            *(LAS unsigned*)(lds + L_MISC + 64 + lane * 4) = gv;
        }
        STEPS_TO(68);
        WAITL0();
        BAR();
    }
    STAMP(9); TRDUMP();
}
}

extern "C" void kernel_launch(void* const* d_in, const int* in_sizes, int n_in, void* d_out, int out_size, void* d_ws, size_t ws_size, hipStream_t stream) {
    (void)in_sizes; (void)n_in; (void)out_size; (void)ws_size;
    (void)hipFuncSetAttribute((const void*)gm::gm_kernel, hipFuncAttributeMaxDynamicSharedMemorySize, gm::LDS_BYTES);
    hipLaunchKernelGGL(gm::gm_kernel, dim3(256), dim3(512), gm::LDS_BYTES, stream, (const u16*)d_in[0], (const u16*)d_in[1], (const u16*)d_in[2], (const u16*)d_in[3],
                       (const u16*)d_in[4], (const u16*)d_in[5], (const u16*)d_in[6], (const u16*)d_in[8], (const u16*)d_in[9], (const u16*)d_in[10],
                       (const u16*)d_in[11], (const u16*)d_in[12], (u16*)d_out, (unsigned char*)d_ws);
}
```

```cpp
#define GM_PWS 4
#include <hip/hip_runtime.h>
#include <stdint.h>
#include <type_traits>

#define LAS __attribute__((address_space(3)))
typedef unsigned short u16;
typedef unsigned long long u64;
typedef short bf16x8 __attribute__((ext_vector_type(8)));
typedef short s16x4 __attribute__((ext_vector_type(4)));
typedef _Float16 f16x8 __attribute__((ext_vector_type(8)));
typedef _Float16 f16x2 __attribute__((ext_vector_type(2)));
typedef __bf16 bf16x2 __attribute__((ext_vector_type(2)));
typedef float f32x4 __attribute__((ext_vector_type(4)));
typedef float f32x2 __attribute__((ext_vector_type(2)));
typedef unsigned u32x4 __attribute__((ext_vector_type(4)));
typedef unsigned u32x2 __attribute__((ext_vector_type(2)));
typedef __amdgpu_buffer_rsrc_t rsrc_t;

namespace gm {
constexpr int SEQ = 2048, DM = 576, FH = 2304;
constexpr unsigned WS_E = 0, WS_FS = 4096, WS_FZ = WS_FS + 65536, WS_FH = WS_FZ + 65536, WS_FA = WS_FH + 131072;
constexpr unsigned WS_ST = 0x60000, WS_GATE = 0xE0000, WS_DBG = 0x1E00000, WS_Z2F = 0x100000, WS_HF = 0xA00000, WS_WOF = 0x1300000, WS_UT = 0x1600000, WS_AT = 0x1900000, WS_END = 0x1C00000, WS_TRC = 0x1C00000;
constexpr int STG = 34816, BOFF = 16384, Z0 = 3 * STG, ZST = 296, T0 = Z0 + 128 * ZST;
constexpr int KT_SZ = 4352, UT_SZ = 4608, Y_SZ = 10240;
constexpr int L_MISC = 163840 - 320;
constexpr int L_KT = L_MISC - 3 * KT_SZ, L_SP = T0, L_KD = Z0 + 3 * 8448, L_UT = Z0 + 3 * Y_SZ;
constexpr int L_AT = Z0, L_R = Z0 + 10240;
constexpr int LDS_BYTES = 163840;
static_assert(L_UT + 3 * UT_SZ <= L_KT && L_KD + 3 * 3072 <= T0 && L_SP + 2048 <= L_KT, "lds map");
#define GM_HPRIO 1
#define GM_OAUX 0
#define GM_PFW(w) ((w) == 3)
#define GM_PFL 13
#define GM_CODE 50176
#define GM_ORD 0
#define GM_PW 3
#define GM_LAG 4

__device__ __forceinline__ float bf2f(unsigned v) { return __uint_as_float(v << 16); }
__device__ __forceinline__ float bflo(unsigned w) { return __uint_as_float(w << 16); }
__device__ __forceinline__ float bfhi(unsigned w) { return __uint_as_float(w & 0xffff0000u); }
__device__ __forceinline__ unsigned pk_bf16(float a, float b) { f32x2 v = {a, b}; return __builtin_bit_cast(unsigned, __builtin_convertvector(v, bf16x2)); }
__device__ __forceinline__ unsigned pk_f16(float a, float b) { f32x2 v = {a, b}; return __builtin_bit_cast(unsigned, __builtin_convertvector(v, f16x2)); }
__device__ __forceinline__ float rbf(float a) { return bflo(pk_bf16(a, a)); }
__device__ __forceinline__ f32x2 gelu2(f32x2 x) {
    const f32x2 w = (x * x) * (-0.10294324f) + (-2.30220819f);
    const f32x2 a = w * x;
    f32x2 e; e.x = __builtin_amdgcn_exp2f(a.x); e.y = __builtin_amdgcn_exp2f(a.y);
    e = e + 1.0f;
    f32x2 r; r.x = __builtin_amdgcn_rcpf(e.x); r.y = __builtin_amdgcn_rcpf(e.y);
    return x * r;
}
__device__ __forceinline__ f32x4 gelu4(f32x4 v) { const f32x2 a = gelu2((f32x2){v[0], v[1]}), b = gelu2((f32x2){v[2], v[3]}); return (f32x4){a.x, a.y, b.x, b.y}; }
__device__ __forceinline__ float allred_q(float v) {
    unsigned u = __float_as_uint(v);
    auto r = __builtin_amdgcn_permlane16_swap(u, u, false, false);
    v = __uint_as_float(r[0]) + __uint_as_float(r[1]);
    u = __float_as_uint(v);
    auto s = __builtin_amdgcn_permlane32_swap(u, u, false, false);
    return __uint_as_float(s[0]) + __uint_as_float(s[1]);
}
#define ACQ() __builtin_amdgcn_fence(__ATOMIC_ACQUIRE, "wavefront")
#define TRP
#define TRA
#define TRDUMP() do { } while (0)
#define BAR() __builtin_amdgcn_s_barrier()
#define PBAR() BAR()
#define PDUMP() TRDUMP()
#define WAITV(n) asm volatile("s_waitcnt vmcnt(" #n ")" ::: "memory")
#define WAITL0() asm volatile("s_waitcnt lgkmcnt(0)" ::: "memory")
#define CFENCE() asm volatile("" ::: "memory")
#define SGB(mask, n, id) __builtin_amdgcn_sched_group_barrier(mask, n, id)
#define MFMA16(a, b, c) __builtin_amdgcn_mfma_f32_16x16x32_bf16(a, b, c, 0, 0, 0)
__device__ __forceinline__ s16x4 tr_read(const LAS unsigned char* p) { return __builtin_amdgcn_ds_read_tr16_b64_v4i16((LAS s16x4*)p); }
__device__ __forceinline__ bf16x8 cat8(s16x4 a, s16x4 b) { return __builtin_shufflevector(a, b, 0, 1, 2, 3, 4, 5, 6, 7); }
template <class P> __device__ __forceinline__ P opq(P p) { asm volatile("" : "+v"(p)); return p; }
__device__ __forceinline__ int nextbuf(int so) { return so == 2 * STG ? 0 : so + STG; }

#define STAMP(i) do { } while (0)

__device__ __forceinline__ u32x2 flag_ld(rsrc_t ws, unsigned off) { return __builtin_amdgcn_raw_buffer_load_b64(ws, off, 0, 16); }
__device__ __forceinline__ bool flag_all(u32x2 v, u32x2 T, bool active) { return __builtin_amdgcn_ballot_w64(active && (v[0] != T[0] || v[1] != T[1])) == 0ull; }
__device__ __forceinline__ void flag_publish(rsrc_t ws, unsigned off, u32x2 T, int lane) {
    WAITV(0);
    if (lane == 0) __builtin_amdgcn_raw_buffer_store_b64(T, ws, off, 0, 16);
}
__device__ __forceinline__ void flag_wait(rsrc_t ws, unsigned base, int n, u32x2 T, int lane) {
    const unsigned off = base + (unsigned)(lane < n ? lane : 0) * 128u;
    const u64 t0 = __builtin_amdgcn_s_memrealtime();
    for (;;) {
        CFENCE();
        const u32x2 v = flag_ld(ws, off);
        if (flag_all(v, T, true)) break;
        if (__builtin_amdgcn_s_memrealtime() - t0 > 300000ull) break;
        __builtin_amdgcn_s_sleep(1);
    }
    ACQ();
}

#define SB0() __builtin_amdgcn_sched_barrier(0)
template <int NT> struct Frag { bf16x8 a[4]; s16x4 lo[NT], hi[NT]; };
template <int NT, bool TR, int HALF>
__device__ __forceinline__ void frag_read(Frag<NT>& f, int i, const LAS unsigned char* pa, const LAS unsigned char* pb) {
    { constexpr int B0 = TR ? 2 : 1; i = i == 0 ? 0 : i <= B0 ? i + 3 : i <= B0 + 3 ? i - B0 : i; }
    if (i < 4) f.a[i] = *(const LAS bf16x8*)(pa + i * 2048);
    else if (TR) { const int n = (i - 4) >> 1; if ((i - 4) & 1) f.hi[n] = tr_read(pb + HALF * 9216 + 4608 + n * 32); else f.lo[n] = tr_read(pb + HALF * 9216 + n * 32); }
    else { const int n = i - 4; const bf16x8 v = *(const LAS bf16x8*)(pb + HALF * 9216 + n * 1024);
           f.lo[n] = __builtin_shufflevector(v, v, 0, 1, 2, 3); f.hi[n] = __builtin_shufflevector(v, v, 4, 5, 6, 7); }
}
template <int NT, bool TR, int LHALF, int S0, int BAT, bool FIRST, bool UPD>
__device__ __forceinline__ void half_step(f32x4 (&acc)[NT][4], const Frag<NT>& c, Frag<NT>& l, const LAS unsigned char*& pa0, const LAS unsigned char*& pa1, const LAS unsigned char*& pb, int delta,
                                          const float* ini, const volatile LAS unsigned* rdy, unsigned& rv TRP) {
    constexpr int NRD = TR ? 4 + 2 * NT : 4 + NT, NM = 4 * NT, NS = NM - S0;
#pragma unroll
    for (int i = 0; i < NM; ++i) {
        const int n = i >> 2, m = i & 3;
        acc[n][m] = MFMA16(cat8(c.lo[n], c.hi[n]), c.a[m], FIRST ? (TR ? ((f32x4){0.f, 0.f, 0.f, 0.f}) : ((f32x4){ini[m], ini[m], ini[m], ini[m]})) : acc[n][m]);
        SB0();
        if (i == BAT) { WAITL0(); BAR(); if (TR) rv = *rdy; }
        if (LHALF >= 0 && i >= S0) {
#pragma unroll
            for (int r = 0; r < NRD; ++r) if ((r * NS) / NRD == i - S0) frag_read<NT, TR, (LHALF > 0 ? 1 : 0)>(l, r, LHALF > 0 ? pa1 : pa0, pb);
        }
        if (UPD) { if (i == NM - 3) { pa0 += delta; asm volatile("" : "+v"(pa0)); } if (i == NM - 2) { pa1 += delta; asm volatile("" : "+v"(pa1)); } if (i == NM - 1) { pb += delta; asm volatile("" : "+v"(pb)); } }
        SB0();
    }
}
template <int NT, bool TR, class HK>
__device__ __forceinline__ void gemm_phase(int nsteps, f32x4 (&acc)[NT][4], int& so, const LAS unsigned char* pa0, const LAS unsigned char* pa1, const LAS unsigned char* pb,
                                           const float* ini, const volatile LAS unsigned* rdy, HK hook TRP) {
    constexpr int NRD = TR ? 4 + 2 * NT : 4 + NT;
    Frag<NT> f0, f1;
    unsigned rv = 0;
    pa0 += so; pa1 += so; pb += so;
    asm volatile("" : "+v"(pa0), "+v"(pa1), "+v"(pb));
    __builtin_amdgcn_s_waitcnt(0xC07F);
    BAR();
#pragma unroll
    for (int i = 0; i < NRD; ++i) frag_read<NT, TR, 0>(f0, i, pa0, pb);
    SB0();
    int delta = so == 2 * STG ? -2 * STG : STG; so += delta;
    half_step<NT, TR, 1, 2, -1, true, true>(acc, f0, f1, pa0, pa1, pb, delta, ini, rdy, rv TRA);
#pragma nounroll
    for (int s = 1; s < nsteps; ++s) {
        delta = so == 2 * STG ? -2 * STG : STG; so += delta;
        half_step<NT, TR, 0, 6, 5, false, false>(acc, f1, f0, pa0, pa1, pb, 0, ini, rdy, rv TRA);
        half_step<NT, TR, 1, 2, -1, false, true>(acc, f0, f1, pa0, pa1, pb, delta, ini, rdy, rv TRA);
        if (TR) hook(rv);
    }
    half_step<NT, TR, -1, 0, -1, false, false>(acc, f1, f0, pa0, pa1, pb, 0, ini, rdy, rv TRA);
}

template <int NT, class LT>
__device__ __forceinline__ void consumer_a(LAS unsigned char* lds, u32x2 (&zp)[5][4], LT late, int wm, int wn, int lane, unsigned char* wsp_ TRP) {
    const int fr = lane & 15, fq = lane >> 4;
    constexpr int N0 = (NT == 5) ? 0 : 5;
    f32x4 acc[NT][4];
    int so = 0;
    const int sw = (fr >> 1) & 7;
    const int a0 = (wm * 64 + fr) * 128 + ((fq ^ sw) << 4), a1 = (wm * 64 + fr) * 128 + (((4 + fq) ^ sw) << 4);
    const int bw = BOFF + (4 * fq + (fr >> 2)) * 288 + (lane & 3) * 8 + N0 * 32;
    const volatile LAS unsigned* rdy = (const volatile LAS unsigned*)(lds + L_MISC + 4 + wm * 4);
    bool done = false;
    auto normalise = [&]() {
        const LAS unsigned char* ps = opq(lds + L_SP + (wm * 64 + fr) * 16);
        LAS unsigned char* pz = opq(lds + Z0 + (wm * 64 + fr) * ZST + N0 * 32 + fq * 8);
#pragma unroll
        for (int m = 0; m < 4; ++m) {
            const f32x2 sr = *(const LAS f32x2*)(ps + m * 256);
#pragma unroll
            for (int n = 0; n < NT; ++n) {
                const f32x2 u = (f32x2){bflo(zp[n][m][0]), bfhi(zp[n][m][0])} * sr.x + sr.y, v = (f32x2){bflo(zp[n][m][1]), bfhi(zp[n][m][1])} * sr.x + sr.y;
                *(LAS u32x2*)(pz + m * 16 * ZST + n * 32) = (u32x2){pk_bf16(u.x, u.y), pk_bf16(v.x, v.y)};
            }
        }
        WAITL0();
        if (lane == 0) *(volatile LAS unsigned*)(lds + L_MISC + 16 + (wm * 2 + wn) * 4) = 1u;
        done = true;
    };
    auto hook = [&](unsigned rv) { if (__builtin_amdgcn_readfirstlane(rv) != 0u && !done) normalise(); };
#pragma unroll
    for (int n = 0; n < NT; ++n)
#pragma unroll
        for (int m = 0; m < 4; ++m) zp[n][m] = (u32x2){0u, 0u};
#pragma nounroll
    for (int it = 0; it < 2; ++it) {
        gemm_phase<NT, true>(9, acc, so, lds + a0, lds + a1, lds + bw, nullptr, rdy, hook TRA);
        STAMP(1 + 2 * it);
        if (it == 1) late();
        if (it == 1 && !done) {
            const u64 t0 = __builtin_amdgcn_s_memrealtime();
            while (__builtin_amdgcn_readfirstlane(*rdy) == 0u && __builtin_amdgcn_s_memrealtime() - t0 < 400000ull) __builtin_amdgcn_s_sleep(1);
            normalise();
        }
#pragma unroll
        for (int m = 0; m < 4; ++m)
#pragma unroll
            for (int n = 0; n < NT; ++n) { acc[n][m] = gelu4(acc[n][m]); zp[n][m] = (u32x2){pk_bf16(acc[n][m][0], acc[n][m][1]), pk_bf16(acc[n][m][2], acc[n][m][3])}; }
        if (it == 0) {
            LAS unsigned char* ps = opq(lds + L_SP + (wm * 64 + fr) * 16 + wn * 8);
#pragma unroll
            for (int m = 0; m < 4; ++m) {
                f32x4 s4 = acc[0][m];
#pragma unroll
                for (int n = 1; n < NT; ++n) s4 += acc[n][m];
                const float mean = opq(allred_q((s4[0] + s4[1]) + (s4[2] + s4[3])) * (1.0f / (16 * NT)));
                f32x4 q4 = {0.f, 0.f, 0.f, 0.f};
#pragma unroll
                for (int n = 0; n < NT; ++n) { const f32x4 d = acc[n][m] - mean; q4 += d * d; }
                const float m2 = allred_q((q4[0] + q4[1]) + (q4[2] + q4[3]));
                if (fq == 0) *(LAS f32x2*)(ps + m * 256) = (f32x2){mean, m2};
            }
            STAMP(2);
        }
    }
    STAMP(4);
}

template <int NT>
__device__ __forceinline__ void consumer_b(LAS unsigned char* lds, rsrc_t ws, u32x2 T, const u32x2 (&z1)[5][4], const float (&bs_)[4], int pm, int pn, int wm, int wn, int lane, unsigned char* wsp_ TRP) {
    float bs[4] = {bf2f(__float_as_uint(opq(bs_[0]))), bf2f(__float_as_uint(opq(bs_[1]))), bf2f(__float_as_uint(opq(bs_[2]))), bf2f(__float_as_uint(opq(bs_[3])))};
    const int fr = lane & 15, fq = lane >> 4;
    constexpr int N0 = (NT == 5) ? 0 : 5;
    f32x4 acc[NT][4];
    int so = 0;
    const int sw = (fr >> 1) & 7;
    const int a0 = (wm * 64 + fr) * 128 + ((fq ^ sw) << 4), a1 = (wm * 64 + fr) * 128 + (((4 + fq) ^ sw) << 4);
    const int bz = BOFF + N0 * 1024 + lane * 16;
    gemm_phase<NT, false>(32, acc, so, lds + a0, lds + a1, lds + bz, bs, nullptr, [](unsigned) {} TRA);
    STAMP(5);
    __builtin_amdgcn_s_setprio(3);
    const int qm = 2 * pm + wm;
    const unsigned hb = WS_HF + (unsigned)qm * (18u * 16384u);
#pragma unroll
    for (int m = 0; m < 4; ++m) {
        u32x2 hp[NT];
#pragma unroll
        for (int n = 0; n < NT; ++n) {
            const f32x2 u = (f32x2){bflo(z1[n][m][0]), bfhi(z1[n][m][0])} * (f32x2){acc[n][m][0], acc[n][m][1]}, v = (f32x2){bflo(z1[n][m][1]), bfhi(z1[n][m][1])} * (f32x2){acc[n][m][2], acc[n][m][3]};
            hp[n] = (u32x2){pk_bf16(u.x, u.y), pk_bf16(v.x, v.y)};
        }
#pragma unroll
        for (int j = 0; j < 2; ++j)
            __builtin_amdgcn_raw_buffer_store_b128((u32x4){hp[2 * j][0], hp[2 * j][1], hp[2 * j + 1][0], hp[2 * j + 1][1]}, ws,
                                                   hb + (unsigned)pn * 16384u + (unsigned)(m * 4 + wn * 2 + j) * 1024u + lane * 16, 0, 16);
        if (NT == 5) __builtin_amdgcn_raw_buffer_store_b64(hp[NT - 1], ws, hb + (unsigned)(16 + (pn >> 3)) * 16384u + m * 4096u + (unsigned)(pn & 7) * 512u + lane * 8, 0, 16);
    }
    flag_publish(ws, WS_FH + (unsigned)(qm * 32 + pn * 2 + wn) * 128u, T, lane);
    __builtin_amdgcn_s_setprio(0);
    STAMP(6);
}

template <int TLO, int THI>
__device__ __forceinline__ void conv_task(LAS unsigned char* lds, int kt, int ut, int yt, int tl, int lane, int& nb TRP) {
#define CSL() do { if (nb < 50) { BAR(); ++nb; } } while (0)
    constexpr int NTL = THI - TLO + 1, ND = THI + 2;
    const int i = lane & 15, g = lane >> 4;
    const LAS unsigned char* pk = opq(lds + kt + (i & 3) * 1088 + ((i >> 2) + g) * 16);
    const LAS unsigned char* pu = opq(lds + ut + (128 + 16 * tl - 32 * g - 4 * (i >> 2) + 4 * (i & 3)) * 2 - 32);
    bf16x8 ub[ND];
#pragma unroll
    for (int d = 0; d < ND; ++d) { if (d == 0 || d == 8) CSL(); ub[d] = cat8(tr_read(pu + 256 * d + 32), tr_read(pu + 256 * d)); }
    f32x4 acc[NTL];
#pragma unroll
    for (int t = 0; t < NTL; ++t) acc[t] = (f32x4){0.f, 0.f, 0.f, 0.f};
#pragma unroll
    for (int s = -1; s <= THI; ++s) {
        if (TLO == 0 ? (s <= 3 || s == 5 || s == 7) : (s & 1) != 0 && s < 12) CSL();
        const bf16x8 kf = *(const LAS bf16x8*)(pk + 64 * (s + 1));
#pragma unroll
        for (int t = TLO; t <= THI; ++t) if (t >= s) acc[t - TLO] = MFMA16(kf, ub[t - s], acc[t - TLO]);
    }
    LAS float* y = opq((LAS float*)(lds + yt) + 32 * g + 16 * tl + i);
    LAS float* cy = opq((LAS float*)(lds + yt + 8192) + (2 * g + tl) * 4 + (i & 3));
#define DPPF(v, ctl) __int_as_float(__builtin_amdgcn_update_dpp(0, __float_as_int(v), ctl, 0xf, 0xf, true))
#pragma unroll
    for (int t = TLO; t <= THI; ++t) {
        if (((t - TLO) % 2) == 0) CSL();
        const f32x4 a = acc[t - TLO];
        y[128 * t] = (a[0] + DPPF(a[1], 0x111)) + (DPPF(a[2], 0x112) + DPPF(a[3], 0x113));
        const float cv = DPPF(a[1], 0x10f) + (DPPF(a[2], 0x10e) + DPPF(a[3], 0x10d));
        if (i < 4) cy[32 * t] = cv;
    }
#undef DPPF
#undef CSL
}
__device__ __forceinline__ int ytab(int c) { return Z0 + c * Y_SZ; }

__global__ __launch_bounds__(512, 2) void gm_kernel(const u16* __restrict__ x, const u16* __restrict__ logA, const u16* __restrict__ Aim, const u16* __restrict__ Cre,
                                                    const u16* __restrict__ Cim, const u16* __restrict__ logdt, const u16* __restrict__ Dsk, const u16* __restrict__ win,
                                                    const u16* __restrict__ wsp, const u16* __restrict__ bsp, const u16* __restrict__ wout, const u16* __restrict__ gw,
                                                    u16* __restrict__ out, unsigned char* wsp_) {
    extern __shared__ __attribute__((aligned(16))) unsigned char lds_[];
    LAS unsigned char* lds = (LAS unsigned char*)lds_;
    const int wid = __builtin_amdgcn_readfirstlane(threadIdx.x >> 6), lane = threadIdx.x & 63;
    const int bid = blockIdx.x, xg = bid & 7, rr = bid >> 3, bi = rr >> 2, bj = rr & 3;
    const int pm = (xg >> 2) * 8 + bi, pn = (xg & 3) * 4 + bj;
    const int qm = 2 * pm + (pn >> 3), qn = pn & 7;
    const int nch = pm < 4 ? 3 : 2;
    const rsrc_t ws = __builtin_amdgcn_make_buffer_rsrc(wsp_, 0, WS_END, 0x00020000);
    STAMP(0);
    unsigned pfw = 0;
    if (GM_PFW(wid)) {
        u64 pc0; asm volatile("s_getpc_b64 %0" : "=s"(pc0));
        const u64 q_ = (pc0 & ~127ull) + (unsigned)min((rr * GM_PFL + (lane >> 1)) * 128 + (lane & 1) * 64, GM_CODE);
        if (lane < 2 * GM_PFL) asm volatile("global_load_dword %0, %1, off" : "+v"(pfw) : "v"(q_) : "memory");
    }

    const int fr = lane & 15, fq = lane >> 4;
    const int h = wid - 6, p = wid - 4;
    const bool is_prod = (wid >> 1) == 2;
    const int ch0 = pn * 36 + pm;
    float bs[4];
    auto late = [&]() {
#pragma unroll
        for (int m = 0; m < 4; ++m) bs[m] = __uint_as_float((unsigned)bsp[pm * 128 + (wid & 1) * 64 + m * 16 + fr]);
    };
    const int phi = (-(bi + bj)) & 3;
    const unsigned rsw = (unsigned)(((lane & 7) ^ ((4 * p + (lane >> 4)) & 7)) * 16);
#define GLDS(src, dst, aux) __builtin_amdgcn_global_load_lds((const unsigned*)(src), (LAS unsigned*)(dst), 16, 0, aux)
    auto issueS = [&](int k, int s) {
        const char* sb = (const char*)wsp + (size_t)(pm * 128 + p * 8) * 4096 + k * 128; const unsigned vs = (unsigned)(lane >> 3) * 4096u + rsw;
#pragma unroll
        for (int i = 0; i < 8; ++i) GLDS(sb + (size_t)i * (16 * 4096) + vs, lds + s + (p + 2 * i) * 1024, 0); };
    auto ord2 = [&](int s) { return GM_LAG == 0 ? s : (((s & 3) == phi) ? s : ((s - GM_LAG) & 31)); };
    u32x2 E, T;
    auto mk_tag = [&]() { u64 z = (((u64)__builtin_amdgcn_readfirstlane(E[1]) << 32) | __builtin_amdgcn_readfirstlane(E[0])) + 0x9E3779B97F4A7C15ull;
        z = (z ^ (z >> 30)) * 0xBF58476D1CE4E5B9ull; z = (z ^ (z >> 27)) * 0x94D049BB133111EBull; z ^= z >> 31; T = (u32x2){(unsigned)z, (unsigned)(z >> 32)}; };
    u32x2 zp[5][4];
#define STEP(lim) do { if (nb < (lim)) { BAR(); ++nb; } } while (0)
#define STEPS_TO(lim) _Pragma("nounroll") while (nb < (lim)) { BAR(); ++nb; }
    int nb = 0;
    if (wid < nch) {
        const int kch = ch0 + 16 * wid, ki = kch * 64 + lane;
        const unsigned pA = ((const unsigned*)logA)[ki], pI = ((const unsigned*)Aim)[ki], pR = ((const unsigned*)Cre)[ki], pC = ((const unsigned*)Cim)[ki], pT = logdt[kch];
        LAS u32x2* tw = opq((LAS u32x2*)(lds + Z0 + wid * 8448) + lane);
        const LAS unsigned char* trd = opq(lds + Z0 + wid * 8448 + fr * 528 + fq * 16);
        const float dt = rbf(__expf(bf2f(pT)));
        const f32x2 ar = (f32x2){rbf(__expf(bflo(pA))), rbf(__expf(bfhi(pA)))} * dt * -1.44269504f, ai = (f32x2){bflo(pI), bfhi(pI)} * dt * 0.15915494f;
        const f32x2 cr = (f32x2){bflo(pR), bfhi(pR)} * dt, ci = (f32x2){bflo(pC), bfhi(pC)} * dt;
        float cm = fmaxf(fmaxf(fabsf(cr.x), fabsf(ci.x)), fmaxf(fabsf(cr.y), fabsf(ci.y)));
#define DPPF(v, ctl) __int_as_float(__builtin_amdgcn_update_dpp(0, __float_as_int(v), ctl, 0xf, 0xf, true))
        cm = fmaxf(cm, DPPF(cm, 0x121)); cm = fmaxf(cm, DPPF(cm, 0x122)); cm = fmaxf(cm, DPPF(cm, 0x124)); cm = fmaxf(cm, DPPF(cm, 0x128));
#undef DPPF
        { auto r = __builtin_amdgcn_permlane16_swap(__float_as_uint(cm), __float_as_uint(cm), false, false); cm = fmaxf(__uint_as_float(r[0]), __uint_as_float(r[1]));
          auto q = __builtin_amdgcn_permlane32_swap(__float_as_uint(cm), __float_as_uint(cm), false, false); cm = fmaxf(fmaxf(__uint_as_float(q[0]), __uint_as_float(q[1])), 1e-30f); }
        struct C2 { f32x2 x, y; };
        auto cexp = [&](float p_) { const f32x2 e = ar * p_, t = ai * p_;
            const f32x2 mg = {__builtin_amdgcn_exp2f(e.x), __builtin_amdgcn_exp2f(e.y)}; const float t0 = __builtin_amdgcn_fractf(t.x), t1 = __builtin_amdgcn_fractf(t.y);
            return C2{mg * (f32x2){__builtin_amdgcn_cosf(t0), __builtin_amdgcn_cosf(t1)}, mg * (f32x2){__builtin_amdgcn_sinf(t0), __builtin_amdgcn_sinf(t1)}}; };
        auto cmul = [](C2 a, C2 b) { return C2{__builtin_elementwise_fma(a.x, b.x, -(a.y * b.y)), __builtin_elementwise_fma(a.x, b.y, a.y * b.x)}; };
        auto put = [&](int row, C2 v, bool conj) { tw[row * 66] = conj ? (u32x2){pk_f16(v.x.x, -v.y.x), pk_f16(v.x.y, -v.y.y)} : (u32x2){pk_f16(v.x.x, v.y.x), pk_f16(v.x.y, v.y.y)}; };
        C2 wp[16], zr[8], vp[4], cv[17];
        wp[1] = cexp(8.f); zr[1] = cexp(1.f); vp[0] = cexp(128.f);
        tw[0] = (u32x2){pk_f16(1.f, 0.f), pk_f16(1.f, 0.f)};
#pragma unroll
        for (int j = 1; j < 16; ++j) { if (j > 1) wp[j] = cmul(wp[j >> 1], wp[j - (j >> 1)]); put(j, wp[j], true); }
        CFENCE();
        u32x4 fb[8];
#pragma unroll
        for (int ks = 0; ks < 8; ++ks) fb[ks] = *(const LAS u32x4*)(trd + ks * 64);
        CFENCE();
#pragma unroll
        for (int r = 2; r < 8; ++r) zr[r] = cmul(zr[r >> 1], zr[r - (r >> 1)]);
#pragma unroll
        for (int k = 1; k < 4; ++k) vp[k] = cmul(vp[k - 1], vp[k - 1]);
        const float rc = __builtin_amdgcn_rcpf(cm);
        cv[0] = C2{cr * rc, ci * rc};
#pragma unroll
        for (int k = 1; k < 8; ++k) { const int lg = k >= 4 ? 2 : k >= 2 ? 1 : 0; cv[k] = cmul(cv[k - (1 << lg)], vp[lg]); }
        LAS float* kb_ = (LAS float*)(lds + L_KD + wid * 3072);
        const float sc = 2.0f * cm;
        u32x4 fa[3][8];
#pragma unroll
        for (int tt = 0; tt < 3; ++tt) {
#pragma unroll
            for (int i = 0; i < (tt == 2 ? 9 : 16); ++i) {
                const int r = tt == 0 ? (i & 7) : tt == 1 ? (i < 8 ? 2 * (i & 3) : 4 * (i & 1)) : 0;
                const int hh = tt == 0 ? (i >> 3) : tt == 1 ? (i < 8 ? 2 + (i >> 2) : 4 + ((i - 8) >> 1)) : 8 + i;
                if (tt == 2) cv[hh] = cmul(cv[hh - 8], vp[3]);
                put(i, r == 0 ? cv[hh] : cmul(cv[hh], zr[r]), false);
            }
            CFENCE();
#pragma unroll
            for (int ks = 0; ks < 8; ++ks) fa[tt][ks] = *(const LAS u32x4*)(trd + ks * 64);
            CFENCE();
            SB0();
        }
        f32x4 kd[3] = {{0.f, 0.f, 0.f, 0.f}, {0.f, 0.f, 0.f, 0.f}, {0.f, 0.f, 0.f, 0.f}};
#pragma unroll
        for (int ks = 0; ks < 8; ++ks)
#pragma unroll
            for (int tt = 0; tt < 3; ++tt) kd[tt] = __builtin_amdgcn_mfma_f32_16x16x32_f16(__builtin_bit_cast(f16x8, fa[tt][ks]), __builtin_bit_cast(f16x8, fb[ks]), kd[tt], 0, 0, 0);
#pragma unroll
        for (int tt = 0; tt < 3; ++tt) {
            LAS float* kw = opq(kb_ + (tt == 0 ? 128 * (fq >> 1) + 4 * (fq & 1) + 8 * fr : tt == 1 ? 256 + 64 * fq + (fq < 2 ? 4 : 2) * fr : 512 + 64 * fq + fr));
            LAS float* kwb = tt == 1 ? opq(kw + (fq < 2 ? 2 : 32)) : kw + (tt == 0 ? 2 : 32);
            kw[0] = kd[tt][0] * sc; kw[tt == 2 ? 16 : 1] = kd[tt][1] * sc; kwb[0] = kd[tt][2] * sc; kwb[tt == 2 ? 16 : 1] = kd[tt][3] * sc;
        }
        WAITL0();
    }
#define IC(n) std::integral_constant<int, n>{}
    if (GM_PFW(wid)) { WAITV(0); asm volatile("" :: "v"(pfw)); }
    if (wid < 6) STAMP(10);
    if (is_prod) {
        const unsigned vx = (unsigned)(lane >> 3) * 1152u + rsw;
        unsigned wv[9];
#pragma unroll
        for (int i = 0; i < 9; ++i) { const int c = (p + 2 * i) * 64 + lane, rho = c / 18, ch = c - rho * 18;
            const int kap = (rho & 32) + 8 * ((rho >> 2) & 3) + 4 * ((rho >> 4) & 1) + (rho & 3); wv[i] = (unsigned)kap * 9216u + (unsigned)ch * 16u; }
        const char* xb = (const char*)x + (size_t)(pm * 128 + p * 8) * 1152;
        const char* wb = (const char*)win + pn * 288;
        auto issueX = [&](int k, int s) {
#pragma unroll
            for (int i = 0; i < 8; ++i) GLDS(xb + (size_t)i * (16 * 1152) + k * 128 + vx, lds + s + (p + 2 * i) * 1024, 0); };
        auto issueW = [&](int k, int coloff, int s) {
            const char* b = wb + (size_t)k * (64 * 9216) + coloff;
#pragma unroll
            for (int i = 0; i < 9; ++i) GLDS(b + wv[i], lds + s + BOFF + (p + 2 * i) * 1024, 0); };
        auto ord1 = [&](int s) { return (GM_LAG == 0 || (s & 3) == phi) ? s : (s >= 4 ? s - 4 : (s == 0 ? 8 : s + 4)); };
        issueX(ord1(0), 0); issueW(ord1(0), FH * 2, 0); issueX(ord1(1), STG); issueW(ord1(1), FH * 2, STG);
        issueX(ord1(2), 2 * STG); issueW(ord1(2), FH * 2, 2 * STG);
        E = flag_ld(ws, WS_E);
        unsigned wm_ = 0;
        auto touch = [&](const void* base, unsigned off, unsigned last) { const char* q_ = (const char*)base + min(off + lane * 64u, last); asm volatile("global_load_dword %0, %1, off" : "+v"(wm_) : "v"(q_) : "memory"); };
        if (GM_PW & 1) {
#pragma unroll
            for (int i = 0; i < 2; ++i) touch(x, bid * 9216u + p * 4608u + i * 4096u, bid * 9216u + p * 4608u + 4544u);
#pragma unroll
            for (int i = 0; i < 3; ++i) touch(win, bid * 20736u + p * 10368u + i * 4096u, bid * 20736u + p * 10368u + 10304u);
        }
        int so = 0;
        WAITV(35); PBAR();
#pragma nounroll
        for (int S = 1; S < 16; ++S) {
            if (S >= 8 && S <= 10) WAITV(9); else WAITV(17);
            PBAR();
            const int t = S + 2, k = ord1(t < 9 ? t : t < 12 ? t - 3 : t - 12);
            if (t < 9 || t >= 12) issueX(k, so);
            issueW(k, t >= 9 ? 0 : FH * 2, so); so = nextbuf(so);
            if ((GM_PW & 2) && S == GM_PWS) {
#pragma unroll
                for (int i = 0; i < 4; ++i) touch(wsp, bid * 32768u + p * 16384u + i * 4096u, 0x7fffffffu);
            }
        }
        WAITV(17); PBAR(); issueS(2 * pm, 0);
        WAITV(8); PBAR(); issueS(2 * pm + 1, STG);
        asm volatile("" :: "v"(wm_));
        STAMP(1);
    } else if (wid < 4) {
        E = flag_ld(ws, WS_E);
        if (wid >> 1) consumer_a<4>(lds, zp, late, wid & 1, 1, lane, wsp_ TRA);
        else consumer_a<5>(lds, zp, late, wid & 1, 0, lane, wsp_ TRA);
    } else {
        if (h == 0 && lane < 12) *(LAS unsigned*)(lds + L_MISC + lane * 4) = 0u;
        const int row0 = lane / 9, chk0 = lane - row0 * 9;
        const int u = (pn >> 3) * 16 + pm, nblk = u < 8 ? 3 : 2;
        u32x2 uc[18]; u32x4 xv[9], gv[9], wv[3][5];
        if (h == 0) {
            { const char* xb = (const char*)x + (size_t)pm * 128 * 1152 + pn * 72; unsigned off = row0 * 1152 + chk0 * 8; int ch = chk0;
#pragma unroll
              for (int i = 0; i < 9; ++i) { uc[i] = *(const u32x2*)(xb + off); uc[9 + i] = *(const u32x2*)(xb + 64 * 1152 + off); off += 7 * 1152 + 8; if (++ch == 9) { ch = 0; off += 1152 - 72; } } }
            { const char* xr = (const char*)x + (size_t)(pm * 128 + pn * 8 + (lane >> 3)) * 1152 + (lane & 7) * 16; const char* gr = (const char*)gw + (lane & 7) * 16;
#pragma unroll
              for (int i = 0; i < 9; ++i) { xv[i] = *(const u32x4*)(xr + i * 128); gv[i] = *(const u32x4*)(gr + i * 128); } }
        } else {
            unsigned wo_[5], hi_[5];
            { int row = row0, ch = chk0;
#pragma unroll
              for (int i = 0; i < 5; ++i) { const int rw = row < 32 ? row : 31; wo_[i] = (rw & 15) * 1152 + ch * 16; hi_[i] = rw >> 4; row += 7; if (++ch == 9) { ch = 0; ++row; } } }
#pragma unroll
            for (int b = 0; b < 3; ++b) {
                const int blk = (b < nblk) ? u + 32 * b : u, st = blk >> 2, jb = blk & 3;
                const int fA = st < 16 ? st * 144 + (jb < 2 ? 32 * jb : 16 + 32 * jb) : ((st - 16) * 8 + 2 * jb) * 144 + 64, dB = (st < 16 ? 16 : 144) * 1152;
                const char* wb_ = (const char*)wout + (size_t)fA * 1152 + qn * 144;
#pragma unroll
                for (int i = 0; i < 5; ++i) wv[b][i] = *(const u32x4*)(wb_ + (wo_[i] + hi_[i] * dB));
            }
        }
        E = flag_ld(ws, WS_E);
        if (lane < 24) *(LAS u32x4*)(lds + L_KT + (lane >> 3) * KT_SZ + (2 * h + ((lane >> 2) & 1)) * 1088 + (lane & 3) * 16) = (u32x4){0u, 0u, 0u, 0u};
        WAITL0();
        STAMP(10);
        __builtin_amdgcn_s_setprio(GM_HPRIO);
        {
            float kv[2][3];
#pragma nounroll
            for (int ei = 0; ei < 9; ++ei) {
                BAR();
                if (ei > 0) {
                    LAS unsigned char* kt = opq(lds + L_KT + (2 * h + ((ei - 1) >> 2)) * 1088 + 64 + ((ei - 1) & 3) * 256 + lane * 2);
#pragma unroll
                    for (int j = 0; j < 2; ++j)
#pragma unroll
                        for (int c = 0; c < 3; ++c) *(LAS u16*)(kt + c * KT_SZ + j * 128) = (u16)pk_bf16(kv[j][c], 0.f);
                }
#pragma unroll
                for (int j = 0; j < 2; ++j) {
                    const int jj = (ei & 3) * 2 + j, e = jj >= 4 ? 3 : jj >= 2 ? 2 : jj, lag = 256 * jj + 4 * lane + 2 * h + ((ei >> 2) & 1);
                    const int idx = e == 0 ? lag : 128 * e + ((lag + (1 << (e - 1)) - 1 + ((lag >> e) & 1)) >> e);
                    const LAS float* q = opq((const LAS float*)(lds + L_KD) + idx);
#pragma unroll
                    for (int c = 0; c < 3; ++c) kv[j][c] = q[c * 768];
                }
            }
        }
        if (h == 0) {
            LAS unsigned char* pzs = opq(lds + Z0 + lane * 8);
#pragma unroll
            for (int i = 0; i < 18; ++i) *(LAS u32x2*)(pzs + i * 512) = uc[i];
            CFENCE();
            { const LAS unsigned char* pt = opq(lds + Z0 + (8 * (4 * fq + (lane & 3)) + (fr >> 2)) * 72);
              const unsigned ub = WS_UT + (unsigned)(pn * 36 + (lane & 3)) * 4096u + (unsigned)(pm * 128 + 8 * (4 * fq + (fr >> 2))) * 2u;
#pragma unroll
              for (int cc = 0; cc < 9; ++cc) __builtin_amdgcn_raw_buffer_store_b128(__builtin_bit_cast(u32x4, cat8(tr_read(pt + cc * 8), tr_read(pt + 288 + cc * 8))), ws, ub + cc * 16384u, 0, 16); }
            float a = 0.f;
#pragma unroll
            for (int i = 0; i < 9; ++i)
#pragma unroll
                for (int j = 0; j < 4; ++j) { const unsigned xa = xv[i][j], ga = gv[i][j]; a = __builtin_amdgcn_fdot2_f32_bf16(__builtin_bit_cast(bf16x2, xa), __builtin_bit_cast(bf16x2, ga), a, false); }
#define DPPF(v, ctl) __int_as_float(__builtin_amdgcn_update_dpp(0, __float_as_int(v), ctl, 0xf, 0xf, true))
            a += DPPF(a, 0xb1); a += DPPF(a, 0x4e); a += DPPF(a, 0x141);
#undef DPPF
            const float gt = rbf(__builtin_amdgcn_rcpf(1.0f + __expf(-rbf(a))));
            if ((lane & 7) == 0) __builtin_amdgcn_raw_buffer_store_b32(__float_as_uint(gt), ws, WS_GATE + (unsigned)(pm * 128 + pn * 8 + (lane >> 3)) * 4u, 0, 16);
        } else {
            LAS unsigned char* pws = opq(lds + Z0 + 9216 + lane * 16);
            const LAS unsigned char* ptw = opq(lds + Z0 + 9216 + (4 * fq + (fr >> 2)) * 144 + (lane & 3) * 8);
#pragma unroll
            for (int b = 0; b < 3; ++b) {
                if (b < nblk) {
#pragma unroll
                    for (int i = 0; i < 5; ++i) if (i < 4 || lane < 32) *(LAS u32x4*)(pws + i * 1024) = wv[b][i];
                    CFENCE();
                    const unsigned ob = WS_WOF + (unsigned)qn * (18u * 18432u) + (unsigned)(u + 32 * b) * 4608u;
#pragma unroll
                    for (int nn = 0; nn < 5; ++nn) {
                        const u32x4 v = __builtin_bit_cast(u32x4, cat8(tr_read(ptw + nn * 32), tr_read(ptw + 2304 + nn * 32)));
                        if (nn < 4) __builtin_amdgcn_raw_buffer_store_b128(v, ws, ob + nn * 1024 + lane * 16, 0, 16);
                        else if (fr < 8) __builtin_amdgcn_raw_buffer_store_b128(v, ws, ob + 4096 + (fq * 8 + fr) * 16, 0, 16);
                    }
                    CFENCE();
                }
            }
        }
        nb = 9;
        mk_tag();
        WAITL0();
        STEPS_TO(10);
        STAMP(1);
        {
            const f32x4 sp = *(const LAS f32x4*)(lds + L_SP + (h * 64 + lane) * 16);
            const float dm = sp[0] - sp[2];
            const f32x2 mine = {(80.f * sp[0] + 64.f * sp[2]) * (1.0f / 144.f), sp[1] + sp[3] + (80.f * 64.f / 144.f) * dm * dm};
            __builtin_amdgcn_raw_buffer_store_b128((u32x4){T[0], __float_as_uint(mine.x), T[1], __float_as_uint(mine.y)}, ws, WS_ST + (unsigned)((pm * 16 + pn) * 128 + h * 64 + lane) * 16u, 0, 16);
            STEP(18);
            const unsigned so_ = WS_ST + (unsigned)(pm * 16 * 128 + h * 64 + lane) * 16u;
            const u64 t0 = __builtin_amdgcn_s_memrealtime();
            u32x4 st[16];
            for (;;) {
                CFENCE();
#pragma unroll
                for (int j = 0; j < 16; ++j) st[j] = __builtin_amdgcn_raw_buffer_load_b128(ws, so_ + j * 2048u, 0, 16);
                STEP(18);
                unsigned bad = 0;
#pragma unroll
                for (int j = 0; j < 16; ++j) bad |= (st[j][0] ^ T[0]) | (st[j][2] ^ T[1]);
                if (__builtin_amdgcn_ballot_w64(bad != 0u) == 0ull) break;
                if (__builtin_amdgcn_s_memrealtime() - t0 > 300000ull) break;
            }
            STAMP(2);
            STEP(18);
            float mean = 0.f, m2 = 0.f;
#pragma unroll
            for (int j = 0; j < 16; ++j) mean += __uint_as_float(st[j][1]);
            mean *= (1.0f / 16.f);
#pragma unroll
            for (int j = 0; j < 16; ++j) { const float d = __uint_as_float(st[j][1]) - mean; m2 += __uint_as_float(st[j][3]) + 144.f * d * d; }
            const float rstd = __builtin_amdgcn_rsqf(m2 * (1.0f / FH) + 1e-5f);
            *(LAS f32x2*)(lds + L_SP + (h * 64 + lane) * 16) = (f32x2){rstd, -mean * rstd};
            WAITL0();
            if (lane == 0) *(volatile LAS unsigned*)(lds + L_MISC + 4 + h * 4) = 1u;
            const volatile LAS unsigned* zd = (const volatile LAS unsigned*)(lds + L_MISC + 16 + h * 8 + (lane & 1) * 4);
            for (;;) {
                STEP(18);
                if (__builtin_amdgcn_ballot_w64(*zd == 0u) == 0ull) break;
                if (__builtin_amdgcn_s_memrealtime() - t0 > 800000ull) break;
            }
            CFENCE();
            const LAS unsigned char* ptz = opq(lds + Z0 + (h * 64 + 8 * fq + (fr >> 2)) * ZST + (lane & 3) * 8);
            const unsigned zo = WS_Z2F + (unsigned)((pn * 64 + pm * 4 + h * 2) * 9) * 1024u + lane * 16;
            u32x4 zf[18];
#pragma unroll
            for (int i = 0; i < 18; ++i) {
                const LAS unsigned char* pt = ptz + (i / 9) * 32 * ZST + (i % 9) * 32;
                zf[i] = __builtin_bit_cast(u32x4, cat8(tr_read(pt), tr_read(pt + 4 * ZST)));
                __builtin_amdgcn_raw_buffer_store_b128(zf[i], ws, zo + i * 1024, 0, 16);
                if (i == 5 || i == 11) STEP(18);
            }
            STEP(18);
            flag_publish(ws, WS_FZ + (unsigned)(pn * 32 + pm * 2 + h) * 128u, T, lane);
            STAMP(3);
            STEPS_TO(18);
            LAS unsigned char* pr_ = opq(lds + h * STG + BOFF + lane * 16);
#pragma unroll
            for (int i = 0; i < 18; ++i) *(LAS u32x4*)(pr_ + i * 1024) = zf[i];
            WAITL0();
        }
    }

    if (is_prod) {
        mk_tag();
        const char* lin = (const char*)wsp_ + p * 1024 + lane * 16;
        auto issueLA = [&](unsigned off, int s) {
#pragma unroll
            for (int i = 0; i < 8; ++i) GLDS(lin + off + i * 2048, lds + s + (p + 2 * i) * 1024, 16); };
        auto issueLB = [&](unsigned off, int s) {
#pragma unroll
            for (int i = 0; i < 9; ++i) GLDS(lin + off + i * 2048, lds + s + BOFF + (p + 2 * i) * 1024, 16); };
        auto ord3 = [&](int s) { return (GM_LAG == 0 || s >= 16) ? s : (((s & 3) == phi) ? s : ((s - GM_LAG) & 15)); };
        u32x2 pv = {0u, 0u}; unsigned rdy = 0; bool pend = false;
        const char* fp = (const char*)wsp_ + WS_FZ + ((unsigned)pn * 32u + (lane & 31)) * 128u;
        auto poll_issue = [&]() { asm volatile("global_load_dwordx2 %0, %1, off sc1" : "+v"(pv) : "v"(fp) : "memory"); };
        auto poll_take = [&]() { asm volatile("" : "+v"(pv)); rdy |= (unsigned)__builtin_amdgcn_ballot_w64(pv[0] == T[0] && pv[1] == T[1]); };
        auto need = [&](unsigned bits) {
            if (pend) { poll_take(); pend = false; }
            if ((rdy & bits) != bits) {
                const u64 t0 = __builtin_amdgcn_s_memrealtime();
                do { poll_issue(); WAITV(0); poll_take(); } while ((rdy & bits) != bits && __builtin_amdgcn_s_memrealtime() - t0 < 300000ull);
            }
            if (rdy != ~0u) { poll_issue(); pend = true; }
        };
        const unsigned zb = WS_Z2F + (unsigned)pn * (64u * 9216u);
        int so = 2 * STG;
        const int r0 = (ord2(0) >> 1) == pm ? ord2(1) : ord2(0), r1 = (ord2(1) >> 1) == pm ? ord2(0) : ord2(1);
        poll_issue(); pend = true; WAITV(0);
        STAMP(2);
#pragma nounroll
        for (int S = 18; S < 48; ++S) {
            PBAR();
            int k = ord2(S - 16); if ((k >> 1) == pm) k = (k & 1) ? r1 : r0;
            need(1u << k); issueS(k, so); issueLB(zb + (unsigned)k * 18432u, so); so = nextbuf(so);
            WAITV(17);
        }
        const unsigned hb = WS_HF + (unsigned)qm * (18u * 16384u), ob = WS_WOF + (unsigned)qn * (18u * 18432u);
        PBAR();
        fp = (const char*)wsp_ + WS_FZ + ((unsigned)(pn ^ 8) * 32u + (lane & 31)) * 128u; WAITV(0); rdy = 0; pend = false;
        need(~0u);
        issueLB(ob + (unsigned)ord3(0) * 18432u, 2 * STG);
        PBAR();
        issueLB(ob + (unsigned)ord3(1) * 18432u, 0);
        STAMP(3);
        fp = (const char*)wsp_ + WS_FH + ((unsigned)qm * 32u + (lane & 31)) * 128u; rdy = 0;
        need(~0u);
        STAMP(4);
        issueLA(hb + (unsigned)ord3(0) * 16384u, 2 * STG); issueLA(hb + (unsigned)ord3(1) * 16384u, 0);
        so = STG;
        WAITV(8);
#pragma nounroll
        for (int S = 50; S < 66; ++S) {
            PBAR();
            const int k = ord3(S - 48); issueLA(hb + (unsigned)k * 16384u, so); issueLB(ob + (unsigned)k * 18432u, so); so = nextbuf(so);
            WAITV(17);
        }
        PBAR();
        WAITV(0); PBAR();
        PBAR();
        STAMP(5); PDUMP();
        if (bid == 0 && p == 0 && lane == 0) __builtin_amdgcn_raw_buffer_store_b64(T, ws, WS_E, 0, 16);
        return;
    }

    if (wid < 4) {
        mk_tag();
        if (wid >> 1) consumer_b<4>(lds, ws, T, zp, bs, pm, pn, wid & 1, 1, lane, wsp_ TRA);
        else consumer_b<5>(lds, ws, T, zp, bs, pm, pn, wid & 1, 0, lane, wsp_ TRA);
    } else {
        __builtin_amdgcn_s_setprio(GM_HPRIO);
        {   const unsigned fo = WS_FZ + (unsigned)(pn * 32 + (lane & 31)) * 128u;
            const u64 t0 = __builtin_amdgcn_s_memrealtime();
            for (;;) {
                STEP(44);
                CFENCE();
                const u32x2 v = flag_ld(ws, fo);
                STEP(44);
                if (flag_all(v, T, true)) break;
                if (__builtin_amdgcn_s_memrealtime() - t0 > 300000ull) break;
            } }
        u32x4 uu[3][2];
#pragma unroll
        for (int c = 0; c < 3; ++c)
#pragma unroll
            for (int j = 0; j < 2; ++j) uu[c][j] = __builtin_amdgcn_raw_buffer_load_b128(ws, WS_UT + (unsigned)(ch0 + (c < nch ? 16 * c : 0)) * 4096u + h * 2048 + j * 1024 + lane * 16, 0, 16);
#pragma unroll
        for (int c = 0; c < 3; ++c) if (lane < 16) *(LAS u32x4*)(lds + L_UT + c * UT_SZ + h * 4352 + lane * 16) = (u32x4){0u, 0u, 0u, 0u};
        BAR(); BAR(); BAR(); nb += 3;
        LAS unsigned char* pu_ = opq(lds + L_UT + 256 + h * 2048 + lane * 16);
#pragma unroll
        for (int c = 0; c < 3; ++c)
#pragma unroll
            for (int j = 0; j < 2; ++j) *(LAS u32x4*)(pu_ + c * UT_SZ + j * 1024) = uu[c][j];
        WAITL0();
        if (lane == 0) *(volatile LAS unsigned*)(lds + L_MISC + 32 + h * 4) = 1u;
        {   const volatile LAS unsigned* uw = (const volatile LAS unsigned*)(lds + L_MISC + 32 + (lane & 1) * 4);
            const u64 t0 = __builtin_amdgcn_s_memrealtime();
            do { STEP(50); } while (__builtin_amdgcn_ballot_w64(*uw == 0u) != 0ull && __builtin_amdgcn_s_memrealtime() - t0 < 800000ull);
            CFENCE(); }
        STAMP(4);
    }

    if (wid < 4) nb = 50;
    for (;;) {
        if (nb > 45) STEPS_TO(50);
        unsigned tk = 0;
        if (lane == 0) tk = __hip_atomic_fetch_add((LAS unsigned*)(lds + L_MISC), 1u, __ATOMIC_RELAXED, __HIP_MEMORY_SCOPE_WORKGROUP);
        tk = __builtin_amdgcn_readfirstlane(tk);
        if (tk >= (unsigned)(4 * nch)) break;
        const int c = tk >> 2, tl = tk & 1;
        if (tk & 2) conv_task<0, 10>(lds, L_KT + c * KT_SZ, L_UT + c * UT_SZ, ytab(c), tl, lane, nb TRA);
        else conv_task<11, 15>(lds, L_KT + c * KT_SZ, L_UT + c * UT_SZ, ytab(c), tl, lane, nb TRA);
    }
    STEPS_TO(50);
    WAITL0();
    STAMP(7);

    if (wid < 4) {
        const int kq = wid >> 1, mh = wid & 1;
        f32x4 acc[5][2];
        struct F3 { s16x4 alo[4], ahi[4]; bf16x8 b[10]; } f0, f1;
        const LAS unsigned char* ph = lds + 2 * STG + mh * 8192 + kq * 2048 + lane * 16;
        const LAS unsigned char* pw = lds + 2 * STG + BOFF + kq * 9216 + lane * 16;
        const LAS unsigned char* pw4 = lds + 2 * STG + BOFF + kq * 9216 + 4096 + (fq * 8 + (fr & 7)) * 16;
        auto rd3 = [&](F3& f, int i, bool single) {
            const int j = i / 7, r = i % 7;
            if (r < 2) {
                if (!single) { const bf16x8 v = *(const LAS bf16x8*)(ph + r * 4096 + j * 1024); f.alo[j * 2 + r] = __builtin_shufflevector(v, v, 0, 1, 2, 3); f.ahi[j * 2 + r] = __builtin_shufflevector(v, v, 4, 5, 6, 7); }
                else { const LAS unsigned char* q = ph - lane * 8 + r * 4096 + j * 1024; f.alo[j * 2 + r] = *(const LAS s16x4*)q; f.ahi[j * 2 + r] = *(const LAS s16x4*)(q + 512); }
            } else if (r < 6) f.b[j * 5 + r - 2] = *(const LAS bf16x8*)(pw + j * 4608 + (r - 2) * 1024);
            else f.b[j * 5 + 4] = *(const LAS bf16x8*)(pw4 + j * 4608);
        };
        int so = 2 * STG;
        auto step3 = [&](const F3& c, F3& l, int mode  , bool first) {
            const int delta = so == 2 * STG ? -2 * STG : STG; so += delta;
#pragma unroll
            for (int i = 0; i < 20; ++i) {
                const int j = i / 10, n = (i % 10) >> 1, m = i & 1;
                acc[n][m] = MFMA16(c.b[j * 5 + n], cat8(c.alo[j * 2 + m], c.ahi[j * 2 + m]), (first && j == 0) ? ((f32x4){0.f, 0.f, 0.f, 0.f}) : acc[n][m]);
                if (mode < 2 && i < 7) { rd3(l, 2 * i, mode == 1); rd3(l, 2 * i + 1, mode == 1); }
                if (mode < 2) { if (i == 8) { ph += delta; asm volatile("" : "+v"(ph)); } if (i == 9) { pw += delta; asm volatile("" : "+v"(pw)); } if (i == 10) { pw4 += delta; asm volatile("" : "+v"(pw4)); } }
                SB0();
            }
        };
        __builtin_amdgcn_s_waitcnt(0xC07F);
        BAR();
#pragma unroll
        for (int i = 0; i < 14; ++i) rd3(f0, i, false);
        ph -= 2 * STG; pw -= 2 * STG; pw4 -= 2 * STG; so = 0;
        WAITL0(); BAR(); step3(f0, f1, 0, true);
#pragma nounroll
        for (int j = 0; j < 7; ++j) {
            WAITL0(); BAR(); step3(f1, f0, 0, false);
            WAITL0(); BAR(); step3(f0, f1, 0, false);
        }
        WAITL0(); BAR(); step3(f1, f0, 1, false);
        WAITL0(); BAR(); step3(f0, f1, 1, false);
        step3(f1, f0, 2, false);
        STAMP(8);
        LAS unsigned char* prw = opq(lds + L_R + wid * 5120 + lane * 16);
#pragma unroll
        for (int n = 0; n < 5; ++n) { const f32x4 x0 = acc[n][0], x1 = acc[n][1]; *(LAS f32x4*)(prw + n * 1024) = kq ? x0 : x1; acc[n][0] = kq ? x1 : x0; }
        WAITL0();
        BAR();
        const int m = mh * 2 + kq;
        const rsrc_t ors = __builtin_amdgcn_make_buffer_rsrc(out, 0, SEQ * DM * 2, 0x00020000);
        const float g = *(const LAS float*)(lds + L_MISC + 64 + (16 * m + fr) * 4), g1 = rbf(1.0f - g);
        const LAS unsigned char* prr = opq(lds + L_R + (wid ^ 2) * 5120 + lane * 16);
        const LAS unsigned char* pat = opq(lds + L_AT + (4 * fq + (fr >> 2)) * 128 + (16 * m + 4 * (lane & 3)) * 2);
#pragma unroll
        for (int n = 0; n < 5; ++n) {
            const f32x4 b = acc[n][0] + *(const LAS f32x4*)(prr + n * 1024);
            const u32x2 a = __builtin_bit_cast(u32x2, tr_read(pat + n * 2048));
            const u32x2 o = {pk_bf16(g * bflo(a[0]) + g1 * b[0], g * bfhi(a[0]) + g1 * b[1]), pk_bf16(g * bflo(a[1]) + g1 * b[2], g * bfhi(a[1]) + g1 * b[3])};
            if (n < 4 || fq < 2) __builtin_amdgcn_raw_buffer_store_b64(o, ors, (unsigned)((qm * 64 + 16 * m + fr) * 1152 + (qn * 72 + 16 * n + 4 * fq) * 2), 0, GM_OAUX);
        }
    } else {
        int d = pn * 36 + pm;
#pragma nounroll
        for (int c = 0; c < nch; ++c) {
            const float dsk = bf2f(Dsk[d]);
#pragma unroll
            for (int k = 0; k < 2; ++k) {
                BAR(); ++nb;
                const int l0 = h * 1024 + k * 512 + lane * 8;
                f32x4 cv = *(const LAS f32x4*)(lds + ytab(c) + 8192 + (l0 >= 16 ? (l0 >> 4) - 1 : 0) * 16);
                if ((lane & 1) || l0 == 0) cv = (f32x4){0.f, 0.f, 0.f, 0.f};
                const f32x4 y0 = *(const LAS f32x4*)(lds + ytab(c) + l0 * 4) + cv, y1 = *(const LAS f32x4*)(lds + ytab(c) + l0 * 4 + 16);
                const u32x4 uv = *(const LAS u32x4*)(lds + L_UT + c * UT_SZ + 256 + l0 * 2);
                u32x4 o;
#pragma unroll
                for (int j = 0; j < 4; ++j) {
                    const float ya = j < 2 ? y0[2 * j] : y1[2 * j - 4], yb = j < 2 ? y0[2 * j + 1] : y1[2 * j - 3];
                    const unsigned yp = pk_bf16(ya, yb), up = pk_bf16(bflo(uv[j]) * dsk, bfhi(uv[j]) * dsk);
                    o[j] = pk_bf16(bflo(yp) + bflo(up), bfhi(yp) + bfhi(up));
                }
                __builtin_amdgcn_raw_buffer_store_b128(o, ws, WS_AT + (unsigned)d * 4096u + l0 * 2, 0, 16);
            }
            d += 16;
        }
        STEPS_TO(58);
        flag_publish(ws, WS_FA + (unsigned)((pn >> 1) * 64 + (pn & 1) * 32 + pm * 2 + h) * 128u, T, lane);
        STAMP(5);
        STEPS_TO(62);
        if (h == 0) {
            const unsigned fo = WS_FA + (unsigned)(qn * 64 + lane) * 128u;
            const u64 t0 = __builtin_amdgcn_s_memrealtime();
            for (;;) {
                CFENCE();
                const u32x2 v = flag_ld(ws, fo);
                STEP(68); STEP(68);
                if (flag_all(v, T, true)) break;
                if (__builtin_amdgcn_s_memrealtime() - t0 > 300000ull) break;
            }
            ACQ();
            u32x4 av[9];
#pragma unroll
            for (int i = 0; i < 9; ++i) av[i] = __builtin_amdgcn_raw_buffer_load_b128(ws, WS_AT + (unsigned)(qn * 72 + i * 8 + (lane >> 3)) * 4096u + qm * 128 + (lane & 7) * 16, 0, 16);
            STEP(68); STEP(68);
            LAS unsigned char* pa_ = opq(lds + L_AT + lane * 16);
#pragma unroll
            for (int i = 0; i < 9; ++i) *(LAS u32x4*)(pa_ + i * 1024) = av[i];
        } else {
            STEPS_TO(65);
            const unsigned gv = __builtin_amdgcn_raw_buffer_load_b32(ws, WS_GATE + (unsigned)(qm * 64 + lane) * 4u, 0, 16);
            BAR(); BAR(); nb += 2;
            *(LAS unsigned*)(lds + L_MISC + 64 + lane * 4) = gv;
        }
        STEPS_TO(68);
        WAITL0();
        BAR();
    }
    STAMP(9); TRDUMP();
}
}

extern "C" void kernel_launch(void* const* d_in, const int* in_sizes, int n_in, void* d_out, int out_size, void* d_ws, size_t ws_size, hipStream_t stream) {
    (void)in_sizes; (void)n_in; (void)out_size; (void)ws_size;
    (void)hipFuncSetAttribute((const void*)gm::gm_kernel, hipFuncAttributeMaxDynamicSharedMemorySize, gm::LDS_BYTES);
    hipLaunchKernelGGL(gm::gm_kernel, dim3(256), dim3(512), gm::LDS_BYTES, stream, (const u16*)d_in[0], (const u16*)d_in[1], (const u16*)d_in[2], (const u16*)d_in[3],
                       (const u16*)d_in[4], (const u16*)d_in[5], (const u16*)d_in[6], (const u16*)d_in[8], (const u16*)d_in[9], (const u16*)d_in[10],
                       (const u16*)d_in[11], (const u16*)d_in[12], (u16*)d_out, (unsigned char*)d_ws);
}
```

```cpp
#define GM_DRY 0
#include <hip/hip_runtime.h>
#include <stdint.h>
#include <type_traits>

#define LAS __attribute__((address_space(3)))
typedef unsigned short u16;
typedef unsigned long long u64;
typedef short bf16x8 __attribute__((ext_vector_type(8)));
typedef short s16x4 __attribute__((ext_vector_type(4)));
typedef _Float16 f16x8 __attribute__((ext_vector_type(8)));
typedef _Float16 f16x2 __attribute__((ext_vector_type(2)));
typedef __bf16 bf16x2 __attribute__((ext_vector_type(2)));
typedef float f32x4 __attribute__((ext_vector_type(4)));
typedef float f32x2 __attribute__((ext_vector_type(2)));
typedef unsigned u32x4 __attribute__((ext_vector_type(4)));
typedef unsigned u32x2 __attribute__((ext_vector_type(2)));
typedef __amdgpu_buffer_rsrc_t rsrc_t;

namespace gm {
constexpr int SEQ = 2048, DM = 576, FH = 2304;
constexpr unsigned WS_E = 0, WS_FS = 4096, WS_FZ = WS_FS + 65536, WS_FH = WS_FZ + 65536, WS_FA = WS_FH + 131072;
constexpr unsigned WS_ST = 0x60000, WS_GATE = 0xE0000, WS_DBG = 0x1E00000, WS_Z2F = 0x100000, WS_HF = 0xA00000, WS_WOF = 0x1300000, WS_UT = 0x1600000, WS_AT = 0x1900000, WS_END = 0x1C00000, WS_TRC = 0x1C00000;
constexpr int STG = 34816, BOFF = 16384, Z0 = 3 * STG, ZST = 296, T0 = Z0 + 128 * ZST;
constexpr int KT_SZ = 4352, UT_SZ = 4608, Y_SZ = 10240;
constexpr int L_MISC = 163840 - 320;
constexpr int L_KT = L_MISC - 3 * KT_SZ, L_SP = T0, L_KD = Z0 + 3 * 8448, L_UT = Z0 + 3 * Y_SZ;
constexpr int L_AT = Z0, L_R = Z0 + 10240;
constexpr int LDS_BYTES = 163840;
static_assert(L_UT + 3 * UT_SZ <= L_KT && L_KD + 3 * 3072 <= T0 && L_SP + 2048 <= L_KT, "lds map");
#define GM_HPRIO 1
#define GM_OAUX 0
#define GM_PFW(w) ((w) == 6)
#define GM_PFL 13
#define GM_CODE 50176
#define GM_ORD 0
#define GM_PW 3
#define GM_PWS 8
#define GM_LAG 4

__device__ __forceinline__ float bf2f(unsigned v) { return __uint_as_float(v << 16); }
__device__ __forceinline__ float bflo(unsigned w) { return __uint_as_float(w << 16); }
__device__ __forceinline__ float bfhi(unsigned w) { return __uint_as_float(w & 0xffff0000u); }
__device__ __forceinline__ unsigned pk_bf16(float a, float b) { f32x2 v = {a, b}; return __builtin_bit_cast(unsigned, __builtin_convertvector(v, bf16x2)); }
__device__ __forceinline__ unsigned pk_f16(float a, float b) { f32x2 v = {a, b}; return __builtin_bit_cast(unsigned, __builtin_convertvector(v, f16x2)); }
__device__ __forceinline__ float rbf(float a) { return bflo(pk_bf16(a, a)); }
__device__ __forceinline__ f32x2 gelu2(f32x2 x) {
    const f32x2 w = (x * x) * (-0.10294324f) + (-2.30220819f);
    const f32x2 a = w * x;
    f32x2 e; e.x = __builtin_amdgcn_exp2f(a.x); e.y = __builtin_amdgcn_exp2f(a.y);
    e = e + 1.0f;
    f32x2 r; r.x = __builtin_amdgcn_rcpf(e.x); r.y = __builtin_amdgcn_rcpf(e.y);
    return x * r;
}
__device__ __forceinline__ f32x4 gelu4(f32x4 v) { const f32x2 a = gelu2((f32x2){v[0], v[1]}), b = gelu2((f32x2){v[2], v[3]}); return (f32x4){a.x, a.y, b.x, b.y}; }
__device__ __forceinline__ float allred_q(float v) {
    unsigned u = __float_as_uint(v);
    auto r = __builtin_amdgcn_permlane16_swap(u, u, false, false);
    v = __uint_as_float(r[0]) + __uint_as_float(r[1]);
    u = __float_as_uint(v);
    auto s = __builtin_amdgcn_permlane32_swap(u, u, false, false);
    return __uint_as_float(s[0]) + __uint_as_float(s[1]);
}
#define ACQ() __builtin_amdgcn_fence(__ATOMIC_ACQUIRE, "wavefront")
#define TRP
#define TRA
#define TRDUMP() do { } while (0)
#define BAR() __builtin_amdgcn_s_barrier()
#define PBAR() BAR()
#define PDUMP() TRDUMP()
#define WAITV(n) asm volatile("s_waitcnt vmcnt(" #n ")" ::: "memory")
#define WAITL0() asm volatile("s_waitcnt lgkmcnt(0)" ::: "memory")
#define CFENCE() asm volatile("" ::: "memory")
#define SGB(mask, n, id) __builtin_amdgcn_sched_group_barrier(mask, n, id)
#define MFMA16(a, b, c) __builtin_amdgcn_mfma_f32_16x16x32_bf16(a, b, c, 0, 0, 0)
__device__ __forceinline__ s16x4 tr_read(const LAS unsigned char* p) { return __builtin_amdgcn_ds_read_tr16_b64_v4i16((LAS s16x4*)p); }
__device__ __forceinline__ bf16x8 cat8(s16x4 a, s16x4 b) { return __builtin_shufflevector(a, b, 0, 1, 2, 3, 4, 5, 6, 7); }
template <class P> __device__ __forceinline__ P opq(P p) { asm volatile("" : "+v"(p)); return p; }
__device__ __forceinline__ int nextbuf(int so) { return so == 2 * STG ? 0 : so + STG; }

#define STAMP(i) do { } while (0)

__device__ __forceinline__ u32x2 flag_ld(rsrc_t ws, unsigned off) { return __builtin_amdgcn_raw_buffer_load_b64(ws, off, 0, 16); }
__device__ __forceinline__ bool flag_all(u32x2 v, u32x2 T, bool active) { return __builtin_amdgcn_ballot_w64(active && (v[0] != T[0] || v[1] != T[1])) == 0ull; }
__device__ __forceinline__ void flag_publish(rsrc_t ws, unsigned off, u32x2 T, int lane) {
    WAITV(0);
    if (lane == 0) __builtin_amdgcn_raw_buffer_store_b64(T, ws, off, 0, 16);
}
__device__ __forceinline__ void flag_wait(rsrc_t ws, unsigned base, int n, u32x2 T, int lane) {
    const unsigned off = base + (unsigned)(lane < n ? lane : 0) * 128u;
    const u64 t0 = __builtin_amdgcn_s_memrealtime();
    for (;;) {
        CFENCE();
        const u32x2 v = flag_ld(ws, off);
        if (flag_all(v, T, true)) break;
        if (__builtin_amdgcn_s_memrealtime() - t0 > 300000ull) break;
        __builtin_amdgcn_s_sleep(1);
    }
    ACQ();
}

#define SB0() __builtin_amdgcn_sched_barrier(0)
template <int NT> struct Frag { bf16x8 a[4]; s16x4 lo[NT], hi[NT]; };
template <int NT, bool TR, int HALF>
__device__ __forceinline__ void frag_read(Frag<NT>& f, int i, const LAS unsigned char* pa, const LAS unsigned char* pb) {
    { constexpr int B0 = TR ? 2 : 1; i = i == 0 ? 0 : i <= B0 ? i + 3 : i <= B0 + 3 ? i - B0 : i; }
    if (i < 4) f.a[i] = *(const LAS bf16x8*)(pa + i * 2048);
    else if (TR) { const int n = (i - 4) >> 1; if ((i - 4) & 1) f.hi[n] = tr_read(pb + HALF * 9216 + 4608 + n * 32); else f.lo[n] = tr_read(pb + HALF * 9216 + n * 32); }
    else { const int n = i - 4; const bf16x8 v = *(const LAS bf16x8*)(pb + HALF * 9216 + n * 1024);
           f.lo[n] = __builtin_shufflevector(v, v, 0, 1, 2, 3); f.hi[n] = __builtin_shufflevector(v, v, 4, 5, 6, 7); }
}
template <int NT, bool TR, int LHALF, int S0, int BAT, bool FIRST, bool UPD>
__device__ __forceinline__ void half_step(f32x4 (&acc)[NT][4], const Frag<NT>& c, Frag<NT>& l, const LAS unsigned char*& pa0, const LAS unsigned char*& pa1, const LAS unsigned char*& pb, int delta,
                                          const float* ini, const volatile LAS unsigned* rdy, unsigned& rv TRP) {
    constexpr int NRD = TR ? 4 + 2 * NT : 4 + NT, NM = 4 * NT, NS = NM - S0;
#pragma unroll
    for (int i = 0; i < NM; ++i) {
        const int n = i >> 2, m = i & 3;
        acc[n][m] = MFMA16(cat8(c.lo[n], c.hi[n]), c.a[m], FIRST ? (TR ? ((f32x4){0.f, 0.f, 0.f, 0.f}) : ((f32x4){ini[m], ini[m], ini[m], ini[m]})) : acc[n][m]);
        SB0();
        if (i == BAT) { WAITL0(); BAR(); if (TR) rv = *rdy; }
        if (LHALF >= 0 && i >= S0) {
#pragma unroll
            for (int r = 0; r < NRD; ++r) if ((r * NS) / NRD == i - S0) frag_read<NT, TR, (LHALF > 0 ? 1 : 0)>(l, r, LHALF > 0 ? pa1 : pa0, pb);
        }
        if (UPD) { if (i == NM - 3) { pa0 += delta; asm volatile("" : "+v"(pa0)); } if (i == NM - 2) { pa1 += delta; asm volatile("" : "+v"(pa1)); } if (i == NM - 1) { pb += delta; asm volatile("" : "+v"(pb)); } }
        SB0();
    }
}
template <int NT, bool TR, class HK>
__device__ __forceinline__ void gemm_phase(int nsteps, f32x4 (&acc)[NT][4], int& so, const LAS unsigned char* pa0, const LAS unsigned char* pa1, const LAS unsigned char* pb,
                                           const float* ini, const volatile LAS unsigned* rdy, HK hook TRP) {
    constexpr int NRD = TR ? 4 + 2 * NT : 4 + NT;
    Frag<NT> f0, f1;
    unsigned rv = 0;
    pa0 += so; pa1 += so; pb += so;
    asm volatile("" : "+v"(pa0), "+v"(pa1), "+v"(pb));
    __builtin_amdgcn_s_waitcnt(0xC07F);
    BAR();
#pragma unroll
    for (int i = 0; i < NRD; ++i) frag_read<NT, TR, 0>(f0, i, pa0, pb);
    SB0();
    int delta = so == 2 * STG ? -2 * STG : STG; so += delta;
    half_step<NT, TR, 1, 2, -1, true, true>(acc, f0, f1, pa0, pa1, pb, delta, ini, rdy, rv TRA);
#pragma nounroll
    for (int s = 1; s < nsteps; ++s) {
        delta = so == 2 * STG ? -2 * STG : STG; so += delta;
        half_step<NT, TR, 0, 6, 5, false, false>(acc, f1, f0, pa0, pa1, pb, 0, ini, rdy, rv TRA);
        half_step<NT, TR, 1, 2, -1, false, true>(acc, f0, f1, pa0, pa1, pb, delta, ini, rdy, rv TRA);
        if (TR) hook(rv);
    }
    half_step<NT, TR, -1, 0, -1, false, false>(acc, f1, f0, pa0, pa1, pb, 0, ini, rdy, rv TRA);
}

template <int NT, class LT>
__device__ __forceinline__ void consumer_a(LAS unsigned char* lds, u32x2 (&zp)[5][4], LT late, bool dry, int wm, int wn, int lane, unsigned char* wsp_ TRP) {
    const int fr = lane & 15, fq = lane >> 4;
    constexpr int N0 = (NT == 5) ? 0 : 5;
    f32x4 acc[NT][4];
    int so = 0;
    const int sw = (fr >> 1) & 7;
    const int a0 = (wm * 64 + fr) * 128 + ((fq ^ sw) << 4), a1 = (wm * 64 + fr) * 128 + (((4 + fq) ^ sw) << 4);
    const int bw = BOFF + (4 * fq + (fr >> 2)) * 288 + (lane & 3) * 8 + N0 * 32;
    const volatile LAS unsigned* rdy = (const volatile LAS unsigned*)(lds + L_MISC + 4 + wm * 4);
    bool done = false;
    auto normalise = [&]() {
        const LAS unsigned char* ps = opq(lds + L_SP + (wm * 64 + fr) * 16);
        LAS unsigned char* pz = opq(lds + Z0 + (wm * 64 + fr) * ZST + N0 * 32 + fq * 8);
#pragma unroll
        for (int m = 0; m < 4; ++m) {
            const f32x2 sr = *(const LAS f32x2*)(ps + m * 256);
#pragma unroll
            for (int n = 0; n < NT; ++n) {
                const f32x2 u = (f32x2){bflo(zp[n][m][0]), bfhi(zp[n][m][0])} * sr.x + sr.y, v = (f32x2){bflo(zp[n][m][1]), bfhi(zp[n][m][1])} * sr.x + sr.y;
                *(LAS u32x2*)(pz + m * 16 * ZST + n * 32) = (u32x2){pk_bf16(u.x, u.y), pk_bf16(v.x, v.y)};
            }
        }
        WAITL0();
        if (lane == 0) *(volatile LAS unsigned*)(lds + L_MISC + 16 + (wm * 2 + wn) * 4) = 1u;
        done = true;
    };
    auto hook = [&](unsigned rv) { if (__builtin_amdgcn_readfirstlane(rv) != 0u && !done) normalise(); };
#pragma unroll
    for (int n = 0; n < NT; ++n)
#pragma unroll
        for (int m = 0; m < 4; ++m) { zp[n][m] = (u32x2){0u, 0u}; asm volatile("" : "=v"(acc[n][m])); }
#pragma nounroll
    for (int it = (GM_DRY && dry) ? -1 : 0; it < 2; ++it) {
        if (it >= 0) gemm_phase<NT, true>(9, acc, so, lds + a0, lds + a1, lds + bw, nullptr, rdy, hook TRA);
        if (it >= 0) STAMP(1 + 2 * it);
        if (it == 1) late();
        if (it == 1 && !done) {
            const u64 t0 = __builtin_amdgcn_s_memrealtime();
            while (__builtin_amdgcn_readfirstlane(*rdy) == 0u && __builtin_amdgcn_s_memrealtime() - t0 < 400000ull) __builtin_amdgcn_s_sleep(1);
            normalise();
        }
#pragma unroll
        for (int m = 0; m < 4; ++m)
#pragma unroll
            for (int n = 0; n < NT; ++n) { acc[n][m] = gelu4(acc[n][m]); zp[n][m] = (u32x2){pk_bf16(acc[n][m][0], acc[n][m][1]), pk_bf16(acc[n][m][2], acc[n][m][3])}; }
        if (it <= 0) {
            LAS unsigned char* ps = opq(lds + L_SP + (wm * 64 + fr) * 16 + wn * 8);
#pragma unroll
            for (int m = 0; m < 4; ++m) {
                f32x4 s4 = acc[0][m];
#pragma unroll
                for (int n = 1; n < NT; ++n) s4 += acc[n][m];
                const float mean = opq(allred_q((s4[0] + s4[1]) + (s4[2] + s4[3])) * (1.0f / (16 * NT)));
                f32x4 q4 = {0.f, 0.f, 0.f, 0.f};
#pragma unroll
                for (int n = 0; n < NT; ++n) { const f32x4 d = acc[n][m] - mean; q4 += d * d; }
                const float m2 = allred_q((q4[0] + q4[1]) + (q4[2] + q4[3]));
                if (fq == 0) *(LAS f32x2*)(ps + m * 256) = (f32x2){mean, m2};
            }
            STAMP(2);
        }
    }
    STAMP(4);
}

template <int NT>
__device__ __forceinline__ void consumer_b(LAS unsigned char* lds, rsrc_t ws, u32x2 T, const u32x2 (&z1)[5][4], const float (&bs_)[4], int pm, int pn, int wm, int wn, int lane, unsigned char* wsp_ TRP) {
    float bs[4] = {bf2f(__float_as_uint(opq(bs_[0]))), bf2f(__float_as_uint(opq(bs_[1]))), bf2f(__float_as_uint(opq(bs_[2]))), bf2f(__float_as_uint(opq(bs_[3])))};
    const int fr = lane & 15, fq = lane >> 4;
    constexpr int N0 = (NT == 5) ? 0 : 5;
    f32x4 acc[NT][4];
    int so = 0;
    const int sw = (fr >> 1) & 7;
    const int a0 = (wm * 64 + fr) * 128 + ((fq ^ sw) << 4), a1 = (wm * 64 + fr) * 128 + (((4 + fq) ^ sw) << 4);
    const int bz = BOFF + N0 * 1024 + lane * 16;
    gemm_phase<NT, false>(32, acc, so, lds + a0, lds + a1, lds + bz, bs, nullptr, [](unsigned) {} TRA);
    STAMP(5);
    __builtin_amdgcn_s_setprio(3);
    const int qm = 2 * pm + wm;
    const unsigned hb = WS_HF + (unsigned)qm * (18u * 16384u);
#pragma unroll
    for (int m = 0; m < 4; ++m) {
        u32x2 hp[NT];
#pragma unroll
        for (int n = 0; n < NT; ++n) {
            const f32x2 u = (f32x2){bflo(z1[n][m][0]), bfhi(z1[n][m][0])} * (f32x2){acc[n][m][0], acc[n][m][1]}, v = (f32x2){bflo(z1[n][m][1]), bfhi(z1[n][m][1])} * (f32x2){acc[n][m][2], acc[n][m][3]};
            hp[n] = (u32x2){pk_bf16(u.x, u.y), pk_bf16(v.x, v.y)};
        }
#pragma unroll
        for (int j = 0; j < 2; ++j)
            __builtin_amdgcn_raw_buffer_store_b128((u32x4){hp[2 * j][0], hp[2 * j][1], hp[2 * j + 1][0], hp[2 * j + 1][1]}, ws,
                                                   hb + (unsigned)pn * 16384u + (unsigned)(m * 4 + wn * 2 + j) * 1024u + lane * 16, 0, 16);
        if (NT == 5) __builtin_amdgcn_raw_buffer_store_b64(hp[NT - 1], ws, hb + (unsigned)(16 + (pn >> 3)) * 16384u + m * 4096u + (unsigned)(pn & 7) * 512u + lane * 8, 0, 16);
    }
    flag_publish(ws, WS_FH + (unsigned)(qm * 32 + pn * 2 + wn) * 128u, T, lane);
    __builtin_amdgcn_s_setprio(0);
    STAMP(6);
}

template <int TLO, int THI>
__device__ __forceinline__ void conv_task(LAS unsigned char* lds, int kt, int ut, int yt, int tl, int lane, int& nb TRP) {
#define CSL() do { if (nb < 50) { BAR(); ++nb; } } while (0)
    constexpr int NTL = THI - TLO + 1, ND = THI + 2;
    const int i = lane & 15, g = lane >> 4;
    const LAS unsigned char* pk = opq(lds + kt + (i & 3) * 1088 + ((i >> 2) + g) * 16);
    const LAS unsigned char* pu = opq(lds + ut + (128 + 16 * tl - 32 * g - 4 * (i >> 2) + 4 * (i & 3)) * 2 - 32);
    bf16x8 ub[ND];
#pragma unroll
    for (int d = 0; d < ND; ++d) { if (d == 0 || d == 8) CSL(); ub[d] = cat8(tr_read(pu + 256 * d + 32), tr_read(pu + 256 * d)); }
    f32x4 acc[NTL];
#pragma unroll
    for (int t = 0; t < NTL; ++t) acc[t] = (f32x4){0.f, 0.f, 0.f, 0.f};
#pragma unroll
    for (int s = -1; s <= THI; ++s) {
        if (TLO == 0 ? (s <= 3 || s == 5 || s == 7) : (s & 1) != 0 && s < 12) CSL();
        const bf16x8 kf = *(const LAS bf16x8*)(pk + 64 * (s + 1));
#pragma unroll
        for (int t = TLO; t <= THI; ++t) if (t >= s) acc[t - TLO] = MFMA16(kf, ub[t - s], acc[t - TLO]);
    }
    LAS float* y = opq((LAS float*)(lds + yt) + 32 * g + 16 * tl + i);
    LAS float* cy = opq((LAS float*)(lds + yt + 8192) + (2 * g + tl) * 4 + (i & 3));
#define DPPF(v, ctl) __int_as_float(__builtin_amdgcn_update_dpp(0, __float_as_int(v), ctl, 0xf, 0xf, true))
#pragma unroll
    for (int t = TLO; t <= THI; ++t) {
        if (((t - TLO) % 2) == 0) CSL();
        const f32x4 a = acc[t - TLO];
        y[128 * t] = (a[0] + DPPF(a[1], 0x111)) + (DPPF(a[2], 0x112) + DPPF(a[3], 0x113));
        const float cv = DPPF(a[1], 0x10f) + (DPPF(a[2], 0x10e) + DPPF(a[3], 0x10d));
        if (i < 4) cy[32 * t] = cv;
    }
#undef DPPF
#undef CSL
}
__device__ __forceinline__ int ytab(int c) { return Z0 + c * Y_SZ; }

__global__ __launch_bounds__(512, 2) void gm_kernel(const u16* __restrict__ x, const u16* __restrict__ logA, const u16* __restrict__ Aim, const u16* __restrict__ Cre,
                                                    const u16* __restrict__ Cim, const u16* __restrict__ logdt, const u16* __restrict__ Dsk, const u16* __restrict__ win,
                                                    const u16* __restrict__ wsp, const u16* __restrict__ bsp, const u16* __restrict__ wout, const u16* __restrict__ gw,
                                                    u16* __restrict__ out, unsigned char* wsp_) {
    extern __shared__ __attribute__((aligned(16))) unsigned char lds_[];
    LAS unsigned char* lds = (LAS unsigned char*)lds_;
    const int wid = __builtin_amdgcn_readfirstlane(threadIdx.x >> 6), lane = threadIdx.x & 63;
    const int bid = blockIdx.x, xg = bid & 7, rr = bid >> 3, bi = rr >> 2, bj = rr & 3;
    const int pm = (xg >> 2) * 8 + bi, pn = (xg & 3) * 4 + bj;
    const int qm = 2 * pm + (pn >> 3), qn = pn & 7;
    const int nch = pm < 4 ? 3 : 2;
    const rsrc_t ws = __builtin_amdgcn_make_buffer_rsrc(wsp_, 0, WS_END, 0x00020000);
    STAMP(0);
    unsigned pfw = 0;
    if (GM_PFW(wid)) {
        u64 pc0; asm volatile("s_getpc_b64 %0" : "=s"(pc0));
        const u64 q_ = (pc0 & ~127ull) + (unsigned)min((rr * GM_PFL + (lane >> 1)) * 128 + (lane & 1) * 64, GM_CODE);
        if (lane < 2 * GM_PFL) asm volatile("global_load_dword %0, %1, off" : "+v"(pfw) : "v"(q_) : "memory");
    }

    const int fr = lane & 15, fq = lane >> 4;
    const int h = wid - 6, p = wid - 4;
    const bool is_prod = (wid >> 1) == 2;
    const int ch0 = pn * 36 + pm;
    float bs[4];
    auto late = [&]() {
#pragma unroll
        for (int m = 0; m < 4; ++m) bs[m] = __uint_as_float((unsigned)bsp[pm * 128 + (wid & 1) * 64 + m * 16 + fr]);
    };
    const int phi = (-(bi + bj)) & 3;
    const unsigned rsw = (unsigned)(((lane & 7) ^ ((4 * p + (lane >> 4)) & 7)) * 16);
#define GLDS(src, dst, aux) __builtin_amdgcn_global_load_lds((const unsigned*)(src), (LAS unsigned*)(dst), 16, 0, aux)
    auto issueS = [&](int k, int s) {
        const char* sb = (const char*)wsp + (size_t)(pm * 128 + p * 8) * 4096 + k * 128; const unsigned vs = (unsigned)(lane >> 3) * 4096u + rsw;
#pragma unroll
        for (int i = 0; i < 8; ++i) GLDS(sb + (size_t)i * (16 * 4096) + vs, lds + s + (p + 2 * i) * 1024, 0); };
    auto ord2 = [&](int s) { return GM_LAG == 0 ? s : (((s & 3) == phi) ? s : ((s - GM_LAG) & 31)); };
    u32x2 E, T;
    auto mk_tag = [&]() { u64 z = (((u64)__builtin_amdgcn_readfirstlane(E[1]) << 32) | __builtin_amdgcn_readfirstlane(E[0])) + 0x9E3779B97F4A7C15ull;
        z = (z ^ (z >> 30)) * 0xBF58476D1CE4E5B9ull; z = (z ^ (z >> 27)) * 0x94D049BB133111EBull; z ^= z >> 31; T = (u32x2){(unsigned)z, (unsigned)(z >> 32)}; };
    u32x2 zp[5][4];
#define STEP(lim) do { if (nb < (lim)) { BAR(); ++nb; } } while (0)
#define STEPS_TO(lim) _Pragma("nounroll") while (nb < (lim)) { BAR(); ++nb; }
    int nb = 0;
    const int kc = wid == 7 ? 2 : wid == 1 || wid == 2 ? wid - 1 : 3;
    if (kc < nch) {
        const int kch = ch0 + 16 * kc, ki = kch * 64 + lane;
        const unsigned pA = ((const unsigned*)logA)[ki], pI = ((const unsigned*)Aim)[ki], pR = ((const unsigned*)Cre)[ki], pC = ((const unsigned*)Cim)[ki], pT = logdt[kch];
        LAS u32x2* tw = opq((LAS u32x2*)(lds + Z0 + kc * 8448) + lane);
        const LAS unsigned char* trd = opq(lds + Z0 + kc * 8448 + fr * 528 + fq * 16);
        const float dt = rbf(__expf(bf2f(pT)));
        const f32x2 ar = (f32x2){rbf(__expf(bflo(pA))), rbf(__expf(bfhi(pA)))} * dt * -1.44269504f, ai = (f32x2){bflo(pI), bfhi(pI)} * dt * 0.15915494f;
        const f32x2 cr = (f32x2){bflo(pR), bfhi(pR)} * dt, ci = (f32x2){bflo(pC), bfhi(pC)} * dt;
        float cm = fmaxf(fmaxf(fabsf(cr.x), fabsf(ci.x)), fmaxf(fabsf(cr.y), fabsf(ci.y)));
#define DPPF(v, ctl) __int_as_float(__builtin_amdgcn_update_dpp(0, __float_as_int(v), ctl, 0xf, 0xf, true))
        cm = fmaxf(cm, DPPF(cm, 0x121)); cm = fmaxf(cm, DPPF(cm, 0x122)); cm = fmaxf(cm, DPPF(cm, 0x124)); cm = fmaxf(cm, DPPF(cm, 0x128));
#undef DPPF
        { auto r = __builtin_amdgcn_permlane16_swap(__float_as_uint(cm), __float_as_uint(cm), false, false); cm = fmaxf(__uint_as_float(r[0]), __uint_as_float(r[1]));
          auto q = __builtin_amdgcn_permlane32_swap(__float_as_uint(cm), __float_as_uint(cm), false, false); cm = fmaxf(fmaxf(__uint_as_float(q[0]), __uint_as_float(q[1])), 1e-30f); }
        struct C2 { f32x2 x, y; };
        auto cexp = [&](float p_) { const f32x2 e = ar * p_, t = ai * p_;
            const f32x2 mg = {__builtin_amdgcn_exp2f(e.x), __builtin_amdgcn_exp2f(e.y)}; const float t0 = __builtin_amdgcn_fractf(t.x), t1 = __builtin_amdgcn_fractf(t.y);
            return C2{mg * (f32x2){__builtin_amdgcn_cosf(t0), __builtin_amdgcn_cosf(t1)}, mg * (f32x2){__builtin_amdgcn_sinf(t0), __builtin_amdgcn_sinf(t1)}}; };
        auto cmul = [](C2 a, C2 b) { return C2{__builtin_elementwise_fma(a.x, b.x, -(a.y * b.y)), __builtin_elementwise_fma(a.x, b.y, a.y * b.x)}; };
        auto put = [&](int row, C2 v, bool conj) { tw[row * 66] = conj ? (u32x2){pk_f16(v.x.x, -v.y.x), pk_f16(v.x.y, -v.y.y)} : (u32x2){pk_f16(v.x.x, v.y.x), pk_f16(v.x.y, v.y.y)}; };
        C2 wp[16], zr[8], vp[4], cv[17];
        wp[1] = cexp(8.f); zr[1] = cexp(1.f); vp[0] = cexp(128.f);
        tw[0] = (u32x2){pk_f16(1.f, 0.f), pk_f16(1.f, 0.f)};
#pragma unroll
        for (int j = 1; j < 16; ++j) { if (j > 1) wp[j] = cmul(wp[j >> 1], wp[j - (j >> 1)]); put(j, wp[j], true); }
        CFENCE();
        u32x4 fb[8];
#pragma unroll
        for (int ks = 0; ks < 8; ++ks) fb[ks] = *(const LAS u32x4*)(trd + ks * 64);
        CFENCE();
#pragma unroll
        for (int r = 2; r < 8; ++r) zr[r] = cmul(zr[r >> 1], zr[r - (r >> 1)]);
#pragma unroll
        for (int k = 1; k < 4; ++k) vp[k] = cmul(vp[k - 1], vp[k - 1]);
        const float rc = __builtin_amdgcn_rcpf(cm);
        cv[0] = C2{cr * rc, ci * rc};
#pragma unroll
        for (int k = 1; k < 8; ++k) { const int lg = k >= 4 ? 2 : k >= 2 ? 1 : 0; cv[k] = cmul(cv[k - (1 << lg)], vp[lg]); }
        LAS float* kb_ = (LAS float*)(lds + L_KD + kc * 3072);
        const float sc = 2.0f * cm;
        u32x4 fa[3][8];
#pragma unroll
        for (int tt = 0; tt < 3; ++tt) {
#pragma unroll
            for (int i = 0; i < (tt == 2 ? 9 : 16); ++i) {
                const int r = tt == 0 ? (i & 7) : tt == 1 ? (i < 8 ? 2 * (i & 3) : 4 * (i & 1)) : 0;
                const int hh = tt == 0 ? (i >> 3) : tt == 1 ? (i < 8 ? 2 + (i >> 2) : 4 + ((i - 8) >> 1)) : 8 + i;
                if (tt == 2) cv[hh] = cmul(cv[hh - 8], vp[3]);
                put(i, r == 0 ? cv[hh] : cmul(cv[hh], zr[r]), false);
            }
            CFENCE();
#pragma unroll
            for (int ks = 0; ks < 8; ++ks) fa[tt][ks] = *(const LAS u32x4*)(trd + ks * 64);
            CFENCE();
            SB0();
        }
        f32x4 kd[3] = {{0.f, 0.f, 0.f, 0.f}, {0.f, 0.f, 0.f, 0.f}, {0.f, 0.f, 0.f, 0.f}};
#pragma unroll
        for (int ks = 0; ks < 8; ++ks)
#pragma unroll
            for (int tt = 0; tt < 3; ++tt) kd[tt] = __builtin_amdgcn_mfma_f32_16x16x32_f16(__builtin_bit_cast(f16x8, fa[tt][ks]), __builtin_bit_cast(f16x8, fb[ks]), kd[tt], 0, 0, 0);
#pragma unroll
        for (int tt = 0; tt < 3; ++tt) {
            LAS float* kw = opq(kb_ + (tt == 0 ? 128 * (fq >> 1) + 4 * (fq & 1) + 8 * fr : tt == 1 ? 256 + 64 * fq + (fq < 2 ? 4 : 2) * fr : 512 + 64 * fq + fr));
            LAS float* kwb = tt == 1 ? opq(kw + (fq < 2 ? 2 : 32)) : kw + (tt == 0 ? 2 : 32);
            kw[0] = kd[tt][0] * sc; kw[tt == 2 ? 16 : 1] = kd[tt][1] * sc; kwb[0] = kd[tt][2] * sc; kwb[tt == 2 ? 16 : 1] = kd[tt][3] * sc;
        }
        WAITL0();
    }
#define IC(n) std::integral_constant<int, n>{}
    if (GM_PFW(wid)) { WAITV(0); asm volatile("" :: "v"(pfw)); }
    if (wid < 6) STAMP(10);
    if (is_prod) {
        const unsigned vx = (unsigned)(lane >> 3) * 1152u + rsw;
        unsigned wv[9];
#pragma unroll
        for (int i = 0; i < 9; ++i) { const int c = (p + 2 * i) * 64 + lane, rho = c / 18, ch = c - rho * 18;
            const int kap = (rho & 32) + 8 * ((rho >> 2) & 3) + 4 * ((rho >> 4) & 1) + (rho & 3); wv[i] = (unsigned)kap * 9216u + (unsigned)ch * 16u; }
        const char* xb = (const char*)x + (size_t)(pm * 128 + p * 8) * 1152;
        const char* wb = (const char*)win + pn * 288;
        auto issueX = [&](int k, int s) {
#pragma unroll
            for (int i = 0; i < 8; ++i) GLDS(xb + (size_t)i * (16 * 1152) + k * 128 + vx, lds + s + (p + 2 * i) * 1024, 0); };
        auto issueW = [&](int k, int coloff, int s) {
            const char* b = wb + (size_t)k * (64 * 9216) + coloff;
#pragma unroll
            for (int i = 0; i < 9; ++i) GLDS(b + wv[i], lds + s + BOFF + (p + 2 * i) * 1024, 0); };
        auto ord1 = [&](int s) { return (GM_LAG == 0 || (s & 3) == phi) ? s : (s >= 4 ? s - 4 : (s == 0 ? 8 : s + 4)); };
        issueX(ord1(0), 0); issueW(ord1(0), FH * 2, 0); issueX(ord1(1), STG); issueW(ord1(1), FH * 2, STG);
        issueX(ord1(2), 2 * STG); issueW(ord1(2), FH * 2, 2 * STG);
        E = flag_ld(ws, WS_E);
        unsigned wm_ = 0;
        auto touch = [&](const void* base, unsigned off, unsigned last) { const char* q_ = (const char*)base + min(off + lane * 64u, last); asm volatile("global_load_dword %0, %1, off" : "+v"(wm_) : "v"(q_) : "memory"); };
        if (GM_PW & 1) {
#pragma unroll
            for (int i = 0; i < 2; ++i) touch(x, bid * 9216u + p * 4608u + i * 4096u, bid * 9216u + p * 4608u + 4544u);
#pragma unroll
            for (int i = 0; i < 3; ++i) touch(win, bid * 20736u + p * 10368u + i * 4096u, bid * 20736u + p * 10368u + 10304u);
        }
        int so = 0;
        WAITV(35); PBAR();
#pragma nounroll
        for (int S = 1; S < 16; ++S) {
            if (S >= 8 && S <= 10) WAITV(9); else WAITV(17);
            PBAR();
            const int t = S + 2, k = ord1(t < 9 ? t : t < 12 ? t - 3 : t - 12);
            if (t < 9 || t >= 12) issueX(k, so);
            issueW(k, t >= 9 ? 0 : FH * 2, so); so = nextbuf(so);
            if ((GM_PW & 2) && S == GM_PWS) {
#pragma unroll
                for (int i = 0; i < 4; ++i) touch(wsp, bid * 32768u + p * 16384u + i * 4096u, 0x7fffffffu);
            }
        }
        WAITV(17); PBAR(); issueS(2 * pm, 0);
        WAITV(8); PBAR(); issueS(2 * pm + 1, STG);
        asm volatile("" :: "v"(wm_));
        STAMP(1);
    } else if (wid < 4) {
        E = flag_ld(ws, WS_E);
        if (wid >> 1) consumer_a<4>(lds, zp, late, wid == 3 && nch == 2, wid & 1, 1, lane, wsp_ TRA);
        else consumer_a<5>(lds, zp, late, wid == 0, wid & 1, 0, lane, wsp_ TRA);
    } else {
        if (h == 0 && lane < 12) *(LAS unsigned*)(lds + L_MISC + lane * 4) = 0u;
        const int row0 = lane / 9, chk0 = lane - row0 * 9;
        const int u = (pn >> 3) * 16 + pm, nblk = u < 8 ? 3 : 2;
        u32x2 uc[18]; u32x4 xv[9], gv[9], wv[3][5];
        if (h == 0) {
            { const char* xb = (const char*)x + (size_t)pm * 128 * 1152 + pn * 72; unsigned off = row0 * 1152 + chk0 * 8; int ch = chk0;
#pragma unroll
              for (int i = 0; i < 9; ++i) { uc[i] = *(const u32x2*)(xb + off); uc[9 + i] = *(const u32x2*)(xb + 64 * 1152 + off); off += 7 * 1152 + 8; if (++ch == 9) { ch = 0; off += 1152 - 72; } } }
            { const char* xr = (const char*)x + (size_t)(pm * 128 + pn * 8 + (lane >> 3)) * 1152 + (lane & 7) * 16; const char* gr = (const char*)gw + (lane & 7) * 16;
#pragma unroll
              for (int i = 0; i < 9; ++i) { xv[i] = *(const u32x4*)(xr + i * 128); gv[i] = *(const u32x4*)(gr + i * 128); } }
        } else {
            unsigned wo_[5], hi_[5];
            { int row = row0, ch = chk0;
#pragma unroll
              for (int i = 0; i < 5; ++i) { const int rw = row < 32 ? row : 31; wo_[i] = (rw & 15) * 1152 + ch * 16; hi_[i] = rw >> 4; row += 7; if (++ch == 9) { ch = 0; ++row; } } }
#pragma unroll
            for (int b = 0; b < 3; ++b) {
                const int blk = (b < nblk) ? u + 32 * b : u, st = blk >> 2, jb = blk & 3;
                const int fA = st < 16 ? st * 144 + (jb < 2 ? 32 * jb : 16 + 32 * jb) : ((st - 16) * 8 + 2 * jb) * 144 + 64, dB = (st < 16 ? 16 : 144) * 1152;
                const char* wb_ = (const char*)wout + (size_t)fA * 1152 + qn * 144;
#pragma unroll
                for (int i = 0; i < 5; ++i) wv[b][i] = *(const u32x4*)(wb_ + (wo_[i] + hi_[i] * dB));
            }
        }
        E = flag_ld(ws, WS_E);
        if (lane < 24) *(LAS u32x4*)(lds + L_KT + (lane >> 3) * KT_SZ + (2 * h + ((lane >> 2) & 1)) * 1088 + (lane & 3) * 16) = (u32x4){0u, 0u, 0u, 0u};
        WAITL0();
        STAMP(10);
        __builtin_amdgcn_s_setprio(GM_HPRIO);
        {
            float kv[2][3];
#pragma nounroll
            for (int ei = 0; ei < 9; ++ei) {
                BAR();
                if (ei > 0) {
                    LAS unsigned char* kt = opq(lds + L_KT + (2 * h + ((ei - 1) >> 2)) * 1088 + 64 + ((ei - 1) & 3) * 256 + lane * 2);
#pragma unroll
                    for (int j = 0; j < 2; ++j)
#pragma unroll
                        for (int c = 0; c < 3; ++c) *(LAS u16*)(kt + c * KT_SZ + j * 128) = (u16)pk_bf16(kv[j][c], 0.f);
                }
#pragma unroll
                for (int j = 0; j < 2; ++j) {
                    const int jj = (ei & 3) * 2 + j, e = jj >= 4 ? 3 : jj >= 2 ? 2 : jj, lag = 256 * jj + 4 * lane + 2 * h + ((ei >> 2) & 1);
                    const int idx = e == 0 ? lag : 128 * e + ((lag + (1 << (e - 1)) - 1 + ((lag >> e) & 1)) >> e);
                    const LAS float* q = opq((const LAS float*)(lds + L_KD) + idx);
#pragma unroll
                    for (int c = 0; c < 3; ++c) kv[j][c] = q[c * 768];
                }
            }
        }
        if (h == 0) {
            LAS unsigned char* pzs = opq(lds + Z0 + lane * 8);
#pragma unroll
            for (int i = 0; i < 18; ++i) *(LAS u32x2*)(pzs + i * 512) = uc[i];
            CFENCE();
            { const LAS unsigned char* pt = opq(lds + Z0 + (8 * (4 * fq + (lane & 3)) + (fr >> 2)) * 72);
              const unsigned ub = WS_UT + (unsigned)(pn * 36 + (lane & 3)) * 4096u + (unsigned)(pm * 128 + 8 * (4 * fq + (fr >> 2))) * 2u;
#pragma unroll
              for (int cc = 0; cc < 9; ++cc) __builtin_amdgcn_raw_buffer_store_b128(__builtin_bit_cast(u32x4, cat8(tr_read(pt + cc * 8), tr_read(pt + 288 + cc * 8))), ws, ub + cc * 16384u, 0, 16); }
            float a = 0.f;
#pragma unroll
            for (int i = 0; i < 9; ++i)
#pragma unroll
                for (int j = 0; j < 4; ++j) { const unsigned xa = xv[i][j], ga = gv[i][j]; a = __builtin_amdgcn_fdot2_f32_bf16(__builtin_bit_cast(bf16x2, xa), __builtin_bit_cast(bf16x2, ga), a, false); }
#define DPPF(v, ctl) __int_as_float(__builtin_amdgcn_update_dpp(0, __float_as_int(v), ctl, 0xf, 0xf, true))
            a += DPPF(a, 0xb1); a += DPPF(a, 0x4e); a += DPPF(a, 0x141);
#undef DPPF
            const float gt = rbf(__builtin_amdgcn_rcpf(1.0f + __expf(-rbf(a))));
            if ((lane & 7) == 0) __builtin_amdgcn_raw_buffer_store_b32(__float_as_uint(gt), ws, WS_GATE + (unsigned)(pm * 128 + pn * 8 + (lane >> 3)) * 4u, 0, 16);
        } else {
            LAS unsigned char* pws = opq(lds + Z0 + 9216 + lane * 16);
            const LAS unsigned char* ptw = opq(lds + Z0 + 9216 + (4 * fq + (fr >> 2)) * 144 + (lane & 3) * 8);
#pragma unroll
            for (int b = 0; b < 3; ++b) {
                if (b < nblk) {
#pragma unroll
                    for (int i = 0; i < 5; ++i) if (i < 4 || lane < 32) *(LAS u32x4*)(pws + i * 1024) = wv[b][i];
                    CFENCE();
                    const unsigned ob = WS_WOF + (unsigned)qn * (18u * 18432u) + (unsigned)(u + 32 * b) * 4608u;
#pragma unroll
                    for (int nn = 0; nn < 5; ++nn) {
                        const u32x4 v = __builtin_bit_cast(u32x4, cat8(tr_read(ptw + nn * 32), tr_read(ptw + 2304 + nn * 32)));
                        if (nn < 4) __builtin_amdgcn_raw_buffer_store_b128(v, ws, ob + nn * 1024 + lane * 16, 0, 16);
                        else if (fr < 8) __builtin_amdgcn_raw_buffer_store_b128(v, ws, ob + 4096 + (fq * 8 + fr) * 16, 0, 16);
                    }
                    CFENCE();
                }
            }
        }
        nb = 9;
        mk_tag();
        WAITL0();
        STEPS_TO(10);
        STAMP(1);
        {
            const f32x4 sp = *(const LAS f32x4*)(lds + L_SP + (h * 64 + lane) * 16);
            const float dm = sp[0] - sp[2];
            const f32x2 mine = {(80.f * sp[0] + 64.f * sp[2]) * (1.0f / 144.f), sp[1] + sp[3] + (80.f * 64.f / 144.f) * dm * dm};
            __builtin_amdgcn_raw_buffer_store_b128((u32x4){T[0], __float_as_uint(mine.x), T[1], __float_as_uint(mine.y)}, ws, WS_ST + (unsigned)((pm * 16 + pn) * 128 + h * 64 + lane) * 16u, 0, 16);
            STEP(18);
            const unsigned so_ = WS_ST + (unsigned)(pm * 16 * 128 + h * 64 + lane) * 16u;
            const u64 t0 = __builtin_amdgcn_s_memrealtime();
            u32x4 st[16];
            for (;;) {
                CFENCE();
#pragma unroll
                for (int j = 0; j < 16; ++j) st[j] = __builtin_amdgcn_raw_buffer_load_b128(ws, so_ + j * 2048u, 0, 16);
                STEP(18);
                unsigned bad = 0;
#pragma unroll
                for (int j = 0; j < 16; ++j) bad |= (st[j][0] ^ T[0]) | (st[j][2] ^ T[1]);
                if (__builtin_amdgcn_ballot_w64(bad != 0u) == 0ull) break;
                if (__builtin_amdgcn_s_memrealtime() - t0 > 300000ull) break;
            }
            STAMP(2);
            STEP(18);
            float mean = 0.f, m2 = 0.f;
#pragma unroll
            for (int j = 0; j < 16; ++j) mean += __uint_as_float(st[j][1]);
            mean *= (1.0f / 16.f);
#pragma unroll
            for (int j = 0; j < 16; ++j) { const float d = __uint_as_float(st[j][1]) - mean; m2 += __uint_as_float(st[j][3]) + 144.f * d * d; }
            const float rstd = __builtin_amdgcn_rsqf(m2 * (1.0f / FH) + 1e-5f);
            *(LAS f32x2*)(lds + L_SP + (h * 64 + lane) * 16) = (f32x2){rstd, -mean * rstd};
            WAITL0();
            if (lane == 0) *(volatile LAS unsigned*)(lds + L_MISC + 4 + h * 4) = 1u;
            const volatile LAS unsigned* zd = (const volatile LAS unsigned*)(lds + L_MISC + 16 + h * 8 + (lane & 1) * 4);
            for (;;) {
                STEP(18);
                if (__builtin_amdgcn_ballot_w64(*zd == 0u) == 0ull) break;
                if (__builtin_amdgcn_s_memrealtime() - t0 > 800000ull) break;
            }
            CFENCE();
            const LAS unsigned char* ptz = opq(lds + Z0 + (h * 64 + 8 * fq + (fr >> 2)) * ZST + (lane & 3) * 8);
            const unsigned zo = WS_Z2F + (unsigned)((pn * 64 + pm * 4 + h * 2) * 9) * 1024u + lane * 16;
            u32x4 zf[18];
#pragma unroll
            for (int i = 0; i < 18; ++i) {
                const LAS unsigned char* pt = ptz + (i / 9) * 32 * ZST + (i % 9) * 32;
                zf[i] = __builtin_bit_cast(u32x4, cat8(tr_read(pt), tr_read(pt + 4 * ZST)));
                __builtin_amdgcn_raw_buffer_store_b128(zf[i], ws, zo + i * 1024, 0, 16);
                if (i == 5 || i == 11) STEP(18);
            }
            STEP(18);
            flag_publish(ws, WS_FZ + (unsigned)(pn * 32 + pm * 2 + h) * 128u, T, lane);
            STAMP(3);
            STEPS_TO(18);
            LAS unsigned char* pr_ = opq(lds + h * STG + BOFF + lane * 16);
#pragma unroll
            for (int i = 0; i < 18; ++i) *(LAS u32x4*)(pr_ + i * 1024) = zf[i];
            WAITL0();
        }
    }

    if (is_prod) {
        mk_tag();
        const char* lin = (const char*)wsp_ + p * 1024 + lane * 16;
        auto issueLA = [&](unsigned off, int s) {
#pragma unroll
            for (int i = 0; i < 8; ++i) GLDS(lin + off + i * 2048, lds + s + (p + 2 * i) * 1024, 16); };
        auto issueLB = [&](unsigned off, int s) {
#pragma unroll
            for (int i = 0; i < 9; ++i) GLDS(lin + off + i * 2048, lds + s + BOFF + (p + 2 * i) * 1024, 16); };
        auto ord3 = [&](int s) { return (GM_LAG == 0 || s >= 16) ? s : (((s & 3) == phi) ? s : ((s - GM_LAG) & 15)); };
        u32x2 pv = {0u, 0u}; unsigned rdy = 0; bool pend = false;
        const char* fp = (const char*)wsp_ + WS_FZ + ((unsigned)pn * 32u + (lane & 31)) * 128u;
        auto poll_issue = [&]() { asm volatile("global_load_dwordx2 %0, %1, off sc1" : "+v"(pv) : "v"(fp) : "memory"); };
        auto poll_take = [&]() { asm volatile("" : "+v"(pv)); rdy |= (unsigned)__builtin_amdgcn_ballot_w64(pv[0] == T[0] && pv[1] == T[1]); };
        auto need = [&](unsigned bits) {
            if (pend) { poll_take(); pend = false; }
            if ((rdy & bits) != bits) {
                const u64 t0 = __builtin_amdgcn_s_memrealtime();
                do { poll_issue(); WAITV(0); poll_take(); } while ((rdy & bits) != bits && __builtin_amdgcn_s_memrealtime() - t0 < 300000ull);
            }
            if (rdy != ~0u) { poll_issue(); pend = true; }
        };
        const unsigned zb = WS_Z2F + (unsigned)pn * (64u * 9216u);
        int so = 2 * STG;
        const int r0 = (ord2(0) >> 1) == pm ? ord2(1) : ord2(0), r1 = (ord2(1) >> 1) == pm ? ord2(0) : ord2(1);
        poll_issue(); pend = true; WAITV(0);
        STAMP(2);
#pragma nounroll
        for (int S = 18; S < 48; ++S) {
            PBAR();
            int k = ord2(S - 16); if ((k >> 1) == pm) k = (k & 1) ? r1 : r0;
            need(1u << k); issueS(k, so); issueLB(zb + (unsigned)k * 18432u, so); so = nextbuf(so);
            WAITV(17);
        }
        const unsigned hb = WS_HF + (unsigned)qm * (18u * 16384u), ob = WS_WOF + (unsigned)qn * (18u * 18432u);
        PBAR();
        fp = (const char*)wsp_ + WS_FZ + ((unsigned)(pn ^ 8) * 32u + (lane & 31)) * 128u; WAITV(0); rdy = 0; pend = false;
        need(~0u);
        issueLB(ob + (unsigned)ord3(0) * 18432u, 2 * STG);
        PBAR();
        issueLB(ob + (unsigned)ord3(1) * 18432u, 0);
        STAMP(3);
        fp = (const char*)wsp_ + WS_FH + ((unsigned)qm * 32u + (lane & 31)) * 128u; rdy = 0;
        need(~0u);
        STAMP(4);
        issueLA(hb + (unsigned)ord3(0) * 16384u, 2 * STG); issueLA(hb + (unsigned)ord3(1) * 16384u, 0);
        so = STG;
        WAITV(8);
#pragma nounroll
        for (int S = 50; S < 66; ++S) {
            PBAR();
            const int k = ord3(S - 48); issueLA(hb + (unsigned)k * 16384u, so); issueLB(ob + (unsigned)k * 18432u, so); so = nextbuf(so);
            WAITV(17);
        }
        PBAR();
        WAITV(0); PBAR();
        PBAR();
        STAMP(5); PDUMP();
        if (bid == 0 && p == 0 && lane == 0) __builtin_amdgcn_raw_buffer_store_b64(T, ws, WS_E, 0, 16);
        return;
    }

    if (wid < 4) {
        mk_tag();
        if (wid >> 1) consumer_b<4>(lds, ws, T, zp, bs, pm, pn, wid & 1, 1, lane, wsp_ TRA);
        else consumer_b<5>(lds, ws, T, zp, bs, pm, pn, wid & 1, 0, lane, wsp_ TRA);
    } else {
        __builtin_amdgcn_s_setprio(GM_HPRIO);
        {   const unsigned fo = WS_FZ + (unsigned)(pn * 32 + (lane & 31)) * 128u;
            const u64 t0 = __builtin_amdgcn_s_memrealtime();
            for (;;) {
                STEP(44);
                CFENCE();
                const u32x2 v = flag_ld(ws, fo);
                STEP(44);
                if (flag_all(v, T, true)) break;
                if (__builtin_amdgcn_s_memrealtime() - t0 > 300000ull) break;
            } }
        u32x4 uu[3][2];
#pragma unroll
        for (int c = 0; c < 3; ++c)
#pragma unroll
            for (int j = 0; j < 2; ++j) uu[c][j] = __builtin_amdgcn_raw_buffer_load_b128(ws, WS_UT + (unsigned)(ch0 + (c < nch ? 16 * c : 0)) * 4096u + h * 2048 + j * 1024 + lane * 16, 0, 16);
#pragma unroll
        for (int c = 0; c < 3; ++c) if (lane < 16) *(LAS u32x4*)(lds + L_UT + c * UT_SZ + h * 4352 + lane * 16) = (u32x4){0u, 0u, 0u, 0u};
        BAR(); BAR(); BAR(); nb += 3;
        LAS unsigned char* pu_ = opq(lds + L_UT + 256 + h * 2048 + lane * 16);
#pragma unroll
        for (int c = 0; c < 3; ++c)
#pragma unroll
            for (int j = 0; j < 2; ++j) *(LAS u32x4*)(pu_ + c * UT_SZ + j * 1024) = uu[c][j];
        WAITL0();
        if (lane == 0) *(volatile LAS unsigned*)(lds + L_MISC + 32 + h * 4) = 1u;
        {   const volatile LAS unsigned* uw = (const volatile LAS unsigned*)(lds + L_MISC + 32 + (lane & 1) * 4);
            const u64 t0 = __builtin_amdgcn_s_memrealtime();
            do { STEP(50); } while (__builtin_amdgcn_ballot_w64(*uw == 0u) != 0ull && __builtin_amdgcn_s_memrealtime() - t0 < 800000ull);
            CFENCE(); }
        STAMP(4);
    }

    if (wid < 4) nb = 50;
    for (;;) {
        if (nb > 45) STEPS_TO(50);
        unsigned tk = 0;
        if (lane == 0) tk = __hip_atomic_fetch_add((LAS unsigned*)(lds + L_MISC), 1u, __ATOMIC_RELAXED, __HIP_MEMORY_SCOPE_WORKGROUP);
        tk = __builtin_amdgcn_readfirstlane(tk);
        if (tk >= (unsigned)(4 * nch)) break;
        const int c = tk >> 2, tl = tk & 1;
        if (tk & 2) conv_task<0, 10>(lds, L_KT + c * KT_SZ, L_UT + c * UT_SZ, ytab(c), tl, lane, nb TRA);
        else conv_task<11, 15>(lds, L_KT + c * KT_SZ, L_UT + c * UT_SZ, ytab(c), tl, lane, nb TRA);
    }
    STEPS_TO(50);
    WAITL0();
    STAMP(7);

    if (wid < 4) {
        const int kq = wid >> 1, mh = wid & 1;
        f32x4 acc[5][2];
        struct F3 { s16x4 alo[4], ahi[4]; bf16x8 b[10]; } f0, f1;
        const LAS unsigned char* ph = lds + 2 * STG + mh * 8192 + kq * 2048 + lane * 16;
        const LAS unsigned char* pw = lds + 2 * STG + BOFF + kq * 9216 + lane * 16;
        const LAS unsigned char* pw4 = lds + 2 * STG + BOFF + kq * 9216 + 4096 + (fq * 8 + (fr & 7)) * 16;
        auto rd3 = [&](F3& f, int i, bool single) {
            const int j = i / 7, r = i % 7;
            if (r < 2) {
                if (!single) { const bf16x8 v = *(const LAS bf16x8*)(ph + r * 4096 + j * 1024); f.alo[j * 2 + r] = __builtin_shufflevector(v, v, 0, 1, 2, 3); f.ahi[j * 2 + r] = __builtin_shufflevector(v, v, 4, 5, 6, 7); }
                else { const LAS unsigned char* q = ph - lane * 8 + r * 4096 + j * 1024; f.alo[j * 2 + r] = *(const LAS s16x4*)q; f.ahi[j * 2 + r] = *(const LAS s16x4*)(q + 512); }
            } else if (r < 6) f.b[j * 5 + r - 2] = *(const LAS bf16x8*)(pw + j * 4608 + (r - 2) * 1024);
            else f.b[j * 5 + 4] = *(const LAS bf16x8*)(pw4 + j * 4608);
        };
        int so = 2 * STG;
        auto step3 = [&](const F3& c, F3& l, int mode  , bool first) {
            const int delta = so == 2 * STG ? -2 * STG : STG; so += delta;
#pragma unroll
            for (int i = 0; i < 20; ++i) {
                const int j = i / 10, n = (i % 10) >> 1, m = i & 1;
                acc[n][m] = MFMA16(c.b[j * 5 + n], cat8(c.alo[j * 2 + m], c.ahi[j * 2 + m]), (first && j == 0) ? ((f32x4){0.f, 0.f, 0.f, 0.f}) : acc[n][m]);
                if (mode < 2 && i < 7) { rd3(l, 2 * i, mode == 1); rd3(l, 2 * i + 1, mode == 1); }
                if (mode < 2) { if (i == 8) { ph += delta; asm volatile("" : "+v"(ph)); } if (i == 9) { pw += delta; asm volatile("" : "+v"(pw)); } if (i == 10) { pw4 += delta; asm volatile("" : "+v"(pw4)); } }
                SB0();
            }
        };
        __builtin_amdgcn_s_waitcnt(0xC07F);
        BAR();
#pragma unroll
        for (int i = 0; i < 14; ++i) rd3(f0, i, false);
        ph -= 2 * STG; pw -= 2 * STG; pw4 -= 2 * STG; so = 0;
        WAITL0(); BAR(); step3(f0, f1, 0, true);
#pragma nounroll
        for (int j = 0; j < 7; ++j) {
            WAITL0(); BAR(); step3(f1, f0, 0, false);
            WAITL0(); BAR(); step3(f0, f1, 0, false);
        }
        WAITL0(); BAR(); step3(f1, f0, 1, false);
        WAITL0(); BAR(); step3(f0, f1, 1, false);
        step3(f1, f0, 2, false);
        STAMP(8);
        LAS unsigned char* prw = opq(lds + L_R + wid * 5120 + lane * 16);
#pragma unroll
        for (int n = 0; n < 5; ++n) { const f32x4 x0 = acc[n][0], x1 = acc[n][1]; *(LAS f32x4*)(prw + n * 1024) = kq ? x0 : x1; acc[n][0] = kq ? x1 : x0; }
        WAITL0();
        BAR();
        const int m = mh * 2 + kq;
        const rsrc_t ors = __builtin_amdgcn_make_buffer_rsrc(out, 0, SEQ * DM * 2, 0x00020000);
        const float g = *(const LAS float*)(lds + L_MISC + 64 + (16 * m + fr) * 4), g1 = rbf(1.0f - g);
        const LAS unsigned char* prr = opq(lds + L_R + (wid ^ 2) * 5120 + lane * 16);
        const LAS unsigned char* pat = opq(lds + L_AT + (4 * fq + (fr >> 2)) * 128 + (16 * m + 4 * (lane & 3)) * 2);
#pragma unroll
        for (int n = 0; n < 5; ++n) {
            const f32x4 b = acc[n][0] + *(const LAS f32x4*)(prr + n * 1024);
            const u32x2 a = __builtin_bit_cast(u32x2, tr_read(pat + n * 2048));
            const u32x2 o = {pk_bf16(g * bflo(a[0]) + g1 * b[0], g * bfhi(a[0]) + g1 * b[1]), pk_bf16(g * bflo(a[1]) + g1 * b[2], g * bfhi(a[1]) + g1 * b[3])};
            if (n < 4 || fq < 2) __builtin_amdgcn_raw_buffer_store_b64(o, ors, (unsigned)((qm * 64 + 16 * m + fr) * 1152 + (qn * 72 + 16 * n + 4 * fq) * 2), 0, GM_OAUX);
        }
    } else {
        int d = pn * 36 + pm;
#pragma nounroll
        for (int c = 0; c < nch; ++c) {
            const float dsk = bf2f(Dsk[d]);
#pragma unroll
            for (int k = 0; k < 2; ++k) {
                BAR(); ++nb;
                const int l0 = h * 1024 + k * 512 + lane * 8;
                f32x4 cv = *(const LAS f32x4*)(lds + ytab(c) + 8192 + (l0 >= 16 ? (l0 >> 4) - 1 : 0) * 16);
                if ((lane & 1) || l0 == 0) cv = (f32x4){0.f, 0.f, 0.f, 0.f};
                const f32x4 y0 = *(const LAS f32x4*)(lds + ytab(c) + l0 * 4) + cv, y1 = *(const LAS f32x4*)(lds + ytab(c) + l0 * 4 + 16);
                const u32x4 uv = *(const LAS u32x4*)(lds + L_UT + c * UT_SZ + 256 + l0 * 2);
                u32x4 o;
#pragma unroll
                for (int j = 0; j < 4; ++j) {
                    const float ya = j < 2 ? y0[2 * j] : y1[2 * j - 4], yb = j < 2 ? y0[2 * j + 1] : y1[2 * j - 3];
                    const unsigned yp = pk_bf16(ya, yb), up = pk_bf16(bflo(uv[j]) * dsk, bfhi(uv[j]) * dsk);
                    o[j] = pk_bf16(bflo(yp) + bflo(up), bfhi(yp) + bfhi(up));
                }
                __builtin_amdgcn_raw_buffer_store_b128(o, ws, WS_AT + (unsigned)d * 4096u + l0 * 2, 0, 16);
            }
            d += 16;
        }
        STEPS_TO(58);
        flag_publish(ws, WS_FA + (unsigned)((pn >> 1) * 64 + (pn & 1) * 32 + pm * 2 + h) * 128u, T, lane);
        STAMP(5);
        STEPS_TO(62);
        if (h == 0) {
            const unsigned fo = WS_FA + (unsigned)(qn * 64 + lane) * 128u;
            const u64 t0 = __builtin_amdgcn_s_memrealtime();
            for (;;) {
                CFENCE();
                const u32x2 v = flag_ld(ws, fo);
                STEP(68); STEP(68);
                if (flag_all(v, T, true)) break;
                if (__builtin_amdgcn_s_memrealtime() - t0 > 300000ull) break;
            }
            ACQ();
            u32x4 av[9];
#pragma unroll
            for (int i = 0; i < 9; ++i) av[i] = __builtin_amdgcn_raw_buffer_load_b128(ws, WS_AT + (unsigned)(qn * 72 + i * 8 + (lane >> 3)) * 4096u + qm * 128 + (lane & 7) * 16, 0, 16);
            STEP(68); STEP(68);
            LAS unsigned char* pa_ = opq(lds + L_AT + lane * 16);
#pragma unroll
            for (int i = 0; i < 9; ++i) *(LAS u32x4*)(pa_ + i * 1024) = av[i];
        } else {
            STEPS_TO(65);
            const unsigned gv = __builtin_amdgcn_raw_buffer_load_b32(ws, WS_GATE + (unsigned)(qm * 64 + lane) * 4u, 0, 16);
            BAR(); BAR(); nb += 2;
            *(LAS unsigned*)(lds + L_MISC + 64 + lane * 4) = gv;
        }
        STEPS_TO(68);
        WAITL0();
        BAR();
    }
    STAMP(9); TRDUMP();
}
}

extern "C" void kernel_launch(void* const* d_in, const int* in_sizes, int n_in, void* d_out, int out_size, void* d_ws, size_t ws_size, hipStream_t stream) {
    (void)in_sizes; (void)n_in; (void)out_size; (void)ws_size;
    (void)hipFuncSetAttribute((const void*)gm::gm_kernel, hipFuncAttributeMaxDynamicSharedMemorySize, gm::LDS_BYTES);
    hipLaunchKernelGGL(gm::gm_kernel, dim3(256), dim3(512), gm::LDS_BYTES, stream, (const u16*)d_in[0], (const u16*)d_in[1], (const u16*)d_in[2], (const u16*)d_in[3],
                       (const u16*)d_in[4], (const u16*)d_in[5], (const u16*)d_in[6], (const u16*)d_in[8], (const u16*)d_in[9], (const u16*)d_in[10],
                       (const u16*)d_in[11], (const u16*)d_in[12], (u16*)d_out, (unsigned char*)d_ws);
}
```

```cpp
#include <hip/hip_runtime.h>
#include <stdint.h>
#include <type_traits>

#define LAS __attribute__((address_space(3)))
typedef unsigned short u16;
typedef unsigned long long u64;
typedef short bf16x8 __attribute__((ext_vector_type(8)));
typedef short s16x4 __attribute__((ext_vector_type(4)));
typedef _Float16 f16x8 __attribute__((ext_vector_type(8)));
typedef _Float16 f16x2 __attribute__((ext_vector_type(2)));
typedef __bf16 bf16x2 __attribute__((ext_vector_type(2)));
typedef float f32x4 __attribute__((ext_vector_type(4)));
typedef float f32x2 __attribute__((ext_vector_type(2)));
typedef unsigned u32x4 __attribute__((ext_vector_type(4)));
typedef unsigned u32x2 __attribute__((ext_vector_type(2)));
typedef __amdgpu_buffer_rsrc_t rsrc_t;

namespace gm {
constexpr int SEQ = 2048, DM = 576, FH = 2304;
constexpr unsigned WS_E = 0, WS_FS = 4096, WS_FZ = WS_FS + 65536, WS_FH = WS_FZ + 65536, WS_FA = WS_FH + 131072;
constexpr unsigned WS_ST = 0x60000, WS_GATE = 0xE0000, WS_DBG = 0x1E00000, WS_Z2F = 0x100000, WS_HF = 0xA00000, WS_WOF = 0x1300000, WS_UT = 0x1600000, WS_AT = 0x1900000, WS_END = 0x1C00000, WS_TRC = 0x1C00000;
constexpr int STG = 34816, BOFF = 16384, Z0 = 3 * STG, ZST = 296, T0 = Z0 + 128 * ZST;
constexpr int KT_SZ = 4352, UT_SZ = 4608, Y_SZ = 10240;
constexpr int L_MISC = 163840 - 320;
constexpr int L_KT = L_MISC - 3 * KT_SZ, L_SP = T0, L_KD = Z0 + 3 * 8448, L_UT = Z0 + 3 * Y_SZ;
constexpr int L_AT = Z0, L_R = Z0 + 10240;
constexpr int SPIN_G = 8192, SPIN_L = 1 << 20;
constexpr int LDS_BYTES = 163840;
static_assert(L_UT + 3 * UT_SZ <= L_KT && L_KD + 3 * 3072 <= T0 && L_SP + 2048 <= L_KT, "lds map");
#define GM_HPRIO 1
#define GM_OAUX 0
#define GM_PFW(w) ((w) == 6)
#define GM_PFL 13
#define GM_CODE 50176
#define GM_ORD 0
#define GM_PW 3
#define GM_PWS 8
#define GM_DRY 0
#define GM_CSM 11
#define GM_PCONV 1
#define GM_LAG 4

__device__ __forceinline__ float bf2f(unsigned v) { return __uint_as_float(v << 16); }
__device__ __forceinline__ float bflo(unsigned w) { return __uint_as_float(w << 16); }
__device__ __forceinline__ float bfhi(unsigned w) { return __uint_as_float(w & 0xffff0000u); }
__device__ __forceinline__ unsigned pk_bf16(float a, float b) { f32x2 v = {a, b}; return __builtin_bit_cast(unsigned, __builtin_convertvector(v, bf16x2)); }
__device__ __forceinline__ unsigned pk_f16(float a, float b) { f32x2 v = {a, b}; return __builtin_bit_cast(unsigned, __builtin_convertvector(v, f16x2)); }
__device__ __forceinline__ float rbf(float a) { return bflo(pk_bf16(a, a)); }
__device__ __forceinline__ f32x2 gelu2(f32x2 x) {
    const f32x2 w = (x * x) * (-0.10294324f) + (-2.30220819f);
    const f32x2 a = w * x;
    f32x2 e; e.x = __builtin_amdgcn_exp2f(a.x); e.y = __builtin_amdgcn_exp2f(a.y);
    e = e + 1.0f;
    f32x2 r; r.x = __builtin_amdgcn_rcpf(e.x); r.y = __builtin_amdgcn_rcpf(e.y);
    return x * r;
}
__device__ __forceinline__ f32x4 gelu4(f32x4 v) { const f32x2 a = gelu2((f32x2){v[0], v[1]}), b = gelu2((f32x2){v[2], v[3]}); return (f32x4){a.x, a.y, b.x, b.y}; }
__device__ __forceinline__ float allred_q(float v) {
    unsigned u = __float_as_uint(v);
    auto r = __builtin_amdgcn_permlane16_swap(u, u, false, false);
    v = __uint_as_float(r[0]) + __uint_as_float(r[1]);
    u = __float_as_uint(v);
    auto s = __builtin_amdgcn_permlane32_swap(u, u, false, false);
    return __uint_as_float(s[0]) + __uint_as_float(s[1]);
}
#define ACQ() __builtin_amdgcn_fence(__ATOMIC_ACQUIRE, "wavefront")
#define TRP
#define TRA
#define TRDUMP() do { } while (0)
#define BAR() do { asm volatile("" ::: "memory"); __builtin_amdgcn_s_barrier(); asm volatile("" ::: "memory"); } while (0)
#define PBAR() BAR()
#define PDUMP() TRDUMP()
#define WAITV(n) asm volatile("s_waitcnt vmcnt(" #n ")" ::: "memory")
#define WAITL0() asm volatile("s_waitcnt lgkmcnt(0)" ::: "memory")
#define CFENCE() asm volatile("" ::: "memory")
#define SGB(mask, n, id) __builtin_amdgcn_sched_group_barrier(mask, n, id)
#define MFMA16(a, b, c) __builtin_amdgcn_mfma_f32_16x16x32_bf16(a, b, c, 0, 0, 0)
__device__ __forceinline__ s16x4 tr_read(const LAS unsigned char* p) { return __builtin_amdgcn_ds_read_tr16_b64_v4i16((LAS s16x4*)p); }
__device__ __forceinline__ bf16x8 cat8(s16x4 a, s16x4 b) { return __builtin_shufflevector(a, b, 0, 1, 2, 3, 4, 5, 6, 7); }
template <class P> __device__ __forceinline__ P opq(P p) { asm volatile("" : "+v"(p)); return p; }
__device__ __forceinline__ int nextbuf(int so) { return so == 2 * STG ? 0 : so + STG; }

#define STAMP(i) do { } while (0)

__device__ __forceinline__ u32x2 flag_ld(rsrc_t ws, unsigned off) { return __builtin_amdgcn_raw_buffer_load_b64(ws, off, 0, 16); }
__device__ __forceinline__ bool flag_all(u32x2 v, u32x2 T, bool active) { return __builtin_amdgcn_ballot_w64(active && (v[0] != T[0] || v[1] != T[1])) == 0ull; }
__device__ __forceinline__ void flag_publish(rsrc_t ws, unsigned off, u32x2 T, int lane) {
    WAITV(0);
    if (lane == 0) __builtin_amdgcn_raw_buffer_store_b64(T, ws, off, 0, 16);
}
#define SB0() __builtin_amdgcn_sched_barrier(0)
template <int NT> struct Frag { bf16x8 a[4]; s16x4 lo[NT], hi[NT]; };
template <int NT, bool TR, int HALF>
__device__ __forceinline__ void frag_read(Frag<NT>& f, int i, const LAS unsigned char* pa, const LAS unsigned char* pb) {
    { constexpr int B0 = TR ? 2 : 1; i = i == 0 ? 0 : i <= B0 ? i + 3 : i <= B0 + 3 ? i - B0 : i; }
    if (i < 4) f.a[i] = *(const LAS bf16x8*)(pa + i * 2048);
    else if (TR) { const int n = (i - 4) >> 1; if ((i - 4) & 1) f.hi[n] = tr_read(pb + HALF * 9216 + 4608 + n * 32); else f.lo[n] = tr_read(pb + HALF * 9216 + n * 32); }
    else { const int n = i - 4; const bf16x8 v = *(const LAS bf16x8*)(pb + HALF * 9216 + n * 1024);
           f.lo[n] = __builtin_shufflevector(v, v, 0, 1, 2, 3); f.hi[n] = __builtin_shufflevector(v, v, 4, 5, 6, 7); }
}
template <int NT, bool TR, int LHALF, int S0, int BAT, bool FIRST, bool UPD>
__device__ __forceinline__ void half_step(f32x4 (&acc)[NT][4], const Frag<NT>& c, Frag<NT>& l, const LAS unsigned char*& pa0, const LAS unsigned char*& pa1, const LAS unsigned char*& pb, int delta,
                                          const float* ini, const volatile LAS unsigned* rdy, unsigned& rv TRP) {
    constexpr int NRD = TR ? 4 + 2 * NT : 4 + NT, NM = 4 * NT, NS = NM - S0;
#pragma unroll
    for (int i = 0; i < NM; ++i) {
        const int n = i >> 2, m = i & 3;
        acc[n][m] = MFMA16(cat8(c.lo[n], c.hi[n]), c.a[m], FIRST ? (TR ? ((f32x4){0.f, 0.f, 0.f, 0.f}) : ((f32x4){ini[m], ini[m], ini[m], ini[m]})) : acc[n][m]);
        SB0();
        if (i == BAT) { WAITL0(); BAR(); if (TR) rv = *rdy; }
        if (LHALF >= 0 && i >= S0) {
#pragma unroll
            for (int r = 0; r < NRD; ++r) if ((r * NS) / NRD == i - S0) frag_read<NT, TR, (LHALF > 0 ? 1 : 0)>(l, r, LHALF > 0 ? pa1 : pa0, pb);
        }
        if (UPD) { if (i == NM - 3) { pa0 += delta; asm volatile("" : "+v"(pa0)); } if (i == NM - 2) { pa1 += delta; asm volatile("" : "+v"(pa1)); } if (i == NM - 1) { pb += delta; asm volatile("" : "+v"(pb)); } }
        SB0();
    }
}
template <int NT, bool TR, class HK>
__device__ __forceinline__ void gemm_phase(int nsteps, f32x4 (&acc)[NT][4], int& so, const LAS unsigned char* pa0, const LAS unsigned char* pa1, const LAS unsigned char* pb,
                                           const float* ini, const volatile LAS unsigned* rdy, HK hook TRP) {
    constexpr int NRD = TR ? 4 + 2 * NT : 4 + NT;
    Frag<NT> f0, f1;
    unsigned rv = 0;
    pa0 += so; pa1 += so; pb += so;
    asm volatile("" : "+v"(pa0), "+v"(pa1), "+v"(pb));
    __builtin_amdgcn_s_waitcnt(0xC07F);
    BAR();
#pragma unroll
    for (int i = 0; i < NRD; ++i) frag_read<NT, TR, 0>(f0, i, pa0, pb);
    SB0();
    int delta = so == 2 * STG ? -2 * STG : STG; so += delta;
    half_step<NT, TR, 1, 2, -1, true, true>(acc, f0, f1, pa0, pa1, pb, delta, ini, rdy, rv TRA);
#pragma nounroll
    for (int s = 1; s < nsteps; ++s) {
        delta = so == 2 * STG ? -2 * STG : STG; so += delta;
        half_step<NT, TR, 0, 6, 5, false, false>(acc, f1, f0, pa0, pa1, pb, 0, ini, rdy, rv TRA);
        half_step<NT, TR, 1, 2, -1, false, true>(acc, f0, f1, pa0, pa1, pb, delta, ini, rdy, rv TRA);
        if (TR) hook(rv);
    }
    half_step<NT, TR, -1, 0, -1, false, false>(acc, f1, f0, pa0, pa1, pb, 0, ini, rdy, rv TRA);
}

template <int NT, class LT>
__device__ __forceinline__ void consumer_a(LAS unsigned char* lds, u32x2 (&zp)[5][4], LT late, bool dry, int wm, int wn, int lane, unsigned char* wsp_ TRP) {
    const int fr = lane & 15, fq = lane >> 4;
    constexpr int N0 = (NT == 5) ? 0 : 5;
    f32x4 acc[NT][4];
    int so = 0;
    const int sw = (fr >> 1) & 7;
    const int a0 = (wm * 64 + fr) * 128 + ((fq ^ sw) << 4), a1 = (wm * 64 + fr) * 128 + (((4 + fq) ^ sw) << 4);
    const int bw = BOFF + (4 * fq + (fr >> 2)) * 288 + (lane & 3) * 8 + N0 * 32;
    const volatile LAS unsigned* rdy = (const volatile LAS unsigned*)(lds + L_MISC + 4 + wm * 4);
    bool done = false;
    auto normalise = [&]() {
        const LAS unsigned char* ps = opq(lds + L_SP + (wm * 64 + fr) * 16);
        LAS unsigned char* pz = opq(lds + Z0 + (wm * 64 + fr) * ZST + N0 * 32 + fq * 8);
#pragma unroll
        for (int m = 0; m < 4; ++m) {
            const f32x2 sr = *(const LAS f32x2*)(ps + m * 256);
#pragma unroll
            for (int n = 0; n < NT; ++n) {
                const f32x2 u = (f32x2){bflo(zp[n][m][0]), bfhi(zp[n][m][0])} * sr.x + sr.y, v = (f32x2){bflo(zp[n][m][1]), bfhi(zp[n][m][1])} * sr.x + sr.y;
                *(LAS u32x2*)(pz + m * 16 * ZST + n * 32) = (u32x2){pk_bf16(u.x, u.y), pk_bf16(v.x, v.y)};
            }
        }
        WAITL0();
        if (lane == 0) *(volatile LAS unsigned*)(lds + L_MISC + 16 + (wm * 2 + wn) * 4) = 1u;
        done = true;
    };
    auto hook = [&](unsigned rv) { if (__builtin_amdgcn_readfirstlane(rv) != 0u && !done) normalise(); };
#pragma unroll
    for (int n = 0; n < NT; ++n)
#pragma unroll
        for (int m = 0; m < 4; ++m) { zp[n][m] = (u32x2){0u, 0u}; asm volatile("" : "=v"(acc[n][m])); }
#pragma nounroll
    for (int it = (GM_DRY && dry) ? -1 : 0; it < 2; ++it) {
        if (it >= 0) gemm_phase<NT, true>(9, acc, so, lds + a0, lds + a1, lds + bw, nullptr, rdy, hook TRA);
        if (it >= 0) STAMP(1 + 2 * it);
        if (it == 1) late();
        if (it == 1 && !done) {
            int t0 = 0;
            while (__builtin_amdgcn_readfirstlane(*rdy) == 0u && ++t0 < SPIN_L) __builtin_amdgcn_s_sleep(1);
            normalise();
        }
#pragma unroll
        for (int m = 0; m < 4; ++m)
#pragma unroll
            for (int n = 0; n < NT; ++n) { acc[n][m] = gelu4(acc[n][m]); zp[n][m] = (u32x2){pk_bf16(acc[n][m][0], acc[n][m][1]), pk_bf16(acc[n][m][2], acc[n][m][3])}; }
        if (it <= 0) {
            LAS unsigned char* ps = opq(lds + L_SP + (wm * 64 + fr) * 16 + wn * 8);
#pragma unroll
            for (int m = 0; m < 4; ++m) {
                f32x4 s4 = acc[0][m];
#pragma unroll
                for (int n = 1; n < NT; ++n) s4 += acc[n][m];
                const float mean = opq(allred_q((s4[0] + s4[1]) + (s4[2] + s4[3])) * (1.0f / (16 * NT)));
                f32x4 q4 = {0.f, 0.f, 0.f, 0.f};
#pragma unroll
                for (int n = 0; n < NT; ++n) { const f32x4 d = acc[n][m] - mean; q4 += d * d; }
                const float m2 = allred_q((q4[0] + q4[1]) + (q4[2] + q4[3]));
                if (fq == 0) *(LAS f32x2*)(ps + m * 256) = (f32x2){mean, m2};
            }
            STAMP(2);
        }
    }
    STAMP(4);
}

template <int NT>
__device__ __forceinline__ void consumer_b(LAS unsigned char* lds, rsrc_t ws, u32x2 T, const u32x2 (&z1)[5][4], const float (&bs_)[4], int pm, int pn, int wm, int wn, int lane, unsigned char* wsp_ TRP) {
    float bs[4] = {bf2f(__float_as_uint(opq(bs_[0]))), bf2f(__float_as_uint(opq(bs_[1]))), bf2f(__float_as_uint(opq(bs_[2]))), bf2f(__float_as_uint(opq(bs_[3])))};
    const int fr = lane & 15, fq = lane >> 4;
    constexpr int N0 = (NT == 5) ? 0 : 5;
    f32x4 acc[NT][4];
    int so = 0;
    const int sw = (fr >> 1) & 7;
    const int a0 = (wm * 64 + fr) * 128 + ((fq ^ sw) << 4), a1 = (wm * 64 + fr) * 128 + (((4 + fq) ^ sw) << 4);
    const int bz = BOFF + N0 * 1024 + lane * 16;
    gemm_phase<NT, false>(32, acc, so, lds + a0, lds + a1, lds + bz, bs, nullptr, [](unsigned) {} TRA);
    STAMP(5);
    __builtin_amdgcn_s_setprio(3);
    const int qm = 2 * pm + wm;
    const unsigned hb = WS_HF + (unsigned)qm * (18u * 16384u);
#pragma unroll
    for (int m = 0; m < 4; ++m) {
        u32x2 hp[NT];
#pragma unroll
        for (int n = 0; n < NT; ++n) {
            const f32x2 u = (f32x2){bflo(z1[n][m][0]), bfhi(z1[n][m][0])} * (f32x2){acc[n][m][0], acc[n][m][1]}, v = (f32x2){bflo(z1[n][m][1]), bfhi(z1[n][m][1])} * (f32x2){acc[n][m][2], acc[n][m][3]};
            hp[n] = (u32x2){pk_bf16(u.x, u.y), pk_bf16(v.x, v.y)};
        }
#pragma unroll
        for (int j = 0; j < 2; ++j)
            __builtin_amdgcn_raw_buffer_store_b128((u32x4){hp[2 * j][0], hp[2 * j][1], hp[2 * j + 1][0], hp[2 * j + 1][1]}, ws,
                                                   hb + (unsigned)pn * 16384u + (unsigned)(m * 4 + wn * 2 + j) * 1024u + lane * 16, 0, 16);
        if (NT == 5) __builtin_amdgcn_raw_buffer_store_b64(hp[NT - 1], ws, hb + (unsigned)(16 + (pn >> 3)) * 16384u + m * 4096u + (unsigned)(pn & 7) * 512u + lane * 8, 0, 16);
    }
    flag_publish(ws, WS_FH + (unsigned)(qm * 32 + pn * 2 + wn) * 128u, T, lane);
    __builtin_amdgcn_s_setprio(0);
    STAMP(6);
}

template <int TLO, int THI>
__device__ __forceinline__ void conv_task(LAS unsigned char* lds, int kt, int ut, int yt, int tl, int lane, int& nb TRP) {
#define CSL() do { if (nb < 50) { BAR(); ++nb; } } while (0)
    constexpr int NTL = THI - TLO + 1, ND = THI + 2;
    const int i = lane & 15, g = lane >> 4;
    const LAS unsigned char* pk = opq(lds + kt + (i & 3) * 1088 + ((i >> 2) + g) * 16);
    const LAS unsigned char* pu = opq(lds + ut + (128 + 16 * tl - 32 * g - 4 * (i >> 2) + 4 * (i & 3)) * 2 - 32);
    bf16x8 ub[ND], kf[ND];
#pragma unroll
    for (int d = 0; d < ND; ++d) { if (d % 6 == 0) CSL(); ub[ND - 1 - d] = cat8(tr_read(pu + 256 * (ND - 1 - d) + 32), tr_read(pu + 256 * (ND - 1 - d))); kf[d] = *(const LAS bf16x8*)(pk + 64 * d); }
    f32x4 acc[NTL];
    int cnt = GM_CSM;
#pragma unroll
    for (int s = -1; s <= THI; ++s) {
#pragma unroll
        for (int t = TLO; t <= THI; ++t) if (t >= s) {
            if (cnt == GM_CSM) { CSL(); cnt = 0; }
            ++cnt;
            acc[t - TLO] = MFMA16(kf[s + 1], ub[t - s], s == -1 ? ((f32x4){0.f, 0.f, 0.f, 0.f}) : acc[t - TLO]);
        }
    }
    LAS float* y = opq((LAS float*)(lds + yt) + 32 * g + 16 * tl + i);
    LAS float* cy = opq((LAS float*)(lds + yt + 8192) + (2 * g + tl) * 4 + (i & 3));
#define DPPF(v, ctl) __int_as_float(__builtin_amdgcn_update_dpp(0, __float_as_int(v), ctl, 0xf, 0xf, true))
#pragma unroll
    for (int t = TLO; t <= THI; ++t) {
        if (((t - TLO) % 3) == 0) CSL();
        const f32x4 a = acc[t - TLO];
        y[128 * t] = (a[0] + DPPF(a[1], 0x111)) + (DPPF(a[2], 0x112) + DPPF(a[3], 0x113));
        const float cv = DPPF(a[1], 0x10f) + (DPPF(a[2], 0x10e) + DPPF(a[3], 0x10d));
        if (i < 4) cy[32 * t] = cv;
    }
#undef DPPF
#undef CSL
}
__device__ __forceinline__ int ytab(int c) { return Z0 + c * Y_SZ; }

__global__ __launch_bounds__(512, 2) void gm_kernel(const u16* __restrict__ x, const u16* __restrict__ logA, const u16* __restrict__ Aim, const u16* __restrict__ Cre,
                                                    const u16* __restrict__ Cim, const u16* __restrict__ logdt, const u16* __restrict__ Dsk, const u16* __restrict__ win,
                                                    const u16* __restrict__ wsp, const u16* __restrict__ bsp, const u16* __restrict__ wout, const u16* __restrict__ gw,
                                                    u16* __restrict__ out, unsigned char* wsp_) {
    extern __shared__ __attribute__((aligned(16))) unsigned char lds_[];
    LAS unsigned char* lds = (LAS unsigned char*)lds_;
    const int wid = __builtin_amdgcn_readfirstlane(threadIdx.x >> 6), lane = threadIdx.x & 63;
    const int bid = blockIdx.x, xg = bid & 7, rr = bid >> 3, bi = rr >> 2, bj = rr & 3;
    const int pm = (xg >> 2) * 8 + bi, pn = (xg & 3) * 4 + bj;
    const int qm = 2 * pm + (pn >> 3), qn = pn & 7;
    const int nch = pm < 4 ? 3 : 2;
    const rsrc_t ws = __builtin_amdgcn_make_buffer_rsrc(wsp_, 0, WS_END, 0x00020000);
    STAMP(0);
    unsigned pfw = 0;
    if (GM_PFW(wid)) {
        u64 pc0; asm volatile("s_getpc_b64 %0" : "=s"(pc0));
        const u64 q_ = (pc0 & ~127ull) + (unsigned)min((rr * GM_PFL + (lane >> 1)) * 128 + (lane & 1) * 64, GM_CODE);
        if (lane < 2 * GM_PFL) asm volatile("global_load_dword %0, %1, off" : "+v"(pfw) : "v"(q_) : "memory");
    }

    const int fr = lane & 15, fq = lane >> 4;
    const int h = wid - 6, p = wid - 4;
    const bool is_prod = (wid >> 1) == 2;
    const int ch0 = pn * 36 + pm;
    float bs[4];
    auto late = [&]() {
#pragma unroll
        for (int m = 0; m < 4; ++m) bs[m] = __uint_as_float((unsigned)bsp[pm * 128 + (wid & 1) * 64 + m * 16 + fr]);
    };
    const int phi = (-(bi + bj)) & 3;
    const unsigned rsw = (unsigned)(((lane & 7) ^ ((4 * p + (lane >> 4)) & 7)) * 16);
#define GLDS(src, dst, aux) __builtin_amdgcn_global_load_lds((const unsigned*)(src), (LAS unsigned*)(dst), 16, 0, aux)
    auto issueS = [&](int k, int s) {
        const char* sb = (const char*)wsp + (size_t)(pm * 128 + p * 8) * 4096 + k * 128; const unsigned vs = (unsigned)(lane >> 3) * 4096u + rsw;
#pragma unroll
        for (int i = 0; i < 8; ++i) GLDS(sb + (size_t)i * (16 * 4096) + vs, lds + s + (p + 2 * i) * 1024, 0); };
    auto ord2 = [&](int s) { return GM_LAG == 0 ? s : (((s & 3) == phi) ? s : ((s - GM_LAG) & 31)); };
    u32x2 E, T;
    auto mk_tag = [&]() { u64 z = (((u64)__builtin_amdgcn_readfirstlane(E[1]) << 32) | __builtin_amdgcn_readfirstlane(E[0])) + 0x9E3779B97F4A7C15ull;
        z = (z ^ (z >> 30)) * 0xBF58476D1CE4E5B9ull; z = (z ^ (z >> 27)) * 0x94D049BB133111EBull; z ^= z >> 31; T = (u32x2){(unsigned)z, (unsigned)(z >> 32)}; };
    u32x2 zp[5][4];
#define STEP(lim) do { if (nb < (lim)) { BAR(); ++nb; } } while (0)
#define STEPS_TO(lim) _Pragma("nounroll") while (nb < (lim)) { BAR(); ++nb; }
    int nb = 0;
    const int kc = wid == 7 ? 2 : wid == 1 || wid == 2 ? wid - 1 : 3;
    if (kc < nch) {
        const int kch = ch0 + 16 * kc, ki = kch * 64 + lane;
        const unsigned pA = ((const unsigned*)logA)[ki], pI = ((const unsigned*)Aim)[ki], pR = ((const unsigned*)Cre)[ki], pC = ((const unsigned*)Cim)[ki], pT = logdt[kch];
        LAS u32x2* tw = opq((LAS u32x2*)(lds + Z0 + kc * 8448) + lane);
        const LAS unsigned char* trd = opq(lds + Z0 + kc * 8448 + fr * 528 + fq * 16);
        const float dt = rbf(__expf(bf2f(pT)));
        const f32x2 ar = (f32x2){rbf(__expf(bflo(pA))), rbf(__expf(bfhi(pA)))} * dt * -1.44269504f, ai = (f32x2){bflo(pI), bfhi(pI)} * dt * 0.15915494f;
        const f32x2 cr = (f32x2){bflo(pR), bfhi(pR)} * dt, ci = (f32x2){bflo(pC), bfhi(pC)} * dt;
        float cm = fmaxf(fmaxf(fabsf(cr.x), fabsf(ci.x)), fmaxf(fabsf(cr.y), fabsf(ci.y)));
#define DPPF(v, ctl) __int_as_float(__builtin_amdgcn_update_dpp(0, __float_as_int(v), ctl, 0xf, 0xf, true))
        cm = fmaxf(cm, DPPF(cm, 0x121)); cm = fmaxf(cm, DPPF(cm, 0x122)); cm = fmaxf(cm, DPPF(cm, 0x124)); cm = fmaxf(cm, DPPF(cm, 0x128));
#undef DPPF
        { auto r = __builtin_amdgcn_permlane16_swap(__float_as_uint(cm), __float_as_uint(cm), false, false); cm = fmaxf(__uint_as_float(r[0]), __uint_as_float(r[1]));
          auto q = __builtin_amdgcn_permlane32_swap(__float_as_uint(cm), __float_as_uint(cm), false, false); cm = fmaxf(fmaxf(__uint_as_float(q[0]), __uint_as_float(q[1])), 1e-30f); }
        struct C2 { f32x2 x, y; };
        auto cexp = [&](float p_) { const f32x2 e = ar * p_, t = ai * p_;
            const f32x2 mg = {__builtin_amdgcn_exp2f(e.x), __builtin_amdgcn_exp2f(e.y)}; const float t0 = __builtin_amdgcn_fractf(t.x), t1 = __builtin_amdgcn_fractf(t.y);
            return C2{mg * (f32x2){__builtin_amdgcn_cosf(t0), __builtin_amdgcn_cosf(t1)}, mg * (f32x2){__builtin_amdgcn_sinf(t0), __builtin_amdgcn_sinf(t1)}}; };
        auto cmul = [](C2 a, C2 b) { return C2{__builtin_elementwise_fma(a.x, b.x, -(a.y * b.y)), __builtin_elementwise_fma(a.x, b.y, a.y * b.x)}; };
        auto put = [&](int row, C2 v, bool conj) { tw[row * 66] = conj ? (u32x2){pk_f16(v.x.x, -v.y.x), pk_f16(v.x.y, -v.y.y)} : (u32x2){pk_f16(v.x.x, v.y.x), pk_f16(v.x.y, v.y.y)}; };
        C2 wp[16], zr[8], vp[4], cv[17];
        wp[1] = cexp(8.f); zr[1] = cexp(1.f); vp[0] = cexp(128.f);
        tw[0] = (u32x2){pk_f16(1.f, 0.f), pk_f16(1.f, 0.f)};
#pragma unroll
        for (int j = 1; j < 16; ++j) { if (j > 1) wp[j] = cmul(wp[j >> 1], wp[j - (j >> 1)]); put(j, wp[j], true); }
        CFENCE();
        u32x4 fb[8];
#pragma unroll
        for (int ks = 0; ks < 8; ++ks) fb[ks] = *(const LAS u32x4*)(trd + ks * 64);
        CFENCE();
#pragma unroll
        for (int r = 2; r < 8; ++r) zr[r] = cmul(zr[r >> 1], zr[r - (r >> 1)]);
#pragma unroll
        for (int k = 1; k < 4; ++k) vp[k] = cmul(vp[k - 1], vp[k - 1]);
        const float rc = __builtin_amdgcn_rcpf(cm);
        cv[0] = C2{cr * rc, ci * rc};
#pragma unroll
        for (int k = 1; k < 8; ++k) { const int lg = k >= 4 ? 2 : k >= 2 ? 1 : 0; cv[k] = cmul(cv[k - (1 << lg)], vp[lg]); }
        LAS float* kb_ = (LAS float*)(lds + L_KD + kc * 3072);
        const float sc = 2.0f * cm;
        u32x4 fa[3][8];
#pragma unroll
        for (int tt = 0; tt < 3; ++tt) {
#pragma unroll
            for (int i = 0; i < (tt == 2 ? 9 : 16); ++i) {
                const int r = tt == 0 ? (i & 7) : tt == 1 ? (i < 8 ? 2 * (i & 3) : 4 * (i & 1)) : 0;
                const int hh = tt == 0 ? (i >> 3) : tt == 1 ? (i < 8 ? 2 + (i >> 2) : 4 + ((i - 8) >> 1)) : 8 + i;
                if (tt == 2) cv[hh] = cmul(cv[hh - 8], vp[3]);
                put(i, r == 0 ? cv[hh] : cmul(cv[hh], zr[r]), false);
            }
            CFENCE();
#pragma unroll
            for (int ks = 0; ks < 8; ++ks) fa[tt][ks] = *(const LAS u32x4*)(trd + ks * 64);
            CFENCE();
            SB0();
        }
        f32x4 kd[3] = {{0.f, 0.f, 0.f, 0.f}, {0.f, 0.f, 0.f, 0.f}, {0.f, 0.f, 0.f, 0.f}};
#pragma unroll
        for (int ks = 0; ks < 8; ++ks)
#pragma unroll
            for (int tt = 0; tt < 3; ++tt) kd[tt] = __builtin_amdgcn_mfma_f32_16x16x32_f16(__builtin_bit_cast(f16x8, fa[tt][ks]), __builtin_bit_cast(f16x8, fb[ks]), kd[tt], 0, 0, 0);
#pragma unroll
        for (int tt = 0; tt < 3; ++tt) {
            LAS float* kw = opq(kb_ + (tt == 0 ? 128 * (fq >> 1) + 4 * (fq & 1) + 8 * fr : tt == 1 ? 256 + 64 * fq + (fq < 2 ? 4 : 2) * fr : 512 + 64 * fq + fr));
            LAS float* kwb = tt == 1 ? opq(kw + (fq < 2 ? 2 : 32)) : kw + (tt == 0 ? 2 : 32);
            kw[0] = kd[tt][0] * sc; kw[tt == 2 ? 16 : 1] = kd[tt][1] * sc; kwb[0] = kd[tt][2] * sc; kwb[tt == 2 ? 16 : 1] = kd[tt][3] * sc;
        }
        WAITL0();
    }
#define IC(n) std::integral_constant<int, n>{}
    if (GM_PFW(wid)) { WAITV(0); asm volatile("" :: "v"(pfw)); }
    if (wid < 6) STAMP(10);
    if (is_prod) {
        const unsigned vx = (unsigned)(lane >> 3) * 1152u + rsw;
        unsigned wv[9];
#pragma unroll
        for (int i = 0; i < 9; ++i) { const int c = (p + 2 * i) * 64 + lane, rho = c / 18, ch = c - rho * 18;
            const int kap = (rho & 32) + 8 * ((rho >> 2) & 3) + 4 * ((rho >> 4) & 1) + (rho & 3); wv[i] = (unsigned)kap * 9216u + (unsigned)ch * 16u; }
        const char* xb = (const char*)x + (size_t)(pm * 128 + p * 8) * 1152;
        const char* wb = (const char*)win + pn * 288;
        auto issueX = [&](int k, int s) {
#pragma unroll
            for (int i = 0; i < 8; ++i) GLDS(xb + (size_t)i * (16 * 1152) + k * 128 + vx, lds + s + (p + 2 * i) * 1024, 0); };
        auto issueW = [&](int k, int coloff, int s) {
            const char* b = wb + (size_t)k * (64 * 9216) + coloff;
#pragma unroll
            for (int i = 0; i < 9; ++i) GLDS(b + wv[i], lds + s + BOFF + (p + 2 * i) * 1024, 0); };
        auto ord1 = [&](int s) { return (GM_LAG == 0 || (s & 3) == phi) ? s : (s >= 4 ? s - 4 : (s == 0 ? 8 : s + 4)); };
        issueX(ord1(0), 0); issueW(ord1(0), FH * 2, 0); issueX(ord1(1), STG); issueW(ord1(1), FH * 2, STG);
        issueX(ord1(2), 2 * STG); issueW(ord1(2), FH * 2, 2 * STG);
        E = flag_ld(ws, WS_E);
        unsigned wm_ = 0;
        auto touch = [&](const void* base, unsigned off, unsigned last) { const char* q_ = (const char*)base + min(off + lane * 64u, last); asm volatile("global_load_dword %0, %1, off" : "+v"(wm_) : "v"(q_) : "memory"); };
        if (GM_PW & 1) {
#pragma unroll
            for (int i = 0; i < 2; ++i) touch(x, bid * 9216u + p * 4608u + i * 4096u, bid * 9216u + p * 4608u + 4544u);
#pragma unroll
            for (int i = 0; i < 3; ++i) touch(win, bid * 20736u + p * 10368u + i * 4096u, bid * 20736u + p * 10368u + 10304u);
        }
        int so = 0;
        WAITV(35); PBAR();
#pragma nounroll
        for (int S = 1; S < 16; ++S) {
            if (S >= 8 && S <= 10) WAITV(9); else WAITV(17);
            PBAR();
            const int t = S + 2, k = ord1(t < 9 ? t : t < 12 ? t - 3 : t - 12);
            if (t < 9 || t >= 12) issueX(k, so);
            issueW(k, t >= 9 ? 0 : FH * 2, so); so = nextbuf(so);
            if ((GM_PW & 2) && S == GM_PWS) {
#pragma unroll
                for (int i = 0; i < 4; ++i) touch(wsp, bid * 32768u + p * 16384u + i * 4096u, 0x7fffffffu);
            }
        }
        WAITV(17); PBAR(); issueS(2 * pm, 0);
        WAITV(8); PBAR(); issueS(2 * pm + 1, STG);
        asm volatile("" :: "v"(wm_));
        STAMP(1);
    } else if (wid < 4) {
        E = flag_ld(ws, WS_E);
        if (wid >> 1) consumer_a<4>(lds, zp, late, wid == 3 && nch == 2, wid & 1, 1, lane, wsp_ TRA);
        else consumer_a<5>(lds, zp, late, wid == 0, wid & 1, 0, lane, wsp_ TRA);
    } else {
        if (h == 0 && lane < 12) *(LAS unsigned*)(lds + L_MISC + lane * 4) = 0u;
        const int row0 = lane / 9, chk0 = lane - row0 * 9;
        const int u = (pn >> 3) * 16 + pm, nblk = u < 8 ? 3 : 2;
        u32x2 uc[18]; u32x4 xv[9], gv[9], wv[3][5];
        if (h == 0) {
            { const char* xb = (const char*)x + (size_t)pm * 128 * 1152 + pn * 72; unsigned off = row0 * 1152 + chk0 * 8; int ch = chk0;
#pragma unroll
              for (int i = 0; i < 9; ++i) { uc[i] = *(const u32x2*)(xb + off); uc[9 + i] = *(const u32x2*)(xb + 64 * 1152 + off); off += 7 * 1152 + 8; if (++ch == 9) { ch = 0; off += 1152 - 72; } } }
            { const char* xr = (const char*)x + (size_t)(pm * 128 + pn * 8 + (lane >> 3)) * 1152 + (lane & 7) * 16; const char* gr = (const char*)gw + (lane & 7) * 16;
#pragma unroll
              for (int i = 0; i < 9; ++i) { xv[i] = *(const u32x4*)(xr + i * 128); gv[i] = *(const u32x4*)(gr + i * 128); } }
        } else {
            unsigned wo_[5], hi_[5];
            { int row = row0, ch = chk0;
#pragma unroll
              for (int i = 0; i < 5; ++i) { const int rw = row < 32 ? row : 31; wo_[i] = (rw & 15) * 1152 + ch * 16; hi_[i] = rw >> 4; row += 7; if (++ch == 9) { ch = 0; ++row; } } }
#pragma unroll
            for (int b = 0; b < 3; ++b) {
                const int blk = (b < nblk) ? u + 32 * b : u, st = blk >> 2, jb = blk & 3;
                const int fA = st < 16 ? st * 144 + (jb < 2 ? 32 * jb : 16 + 32 * jb) : ((st - 16) * 8 + 2 * jb) * 144 + 64, dB = (st < 16 ? 16 : 144) * 1152;
                const char* wb_ = (const char*)wout + (size_t)fA * 1152 + qn * 144;
#pragma unroll
                for (int i = 0; i < 5; ++i) wv[b][i] = *(const u32x4*)(wb_ + (wo_[i] + hi_[i] * dB));
            }
        }
        E = flag_ld(ws, WS_E);
        if (lane < 24) *(LAS u32x4*)(lds + L_KT + (lane >> 3) * KT_SZ + (2 * h + ((lane >> 2) & 1)) * 1088 + (lane & 3) * 16) = (u32x4){0u, 0u, 0u, 0u};
        WAITL0();
        STAMP(10);
        __builtin_amdgcn_s_setprio(GM_HPRIO);
        {
            float kv[2][3];
#pragma nounroll
            for (int ei = 0; ei < 9; ++ei) {
                BAR();
                if (ei > 0) {
                    LAS unsigned char* kt = opq(lds + L_KT + (2 * h + ((ei - 1) >> 2)) * 1088 + 64 + ((ei - 1) & 3) * 256 + lane * 2);
#pragma unroll
                    for (int j = 0; j < 2; ++j)
#pragma unroll
                        for (int c = 0; c < 3; ++c) *(LAS u16*)(kt + c * KT_SZ + j * 128) = (u16)pk_bf16(kv[j][c], 0.f);
                }
#pragma unroll
                for (int j = 0; j < 2; ++j) {
                    const int jj = (ei & 3) * 2 + j, e = jj >= 4 ? 3 : jj >= 2 ? 2 : jj, lag = 256 * jj + 4 * lane + 2 * h + ((ei >> 2) & 1);
                    const int idx = e == 0 ? lag : 128 * e + ((lag + (1 << (e - 1)) - 1 + ((lag >> e) & 1)) >> e);
                    const LAS float* q = opq((const LAS float*)(lds + L_KD) + idx);
#pragma unroll
                    for (int c = 0; c < 3; ++c) kv[j][c] = q[c * 768];
                }
            }
        }
        if (h == 0) {
            LAS unsigned char* pzs = opq(lds + Z0 + lane * 8);
#pragma unroll
            for (int i = 0; i < 18; ++i) *(LAS u32x2*)(pzs + i * 512) = uc[i];
            CFENCE();
            { const LAS unsigned char* pt = opq(lds + Z0 + (8 * (4 * fq + (lane & 3)) + (fr >> 2)) * 72);
              const unsigned ub = WS_UT + (unsigned)(pn * 36 + (lane & 3)) * 4096u + (unsigned)(pm * 128 + 8 * (4 * fq + (fr >> 2))) * 2u;
#pragma unroll
              for (int cc = 0; cc < 9; ++cc) __builtin_amdgcn_raw_buffer_store_b128(__builtin_bit_cast(u32x4, cat8(tr_read(pt + cc * 8), tr_read(pt + 288 + cc * 8))), ws, ub + cc * 16384u, 0, 16); }
            float a = 0.f;
#pragma unroll
            for (int i = 0; i < 9; ++i)
#pragma unroll
                for (int j = 0; j < 4; ++j) { const unsigned xa = xv[i][j], ga = gv[i][j]; a = __builtin_amdgcn_fdot2_f32_bf16(__builtin_bit_cast(bf16x2, xa), __builtin_bit_cast(bf16x2, ga), a, false); }
#define DPPF(v, ctl) __int_as_float(__builtin_amdgcn_update_dpp(0, __float_as_int(v), ctl, 0xf, 0xf, true))
            a += DPPF(a, 0xb1); a += DPPF(a, 0x4e); a += DPPF(a, 0x141);
#undef DPPF
            const float gt = rbf(__builtin_amdgcn_rcpf(1.0f + __expf(-rbf(a))));
            if ((lane & 7) == 0) __builtin_amdgcn_raw_buffer_store_b32(__float_as_uint(gt), ws, WS_GATE + (unsigned)(pm * 128 + pn * 8 + (lane >> 3)) * 4u, 0, 16);
        } else {
            LAS unsigned char* pws = opq(lds + Z0 + 9216 + lane * 16);
            const LAS unsigned char* ptw = opq(lds + Z0 + 9216 + (4 * fq + (fr >> 2)) * 144 + (lane & 3) * 8);
#pragma unroll
            for (int b = 0; b < 3; ++b) {
                if (b < nblk) {
#pragma unroll
                    for (int i = 0; i < 5; ++i) if (i < 4 || lane < 32) *(LAS u32x4*)(pws + i * 1024) = wv[b][i];
                    CFENCE();
                    const unsigned ob = WS_WOF + (unsigned)qn * (18u * 18432u) + (unsigned)(u + 32 * b) * 4608u;
#pragma unroll
                    for (int nn = 0; nn < 5; ++nn) {
                        const u32x4 v = __builtin_bit_cast(u32x4, cat8(tr_read(ptw + nn * 32), tr_read(ptw + 2304 + nn * 32)));
                        if (nn < 4) __builtin_amdgcn_raw_buffer_store_b128(v, ws, ob + nn * 1024 + lane * 16, 0, 16);
                        else if (fr < 8) __builtin_amdgcn_raw_buffer_store_b128(v, ws, ob + 4096 + (fq * 8 + fr) * 16, 0, 16);
                    }
                    CFENCE();
                }
            }
        }
        nb = 9;
        mk_tag();
        WAITL0();
        STEPS_TO(10);
        STAMP(1);
        {
            const f32x4 sp = *(const LAS f32x4*)(lds + L_SP + (h * 64 + lane) * 16);
            const float dm = sp[0] - sp[2];
            const f32x2 mine = {(80.f * sp[0] + 64.f * sp[2]) * (1.0f / 144.f), sp[1] + sp[3] + (80.f * 64.f / 144.f) * dm * dm};
            __builtin_amdgcn_raw_buffer_store_b128((u32x4){T[0], __float_as_uint(mine.x), T[1], __float_as_uint(mine.y)}, ws, WS_ST + (unsigned)((pm * 16 + pn) * 128 + h * 64 + lane) * 16u, 0, 16);
            STEP(18);
            const unsigned so_ = WS_ST + (unsigned)(pm * 16 * 128 + h * 64 + lane) * 16u;
            int t0 = 0;
            u32x4 st[16];
            for (;;) {
                CFENCE();
#pragma unroll
                for (int j = 0; j < 16; ++j) st[j] = __builtin_amdgcn_raw_buffer_load_b128(ws, so_ + j * 2048u, 0, 16);
                STEP(18);
                unsigned bad = 0;
#pragma unroll
                for (int j = 0; j < 16; ++j) bad |= (st[j][0] ^ T[0]) | (st[j][2] ^ T[1]);
                if (__builtin_amdgcn_ballot_w64(bad != 0u) == 0ull) break;
                if (++t0 > SPIN_G) break;
            }
            STAMP(2);
            STEP(18);
            float mean = 0.f, m2 = 0.f;
#pragma unroll
            for (int j = 0; j < 16; ++j) mean += __uint_as_float(st[j][1]);
            mean *= (1.0f / 16.f);
#pragma unroll
            for (int j = 0; j < 16; ++j) { const float d = __uint_as_float(st[j][1]) - mean; m2 += __uint_as_float(st[j][3]) + 144.f * d * d; }
            const float rstd = __builtin_amdgcn_rsqf(m2 * (1.0f / FH) + 1e-5f);
            *(LAS f32x2*)(lds + L_SP + (h * 64 + lane) * 16) = (f32x2){rstd, -mean * rstd};
            WAITL0();
            if (lane == 0) *(volatile LAS unsigned*)(lds + L_MISC + 4 + h * 4) = 1u;
            const volatile LAS unsigned* zd = (const volatile LAS unsigned*)(lds + L_MISC + 16 + h * 8 + (lane & 1) * 4);
            for (;;) {
                STEP(18);
                if (__builtin_amdgcn_ballot_w64(*zd == 0u) == 0ull) break;
                if (++t0 > SPIN_L) break;
            }
            CFENCE();
            const LAS unsigned char* ptz = opq(lds + Z0 + (h * 64 + 8 * fq + (fr >> 2)) * ZST + (lane & 3) * 8);
            const unsigned zo = WS_Z2F + (unsigned)((pn * 64 + pm * 4 + h * 2) * 9) * 1024u + lane * 16;
            u32x4 zf[18];
#pragma unroll
            for (int i = 0; i < 18; ++i) {
                const LAS unsigned char* pt = ptz + (i / 9) * 32 * ZST + (i % 9) * 32;
                zf[i] = __builtin_bit_cast(u32x4, cat8(tr_read(pt), tr_read(pt + 4 * ZST)));
                __builtin_amdgcn_raw_buffer_store_b128(zf[i], ws, zo + i * 1024, 0, 16);
                if (i == 5 || i == 11) STEP(18);
            }
            STEP(18);
            flag_publish(ws, WS_FZ + (unsigned)(pn * 32 + pm * 2 + h) * 128u, T, lane);
            STAMP(3);
            STEPS_TO(18);
            LAS unsigned char* pr_ = opq(lds + h * STG + BOFF + lane * 16);
#pragma unroll
            for (int i = 0; i < 18; ++i) *(LAS u32x4*)(pr_ + i * 1024) = zf[i];
            WAITL0();
        }
    }

    const char* lin = (const char*)wsp_ + p * 1024 + lane * 16;
    auto issueLA = [&](unsigned off, int s) {
#pragma unroll
        for (int i = 0; i < 8; ++i) GLDS(lin + off + i * 2048, lds + s + (p + 2 * i) * 1024, 16); };
    auto issueLB = [&](unsigned off, int s) {
#pragma unroll
        for (int i = 0; i < 9; ++i) GLDS(lin + off + i * 2048, lds + s + BOFF + (p + 2 * i) * 1024, 16); };
    auto ord3 = [&](int s) { return (GM_LAG == 0 || s >= 16) ? s : (((s & 3) == phi) ? s : ((s - GM_LAG) & 15)); };
    u32x2 pv = {0u, 0u}; unsigned rdy = 0; bool pend = false;
    const char* fp = (const char*)wsp_ + WS_FZ + ((unsigned)pn * 32u + (lane & 31)) * 128u;
    auto poll_issue = [&]() { asm volatile("global_load_dwordx2 %0, %1, off sc1" : "+v"(pv) : "v"(fp) : "memory"); };
    auto poll_take = [&]() { asm volatile("" : "+v"(pv)); rdy |= (unsigned)__builtin_amdgcn_ballot_w64(pv[0] == T[0] && pv[1] == T[1]); };
    auto need = [&](unsigned bits) {
        if (pend) { poll_take(); pend = false; }
        if ((rdy & bits) != bits) {
            int t0 = 0;
            do { poll_issue(); WAITV(0); poll_take(); } while ((rdy & bits) != bits && ++t0 < SPIN_G);
        }
        if (rdy != ~0u) { poll_issue(); pend = true; }
    };
    const unsigned hb = WS_HF + (unsigned)qm * (18u * 16384u), ob = WS_WOF + (unsigned)qn * (18u * 18432u);
    if (is_prod) {
        mk_tag();
        const unsigned zb = WS_Z2F + (unsigned)pn * (64u * 9216u);
        int so = 2 * STG;
        const int r0 = (ord2(0) >> 1) == pm ? ord2(1) : ord2(0), r1 = (ord2(1) >> 1) == pm ? ord2(0) : ord2(1);
        poll_issue(); pend = true; WAITV(0);
        STAMP(2);
#pragma nounroll
        for (int S = 18; S < 48; ++S) {
            PBAR();
            int k = ord2(S - 16); if ((k >> 1) == pm) k = (k & 1) ? r1 : r0;
            need(1u << k); issueS(k, so); issueLB(zb + (unsigned)k * 18432u, so); so = nextbuf(so);
            WAITV(17);
        }
        PBAR();
        fp = (const char*)wsp_ + WS_FZ + ((unsigned)(pn ^ 8) * 32u + (lane & 31)) * 128u; WAITV(0); rdy = 0; pend = false;
        need(~0u);
        issueLB(ob + (unsigned)ord3(0) * 18432u, 2 * STG);
        PBAR();
        issueLB(ob + (unsigned)ord3(1) * 18432u, 0);
        STAMP(3);
    } else
    if (wid < 4) {
        mk_tag();
        if (wid >> 1) consumer_b<4>(lds, ws, T, zp, bs, pm, pn, wid & 1, 1, lane, wsp_ TRA);
        else consumer_b<5>(lds, ws, T, zp, bs, pm, pn, wid & 1, 0, lane, wsp_ TRA);
    } else {
        __builtin_amdgcn_s_setprio(GM_HPRIO);
        {   const unsigned fo = WS_FZ + (unsigned)(pn * 32 + (lane & 31)) * 128u;
            int t0 = 0;
            for (;;) {
                STEP(43);
                CFENCE();
                const u32x2 v = flag_ld(ws, fo);
                STEP(43);
                if (flag_all(v, T, true)) break;
                if (++t0 > SPIN_G) break;
            } }
        u32x4 uu[3][2];
#pragma unroll
        for (int c = 0; c < 3; ++c)
#pragma unroll
            for (int j = 0; j < 2; ++j) uu[c][j] = __builtin_amdgcn_raw_buffer_load_b128(ws, WS_UT + (unsigned)(ch0 + (c < nch ? 16 * c : 0)) * 4096u + h * 2048 + j * 1024 + lane * 16, 0, 16);
#pragma unroll
        for (int c = 0; c < 3; ++c) if (lane < 16) *(LAS u32x4*)(lds + L_UT + c * UT_SZ + h * 4352 + lane * 16) = (u32x4){0u, 0u, 0u, 0u};
        BAR(); BAR(); BAR(); nb += 3;
        LAS unsigned char* pu_ = opq(lds + L_UT + 256 + h * 2048 + lane * 16);
#pragma unroll
        for (int c = 0; c < 3; ++c)
#pragma unroll
            for (int j = 0; j < 2; ++j) *(LAS u32x4*)(pu_ + c * UT_SZ + j * 1024) = uu[c][j];
        WAITL0();
        if (lane == 0) *(volatile LAS unsigned*)(lds + L_MISC + 32 + h * 4) = 1u;
        {   const volatile LAS unsigned* uw = (const volatile LAS unsigned*)(lds + L_MISC + 32 + (lane & 1) * 4);
            int t0 = 0;
            while (__builtin_amdgcn_ballot_w64(*uw == 0u) != 0ull && ++t0 < SPIN_L) STEP(50);
            CFENCE(); }
        STAMP(4);
    }

    if (wid < 6) nb = 50;
    while (!is_prod || (GM_PCONV && nch == 3)) {
        if (nb > 45) STEPS_TO(50);
        STEP(50);
        unsigned tk = 0;
        if (lane == 0) tk = __hip_atomic_fetch_add((LAS unsigned*)(lds + L_MISC), 1u, __ATOMIC_RELAXED, __HIP_MEMORY_SCOPE_WORKGROUP);
        tk = __builtin_amdgcn_readfirstlane(tk);
        if (tk >= (unsigned)(4 * nch)) break;
        const int c = tk >> 2, tl = tk & 1;
        if (tk & 2) conv_task<0, 10>(lds, L_KT + c * KT_SZ, L_UT + c * UT_SZ, ytab(c), tl, lane, nb TRA);
        else conv_task<11, 15>(lds, L_KT + c * KT_SZ, L_UT + c * UT_SZ, ytab(c), tl, lane, nb TRA);
    }
    STEPS_TO(50);
    WAITL0();
    STAMP(7);
    if (is_prod) {
        fp = (const char*)wsp_ + WS_FH + ((unsigned)qm * 32u + (lane & 31)) * 128u; rdy = 0;
        need(~0u);
        STAMP(4);
        issueLA(hb + (unsigned)ord3(0) * 16384u, 2 * STG); issueLA(hb + (unsigned)ord3(1) * 16384u, 0);
        int so = STG;
        WAITV(8);
#pragma nounroll
        for (int S = 50; S < 66; ++S) {
            PBAR();
            const int k = ord3(S - 48); issueLA(hb + (unsigned)k * 16384u, so); issueLB(ob + (unsigned)k * 18432u, so); so = nextbuf(so);
            WAITV(17);
        }
        PBAR();
        WAITV(0); PBAR();
        PBAR();
        STAMP(5); PDUMP();
        if (bid == 0 && p == 0 && lane == 0) __builtin_amdgcn_raw_buffer_store_b64(T, ws, WS_E, 0, 16);
        return;
    }

    if (wid < 4) {
        const int kq = wid >> 1, mh = wid & 1;
        f32x4 acc[5][2];
        struct F3 { s16x4 alo[4], ahi[4]; bf16x8 b[10]; } f0, f1;
        const LAS unsigned char* ph = lds + 2 * STG + mh * 8192 + kq * 2048 + lane * 16;
        const LAS unsigned char* pw = lds + 2 * STG + BOFF + kq * 9216 + lane * 16;
        const LAS unsigned char* pw4 = lds + 2 * STG + BOFF + kq * 9216 + 4096 + (fq * 8 + (fr & 7)) * 16;
        auto rd3 = [&](F3& f, int i, bool single) {
            const int j = i / 7, r = i % 7;
            if (r < 2) {
                if (!single) { const bf16x8 v = *(const LAS bf16x8*)(ph + r * 4096 + j * 1024); f.alo[j * 2 + r] = __builtin_shufflevector(v, v, 0, 1, 2, 3); f.ahi[j * 2 + r] = __builtin_shufflevector(v, v, 4, 5, 6, 7); }
                else { const LAS unsigned char* q = ph + r * 4096 + j * 1024; f.alo[j * 2 + r] = *(const LAS s16x4*)q; f.ahi[j * 2 + r] = *(const LAS s16x4*)(q + 512); }
            } else if (r < 6) f.b[j * 5 + r - 2] = *(const LAS bf16x8*)(pw + j * 4608 + (r - 2) * 1024);
            else f.b[j * 5 + 4] = *(const LAS bf16x8*)(pw4 + j * 4608);
        };
        int so = 2 * STG;
        auto step3 = [&](const F3& c, F3& l, int mode  , bool first) {
            const int delta = so == 2 * STG ? -2 * STG : STG; so += delta;
#pragma unroll
            for (int i = 0; i < 20; ++i) {
                const int j = i / 10, n = (i % 10) >> 1, m = i & 1;
                acc[n][m] = MFMA16(c.b[j * 5 + n], cat8(c.alo[j * 2 + m], c.ahi[j * 2 + m]), (first && j == 0) ? ((f32x4){0.f, 0.f, 0.f, 0.f}) : acc[n][m]);
                if (mode < 2 && i < 7) { rd3(l, 2 * i, mode == 1); rd3(l, 2 * i + 1, mode == 1); }
                if (mode < 2) { if (i == 8) { ph += delta; asm volatile("" : "+v"(ph)); } if (i == 9) { pw += delta; asm volatile("" : "+v"(pw)); } if (i == 10) { pw4 += delta; asm volatile("" : "+v"(pw4)); } }
                SB0();
            }
        };
        __builtin_amdgcn_s_waitcnt(0xC07F);
        BAR();
#pragma unroll
        for (int i = 0; i < 14; ++i) rd3(f0, i, false);
        ph -= 2 * STG; pw -= 2 * STG; pw4 -= 2 * STG; so = 0;
        WAITL0(); BAR(); step3(f0, f1, 0, true);
#pragma nounroll
        for (int j = 0; j < 7; ++j) {
            WAITL0(); BAR(); step3(f1, f0, 0, false);
            WAITL0(); BAR(); step3(f0, f1, 0, false);
        }
        ph = opq(ph - lane * 8);
        WAITL0(); BAR(); step3(f1, f0, 1, false);
        WAITL0(); BAR(); step3(f0, f1, 1, false);
        step3(f1, f0, 2, false);
        STAMP(8);
        LAS unsigned char* prw = opq(lds + L_R + wid * 5120 + lane * 16);
#pragma unroll
        for (int n = 0; n < 5; ++n) { const f32x4 x0 = acc[n][0], x1 = acc[n][1]; *(LAS f32x4*)(prw + n * 1024) = kq ? x0 : x1; acc[n][0] = kq ? x1 : x0; }
        WAITL0();
        BAR();
        const int m = mh * 2 + kq;
        const rsrc_t ors = __builtin_amdgcn_make_buffer_rsrc(out, 0, SEQ * DM * 2, 0x00020000);
        const float g = *(const LAS float*)(lds + L_MISC + 64 + (16 * m + fr) * 4), g1 = rbf(1.0f - g);
        const LAS unsigned char* prr = opq(lds + L_R + (wid ^ 2) * 5120 + lane * 16);
        const LAS unsigned char* pat = opq(lds + L_AT + (4 * fq + (fr >> 2)) * 128 + (16 * m + 4 * (lane & 3)) * 2);
#pragma unroll
        for (int n = 0; n < 5; ++n) {
            const f32x4 b = acc[n][0] + *(const LAS f32x4*)(prr + n * 1024);
            const u32x2 a = __builtin_bit_cast(u32x2, tr_read(pat + n * 2048));
            const u32x2 o = {pk_bf16(g * bflo(a[0]) + g1 * b[0], g * bfhi(a[0]) + g1 * b[1]), pk_bf16(g * bflo(a[1]) + g1 * b[2], g * bfhi(a[1]) + g1 * b[3])};
            if (n < 4 || fq < 2) __builtin_amdgcn_raw_buffer_store_b64(o, ors, (unsigned)((qm * 64 + 16 * m + fr) * 1152 + (qn * 72 + 16 * n + 4 * fq) * 2), 0, GM_OAUX);
        }
    } else {
        int d = pn * 36 + pm;
#pragma nounroll
        for (int c = 0; c < nch; ++c) {
            const float dsk = bf2f(Dsk[d]);
#pragma unroll
            for (int k = 0; k < 2; ++k) {
                BAR(); ++nb;
                const int l0 = h * 1024 + k * 512 + lane * 8;
                f32x4 cv = *(const LAS f32x4*)(lds + ytab(c) + 8192 + (l0 >= 16 ? (l0 >> 4) - 1 : 0) * 16);
                if ((lane & 1) || l0 == 0) cv = (f32x4){0.f, 0.f, 0.f, 0.f};
                const f32x4 y0 = *(const LAS f32x4*)(lds + ytab(c) + l0 * 4) + cv, y1 = *(const LAS f32x4*)(lds + ytab(c) + l0 * 4 + 16);
                const u32x4 uv = *(const LAS u32x4*)(lds + L_UT + c * UT_SZ + 256 + l0 * 2);
                u32x4 o;
#pragma unroll
                for (int j = 0; j < 4; ++j) {
                    const float ya = j < 2 ? y0[2 * j] : y1[2 * j - 4], yb = j < 2 ? y0[2 * j + 1] : y1[2 * j - 3];
                    const unsigned yp = pk_bf16(ya, yb), up = pk_bf16(bflo(uv[j]) * dsk, bfhi(uv[j]) * dsk);
                    o[j] = pk_bf16(bflo(yp) + bflo(up), bfhi(yp) + bfhi(up));
                }
                __builtin_amdgcn_raw_buffer_store_b128(o, ws, WS_AT + (unsigned)d * 4096u + l0 * 2, 0, 16);
            }
            d += 16;
        }
        STEPS_TO(58);
        flag_publish(ws, WS_FA + (unsigned)((pn >> 1) * 64 + (pn & 1) * 32 + pm * 2 + h) * 128u, T, lane);
        STAMP(5);
        STEPS_TO(62);
        if (h == 0) {
            const unsigned fo = WS_FA + (unsigned)(qn * 64 + lane) * 128u;
            int t0 = 0;
            for (;;) {
                CFENCE();
                const u32x2 v = flag_ld(ws, fo);
                STEP(68); STEP(68);
                if (flag_all(v, T, true)) break;
                if (++t0 > SPIN_G) break;
            }
            ACQ();
            u32x4 av[9];
#pragma unroll
            for (int i = 0; i < 9; ++i) av[i] = __builtin_amdgcn_raw_buffer_load_b128(ws, WS_AT + (unsigned)(qn * 72 + i * 8 + (lane >> 3)) * 4096u + qm * 128 + (lane & 7) * 16, 0, 16);
            STEP(68); STEP(68);
            LAS unsigned char* pa_ = opq(lds + L_AT + lane * 16);
#pragma unroll
            for (int i = 0; i < 9; ++i) *(LAS u32x4*)(pa_ + i * 1024) = av[i];
        } else {
            STEPS_TO(65);
            const unsigned gv = __builtin_amdgcn_raw_buffer_load_b32(ws, WS_GATE + (unsigned)(qm * 64 + lane) * 4u, 0, 16);
            BAR(); BAR(); nb += 2;
            *(LAS unsigned*)(lds + L_MISC + 64 + lane * 4) = gv;
        }
        STEPS_TO(68);
        WAITL0();
        BAR();
    }
    STAMP(9); TRDUMP();
}
}

extern "C" void kernel_launch(void* const* d_in, const int* in_sizes, int n_in, void* d_out, int out_size, void* d_ws, size_t ws_size, hipStream_t stream) {
    (void)in_sizes; (void)n_in; (void)out_size; (void)ws_size;
    (void)hipFuncSetAttribute((const void*)gm::gm_kernel, hipFuncAttributeMaxDynamicSharedMemorySize, gm::LDS_BYTES);
    hipLaunchKernelGGL(gm::gm_kernel, dim3(256), dim3(512), gm::LDS_BYTES, stream, (const u16*)d_in[0], (const u16*)d_in[1], (const u16*)d_in[2], (const u16*)d_in[3],
                       (const u16*)d_in[4], (const u16*)d_in[5], (const u16*)d_in[6], (const u16*)d_in[8], (const u16*)d_in[9], (const u16*)d_in[10],
                       (const u16*)d_in[11], (const u16*)d_in[12], (u16*)d_out, (unsigned char*)d_ws);
}
```

```cpp
#include <hip/hip_runtime.h>
#include <stdint.h>
#include <type_traits>

#define LAS __attribute__((address_space(3)))
typedef unsigned short u16;
typedef unsigned long long u64;
typedef short bf16x8 __attribute__((ext_vector_type(8)));
typedef short s16x4 __attribute__((ext_vector_type(4)));
typedef _Float16 f16x8 __attribute__((ext_vector_type(8)));
typedef _Float16 f16x2 __attribute__((ext_vector_type(2)));
typedef __bf16 bf16x2 __attribute__((ext_vector_type(2)));
typedef float f32x4 __attribute__((ext_vector_type(4)));
typedef float f32x2 __attribute__((ext_vector_type(2)));
typedef unsigned u32x4 __attribute__((ext_vector_type(4)));
typedef unsigned u32x2 __attribute__((ext_vector_type(2)));
typedef __amdgpu_buffer_rsrc_t rsrc_t;

namespace gm {
constexpr int SEQ = 2048, DM = 576, FH = 2304;
constexpr unsigned WS_E = 0, WS_FS = 4096, WS_FZ = WS_FS + 65536, WS_FH = WS_FZ + 65536, WS_FA = WS_FH + 131072;
constexpr unsigned WS_ST = 0x60000, WS_GATE = 0xE0000, WS_DBG = 0x1E00000, WS_Z2F = 0x100000, WS_HF = 0xA00000, WS_WOF = 0x1300000, WS_UT = 0x1600000, WS_AT = 0x1900000, WS_END = 0x1C00000, WS_TRC = 0x1C00000;
constexpr int STG = 34816, BOFF = 16384, Z0 = 3 * STG, ZST = 296, T0 = Z0 + 128 * ZST;
constexpr int KT_SZ = 4352, UT_SZ = 4608, Y_SZ = 10240;
constexpr int L_MISC = 163840 - 320;
constexpr int L_KT = L_MISC - 3 * KT_SZ, L_SP = T0, L_KD = Z0 + 3 * 8448, L_UT = Z0 + 3 * Y_SZ;
constexpr int L_AT = Z0, L_R = Z0 + 10240;
constexpr int SPIN_G = 8192, SPIN_L = 1 << 20;
constexpr int LDS_BYTES = 163840;
static_assert(L_UT + 3 * UT_SZ <= L_KT && L_KD + 3 * 3072 <= T0 && L_SP + 2048 <= L_KT, "lds map");
#define GM_HPRIO 1
#define GM_OAUX 0
#define GM_PFW(w) ((w) == 6)
#define GM_PFL 13
#define GM_CODE 50176
#define GM_ORD 0
#define GM_PW 3
#define GM_PWS 8
#define GM_DRY 0
#define GM_CSM 11
#define GM_PCONV 1
#define GM_PREPOLL 1
#define GM_LAG 4

__device__ __forceinline__ float bf2f(unsigned v) { return __uint_as_float(v << 16); }
__device__ __forceinline__ float bflo(unsigned w) { return __uint_as_float(w << 16); }
__device__ __forceinline__ float bfhi(unsigned w) { return __uint_as_float(w & 0xffff0000u); }
__device__ __forceinline__ unsigned pk_bf16(float a, float b) { f32x2 v = {a, b}; return __builtin_bit_cast(unsigned, __builtin_convertvector(v, bf16x2)); }
__device__ __forceinline__ unsigned pk_f16(float a, float b) { f32x2 v = {a, b}; return __builtin_bit_cast(unsigned, __builtin_convertvector(v, f16x2)); }
__device__ __forceinline__ float rbf(float a) { return bflo(pk_bf16(a, a)); }
__device__ __forceinline__ f32x2 gelu2(f32x2 x) {
    const f32x2 w = (x * x) * (-0.10294324f) + (-2.30220819f);
    const f32x2 a = w * x;
    f32x2 e; e.x = __builtin_amdgcn_exp2f(a.x); e.y = __builtin_amdgcn_exp2f(a.y);
    e = e + 1.0f;
    f32x2 r; r.x = __builtin_amdgcn_rcpf(e.x); r.y = __builtin_amdgcn_rcpf(e.y);
    return x * r;
}
__device__ __forceinline__ f32x4 gelu4(f32x4 v) { const f32x2 a = gelu2((f32x2){v[0], v[1]}), b = gelu2((f32x2){v[2], v[3]}); return (f32x4){a.x, a.y, b.x, b.y}; }
__device__ __forceinline__ float allred_q(float v) {
    unsigned u = __float_as_uint(v);
    auto r = __builtin_amdgcn_permlane16_swap(u, u, false, false);
    v = __uint_as_float(r[0]) + __uint_as_float(r[1]);
    u = __float_as_uint(v);
    auto s = __builtin_amdgcn_permlane32_swap(u, u, false, false);
    return __uint_as_float(s[0]) + __uint_as_float(s[1]);
}
#define ACQ() __builtin_amdgcn_fence(__ATOMIC_ACQUIRE, "wavefront")
#define TRP
#define TRA
#define TRDUMP() do { } while (0)
#define BAR() do { asm volatile("" ::: "memory"); __builtin_amdgcn_s_barrier(); asm volatile("" ::: "memory"); } while (0)
#define PBAR() BAR()
#define PDUMP() TRDUMP()
#define WAITV(n) asm volatile("s_waitcnt vmcnt(" #n ")" ::: "memory")
#define WAITL0() asm volatile("s_waitcnt lgkmcnt(0)" ::: "memory")
#define CFENCE() asm volatile("" ::: "memory")
#define SGB(mask, n, id) __builtin_amdgcn_sched_group_barrier(mask, n, id)
#define MFMA16(a, b, c) __builtin_amdgcn_mfma_f32_16x16x32_bf16(a, b, c, 0, 0, 0)
__device__ __forceinline__ s16x4 tr_read(const LAS unsigned char* p) { return __builtin_amdgcn_ds_read_tr16_b64_v4i16((LAS s16x4*)p); }
__device__ __forceinline__ bf16x8 cat8(s16x4 a, s16x4 b) { return __builtin_shufflevector(a, b, 0, 1, 2, 3, 4, 5, 6, 7); }
template <class P> __device__ __forceinline__ P opq(P p) { asm volatile("" : "+v"(p)); return p; }
__device__ __forceinline__ int nextbuf(int so) { return so == 2 * STG ? 0 : so + STG; }

#define STAMP(i) do { } while (0)

__device__ __forceinline__ u32x2 flag_ld(rsrc_t ws, unsigned off) { return __builtin_amdgcn_raw_buffer_load_b64(ws, off, 0, 16); }
__device__ __forceinline__ bool flag_all(u32x2 v, u32x2 T, bool active) { return __builtin_amdgcn_ballot_w64(active && (v[0] != T[0] || v[1] != T[1])) == 0ull; }
__device__ __forceinline__ void flag_publish(rsrc_t ws, unsigned off, u32x2 T, int lane) {
    WAITV(0);
    if (lane == 0) __builtin_amdgcn_raw_buffer_store_b64(T, ws, off, 0, 16);
}
#define SB0() __builtin_amdgcn_sched_barrier(0)
template <int NT> struct Frag { bf16x8 a[4]; s16x4 lo[NT], hi[NT]; };
template <int NT, bool TR, int HALF>
__device__ __forceinline__ void frag_read(Frag<NT>& f, int i, const LAS unsigned char* pa, const LAS unsigned char* pb) {
    { constexpr int B0 = TR ? 2 : 1; i = i == 0 ? 0 : i <= B0 ? i + 3 : i <= B0 + 3 ? i - B0 : i; }
    if (i < 4) f.a[i] = *(const LAS bf16x8*)(pa + i * 2048);
    else if (TR) { const int n = (i - 4) >> 1; if ((i - 4) & 1) f.hi[n] = tr_read(pb + HALF * 9216 + 4608 + n * 32); else f.lo[n] = tr_read(pb + HALF * 9216 + n * 32); }
    else { const int n = i - 4; const bf16x8 v = *(const LAS bf16x8*)(pb + HALF * 9216 + n * 1024);
           f.lo[n] = __builtin_shufflevector(v, v, 0, 1, 2, 3); f.hi[n] = __builtin_shufflevector(v, v, 4, 5, 6, 7); }
}
template <int NT, bool TR, int LHALF, int S0, int BAT, bool FIRST, bool UPD>
__device__ __forceinline__ void half_step(f32x4 (&acc)[NT][4], const Frag<NT>& c, Frag<NT>& l, const LAS unsigned char*& pa0, const LAS unsigned char*& pa1, const LAS unsigned char*& pb, int delta,
                                          const float* ini, const volatile LAS unsigned* rdy, unsigned& rv TRP) {
    constexpr int NRD = TR ? 4 + 2 * NT : 4 + NT, NM = 4 * NT, NS = NM - S0;
#pragma unroll
    for (int i = 0; i < NM; ++i) {
        const int n = i >> 2, m = i & 3;
        acc[n][m] = MFMA16(cat8(c.lo[n], c.hi[n]), c.a[m], FIRST ? (TR ? ((f32x4){0.f, 0.f, 0.f, 0.f}) : ((f32x4){ini[m], ini[m], ini[m], ini[m]})) : acc[n][m]);
        SB0();
        if (i == BAT) { WAITL0(); BAR(); if (TR) rv = *rdy; }
        if (LHALF >= 0 && i >= S0) {
#pragma unroll
            for (int r = 0; r < NRD; ++r) if ((r * NS) / NRD == i - S0) frag_read<NT, TR, (LHALF > 0 ? 1 : 0)>(l, r, LHALF > 0 ? pa1 : pa0, pb);
        }
        if (UPD) { if (i == NM - 3) { pa0 += delta; asm volatile("" : "+v"(pa0)); } if (i == NM - 2) { pa1 += delta; asm volatile("" : "+v"(pa1)); } if (i == NM - 1) { pb += delta; asm volatile("" : "+v"(pb)); } }
        SB0();
    }
}
template <int NT, bool TR, class HK>
__device__ __forceinline__ void gemm_phase(int nsteps, f32x4 (&acc)[NT][4], int& so, const LAS unsigned char* pa0, const LAS unsigned char* pa1, const LAS unsigned char* pb,
                                           const float* ini, const volatile LAS unsigned* rdy, HK hook TRP) {
    constexpr int NRD = TR ? 4 + 2 * NT : 4 + NT;
    Frag<NT> f0, f1;
    unsigned rv = 0;
    pa0 += so; pa1 += so; pb += so;
    asm volatile("" : "+v"(pa0), "+v"(pa1), "+v"(pb));
    __builtin_amdgcn_s_waitcnt(0xC07F);
    BAR();
#pragma unroll
    for (int i = 0; i < NRD; ++i) frag_read<NT, TR, 0>(f0, i, pa0, pb);
    SB0();
    int delta = so == 2 * STG ? -2 * STG : STG; so += delta;
    half_step<NT, TR, 1, 2, -1, true, true>(acc, f0, f1, pa0, pa1, pb, delta, ini, rdy, rv TRA);
#pragma nounroll
    for (int s = 1; s < nsteps; ++s) {
        delta = so == 2 * STG ? -2 * STG : STG; so += delta;
        half_step<NT, TR, 0, 6, 5, false, false>(acc, f1, f0, pa0, pa1, pb, 0, ini, rdy, rv TRA);
        half_step<NT, TR, 1, 2, -1, false, true>(acc, f0, f1, pa0, pa1, pb, delta, ini, rdy, rv TRA);
        if (TR) hook(rv);
    }
    if (TR) half_step<NT, TR, -1, 0, -1, false, false>(acc, f1, f0, pa0, pa1, pb, 0, ini, rdy, rv TRA);
    else {
#pragma unroll
        for (int m = 0; m <= 4; ++m) {
            if (m < 4) {
#pragma unroll
                for (int n = 0; n < NT; ++n) acc[n][m] = MFMA16(cat8(f1.lo[n], f1.hi[n]), f1.a[m], acc[n][m]);
            }
            SB0();
            if (m > 0) hook(m - 1);
            SB0();
        }
    }
}

template <int NT, class LT>
__device__ __forceinline__ void consumer_a(LAS unsigned char* lds, u32x2 (&zp)[5][4], LT late, bool dry, int wm, int wn, int lane, unsigned char* wsp_ TRP) {
    const int fr = lane & 15, fq = lane >> 4;
    constexpr int N0 = (NT == 5) ? 0 : 5;
    f32x4 acc[NT][4];
    int so = 0;
    const int sw = (fr >> 1) & 7;
    const int a0 = (wm * 64 + fr) * 128 + ((fq ^ sw) << 4), a1 = (wm * 64 + fr) * 128 + (((4 + fq) ^ sw) << 4);
    const int bw = BOFF + (4 * fq + (fr >> 2)) * 288 + (lane & 3) * 8 + N0 * 32;
    const volatile LAS unsigned* rdy = (const volatile LAS unsigned*)(lds + L_MISC + 4 + wm * 4);
    bool done = false;
    auto normalise = [&]() {
        const LAS unsigned char* ps = opq(lds + L_SP + (wm * 64 + fr) * 16);
        LAS unsigned char* pz = opq(lds + Z0 + (wm * 64 + fr) * ZST + N0 * 32 + fq * 8);
#pragma unroll
        for (int m = 0; m < 4; ++m) {
            const f32x2 sr = *(const LAS f32x2*)(ps + m * 256);
#pragma unroll
            for (int n = 0; n < NT; ++n) {
                const f32x2 u = (f32x2){bflo(zp[n][m][0]), bfhi(zp[n][m][0])} * sr.x + sr.y, v = (f32x2){bflo(zp[n][m][1]), bfhi(zp[n][m][1])} * sr.x + sr.y;
                *(LAS u32x2*)(pz + m * 16 * ZST + n * 32) = (u32x2){pk_bf16(u.x, u.y), pk_bf16(v.x, v.y)};
            }
        }
        CFENCE();
        if (lane == 0) *(volatile LAS unsigned*)(lds + L_MISC + 16 + (wm * 2 + wn) * 4) = 1u;
        done = true;
    };
    auto hook = [&](unsigned rv) { if (__builtin_amdgcn_readfirstlane(rv) != 0u && !done) normalise(); };
#pragma unroll
    for (int n = 0; n < NT; ++n)
#pragma unroll
        for (int m = 0; m < 4; ++m) { zp[n][m] = (u32x2){0u, 0u}; asm volatile("" : "=v"(acc[n][m])); }
#pragma nounroll
    for (int it = (GM_DRY && dry) ? -1 : 0; it < 2; ++it) {
        if (it >= 0) gemm_phase<NT, true>(9, acc, so, lds + a0, lds + a1, lds + bw, nullptr, rdy, hook TRA);
        if (it >= 0) STAMP(1 + 2 * it);
        if (it == 1) late();
        if (it == 1 && !done) {
            int t0 = 0;
            while (__builtin_amdgcn_readfirstlane(*rdy) == 0u && ++t0 < SPIN_L) __builtin_amdgcn_s_sleep(1);
            normalise();
        }
#pragma unroll
        for (int m = 0; m < 4; ++m)
#pragma unroll
            for (int n = 0; n < NT; ++n) { acc[n][m] = gelu4(acc[n][m]); zp[n][m] = (u32x2){pk_bf16(acc[n][m][0], acc[n][m][1]), pk_bf16(acc[n][m][2], acc[n][m][3])}; }
        if (it <= 0) {
            LAS unsigned char* ps = opq(lds + L_SP + (wm * 64 + fr) * 16 + wn * 8);
#pragma unroll
            for (int m = 0; m < 4; ++m) {
                f32x4 s4 = acc[0][m];
#pragma unroll
                for (int n = 1; n < NT; ++n) s4 += acc[n][m];
                const float mean = opq(allred_q((s4[0] + s4[1]) + (s4[2] + s4[3])) * (1.0f / (16 * NT)));
                f32x4 q4 = {0.f, 0.f, 0.f, 0.f};
#pragma unroll
                for (int n = 0; n < NT; ++n) { const f32x4 d = acc[n][m] - mean; q4 += d * d; }
                const float m2 = allred_q((q4[0] + q4[1]) + (q4[2] + q4[3]));
                if (fq == 0) *(LAS f32x2*)(ps + m * 256) = (f32x2){mean, m2};
            }
            STAMP(2);
        }
    }
    STAMP(4);
}

template <int NT>
__device__ __forceinline__ void consumer_b(LAS unsigned char* lds, rsrc_t ws, u32x2 T, const u32x2 (&z1)[5][4], const float (&bs_)[4], int pm, int pn, int wm, int wn, int lane, unsigned char* wsp_ TRP) {
    float bs[4] = {bf2f(__float_as_uint(opq(bs_[0]))), bf2f(__float_as_uint(opq(bs_[1]))), bf2f(__float_as_uint(opq(bs_[2]))), bf2f(__float_as_uint(opq(bs_[3])))};
    const int fr = lane & 15, fq = lane >> 4;
    constexpr int N0 = (NT == 5) ? 0 : 5;
    f32x4 acc[NT][4];
    int so = 0;
    const int sw = (fr >> 1) & 7;
    const int a0 = (wm * 64 + fr) * 128 + ((fq ^ sw) << 4), a1 = (wm * 64 + fr) * 128 + (((4 + fq) ^ sw) << 4);
    const int bz = BOFF + N0 * 1024 + lane * 16;
    const int qm = 2 * pm + wm;
    const unsigned hb = WS_HF + (unsigned)qm * (18u * 16384u);
    auto epi = [&](int m) {
        if (m == 0) __builtin_amdgcn_s_setprio(3);
        u32x2 hp[NT];
#pragma unroll
        for (int n = 0; n < NT; ++n) {
            const f32x2 u = (f32x2){bflo(z1[n][m][0]), bfhi(z1[n][m][0])} * (f32x2){acc[n][m][0], acc[n][m][1]}, v = (f32x2){bflo(z1[n][m][1]), bfhi(z1[n][m][1])} * (f32x2){acc[n][m][2], acc[n][m][3]};
            hp[n] = (u32x2){pk_bf16(u.x, u.y), pk_bf16(v.x, v.y)};
        }
#pragma unroll
        for (int j = 0; j < 2; ++j)
            __builtin_amdgcn_raw_buffer_store_b128((u32x4){hp[2 * j][0], hp[2 * j][1], hp[2 * j + 1][0], hp[2 * j + 1][1]}, ws,
                                                   hb + (unsigned)pn * 16384u + (unsigned)(m * 4 + wn * 2 + j) * 1024u + lane * 16, 0, 16);
        if (NT == 5) __builtin_amdgcn_raw_buffer_store_b64(hp[NT - 1], ws, hb + (unsigned)(16 + (pn >> 3)) * 16384u + m * 4096u + (unsigned)(pn & 7) * 512u + lane * 8, 0, 16);
    };
    gemm_phase<NT, false>(32, acc, so, lds + a0, lds + a1, lds + bz, bs, nullptr, epi TRA);
    STAMP(5);
    flag_publish(ws, WS_FH + (unsigned)(qm * 32 + pn * 2 + wn) * 128u, T, lane);
    __builtin_amdgcn_s_setprio(0);
    STAMP(6);
}

template <int TLO, int THI>
__device__ __forceinline__ void conv_task(LAS unsigned char* lds, int kt, int ut, int yt, int tl, int lane, int& nb TRP) {
#define CSL() do { if (nb < 50) { BAR(); ++nb; } } while (0)
    constexpr int NTL = THI - TLO + 1, ND = THI + 2;
    const int i = lane & 15, g = lane >> 4;
    const LAS unsigned char* pk = opq(lds + kt + (i & 3) * 1088 + ((i >> 2) + g) * 16);
    const LAS unsigned char* pu = opq(lds + ut + (128 + 16 * tl - 32 * g - 4 * (i >> 2) + 4 * (i & 3)) * 2 - 32);
    bf16x8 ub[ND], kf[ND];
#pragma unroll
    for (int d = 0; d < ND; ++d) { if (d % 6 == 0) CSL(); ub[ND - 1 - d] = cat8(tr_read(pu + 256 * (ND - 1 - d) + 32), tr_read(pu + 256 * (ND - 1 - d))); kf[d] = *(const LAS bf16x8*)(pk + 64 * d); }
    f32x4 acc[NTL];
    int cnt = GM_CSM;
#pragma unroll
    for (int s = -1; s <= THI; ++s) {
#pragma unroll
        for (int t = TLO; t <= THI; ++t) if (t >= s) {
            if (cnt == GM_CSM) { CSL(); cnt = 0; }
            ++cnt;
            acc[t - TLO] = MFMA16(kf[s + 1], ub[t - s], s == -1 ? ((f32x4){0.f, 0.f, 0.f, 0.f}) : acc[t - TLO]);
        }
    }
    LAS float* y = opq((LAS float*)(lds + yt) + 32 * g + 16 * tl + i);
    LAS float* cy = opq((LAS float*)(lds + yt + 8192) + (2 * g + tl) * 4 + (i & 3));
#define DPPF(v, ctl) __int_as_float(__builtin_amdgcn_update_dpp(0, __float_as_int(v), ctl, 0xf, 0xf, true))
#pragma unroll
    for (int t = TLO; t <= THI; ++t) {
        if (((t - TLO) % 3) == 0) CSL();
        const f32x4 a = acc[t - TLO];
        y[128 * t] = (a[0] + DPPF(a[1], 0x111)) + (DPPF(a[2], 0x112) + DPPF(a[3], 0x113));
        const float cv = DPPF(a[1], 0x10f) + (DPPF(a[2], 0x10e) + DPPF(a[3], 0x10d));
        if (i < 4) cy[32 * t] = cv;
    }
#undef DPPF
#undef CSL
}
__device__ __forceinline__ int ytab(int c) { return Z0 + c * Y_SZ; }

__global__ __launch_bounds__(512, 2) void gm_kernel(const u16* __restrict__ x, const u16* __restrict__ logA, const u16* __restrict__ Aim, const u16* __restrict__ Cre,
                                                    const u16* __restrict__ Cim, const u16* __restrict__ logdt, const u16* __restrict__ Dsk, const u16* __restrict__ win,
                                                    const u16* __restrict__ wsp, const u16* __restrict__ bsp, const u16* __restrict__ wout, const u16* __restrict__ gw,
                                                    u16* __restrict__ out, unsigned char* wsp_) {
    extern __shared__ __attribute__((aligned(16))) unsigned char lds_[];
    LAS unsigned char* lds = (LAS unsigned char*)lds_;
    const int wid = __builtin_amdgcn_readfirstlane(threadIdx.x >> 6), lane = threadIdx.x & 63;
    const int bid = blockIdx.x, xg = bid & 7, rr = bid >> 3, bi = rr >> 2, bj = rr & 3;
    const int pm = (xg >> 2) * 8 + bi, pn = (xg & 3) * 4 + bj;
    const int qm = 2 * pm + (pn >> 3), qn = pn & 7;
    const int nch = pm < 4 ? 3 : 2;
    const rsrc_t ws = __builtin_amdgcn_make_buffer_rsrc(wsp_, 0, WS_END, 0x00020000);
    STAMP(0);
    unsigned pfw = 0;
    if (GM_PFW(wid)) {
        u64 pc0; asm volatile("s_getpc_b64 %0" : "=s"(pc0));
        const u64 q_ = (pc0 & ~127ull) + (unsigned)min((rr * GM_PFL + (lane >> 1)) * 128 + (lane & 1) * 64, GM_CODE);
        if (lane < 2 * GM_PFL) asm volatile("global_load_dword %0, %1, off" : "+v"(pfw) : "v"(q_) : "memory");
    }

    const int fr = lane & 15, fq = lane >> 4;
    const int h = wid - 6, p = wid - 4;
    const bool is_prod = (wid >> 1) == 2;
    const int ch0 = pn * 36 + pm;
    float bs[4];
    auto late = [&]() {
#pragma unroll
        for (int m = 0; m < 4; ++m) bs[m] = __uint_as_float((unsigned)bsp[pm * 128 + (wid & 1) * 64 + m * 16 + fr]);
    };
    const int phi = (-(bi + bj)) & 3;
    const unsigned rsw = (unsigned)(((lane & 7) ^ ((4 * p + (lane >> 4)) & 7)) * 16);
#define GLDS(src, dst, aux) __builtin_amdgcn_global_load_lds((const unsigned*)(src), (LAS unsigned*)(dst), 16, 0, aux)
    auto issueS = [&](int k, int s) {
        const char* sb = (const char*)wsp + (size_t)(pm * 128 + p * 8) * 4096 + k * 128; const unsigned vs = (unsigned)(lane >> 3) * 4096u + rsw;
#pragma unroll
        for (int i = 0; i < 8; ++i) GLDS(sb + (size_t)i * (16 * 4096) + vs, lds + s + (p + 2 * i) * 1024, 0); };
    auto ord2 = [&](int s) { return GM_LAG == 0 ? s : (((s & 3) == phi) ? s : ((s - GM_LAG) & 31)); };
    u32x2 E, T;
    auto mk_tag = [&]() { u64 z = (((u64)__builtin_amdgcn_readfirstlane(E[1]) << 32) | __builtin_amdgcn_readfirstlane(E[0])) + 0x9E3779B97F4A7C15ull;
        z = (z ^ (z >> 30)) * 0xBF58476D1CE4E5B9ull; z = (z ^ (z >> 27)) * 0x94D049BB133111EBull; z ^= z >> 31; T = (u32x2){(unsigned)z, (unsigned)(z >> 32)}; };
    u32x2 zp[5][4];
#define STEP(lim) do { if (nb < (lim)) { BAR(); ++nb; } } while (0)
#define STEPS_TO(lim) _Pragma("nounroll") while (nb < (lim)) { BAR(); ++nb; }
    int nb = 0;
    const int kc = wid == 7 ? 2 : wid == 1 || wid == 2 ? wid - 1 : 3;
    if (kc < nch) {
        const int kch = ch0 + 16 * kc, ki = kch * 64 + lane;
        const unsigned pA = ((const unsigned*)logA)[ki], pI = ((const unsigned*)Aim)[ki], pR = ((const unsigned*)Cre)[ki], pC = ((const unsigned*)Cim)[ki], pT = logdt[kch];
        LAS u32x2* tw = opq((LAS u32x2*)(lds + Z0 + kc * 8448) + lane);
        const LAS unsigned char* trd = opq(lds + Z0 + kc * 8448 + fr * 528 + fq * 16);
        const float dt = rbf(__expf(bf2f(pT)));
        const f32x2 ar = (f32x2){rbf(__expf(bflo(pA))), rbf(__expf(bfhi(pA)))} * dt * -1.44269504f, ai = (f32x2){bflo(pI), bfhi(pI)} * dt * 0.15915494f;
        const f32x2 cr = (f32x2){bflo(pR), bfhi(pR)} * dt, ci = (f32x2){bflo(pC), bfhi(pC)} * dt;
        float cm = fmaxf(fmaxf(fabsf(cr.x), fabsf(ci.x)), fmaxf(fabsf(cr.y), fabsf(ci.y)));
#define DPPF(v, ctl) __int_as_float(__builtin_amdgcn_update_dpp(0, __float_as_int(v), ctl, 0xf, 0xf, true))
        cm = fmaxf(cm, DPPF(cm, 0x121)); cm = fmaxf(cm, DPPF(cm, 0x122)); cm = fmaxf(cm, DPPF(cm, 0x124)); cm = fmaxf(cm, DPPF(cm, 0x128));
#undef DPPF
        { auto r = __builtin_amdgcn_permlane16_swap(__float_as_uint(cm), __float_as_uint(cm), false, false); cm = fmaxf(__uint_as_float(r[0]), __uint_as_float(r[1]));
          auto q = __builtin_amdgcn_permlane32_swap(__float_as_uint(cm), __float_as_uint(cm), false, false); cm = fmaxf(fmaxf(__uint_as_float(q[0]), __uint_as_float(q[1])), 1e-30f); }
        struct C2 { f32x2 x, y; };
        auto cexp = [&](float p_) { const f32x2 e = ar * p_, t = ai * p_;
            const f32x2 mg = {__builtin_amdgcn_exp2f(e.x), __builtin_amdgcn_exp2f(e.y)}; const float t0 = __builtin_amdgcn_fractf(t.x), t1 = __builtin_amdgcn_fractf(t.y);
            return C2{mg * (f32x2){__builtin_amdgcn_cosf(t0), __builtin_amdgcn_cosf(t1)}, mg * (f32x2){__builtin_amdgcn_sinf(t0), __builtin_amdgcn_sinf(t1)}}; };
        auto cmul = [](C2 a, C2 b) { return C2{__builtin_elementwise_fma(a.x, b.x, -(a.y * b.y)), __builtin_elementwise_fma(a.x, b.y, a.y * b.x)}; };
        auto put = [&](int row, C2 v, bool conj) { tw[row * 66] = conj ? (u32x2){pk_f16(v.x.x, -v.y.x), pk_f16(v.x.y, -v.y.y)} : (u32x2){pk_f16(v.x.x, v.y.x), pk_f16(v.x.y, v.y.y)}; };
        C2 wp[16], zr[8], vp[4], cv[17];
        wp[1] = cexp(8.f); zr[1] = cexp(1.f); vp[0] = cexp(128.f);
        tw[0] = (u32x2){pk_f16(1.f, 0.f), pk_f16(1.f, 0.f)};
#pragma unroll
        for (int j = 1; j < 16; ++j) { if (j > 1) wp[j] = cmul(wp[j >> 1], wp[j - (j >> 1)]); put(j, wp[j], true); }
        CFENCE();
        u32x4 fb[8];
#pragma unroll
        for (int ks = 0; ks < 8; ++ks) fb[ks] = *(const LAS u32x4*)(trd + ks * 64);
        CFENCE();
#pragma unroll
        for (int r = 2; r < 8; ++r) zr[r] = cmul(zr[r >> 1], zr[r - (r >> 1)]);
#pragma unroll
        for (int k = 1; k < 4; ++k) vp[k] = cmul(vp[k - 1], vp[k - 1]);
        const float rc = __builtin_amdgcn_rcpf(cm);
        cv[0] = C2{cr * rc, ci * rc};
#pragma unroll
        for (int k = 1; k < 8; ++k) { const int lg = k >= 4 ? 2 : k >= 2 ? 1 : 0; cv[k] = cmul(cv[k - (1 << lg)], vp[lg]); }
        LAS float* kb_ = (LAS float*)(lds + L_KD + kc * 3072);
        const float sc = 2.0f * cm;
        u32x4 fa[3][8];
#pragma unroll
        for (int tt = 0; tt < 3; ++tt) {
#pragma unroll
            for (int i = 0; i < (tt == 2 ? 9 : 16); ++i) {
                const int r = tt == 0 ? (i & 7) : tt == 1 ? (i < 8 ? 2 * (i & 3) : 4 * (i & 1)) : 0;
                const int hh = tt == 0 ? (i >> 3) : tt == 1 ? (i < 8 ? 2 + (i >> 2) : 4 + ((i - 8) >> 1)) : 8 + i;
                if (tt == 2) cv[hh] = cmul(cv[hh - 8], vp[3]);
                put(i, r == 0 ? cv[hh] : cmul(cv[hh], zr[r]), false);
            }
            CFENCE();
#pragma unroll
            for (int ks = 0; ks < 8; ++ks) fa[tt][ks] = *(const LAS u32x4*)(trd + ks * 64);
            CFENCE();
            SB0();
        }
        f32x4 kd[3] = {{0.f, 0.f, 0.f, 0.f}, {0.f, 0.f, 0.f, 0.f}, {0.f, 0.f, 0.f, 0.f}};
#pragma unroll
        for (int ks = 0; ks < 8; ++ks)
#pragma unroll
            for (int tt = 0; tt < 3; ++tt) kd[tt] = __builtin_amdgcn_mfma_f32_16x16x32_f16(__builtin_bit_cast(f16x8, fa[tt][ks]), __builtin_bit_cast(f16x8, fb[ks]), kd[tt], 0, 0, 0);
#pragma unroll
        for (int tt = 0; tt < 3; ++tt) {
            LAS float* kw = opq(kb_ + (tt == 0 ? 128 * (fq >> 1) + 4 * (fq & 1) + 8 * fr : tt == 1 ? 256 + 64 * fq + (fq < 2 ? 4 : 2) * fr : 512 + 64 * fq + fr));
            LAS float* kwb = tt == 1 ? opq(kw + (fq < 2 ? 2 : 32)) : kw + (tt == 0 ? 2 : 32);
            kw[0] = kd[tt][0] * sc; kw[tt == 2 ? 16 : 1] = kd[tt][1] * sc; kwb[0] = kd[tt][2] * sc; kwb[tt == 2 ? 16 : 1] = kd[tt][3] * sc;
        }
        WAITL0();
    }
#define IC(n) std::integral_constant<int, n>{}
    if (GM_PFW(wid)) { WAITV(0); asm volatile("" :: "v"(pfw)); }
    if (wid < 6) STAMP(10);
    if (is_prod) {
        const unsigned vx = (unsigned)(lane >> 3) * 1152u + rsw;
        unsigned wv[9];
        const char* xb = (const char*)x + (size_t)(pm * 128 + p * 8) * 1152;
        const char* wb = (const char*)win + pn * 288;
        auto issueX = [&](int k, int s) {
#pragma unroll
            for (int i = 0; i < 8; ++i) GLDS(xb + (size_t)i * (16 * 1152) + k * 128 + vx, lds + s + (p + 2 * i) * 1024, 0); };
        auto issueW = [&](int k, int coloff, int s) {
            const char* b = wb + (size_t)k * (64 * 9216) + coloff;
#pragma unroll
            for (int i = 0; i < 9; ++i) GLDS(b + wv[i], lds + s + BOFF + (p + 2 * i) * 1024, 0); };
        auto ord1 = [&](int s) { return (GM_LAG == 0 || (s & 3) == phi) ? s : (s >= 4 ? s - 4 : (s == 0 ? 8 : s + 4)); };
        issueX(ord1(0), 0);
        SB0();
#pragma unroll
        for (int i = 0; i < 9; ++i) { const int c = (p + 2 * i) * 64 + lane, rho = c / 18, ch = c - rho * 18;
            const int kap = (rho & 32) + 8 * ((rho >> 2) & 3) + 4 * ((rho >> 4) & 1) + (rho & 3); wv[i] = (unsigned)kap * 9216u + (unsigned)ch * 16u; }
        issueW(ord1(0), FH * 2, 0); issueX(ord1(1), STG); issueW(ord1(1), FH * 2, STG);
        issueX(ord1(2), 2 * STG); issueW(ord1(2), FH * 2, 2 * STG);
        E = flag_ld(ws, WS_E);
        unsigned wm_ = 0;
        auto touch = [&](const void* base, unsigned off, unsigned last) { const char* q_ = (const char*)base + min(off + lane * 64u, last); asm volatile("global_load_dword %0, %1, off" : "+v"(wm_) : "v"(q_) : "memory"); };
        if (GM_PW & 1) {
#pragma unroll
            for (int i = 0; i < 2; ++i) touch(x, bid * 9216u + p * 4608u + i * 4096u, bid * 9216u + p * 4608u + 4544u);
#pragma unroll
            for (int i = 0; i < 3; ++i) touch(win, bid * 20736u + p * 10368u + i * 4096u, bid * 20736u + p * 10368u + 10304u);
        }
        int so = 0;
        WAITV(35); PBAR();
#pragma nounroll
        for (int S = 1; S < 16; ++S) {
            if (S >= 8 && S <= 10) WAITV(9); else WAITV(17);
            PBAR();
            const int t = S + 2, k = ord1(t < 9 ? t : t < 12 ? t - 3 : t - 12);
            if (t < 9 || t >= 12) issueX(k, so);
            issueW(k, t >= 9 ? 0 : FH * 2, so); so = nextbuf(so);
            if ((GM_PW & 2) && S == GM_PWS) {
#pragma unroll
                for (int i = 0; i < 4; ++i) touch(wsp, bid * 32768u + p * 16384u + i * 4096u, 0x7fffffffu);
            }
        }
        WAITV(17); PBAR(); issueS(2 * pm, 0);
        WAITV(8); PBAR(); issueS(2 * pm + 1, STG);
        asm volatile("" :: "v"(wm_));
        STAMP(1);
    } else if (wid < 4) {
        E = flag_ld(ws, WS_E);
        if (wid >> 1) consumer_a<4>(lds, zp, late, wid == 3 && nch == 2, wid & 1, 1, lane, wsp_ TRA);
        else consumer_a<5>(lds, zp, late, wid == 0, wid & 1, 0, lane, wsp_ TRA);
    } else {
        if (h == 0 && lane < 12) *(LAS unsigned*)(lds + L_MISC + lane * 4) = 0u;
        const int row0 = lane / 9, chk0 = lane - row0 * 9;
        const int u = (pn >> 3) * 16 + pm, nblk = u < 8 ? 3 : 2;
        u32x2 uc[18]; u32x4 xv[9], gv[9], wv[3][5];
        if (h == 0) {
            { const char* xb = (const char*)x + (size_t)pm * 128 * 1152 + pn * 72; unsigned off = row0 * 1152 + chk0 * 8; int ch = chk0;
#pragma unroll
              for (int i = 0; i < 9; ++i) { uc[i] = *(const u32x2*)(xb + off); uc[9 + i] = *(const u32x2*)(xb + 64 * 1152 + off); off += 7 * 1152 + 8; if (++ch == 9) { ch = 0; off += 1152 - 72; } } }
            { const char* xr = (const char*)x + (size_t)(pm * 128 + pn * 8 + (lane >> 3)) * 1152 + (lane & 7) * 16; const char* gr = (const char*)gw + (lane & 7) * 16;
#pragma unroll
              for (int i = 0; i < 9; ++i) { xv[i] = *(const u32x4*)(xr + i * 128); gv[i] = *(const u32x4*)(gr + i * 128); } }
        } else {
            unsigned wo_[5], hi_[5];
            { int row = row0, ch = chk0;
#pragma unroll
              for (int i = 0; i < 5; ++i) { const int rw = row < 32 ? row : 31; wo_[i] = (rw & 15) * 1152 + ch * 16; hi_[i] = rw >> 4; row += 7; if (++ch == 9) { ch = 0; ++row; } } }
#pragma unroll
            for (int b = 0; b < 3; ++b) {
                const int blk = (b < nblk) ? u + 32 * b : u, st = blk >> 2, jb = blk & 3;
                const int fA = st < 16 ? st * 144 + (jb < 2 ? 32 * jb : 16 + 32 * jb) : ((st - 16) * 8 + 2 * jb) * 144 + 64, dB = (st < 16 ? 16 : 144) * 1152;
                const char* wb_ = (const char*)wout + (size_t)fA * 1152 + qn * 144;
#pragma unroll
                for (int i = 0; i < 5; ++i) wv[b][i] = *(const u32x4*)(wb_ + (wo_[i] + hi_[i] * dB));
            }
        }
        E = flag_ld(ws, WS_E);
        if (lane < 24) *(LAS u32x4*)(lds + L_KT + (lane >> 3) * KT_SZ + (2 * h + ((lane >> 2) & 1)) * 1088 + (lane & 3) * 16) = (u32x4){0u, 0u, 0u, 0u};
        WAITL0();
        STAMP(10);
        __builtin_amdgcn_s_setprio(GM_HPRIO);
        {
            float kv[2][3];
#pragma nounroll
            for (int ei = 0; ei < 9; ++ei) {
                BAR();
                if (ei > 0) {
                    LAS unsigned char* kt = opq(lds + L_KT + (2 * h + ((ei - 1) >> 2)) * 1088 + 64 + ((ei - 1) & 3) * 256 + lane * 2);
#pragma unroll
                    for (int j = 0; j < 2; ++j)
#pragma unroll
                        for (int c = 0; c < 3; ++c) *(LAS u16*)(kt + c * KT_SZ + j * 128) = (u16)pk_bf16(kv[j][c], 0.f);
                }
#pragma unroll
                for (int j = 0; j < 2; ++j) {
                    const int jj = (ei & 3) * 2 + j, e = jj >= 4 ? 3 : jj >= 2 ? 2 : jj, lag = 256 * jj + 4 * lane + 2 * h + ((ei >> 2) & 1);
                    const int idx = e == 0 ? lag : 128 * e + ((lag + (1 << (e - 1)) - 1 + ((lag >> e) & 1)) >> e);
                    const LAS float* q = opq((const LAS float*)(lds + L_KD) + idx);
#pragma unroll
                    for (int c = 0; c < 3; ++c) kv[j][c] = q[c * 768];
                }
            }
        }
        if (h == 0) {
            LAS unsigned char* pzs = opq(lds + Z0 + lane * 8);
#pragma unroll
            for (int i = 0; i < 18; ++i) *(LAS u32x2*)(pzs + i * 512) = uc[i];
            CFENCE();
            { const LAS unsigned char* pt = opq(lds + Z0 + (8 * (4 * fq + (lane & 3)) + (fr >> 2)) * 72);
              const unsigned ub = WS_UT + (unsigned)(pn * 36 + (lane & 3)) * 4096u + (unsigned)(pm * 128 + 8 * (4 * fq + (fr >> 2))) * 2u;
#pragma unroll
              for (int cc = 0; cc < 9; ++cc) __builtin_amdgcn_raw_buffer_store_b128(__builtin_bit_cast(u32x4, cat8(tr_read(pt + cc * 8), tr_read(pt + 288 + cc * 8))), ws, ub + cc * 16384u, 0, 16); }
            float a = 0.f;
#pragma unroll
            for (int i = 0; i < 9; ++i)
#pragma unroll
                for (int j = 0; j < 4; ++j) { const unsigned xa = xv[i][j], ga = gv[i][j]; a = __builtin_amdgcn_fdot2_f32_bf16(__builtin_bit_cast(bf16x2, xa), __builtin_bit_cast(bf16x2, ga), a, false); }
#define DPPF(v, ctl) __int_as_float(__builtin_amdgcn_update_dpp(0, __float_as_int(v), ctl, 0xf, 0xf, true))
            a += DPPF(a, 0xb1); a += DPPF(a, 0x4e); a += DPPF(a, 0x141);
#undef DPPF
            const float gt = rbf(__builtin_amdgcn_rcpf(1.0f + __expf(-rbf(a))));
            if ((lane & 7) == 0) __builtin_amdgcn_raw_buffer_store_b32(__float_as_uint(gt), ws, WS_GATE + (unsigned)(pm * 128 + pn * 8 + (lane >> 3)) * 4u, 0, 16);
        } else {
            LAS unsigned char* pws = opq(lds + Z0 + 9216 + lane * 16);
            const LAS unsigned char* ptw = opq(lds + Z0 + 9216 + (4 * fq + (fr >> 2)) * 144 + (lane & 3) * 8);
#pragma unroll
            for (int b = 0; b < 3; ++b) {
                if (b < nblk) {
#pragma unroll
                    for (int i = 0; i < 5; ++i) if (i < 4 || lane < 32) *(LAS u32x4*)(pws + i * 1024) = wv[b][i];
                    CFENCE();
                    const unsigned ob = WS_WOF + (unsigned)qn * (18u * 18432u) + (unsigned)(u + 32 * b) * 4608u;
#pragma unroll
                    for (int nn = 0; nn < 5; ++nn) {
                        const u32x4 v = __builtin_bit_cast(u32x4, cat8(tr_read(ptw + nn * 32), tr_read(ptw + 2304 + nn * 32)));
                        if (nn < 4) __builtin_amdgcn_raw_buffer_store_b128(v, ws, ob + nn * 1024 + lane * 16, 0, 16);
                        else if (fr < 8) __builtin_amdgcn_raw_buffer_store_b128(v, ws, ob + 4096 + (fq * 8 + fr) * 16, 0, 16);
                    }
                    CFENCE();
                }
            }
        }
        nb = 9;
        mk_tag();
        WAITL0();
        STEPS_TO(10);
        STAMP(1);
        {
            const f32x4 sp = *(const LAS f32x4*)(lds + L_SP + (h * 64 + lane) * 16);
            const float dm = sp[0] - sp[2];
            const f32x2 mine = {(80.f * sp[0] + 64.f * sp[2]) * (1.0f / 144.f), sp[1] + sp[3] + (80.f * 64.f / 144.f) * dm * dm};
            __builtin_amdgcn_raw_buffer_store_b128((u32x4){T[0], __float_as_uint(mine.x), T[1], __float_as_uint(mine.y)}, ws, WS_ST + (unsigned)((pm * 16 + pn) * 128 + h * 64 + lane) * 16u, 0, 16);
            STEP(18);
            const unsigned so_ = WS_ST + (unsigned)(pm * 16 * 128 + h * 64 + lane) * 16u;
            int t0 = 0;
            u32x4 st[16];
            for (;;) {
                CFENCE();
#pragma unroll
                for (int j = 0; j < 16; ++j) st[j] = __builtin_amdgcn_raw_buffer_load_b128(ws, so_ + j * 2048u, 0, 16);
                STEP(18);
                unsigned bad = 0;
#pragma unroll
                for (int j = 0; j < 16; ++j) bad |= (st[j][0] ^ T[0]) | (st[j][2] ^ T[1]);
                if (__builtin_amdgcn_ballot_w64(bad != 0u) == 0ull) break;
                if (++t0 > SPIN_G) break;
            }
            STAMP(2);
            STEP(18);
            float mean = 0.f, m2 = 0.f;
#pragma unroll
            for (int j = 0; j < 16; ++j) mean += __uint_as_float(st[j][1]);
            mean *= (1.0f / 16.f);
#pragma unroll
            for (int j = 0; j < 16; ++j) { const float d = __uint_as_float(st[j][1]) - mean; m2 += __uint_as_float(st[j][3]) + 144.f * d * d; }
            const float rstd = __builtin_amdgcn_rsqf(m2 * (1.0f / FH) + 1e-5f);
            *(LAS f32x2*)(lds + L_SP + (h * 64 + lane) * 16) = (f32x2){rstd, -mean * rstd};
            CFENCE();
            if (lane == 0) *(volatile LAS unsigned*)(lds + L_MISC + 4 + h * 4) = 1u;
            const volatile LAS unsigned* zd = (const volatile LAS unsigned*)(lds + L_MISC + 16 + h * 8 + (lane & 1) * 4);
            for (;;) {
                STEP(18);
                if (__builtin_amdgcn_ballot_w64(*zd == 0u) == 0ull) break;
                if (++t0 > SPIN_L) break;
            }
            CFENCE();
            const LAS unsigned char* ptz = opq(lds + Z0 + (h * 64 + 8 * fq + (fr >> 2)) * ZST + (lane & 3) * 8);
            const unsigned zo = WS_Z2F + (unsigned)((pn * 64 + pm * 4 + h * 2) * 9) * 1024u + lane * 16;
            u32x4 zf[18];
#pragma unroll
            for (int i = 0; i < 18; ++i) {
                const LAS unsigned char* pt = ptz + (i / 9) * 32 * ZST + (i % 9) * 32;
                zf[i] = __builtin_bit_cast(u32x4, cat8(tr_read(pt), tr_read(pt + 4 * ZST)));
                __builtin_amdgcn_raw_buffer_store_b128(zf[i], ws, zo + i * 1024, 0, 16);
                if (i == 5 || i == 11) STEP(18);
            }
            STEP(18);
            flag_publish(ws, WS_FZ + (unsigned)(pn * 32 + pm * 2 + h) * 128u, T, lane);
            STAMP(3);
            STEPS_TO(18);
            LAS unsigned char* pr_ = opq(lds + h * STG + BOFF + lane * 16);
#pragma unroll
            for (int i = 0; i < 18; ++i) *(LAS u32x4*)(pr_ + i * 1024) = zf[i];
            WAITL0();
        }
    }

    const char* lin = (const char*)wsp_ + p * 1024 + lane * 16;
    auto issueLA = [&](unsigned off, int s) {
#pragma unroll
        for (int i = 0; i < 8; ++i) GLDS(lin + off + i * 2048, lds + s + (p + 2 * i) * 1024, 16); };
    auto issueLB = [&](unsigned off, int s) {
#pragma unroll
        for (int i = 0; i < 9; ++i) GLDS(lin + off + i * 2048, lds + s + BOFF + (p + 2 * i) * 1024, 16); };
    auto ord3 = [&](int s) { return (GM_LAG == 0 || s >= 16) ? s : (((s & 3) == phi) ? s : ((s - GM_LAG) & 15)); };
    u32x2 pv = {0u, 0u}; unsigned rdy = 0; bool pend = false;
    const char* fp = (const char*)wsp_ + WS_FZ + ((unsigned)pn * 32u + (lane & 31)) * 128u;
    auto poll_issue = [&]() { asm volatile("global_load_dwordx2 %0, %1, off sc1" : "+v"(pv) : "v"(fp) : "memory"); };
    auto poll_take = [&]() { asm volatile("" : "+v"(pv)); rdy |= (unsigned)__builtin_amdgcn_ballot_w64(pv[0] == T[0] && pv[1] == T[1]); };
    auto need = [&](unsigned bits) {
        if (pend) { poll_take(); pend = false; }
        if ((rdy & bits) != bits) {
            int t0 = 0;
            do { poll_issue(); WAITV(0); poll_take(); } while ((rdy & bits) != bits && ++t0 < SPIN_G);
        }
        if (rdy != ~0u) { poll_issue(); pend = true; }
    };
    const unsigned hb = WS_HF + (unsigned)qm * (18u * 16384u), ob = WS_WOF + (unsigned)qn * (18u * 18432u);
    if (is_prod) {
        mk_tag();
        const unsigned zb = WS_Z2F + (unsigned)pn * (64u * 9216u);
        int so = 2 * STG;
        const int r0 = (ord2(0) >> 1) == pm ? ord2(1) : ord2(0), r1 = (ord2(1) >> 1) == pm ? ord2(0) : ord2(1);
        { int k2 = ord2(2), k3 = ord2(3); if ((k2 >> 1) == pm) k2 = (k2 & 1) ? r1 : r0; if ((k3 >> 1) == pm) k3 = (k3 & 1) ? r1 : r0;
          const unsigned b_ = (1u << k2) | (1u << k3);
#pragma nounroll
          for (int i = 0; i < GM_PREPOLL && (rdy & b_) != b_; ++i) { poll_issue(); WAITV(0); poll_take(); }
          WAITV(0); }
        STAMP(2);
#pragma nounroll
        for (int S = 18; S < 48; ++S) {
            PBAR();
            int k = ord2(S - 16); if ((k >> 1) == pm) k = (k & 1) ? r1 : r0;
            need(1u << k); issueS(k, so); issueLB(zb + (unsigned)k * 18432u, so); so = nextbuf(so);
            WAITV(17);
        }
        PBAR();
        fp = (const char*)wsp_ + WS_FZ + ((unsigned)(pn ^ 8) * 32u + (lane & 31)) * 128u; WAITV(0); rdy = 0; pend = false;
        need(~0u);
        issueLB(ob + (unsigned)ord3(0) * 18432u, 2 * STG);
        PBAR();
        issueLB(ob + (unsigned)ord3(1) * 18432u, 0);
        STAMP(3);
    } else
    if (wid < 4) {
        mk_tag();
        if (wid >> 1) consumer_b<4>(lds, ws, T, zp, bs, pm, pn, wid & 1, 1, lane, wsp_ TRA);
        else consumer_b<5>(lds, ws, T, zp, bs, pm, pn, wid & 1, 0, lane, wsp_ TRA);
    } else {
        __builtin_amdgcn_s_setprio(GM_HPRIO);
        {   const unsigned fo = WS_FZ + (unsigned)(pn * 32 + (lane & 31)) * 128u;
            int t0 = 0;
            for (;;) {
                STEP(43);
                CFENCE();
                const u32x2 v = flag_ld(ws, fo);
                STEP(43);
                if (flag_all(v, T, true)) break;
                if (++t0 > SPIN_G) break;
            } }
        u32x4 uu[3][2];
#pragma unroll
        for (int c = 0; c < 3; ++c)
#pragma unroll
            for (int j = 0; j < 2; ++j) uu[c][j] = __builtin_amdgcn_raw_buffer_load_b128(ws, WS_UT + (unsigned)(ch0 + (c < nch ? 16 * c : 0)) * 4096u + h * 2048 + j * 1024 + lane * 16, 0, 16);
#pragma unroll
        for (int c = 0; c < 3; ++c) if (lane < 16) *(LAS u32x4*)(lds + L_UT + c * UT_SZ + h * 4352 + lane * 16) = (u32x4){0u, 0u, 0u, 0u};
        BAR(); BAR(); BAR(); nb += 3;
        LAS unsigned char* pu_ = opq(lds + L_UT + 256 + h * 2048 + lane * 16);
#pragma unroll
        for (int c = 0; c < 3; ++c)
#pragma unroll
            for (int j = 0; j < 2; ++j) *(LAS u32x4*)(pu_ + c * UT_SZ + j * 1024) = uu[c][j];
        CFENCE();
        if (lane == 0) *(volatile LAS unsigned*)(lds + L_MISC + 32 + h * 4) = 1u;
        STEP(50);
        {   const volatile LAS unsigned* uw = (const volatile LAS unsigned*)(lds + L_MISC + 32 + (lane & 1) * 4);
            int t0 = 0;
            while (__builtin_amdgcn_ballot_w64(*uw == 0u) != 0ull && ++t0 < SPIN_L) STEP(50);
            CFENCE(); }
        STAMP(4);
    }

    if (wid < 6) nb = 50;
    while (!is_prod || (GM_PCONV && nch == 3)) {
        if (nb > 45) STEPS_TO(50);
        STEP(50);
        __builtin_amdgcn_wave_barrier();
        unsigned tk = 0;
        if (lane == 0) tk = __hip_atomic_fetch_add((LAS unsigned*)(lds + L_MISC), 1u, __ATOMIC_RELAXED, __HIP_MEMORY_SCOPE_WORKGROUP);
        __builtin_amdgcn_wave_barrier();
        tk = __builtin_amdgcn_readfirstlane(tk);
        if (tk >= (unsigned)(4 * nch)) break;
        const int c = tk >> 2, tl = tk & 1;
        if (tk & 2) conv_task<0, 10>(lds, L_KT + c * KT_SZ, L_UT + c * UT_SZ, ytab(c), tl, lane, nb TRA);
        else conv_task<11, 15>(lds, L_KT + c * KT_SZ, L_UT + c * UT_SZ, ytab(c), tl, lane, nb TRA);
    }
    STEPS_TO(50);
    WAITL0();
    STAMP(7);
    if (is_prod) {
        fp = (const char*)wsp_ + WS_FH + ((unsigned)qm * 32u + (lane & 31)) * 128u; rdy = 0;
        need(~0u);
        STAMP(4);
        issueLA(hb + (unsigned)ord3(0) * 16384u, 2 * STG); issueLA(hb + (unsigned)ord3(1) * 16384u, 0);
        int so = STG;
        WAITV(8);
#pragma nounroll
        for (int S = 50; S < 66; ++S) {
            PBAR();
            const int k = ord3(S - 48); issueLA(hb + (unsigned)k * 16384u, so); issueLB(ob + (unsigned)k * 18432u, so); so = nextbuf(so);
            WAITV(17);
        }
        PBAR();
        WAITV(0); PBAR();
        STAMP(5); PDUMP();
        if (bid == 0 && p == 0 && lane == 0) __builtin_amdgcn_raw_buffer_store_b64(T, ws, WS_E, 0, 16);
        return;
    }

    if (wid < 4) {
        f32x4 acc[5];
        struct F3 { s16x4 alo[4], ahi[4]; bf16x8 b[20]; } f0, f1;
        const LAS unsigned char* ph = lds + 2 * STG + wid * 4096 + lane * 16;
        const LAS unsigned char* pw = lds + 2 * STG + BOFF + lane * 16;
        const LAS unsigned char* pw4 = lds + 2 * STG + BOFF + 4096 + (fq * 8 + (fr & 7)) * 16;
        auto rd3 = [&](F3& f, int i, bool single) {
            const int j = i / 6, r = i % 6;
            if (r == 0) {
                if (!single) { const bf16x8 v = *(const LAS bf16x8*)(ph + j * 1024); f.alo[j] = __builtin_shufflevector(v, v, 0, 1, 2, 3); f.ahi[j] = __builtin_shufflevector(v, v, 4, 5, 6, 7); }
                else { f.alo[j] = *(const LAS s16x4*)(ph + j * 1024); f.ahi[j] = *(const LAS s16x4*)(ph + j * 1024 + 512); }
            } else if (r < 5) f.b[j * 5 + r - 1] = *(const LAS bf16x8*)(pw + j * 4608 + (r - 1) * 1024);
            else f.b[j * 5 + 4] = *(const LAS bf16x8*)(pw4 + j * 4608);
        };
        int so = 2 * STG;
        auto step3 = [&](const F3& c, F3& l, int mode  , bool first) {
            const int delta = so == 2 * STG ? -2 * STG : STG; so += delta;
#pragma unroll
            for (int i = 0; i < 20; ++i) {
                const int j = i / 5, n = i % 5;
                acc[n] = MFMA16(c.b[i], cat8(c.alo[j], c.ahi[j]), (first && j == 0) ? ((f32x4){0.f, 0.f, 0.f, 0.f}) : acc[n]);
                if (mode < 2 && i < 12) { rd3(l, 2 * i, mode == 1); rd3(l, 2 * i + 1, mode == 1); }
                if (mode < 2) { if (i == 13) { ph += delta; asm volatile("" : "+v"(ph)); } if (i == 14) { pw += delta; asm volatile("" : "+v"(pw)); } if (i == 15) { pw4 += delta; asm volatile("" : "+v"(pw4)); } }
                SB0();
            }
        };
        __builtin_amdgcn_s_waitcnt(0xC07F);
        BAR();
#pragma unroll
        for (int i = 0; i < 24; ++i) rd3(f0, i, false);
        ph -= 2 * STG; pw -= 2 * STG; pw4 -= 2 * STG; so = 0;
        WAITL0(); BAR(); step3(f0, f1, 0, true);
#pragma nounroll
        for (int j = 0; j < 7; ++j) {
            WAITL0(); BAR(); step3(f1, f0, 0, false);
            WAITL0(); BAR(); step3(f0, f1, 0, false);
        }
        ph = opq(ph - lane * 8);
        WAITL0(); BAR(); step3(f1, f0, 1, false);
        WAITL0(); BAR();
        const float g = *(const LAS float*)(lds + L_MISC + 64 + (16 * wid + fr) * 4);
        const LAS unsigned char* pat = opq(lds + L_AT + (4 * fq + (fr >> 2)) * 128 + (16 * wid + 4 * (lane & 3)) * 2);
        u32x2 av[5];
#pragma unroll
        for (int n = 0; n < 5; ++n) av[n] = __builtin_bit_cast(u32x2, tr_read(pat + n * 2048));
        step3(f0, f1, 1, false);
        step3(f1, f0, 2, false);
        STAMP(8);
        const rsrc_t ors = __builtin_amdgcn_make_buffer_rsrc(out, 0, SEQ * DM * 2, 0x00020000);
        const float g1 = rbf(1.0f - g);
#pragma unroll
        for (int n = 0; n < 5; ++n) {
            const f32x4 b = acc[n]; const u32x2 a = av[n];
            const u32x2 o = {pk_bf16(g * bflo(a[0]) + g1 * b[0], g * bfhi(a[0]) + g1 * b[1]), pk_bf16(g * bflo(a[1]) + g1 * b[2], g * bfhi(a[1]) + g1 * b[3])};
            if (n < 4 || fq < 2) __builtin_amdgcn_raw_buffer_store_b64(o, ors, (unsigned)((qm * 64 + 16 * wid + fr) * 1152 + (qn * 72 + 16 * n + 4 * fq) * 2), 0, GM_OAUX);
        }
    } else {
        int d = pn * 36 + pm;
#pragma nounroll
        for (int c = 0; c < nch; ++c) {
            const float dsk = bf2f(Dsk[d]);
#pragma unroll
            for (int k = 0; k < 2; ++k) {
                BAR(); ++nb;
                const int l0 = h * 1024 + k * 512 + lane * 8;
                f32x4 cv = *(const LAS f32x4*)(lds + ytab(c) + 8192 + (l0 >= 16 ? (l0 >> 4) - 1 : 0) * 16);
                if ((lane & 1) || l0 == 0) cv = (f32x4){0.f, 0.f, 0.f, 0.f};
                const f32x4 y0 = *(const LAS f32x4*)(lds + ytab(c) + l0 * 4) + cv, y1 = *(const LAS f32x4*)(lds + ytab(c) + l0 * 4 + 16);
                const u32x4 uv = *(const LAS u32x4*)(lds + L_UT + c * UT_SZ + 256 + l0 * 2);
                u32x4 o;
#pragma unroll
                for (int j = 0; j < 4; ++j) {
                    const float ya = j < 2 ? y0[2 * j] : y1[2 * j - 4], yb = j < 2 ? y0[2 * j + 1] : y1[2 * j - 3];
                    const unsigned yp = pk_bf16(ya, yb), up = pk_bf16(bflo(uv[j]) * dsk, bfhi(uv[j]) * dsk);
                    o[j] = pk_bf16(bflo(yp) + bflo(up), bfhi(yp) + bfhi(up));
                }
                __builtin_amdgcn_raw_buffer_store_b128(o, ws, WS_AT + (unsigned)d * 4096u + l0 * 2, 0, 16);
            }
            d += 16;
        }
        STEPS_TO(58);
        flag_publish(ws, WS_FA + (unsigned)((pn >> 1) * 64 + (pn & 1) * 32 + pm * 2 + h) * 128u, T, lane);
        STAMP(5);
        STEPS_TO(61);
        if (h == 0) {
            const unsigned fo = WS_FA + (unsigned)(qn * 64 + lane) * 128u;
            int t0 = 0;
            for (;;) {
                CFENCE();
                const u32x2 v = flag_ld(ws, fo);
                STEP(67); STEP(67);
                if (flag_all(v, T, true)) break;
                if (++t0 > SPIN_G) break;
            }
            ACQ();
            u32x4 av[9];
#pragma unroll
            for (int i = 0; i < 9; ++i) av[i] = __builtin_amdgcn_raw_buffer_load_b128(ws, WS_AT + (unsigned)(qn * 72 + i * 8 + (lane >> 3)) * 4096u + qm * 128 + (lane & 7) * 16, 0, 16);
            STEP(67); STEP(67);
            LAS unsigned char* pa_ = opq(lds + L_AT + lane * 16);
#pragma unroll
            for (int i = 0; i < 9; ++i) *(LAS u32x4*)(pa_ + i * 1024) = av[i];
        } else {
            STEPS_TO(64);
            const unsigned gv = __builtin_amdgcn_raw_buffer_load_b32(ws, WS_GATE + (unsigned)(qm * 64 + lane) * 4u, 0, 16);
            BAR(); BAR(); nb += 2;
            *(LAS unsigned*)(lds + L_MISC + 64 + lane * 4) = gv;
        }
        STEPS_TO(67);
        WAITL0();
        BAR();
    }
    STAMP(9); TRDUMP();
}
}

extern "C" void kernel_launch(void* const* d_in, const int* in_sizes, int n_in, void* d_out, int out_size, void* d_ws, size_t ws_size, hipStream_t stream) {
    (void)in_sizes; (void)n_in; (void)out_size; (void)ws_size;
    (void)hipFuncSetAttribute((const void*)gm::gm_kernel, hipFuncAttributeMaxDynamicSharedMemorySize, gm::LDS_BYTES);
    hipLaunchKernelGGL(gm::gm_kernel, dim3(256), dim3(512), gm::LDS_BYTES, stream, (const u16*)d_in[0], (const u16*)d_in[1], (const u16*)d_in[2], (const u16*)d_in[3],
                       (const u16*)d_in[4], (const u16*)d_in[5], (const u16*)d_in[6], (const u16*)d_in[8], (const u16*)d_in[9], (const u16*)d_in[10],
                       (const u16*)d_in[11], (const u16*)d_in[12], (u16*)d_out, (unsigned char*)d_ws);
}
```

```cpp
#include <hip/hip_runtime.h>
#include <stdint.h>

#define LAS __attribute__((address_space(3)))
typedef unsigned short u16;
typedef unsigned long long u64;
typedef short bf16x8 __attribute__((ext_vector_type(8)));
typedef short s16x4 __attribute__((ext_vector_type(4)));
typedef _Float16 f16x8 __attribute__((ext_vector_type(8)));
typedef _Float16 f16x2 __attribute__((ext_vector_type(2)));
typedef __bf16 bf16x2 __attribute__((ext_vector_type(2)));
typedef float f32x4 __attribute__((ext_vector_type(4)));
typedef float f32x2 __attribute__((ext_vector_type(2)));
typedef unsigned u32x4 __attribute__((ext_vector_type(4)));
typedef unsigned u32x2 __attribute__((ext_vector_type(2)));
typedef __amdgpu_buffer_rsrc_t rsrc_t;

namespace gm {
constexpr int SEQ = 2048, DM = 576, FH = 2304;
constexpr unsigned WS_E = 0, WS_FZ = 4096, WS_FH = WS_FZ + 65536, WS_FA = WS_FH + 131072;
constexpr unsigned WS_ST = 0x60000, WS_GATE = 0xE0000, WS_Z2F = 0x100000, WS_HF = 0xA00000, WS_WOF = 0x1300000, WS_UT = 0x1600000, WS_AT = 0x1900000, WS_END = 0x1C00000;
constexpr int STG = 34816, BOFF = 16384, Z0 = 3 * STG, ZST = 296, T0 = Z0 + 128 * ZST;
constexpr int KT_SZ = 4352, UT_SZ = 4608, Y_SZ = 10240;
constexpr int L_MISC = 163840 - 320;
constexpr int L_KT = L_MISC - 3 * KT_SZ, L_SP = T0, L_KD = Z0 + 3 * 8448, L_UT = Z0 + 3 * Y_SZ;
constexpr int L_AT = Z0;
constexpr int SPIN_G = 8192, SPIN_L = 1 << 20;
constexpr int LDS_BYTES = 163840;
static_assert(L_UT + 3 * UT_SZ <= L_KT && L_KD + 3 * 3072 <= T0 && L_SP + 2048 <= L_KT, "lds map");
constexpr int CODE_LINES = 13, CODE_BYTES = 50176;
constexpr int CONV_SLICE = 11;

__device__ __forceinline__ float bf2f(unsigned v) { return __uint_as_float(v << 16); }
__device__ __forceinline__ float bflo(unsigned w) { return __uint_as_float(w << 16); }
__device__ __forceinline__ float bfhi(unsigned w) { return __uint_as_float(w & 0xffff0000u); }
__device__ __forceinline__ unsigned pk_bf16(float a, float b) { f32x2 v = {a, b}; return __builtin_bit_cast(unsigned, __builtin_convertvector(v, bf16x2)); }
__device__ __forceinline__ unsigned pk_f16(float a, float b) { f32x2 v = {a, b}; return __builtin_bit_cast(unsigned, __builtin_convertvector(v, f16x2)); }
__device__ __forceinline__ float rbf(float a) { return bflo(pk_bf16(a, a)); }
__device__ __forceinline__ f32x2 gelu2(f32x2 x) {
    const f32x2 w = (x * x) * (-0.10294324f) + (-2.30220819f);
    const f32x2 a = w * x;
    f32x2 e; e.x = __builtin_amdgcn_exp2f(a.x); e.y = __builtin_amdgcn_exp2f(a.y);
    e = e + 1.0f;
    f32x2 r; r.x = __builtin_amdgcn_rcpf(e.x); r.y = __builtin_amdgcn_rcpf(e.y);
    return x * r;
}
__device__ __forceinline__ f32x4 gelu4(f32x4 v) { const f32x2 a = gelu2((f32x2){v[0], v[1]}), b = gelu2((f32x2){v[2], v[3]}); return (f32x4){a.x, a.y, b.x, b.y}; }
__device__ __forceinline__ float allred_q(float v) {
    unsigned u = __float_as_uint(v);
    auto r = __builtin_amdgcn_permlane16_swap(u, u, false, false);
    v = __uint_as_float(r[0]) + __uint_as_float(r[1]);
    u = __float_as_uint(v);
    auto s = __builtin_amdgcn_permlane32_swap(u, u, false, false);
    return __uint_as_float(s[0]) + __uint_as_float(s[1]);
}
#define BAR() do { asm volatile("" ::: "memory"); __builtin_amdgcn_s_barrier(); asm volatile("" ::: "memory"); } while (0)
#define WAITV(n) asm volatile("s_waitcnt vmcnt(" #n ")" ::: "memory")
#define WAITL0() asm volatile("s_waitcnt lgkmcnt(0)" ::: "memory")
#define CFENCE() asm volatile("" ::: "memory")
#define MFMA16(a, b, c) __builtin_amdgcn_mfma_f32_16x16x32_bf16(a, b, c, 0, 0, 0)
__device__ __forceinline__ s16x4 tr_read(const LAS unsigned char* p) { return __builtin_amdgcn_ds_read_tr16_b64_v4i16((LAS s16x4*)p); }
__device__ __forceinline__ bf16x8 cat8(s16x4 a, s16x4 b) { return __builtin_shufflevector(a, b, 0, 1, 2, 3, 4, 5, 6, 7); }
template <class P> __device__ __forceinline__ P opq(P p) { asm volatile("" : "+v"(p)); return p; }
__device__ __forceinline__ int nextbuf(int so) { return so == 2 * STG ? 0 : so + STG; }

__device__ __forceinline__ u32x2 flag_ld(rsrc_t ws, unsigned off) { return __builtin_amdgcn_raw_buffer_load_b64(ws, off, 0, 16); }
__device__ __forceinline__ bool flag_all(u32x2 v, u32x2 T, bool active) { return __builtin_amdgcn_ballot_w64(active && (v[0] != T[0] || v[1] != T[1])) == 0ull; }
__device__ __forceinline__ void flag_publish(rsrc_t ws, unsigned off, u32x2 T, int lane) {
    WAITV(0);
    if (lane == 0) __builtin_amdgcn_raw_buffer_store_b64(T, ws, off, 0, 16);
}
#define SB0() __builtin_amdgcn_sched_barrier(0)
template <int NT> struct Frag { bf16x8 a[4]; s16x4 lo[NT], hi[NT]; };
template <int NT, bool TR, int HALF>
__device__ __forceinline__ void frag_read(Frag<NT>& f, int i, const LAS unsigned char* pa, const LAS unsigned char* pb) {
    { constexpr int B0 = TR ? 2 : 1; i = i == 0 ? 0 : i <= B0 ? i + 3 : i <= B0 + 3 ? i - B0 : i; }
    if (i < 4) f.a[i] = *(const LAS bf16x8*)(pa + i * 2048);
    else if (TR) { const int n = (i - 4) >> 1; if ((i - 4) & 1) f.hi[n] = tr_read(pb + HALF * 9216 + 4608 + n * 32); else f.lo[n] = tr_read(pb + HALF * 9216 + n * 32); }
    else { const int n = i - 4; const bf16x8 v = *(const LAS bf16x8*)(pb + HALF * 9216 + n * 1024);
           f.lo[n] = __builtin_shufflevector(v, v, 0, 1, 2, 3); f.hi[n] = __builtin_shufflevector(v, v, 4, 5, 6, 7); }
}
template <int NT, bool TR, int LHALF, int S0, int BAT, bool FIRST, bool UPD>
__device__ __forceinline__ void half_step(f32x4 (&acc)[NT][4], const Frag<NT>& c, Frag<NT>& l, const LAS unsigned char*& pa0, const LAS unsigned char*& pa1, const LAS unsigned char*& pb, int delta,
                                          const float* ini, const volatile LAS unsigned* rdy, unsigned& rv) {
    constexpr int NRD = TR ? 4 + 2 * NT : 4 + NT, NM = 4 * NT, NS = NM - S0;
#pragma unroll
    for (int i = 0; i < NM; ++i) {
        const int n = i >> 2, m = i & 3;
        acc[n][m] = MFMA16(cat8(c.lo[n], c.hi[n]), c.a[m], FIRST ? (TR ? ((f32x4){0.f, 0.f, 0.f, 0.f}) : ((f32x4){ini[m], ini[m], ini[m], ini[m]})) : acc[n][m]);
        SB0();
        if (i == BAT) { WAITL0(); BAR(); if (TR) rv = *rdy; }
        if (LHALF >= 0 && i >= S0) {
#pragma unroll
            for (int r = 0; r < NRD; ++r) if ((r * NS) / NRD == i - S0) frag_read<NT, TR, (LHALF > 0 ? 1 : 0)>(l, r, LHALF > 0 ? pa1 : pa0, pb);
        }
        if (UPD) { if (i == NM - 3) { pa0 += delta; asm volatile("" : "+v"(pa0)); } if (i == NM - 2) { pa1 += delta; asm volatile("" : "+v"(pa1)); } if (i == NM - 1) { pb += delta; asm volatile("" : "+v"(pb)); } }
        SB0();
    }
}
template <int NT, bool TR, class HK>
__device__ __forceinline__ void gemm_phase(int nsteps, f32x4 (&acc)[NT][4], int& so, const LAS unsigned char* pa0, const LAS unsigned char* pa1, const LAS unsigned char* pb,
                                           const float* ini, const volatile LAS unsigned* rdy, HK hook) {
    constexpr int NRD = TR ? 4 + 2 * NT : 4 + NT;
    Frag<NT> f0, f1;
    unsigned rv = 0;
    pa0 += so; pa1 += so; pb += so;
    asm volatile("" : "+v"(pa0), "+v"(pa1), "+v"(pb));
    __builtin_amdgcn_s_waitcnt(0xC07F);
    BAR();
#pragma unroll
    for (int i = 0; i < NRD; ++i) frag_read<NT, TR, 0>(f0, i, pa0, pb);
    SB0();
    int delta = so == 2 * STG ? -2 * STG : STG; so += delta;
    half_step<NT, TR, 1, 2, -1, true, true>(acc, f0, f1, pa0, pa1, pb, delta, ini, rdy, rv);
#pragma nounroll
    for (int s = 1; s < nsteps; ++s) {
        delta = so == 2 * STG ? -2 * STG : STG; so += delta;
        half_step<NT, TR, 0, 6, 5, false, false>(acc, f1, f0, pa0, pa1, pb, 0, ini, rdy, rv);
        half_step<NT, TR, 1, 2, -1, false, true>(acc, f0, f1, pa0, pa1, pb, delta, ini, rdy, rv);
        if (TR) hook(rv);
    }
    if (TR) half_step<NT, TR, -1, 0, -1, false, false>(acc, f1, f0, pa0, pa1, pb, 0, ini, rdy, rv);
    else {
#pragma unroll
        for (int m = 0; m <= 4; ++m) {
            if (m < 4) {
#pragma unroll
                for (int n = 0; n < NT; ++n) acc[n][m] = MFMA16(cat8(f1.lo[n], f1.hi[n]), f1.a[m], acc[n][m]);
            }
            SB0();
            if (m > 0) hook(m - 1);
            SB0();
        }
    }
}

template <int NT, class LT>
__device__ __forceinline__ void consumer_a(LAS unsigned char* lds, u32x2 (&zp)[5][4], LT late, int wm, int wn, int lane) {
    const int fr = lane & 15, fq = lane >> 4;
    constexpr int N0 = (NT == 5) ? 0 : 5;
    f32x4 acc[NT][4];
    int so = 0;
    const int sw = (fr >> 1) & 7;
    const int a0 = (wm * 64 + fr) * 128 + ((fq ^ sw) << 4), a1 = (wm * 64 + fr) * 128 + (((4 + fq) ^ sw) << 4);
    const int bw = BOFF + (4 * fq + (fr >> 2)) * 288 + (lane & 3) * 8 + N0 * 32;
    const volatile LAS unsigned* rdy = (const volatile LAS unsigned*)(lds + L_MISC + 4 + wm * 4);
    bool done = false;
    auto normalise = [&]() {
        const LAS unsigned char* ps = opq(lds + L_SP + (wm * 64 + fr) * 16);
        LAS unsigned char* pz = opq(lds + Z0 + (wm * 64 + fr) * ZST + N0 * 32 + fq * 8);
#pragma unroll
        for (int m = 0; m < 4; ++m) {
            const f32x2 sr = *(const LAS f32x2*)(ps + m * 256);
#pragma unroll
            for (int n = 0; n < NT; ++n) {
                const f32x2 u = (f32x2){bflo(zp[n][m][0]), bfhi(zp[n][m][0])} * sr.x + sr.y, v = (f32x2){bflo(zp[n][m][1]), bfhi(zp[n][m][1])} * sr.x + sr.y;
                *(LAS u32x2*)(pz + m * 16 * ZST + n * 32) = (u32x2){pk_bf16(u.x, u.y), pk_bf16(v.x, v.y)};
            }
        }
        CFENCE();
        if (lane == 0) *(volatile LAS unsigned*)(lds + L_MISC + 16 + (wm * 2 + wn) * 4) = 1u;
        done = true;
    };
    auto hook = [&](unsigned rv) { if (__builtin_amdgcn_readfirstlane(rv) != 0u && !done) normalise(); };
#pragma unroll
    for (int n = 0; n < NT; ++n)
#pragma unroll
        for (int m = 0; m < 4; ++m) zp[n][m] = (u32x2){0u, 0u};
#pragma nounroll
    for (int it = 0; it < 2; ++it) {
        gemm_phase<NT, true>(9, acc, so, lds + a0, lds + a1, lds + bw, nullptr, rdy, hook);
        if (it == 1) late();
        if (it == 1 && !done) {
            int t0 = 0;
            while (__builtin_amdgcn_readfirstlane(*rdy) == 0u && ++t0 < SPIN_L) __builtin_amdgcn_s_sleep(1);
            normalise();
        }
#pragma unroll
        for (int m = 0; m < 4; ++m)
#pragma unroll
            for (int n = 0; n < NT; ++n) { acc[n][m] = gelu4(acc[n][m]); zp[n][m] = (u32x2){pk_bf16(acc[n][m][0], acc[n][m][1]), pk_bf16(acc[n][m][2], acc[n][m][3])}; }
        if (it == 0) {
            LAS unsigned char* ps = opq(lds + L_SP + (wm * 64 + fr) * 16 + wn * 8);
#pragma unroll
            for (int m = 0; m < 4; ++m) {
                f32x4 s4 = acc[0][m];
#pragma unroll
                for (int n = 1; n < NT; ++n) s4 += acc[n][m];
                const float mean = opq(allred_q((s4[0] + s4[1]) + (s4[2] + s4[3])) * (1.0f / (16 * NT)));
                f32x4 q4 = {0.f, 0.f, 0.f, 0.f};
#pragma unroll
                for (int n = 0; n < NT; ++n) { const f32x4 d = acc[n][m] - mean; q4 += d * d; }
                const float m2 = allred_q((q4[0] + q4[1]) + (q4[2] + q4[3]));
                if (fq == 0) *(LAS f32x2*)(ps + m * 256) = (f32x2){mean, m2};
            }
        }
    }
}

template <int NT>
__device__ __forceinline__ void consumer_b(LAS unsigned char* lds, rsrc_t ws, u32x2 T, const u32x2 (&z1)[5][4], const float (&bs_)[4], int pm, int pn, int wm, int wn, int lane) {
    float bs[4] = {bf2f(__float_as_uint(opq(bs_[0]))), bf2f(__float_as_uint(opq(bs_[1]))), bf2f(__float_as_uint(opq(bs_[2]))), bf2f(__float_as_uint(opq(bs_[3])))};
    const int fr = lane & 15, fq = lane >> 4;
    constexpr int N0 = (NT == 5) ? 0 : 5;
    f32x4 acc[NT][4];
    int so = 0;
    const int sw = (fr >> 1) & 7;
    const int a0 = (wm * 64 + fr) * 128 + ((fq ^ sw) << 4), a1 = (wm * 64 + fr) * 128 + (((4 + fq) ^ sw) << 4);
    const int bz = BOFF + N0 * 1024 + lane * 16;
    const int qm = 2 * pm + wm;
    const unsigned hb = WS_HF + (unsigned)qm * (18u * 16384u);
    auto epi = [&](int m) {
        if (m == 0) __builtin_amdgcn_s_setprio(3);
        u32x2 hp[NT];
#pragma unroll
        for (int n = 0; n < NT; ++n) {
            const f32x2 u = (f32x2){bflo(z1[n][m][0]), bfhi(z1[n][m][0])} * (f32x2){acc[n][m][0], acc[n][m][1]}, v = (f32x2){bflo(z1[n][m][1]), bfhi(z1[n][m][1])} * (f32x2){acc[n][m][2], acc[n][m][3]};
            hp[n] = (u32x2){pk_bf16(u.x, u.y), pk_bf16(v.x, v.y)};
        }
#pragma unroll
        for (int j = 0; j < 2; ++j)
            __builtin_amdgcn_raw_buffer_store_b128((u32x4){hp[2 * j][0], hp[2 * j][1], hp[2 * j + 1][0], hp[2 * j + 1][1]}, ws,
                                                   hb + (unsigned)pn * 16384u + (unsigned)(m * 4 + wn * 2 + j) * 1024u + lane * 16, 0, 16);
        if (NT == 5) __builtin_amdgcn_raw_buffer_store_b64(hp[NT - 1], ws, hb + (unsigned)(16 + (pn >> 3)) * 16384u + m * 4096u + (unsigned)(pn & 7) * 512u + lane * 8, 0, 16);
    };
    gemm_phase<NT, false>(32, acc, so, lds + a0, lds + a1, lds + bz, bs, nullptr, epi);
    flag_publish(ws, WS_FH + (unsigned)(qm * 32 + pn * 2 + wn) * 128u, T, lane);
    __builtin_amdgcn_s_setprio(0);
}

template <int TLO, int THI>
__device__ __forceinline__ void conv_task(LAS unsigned char* lds, int kt, int ut, int yt, int tl, int lane, int& nb) {
#define CSL() do { if (nb < 50) { BAR(); ++nb; } } while (0)
    constexpr int NTL = THI - TLO + 1, ND = THI + 2;
    const int i = lane & 15, g = lane >> 4;
    const LAS unsigned char* pk = opq(lds + kt + (i & 3) * 1088 + ((i >> 2) + g) * 16);
    const LAS unsigned char* pu = opq(lds + ut + (128 + 16 * tl - 32 * g - 4 * (i >> 2) + 4 * (i & 3)) * 2 - 32);
    bf16x8 ub[ND], kf[ND];
#pragma unroll
    for (int d = 0; d < ND; ++d) { if (d % 6 == 0) CSL(); ub[ND - 1 - d] = cat8(tr_read(pu + 256 * (ND - 1 - d) + 32), tr_read(pu + 256 * (ND - 1 - d))); kf[d] = *(const LAS bf16x8*)(pk + 64 * d); }
    f32x4 acc[NTL];
    int cnt = CONV_SLICE;
#pragma unroll
    for (int s = -1; s <= THI; ++s) {
#pragma unroll
        for (int t = TLO; t <= THI; ++t) if (t >= s) {
            if (cnt == CONV_SLICE) { CSL(); cnt = 0; }
            ++cnt;
            acc[t - TLO] = MFMA16(kf[s + 1], ub[t - s], s == -1 ? ((f32x4){0.f, 0.f, 0.f, 0.f}) : acc[t - TLO]);
        }
    }
    LAS float* y = opq((LAS float*)(lds + yt) + 32 * g + 16 * tl + i);
    LAS float* cy = opq((LAS float*)(lds + yt + 8192) + (2 * g + tl) * 4 + (i & 3));
#define DPPF(v, ctl) __int_as_float(__builtin_amdgcn_update_dpp(0, __float_as_int(v), ctl, 0xf, 0xf, true))
#pragma unroll
    for (int t = TLO; t <= THI; ++t) {
        if (((t - TLO) % 3) == 0) CSL();
        const f32x4 a = acc[t - TLO];
        y[128 * t] = (a[0] + DPPF(a[1], 0x111)) + (DPPF(a[2], 0x112) + DPPF(a[3], 0x113));
        const float cv = DPPF(a[1], 0x10f) + (DPPF(a[2], 0x10e) + DPPF(a[3], 0x10d));
        if (i < 4) cy[32 * t] = cv;
    }
#undef DPPF
#undef CSL
}
__device__ __forceinline__ int ytab(int c) { return Z0 + c * Y_SZ; }

__global__ __launch_bounds__(512, 2) void gm_kernel(const u16* __restrict__ x, const u16* __restrict__ logA, const u16* __restrict__ Aim, const u16* __restrict__ Cre,
                                                    const u16* __restrict__ Cim, const u16* __restrict__ logdt, const u16* __restrict__ Dsk, const u16* __restrict__ win,
                                                    const u16* __restrict__ wsp, const u16* __restrict__ bsp, const u16* __restrict__ wout, const u16* __restrict__ gw,
                                                    u16* __restrict__ out, unsigned char* wsp_) {
    extern __shared__ __attribute__((aligned(16))) unsigned char lds_[];
    LAS unsigned char* lds = (LAS unsigned char*)lds_;
    const int wid = __builtin_amdgcn_readfirstlane(threadIdx.x >> 6), lane = threadIdx.x & 63;
    const int bid = blockIdx.x, xg = bid & 7, rr = bid >> 3, bi = rr >> 2, bj = rr & 3;
    const int pm = (xg >> 2) * 8 + bi, pn = (xg & 3) * 4 + bj;
    const int qm = 2 * pm + (pn >> 3), qn = pn & 7;
    const int nch = pm < 4 ? 3 : 2;
    const rsrc_t ws = __builtin_amdgcn_make_buffer_rsrc(wsp_, 0, WS_END, 0x00020000);
    unsigned pfw = 0;
    if (wid == 6) {
        u64 pc0; asm volatile("s_getpc_b64 %0" : "=s"(pc0));
        const u64 q_ = (pc0 & ~127ull) + (unsigned)min((rr * CODE_LINES + (lane >> 1)) * 128 + (lane & 1) * 64, CODE_BYTES);
        if (lane < 2 * CODE_LINES) asm volatile("global_load_dword %0, %1, off" : "+v"(pfw) : "v"(q_) : "memory");
    }

    const int fr = lane & 15, fq = lane >> 4;
    const int h = wid - 6, p = wid - 4;
    const bool is_prod = (wid >> 1) == 2;
    const int ch0 = pn * 36 + pm;
    float bs[4];
    auto late = [&]() {
#pragma unroll
        for (int m = 0; m < 4; ++m) bs[m] = __uint_as_float((unsigned)bsp[pm * 128 + (wid & 1) * 64 + m * 16 + fr]);
    };
    const int phi = (-(bi + bj)) & 3;
    const unsigned rsw = (unsigned)(((lane & 7) ^ ((4 * p + (lane >> 4)) & 7)) * 16);
#define GLDS(src, dst, aux) __builtin_amdgcn_global_load_lds((const unsigned*)(src), (LAS unsigned*)(dst), 16, 0, aux)
    auto issueS = [&](int k, int s) {
        const char* sb = (const char*)wsp + (size_t)(pm * 128 + p * 8) * 4096 + k * 128; const unsigned vs = (unsigned)(lane >> 3) * 4096u + rsw;
#pragma unroll
        for (int i = 0; i < 8; ++i) GLDS(sb + (size_t)i * (16 * 4096) + vs, lds + s + (p + 2 * i) * 1024, 0); };
    auto ord2 = [&](int s) { return ((s & 3) == phi) ? s : ((s - 4) & 31); };
    u32x2 E, T;
    auto mk_tag = [&]() { u64 z = (((u64)__builtin_amdgcn_readfirstlane(E[1]) << 32) | __builtin_amdgcn_readfirstlane(E[0])) + 0x9E3779B97F4A7C15ull;
        z = (z ^ (z >> 30)) * 0xBF58476D1CE4E5B9ull; z = (z ^ (z >> 27)) * 0x94D049BB133111EBull; z ^= z >> 31; T = (u32x2){(unsigned)z, (unsigned)(z >> 32)}; };
    u32x2 zp[5][4];
#define STEP(lim) do { if (nb < (lim)) { BAR(); ++nb; } } while (0)
#define STEPS_TO(lim) _Pragma("nounroll") while (nb < (lim)) { BAR(); ++nb; }
    int nb = 0;
    const int kc = wid == 7 ? 2 : wid == 1 || wid == 2 ? wid - 1 : 3;
    if (kc < nch) {
        const int kch = ch0 + 16 * kc, ki = kch * 64 + lane;
        const unsigned pA = ((const unsigned*)logA)[ki], pI = ((const unsigned*)Aim)[ki], pR = ((const unsigned*)Cre)[ki], pC = ((const unsigned*)Cim)[ki], pT = logdt[kch];
        LAS u32x2* tw = opq((LAS u32x2*)(lds + Z0 + kc * 8448) + lane);
        const LAS unsigned char* trd = opq(lds + Z0 + kc * 8448 + fr * 528 + fq * 16);
        const float dt = rbf(__expf(bf2f(pT)));
        const f32x2 ar = (f32x2){rbf(__expf(bflo(pA))), rbf(__expf(bfhi(pA)))} * dt * -1.44269504f, ai = (f32x2){bflo(pI), bfhi(pI)} * dt * 0.15915494f;
        const f32x2 cr = (f32x2){bflo(pR), bfhi(pR)} * dt, ci = (f32x2){bflo(pC), bfhi(pC)} * dt;
        float cm = fmaxf(fmaxf(fabsf(cr.x), fabsf(ci.x)), fmaxf(fabsf(cr.y), fabsf(ci.y)));
#define DPPF(v, ctl) __int_as_float(__builtin_amdgcn_update_dpp(0, __float_as_int(v), ctl, 0xf, 0xf, true))
        cm = fmaxf(cm, DPPF(cm, 0x121)); cm = fmaxf(cm, DPPF(cm, 0x122)); cm = fmaxf(cm, DPPF(cm, 0x124)); cm = fmaxf(cm, DPPF(cm, 0x128));
#undef DPPF
        { auto r = __builtin_amdgcn_permlane16_swap(__float_as_uint(cm), __float_as_uint(cm), false, false); cm = fmaxf(__uint_as_float(r[0]), __uint_as_float(r[1]));
          auto q = __builtin_amdgcn_permlane32_swap(__float_as_uint(cm), __float_as_uint(cm), false, false); cm = fmaxf(fmaxf(__uint_as_float(q[0]), __uint_as_float(q[1])), 1e-30f); }
        struct C2 { f32x2 x, y; };
        auto cexp = [&](float p_) { const f32x2 e = ar * p_, t = ai * p_;
            const f32x2 mg = {__builtin_amdgcn_exp2f(e.x), __builtin_amdgcn_exp2f(e.y)}; const float t0 = __builtin_amdgcn_fractf(t.x), t1 = __builtin_amdgcn_fractf(t.y);
            return C2{mg * (f32x2){__builtin_amdgcn_cosf(t0), __builtin_amdgcn_cosf(t1)}, mg * (f32x2){__builtin_amdgcn_sinf(t0), __builtin_amdgcn_sinf(t1)}}; };
        auto cmul = [](C2 a, C2 b) { return C2{__builtin_elementwise_fma(a.x, b.x, -(a.y * b.y)), __builtin_elementwise_fma(a.x, b.y, a.y * b.x)}; };
        auto put = [&](int row, C2 v, bool conj) { tw[row * 66] = conj ? (u32x2){pk_f16(v.x.x, -v.y.x), pk_f16(v.x.y, -v.y.y)} : (u32x2){pk_f16(v.x.x, v.y.x), pk_f16(v.x.y, v.y.y)}; };
        C2 wp[16], zr[8], vp[4], cv[17];
        wp[1] = cexp(8.f); zr[1] = cexp(1.f); vp[0] = cexp(128.f);
        tw[0] = (u32x2){pk_f16(1.f, 0.f), pk_f16(1.f, 0.f)};
#pragma unroll
        for (int j = 1; j < 16; ++j) { if (j > 1) wp[j] = cmul(wp[j >> 1], wp[j - (j >> 1)]); put(j, wp[j], true); }
        CFENCE();
        u32x4 fb[8];
#pragma unroll
        for (int ks = 0; ks < 8; ++ks) fb[ks] = *(const LAS u32x4*)(trd + ks * 64);
        CFENCE();
#pragma unroll
        for (int r = 2; r < 8; ++r) zr[r] = cmul(zr[r >> 1], zr[r - (r >> 1)]);
#pragma unroll
        for (int k = 1; k < 4; ++k) vp[k] = cmul(vp[k - 1], vp[k - 1]);
        const float rc = __builtin_amdgcn_rcpf(cm);
        cv[0] = C2{cr * rc, ci * rc};
#pragma unroll
        for (int k = 1; k < 8; ++k) { const int lg = k >= 4 ? 2 : k >= 2 ? 1 : 0; cv[k] = cmul(cv[k - (1 << lg)], vp[lg]); }
        LAS float* kb_ = (LAS float*)(lds + L_KD + kc * 3072);
        const float sc = 2.0f * cm;
        u32x4 fa[3][8];
#pragma unroll
        for (int tt = 0; tt < 3; ++tt) {
#pragma unroll
            for (int i = 0; i < (tt == 2 ? 9 : 16); ++i) {
                const int r = tt == 0 ? (i & 7) : tt == 1 ? (i < 8 ? 2 * (i & 3) : 4 * (i & 1)) : 0;
                const int hh = tt == 0 ? (i >> 3) : tt == 1 ? (i < 8 ? 2 + (i >> 2) : 4 + ((i - 8) >> 1)) : 8 + i;
                if (tt == 2) cv[hh] = cmul(cv[hh - 8], vp[3]);
                put(i, r == 0 ? cv[hh] : cmul(cv[hh], zr[r]), false);
            }
            CFENCE();
#pragma unroll
            for (int ks = 0; ks < 8; ++ks) fa[tt][ks] = *(const LAS u32x4*)(trd + ks * 64);
            CFENCE();
            SB0();
        }
        f32x4 kd[3] = {{0.f, 0.f, 0.f, 0.f}, {0.f, 0.f, 0.f, 0.f}, {0.f, 0.f, 0.f, 0.f}};
#pragma unroll
        for (int ks = 0; ks < 8; ++ks)
#pragma unroll
            for (int tt = 0; tt < 3; ++tt) kd[tt] = __builtin_amdgcn_mfma_f32_16x16x32_f16(__builtin_bit_cast(f16x8, fa[tt][ks]), __builtin_bit_cast(f16x8, fb[ks]), kd[tt], 0, 0, 0);
#pragma unroll
        for (int tt = 0; tt < 3; ++tt) {
            LAS float* kw = opq(kb_ + (tt == 0 ? 128 * (fq >> 1) + 4 * (fq & 1) + 8 * fr : tt == 1 ? 256 + 64 * fq + (fq < 2 ? 4 : 2) * fr : 512 + 64 * fq + fr));
            LAS float* kwb = tt == 1 ? opq(kw + (fq < 2 ? 2 : 32)) : kw + (tt == 0 ? 2 : 32);
            kw[0] = kd[tt][0] * sc; kw[tt == 2 ? 16 : 1] = kd[tt][1] * sc; kwb[0] = kd[tt][2] * sc; kwb[tt == 2 ? 16 : 1] = kd[tt][3] * sc;
        }
        WAITL0();
    }
    if (wid == 6) { WAITV(0); asm volatile("" :: "v"(pfw)); }
    if (is_prod) {
        const unsigned vx = (unsigned)(lane >> 3) * 1152u + rsw;
        unsigned wv[9];
        const char* xb = (const char*)x + (size_t)(pm * 128 + p * 8) * 1152;
        const char* wb = (const char*)win + pn * 288;
        auto issueX = [&](int k, int s) {
#pragma unroll
            for (int i = 0; i < 8; ++i) GLDS(xb + (size_t)i * (16 * 1152) + k * 128 + vx, lds + s + (p + 2 * i) * 1024, 0); };
        auto issueW = [&](int k, int coloff, int s) {
            const char* b = wb + (size_t)k * (64 * 9216) + coloff;
#pragma unroll
            for (int i = 0; i < 9; ++i) GLDS(b + wv[i], lds + s + BOFF + (p + 2 * i) * 1024, 0); };
        auto ord1 = [&](int s) { return ((s & 3) == phi) ? s : (s >= 4 ? s - 4 : (s == 0 ? 8 : s + 4)); };
        issueX(ord1(0), 0);
        SB0();
#pragma unroll
        for (int i = 0; i < 9; ++i) { const int c = (p + 2 * i) * 64 + lane, rho = c / 18, ch = c - rho * 18;
            const int kap = (rho & 32) + 8 * ((rho >> 2) & 3) + 4 * ((rho >> 4) & 1) + (rho & 3); wv[i] = (unsigned)kap * 9216u + (unsigned)ch * 16u; }
        issueW(ord1(0), FH * 2, 0); issueX(ord1(1), STG); issueW(ord1(1), FH * 2, STG);
        issueX(ord1(2), 2 * STG); issueW(ord1(2), FH * 2, 2 * STG);
        E = flag_ld(ws, WS_E);
        unsigned wm_ = 0;
        auto touch = [&](const void* base, unsigned off, unsigned last) { const char* q_ = (const char*)base + min(off + lane * 64u, last); asm volatile("global_load_dword %0, %1, off" : "+v"(wm_) : "v"(q_) : "memory"); };
        {
#pragma unroll
            for (int i = 0; i < 2; ++i) touch(x, bid * 9216u + p * 4608u + i * 4096u, bid * 9216u + p * 4608u + 4544u);
#pragma unroll
            for (int i = 0; i < 3; ++i) touch(win, bid * 20736u + p * 10368u + i * 4096u, bid * 20736u + p * 10368u + 10304u);
        }
        int so = 0;
        WAITV(35); BAR();
#pragma nounroll
        for (int S = 1; S < 16; ++S) {
            if (S >= 8 && S <= 10) WAITV(9); else WAITV(17);
            BAR();
            const int t = S + 2, k = ord1(t < 9 ? t : t < 12 ? t - 3 : t - 12);
            if (t < 9 || t >= 12) issueX(k, so);
            issueW(k, t >= 9 ? 0 : FH * 2, so); so = nextbuf(so);
            if (S == 8) {
#pragma unroll
                for (int i = 0; i < 4; ++i) touch(wsp, bid * 32768u + p * 16384u + i * 4096u, 0x7fffffffu);
            }
        }
        WAITV(17); BAR(); issueS(2 * pm, 0);
        WAITV(8); BAR(); issueS(2 * pm + 1, STG);
        asm volatile("" :: "v"(wm_));
    } else if (wid < 4) {
        E = flag_ld(ws, WS_E);
        if (wid >> 1) consumer_a<4>(lds, zp, late, wid & 1, 1, lane);
        else consumer_a<5>(lds, zp, late, wid & 1, 0, lane);
    } else {
        if (h == 0 && lane < 12) *(LAS unsigned*)(lds + L_MISC + lane * 4) = 0u;
        const int row0 = lane / 9, chk0 = lane - row0 * 9;
        const int u = (pn >> 3) * 16 + pm, nblk = u < 8 ? 3 : 2;
        u32x2 uc[18]; u32x4 xv[9], gv[9], wv[3][5];
        if (h == 0) {
            { const char* xb = (const char*)x + (size_t)pm * 128 * 1152 + pn * 72; unsigned off = row0 * 1152 + chk0 * 8; int ch = chk0;
#pragma unroll
              for (int i = 0; i < 9; ++i) { uc[i] = *(const u32x2*)(xb + off); uc[9 + i] = *(const u32x2*)(xb + 64 * 1152 + off); off += 7 * 1152 + 8; if (++ch == 9) { ch = 0; off += 1152 - 72; } } }
            { const char* xr = (const char*)x + (size_t)(pm * 128 + pn * 8 + (lane >> 3)) * 1152 + (lane & 7) * 16; const char* gr = (const char*)gw + (lane & 7) * 16;
#pragma unroll
              for (int i = 0; i < 9; ++i) { xv[i] = *(const u32x4*)(xr + i * 128); gv[i] = *(const u32x4*)(gr + i * 128); } }
        } else {
            unsigned wo_[5], hi_[5];
            { int row = row0, ch = chk0;
#pragma unroll
              for (int i = 0; i < 5; ++i) { const int rw = row < 32 ? row : 31; wo_[i] = (rw & 15) * 1152 + ch * 16; hi_[i] = rw >> 4; row += 7; if (++ch == 9) { ch = 0; ++row; } } }
#pragma unroll
            for (int b = 0; b < 3; ++b) {
                const int blk = (b < nblk) ? u + 32 * b : u, st = blk >> 2, jb = blk & 3;
                const int fA = st < 16 ? st * 144 + (jb < 2 ? 32 * jb : 16 + 32 * jb) : ((st - 16) * 8 + 2 * jb) * 144 + 64, dB = (st < 16 ? 16 : 144) * 1152;
                const char* wb_ = (const char*)wout + (size_t)fA * 1152 + qn * 144;
#pragma unroll
                for (int i = 0; i < 5; ++i) wv[b][i] = *(const u32x4*)(wb_ + (wo_[i] + hi_[i] * dB));
            }
        }
        E = flag_ld(ws, WS_E);
        if (lane < 24) *(LAS u32x4*)(lds + L_KT + (lane >> 3) * KT_SZ + (2 * h + ((lane >> 2) & 1)) * 1088 + (lane & 3) * 16) = (u32x4){0u, 0u, 0u, 0u};
        WAITL0();
        __builtin_amdgcn_s_setprio(1);
        {
            float kv[2][3];
#pragma nounroll
            for (int ei = 0; ei < 9; ++ei) {
                BAR();
                if (ei > 0) {
                    LAS unsigned char* kt = opq(lds + L_KT + (2 * h + ((ei - 1) >> 2)) * 1088 + 64 + ((ei - 1) & 3) * 256 + lane * 2);
#pragma unroll
                    for (int j = 0; j < 2; ++j)
#pragma unroll
                        for (int c = 0; c < 3; ++c) *(LAS u16*)(kt + c * KT_SZ + j * 128) = (u16)pk_bf16(kv[j][c], 0.f);
                }
#pragma unroll
                for (int j = 0; j < 2; ++j) {
                    const int jj = (ei & 3) * 2 + j, e = jj >= 4 ? 3 : jj >= 2 ? 2 : jj, lag = 256 * jj + 4 * lane + 2 * h + ((ei >> 2) & 1);
                    const int idx = e == 0 ? lag : 128 * e + ((lag + (1 << (e - 1)) - 1 + ((lag >> e) & 1)) >> e);
                    const LAS float* q = opq((const LAS float*)(lds + L_KD) + idx);
#pragma unroll
                    for (int c = 0; c < 3; ++c) kv[j][c] = q[c * 768];
                }
            }
        }
        if (h == 0) {
            LAS unsigned char* pzs = opq(lds + Z0 + lane * 8);
#pragma unroll
            for (int i = 0; i < 18; ++i) *(LAS u32x2*)(pzs + i * 512) = uc[i];
            CFENCE();
            { const LAS unsigned char* pt = opq(lds + Z0 + (8 * (4 * fq + (lane & 3)) + (fr >> 2)) * 72);
              const unsigned ub = WS_UT + (unsigned)(pn * 36 + (lane & 3)) * 4096u + (unsigned)(pm * 128 + 8 * (4 * fq + (fr >> 2))) * 2u;
#pragma unroll
              for (int cc = 0; cc < 9; ++cc) __builtin_amdgcn_raw_buffer_store_b128(__builtin_bit_cast(u32x4, cat8(tr_read(pt + cc * 8), tr_read(pt + 288 + cc * 8))), ws, ub + cc * 16384u, 0, 16); }
            float a = 0.f;
#pragma unroll
            for (int i = 0; i < 9; ++i)
#pragma unroll
                for (int j = 0; j < 4; ++j) { const unsigned xa = xv[i][j], ga = gv[i][j]; a = __builtin_amdgcn_fdot2_f32_bf16(__builtin_bit_cast(bf16x2, xa), __builtin_bit_cast(bf16x2, ga), a, false); }
#define DPPF(v, ctl) __int_as_float(__builtin_amdgcn_update_dpp(0, __float_as_int(v), ctl, 0xf, 0xf, true))
            a += DPPF(a, 0xb1); a += DPPF(a, 0x4e); a += DPPF(a, 0x141);
#undef DPPF
            const float gt = rbf(__builtin_amdgcn_rcpf(1.0f + __expf(-rbf(a))));
            if ((lane & 7) == 0) __builtin_amdgcn_raw_buffer_store_b32(__float_as_uint(gt), ws, WS_GATE + (unsigned)(pm * 128 + pn * 8 + (lane >> 3)) * 4u, 0, 16);
        } else {
            LAS unsigned char* pws = opq(lds + Z0 + 9216 + lane * 16);
            const LAS unsigned char* ptw = opq(lds + Z0 + 9216 + (4 * fq + (fr >> 2)) * 144 + (lane & 3) * 8);
#pragma unroll
            for (int b = 0; b < 3; ++b) {
                if (b < nblk) {
#pragma unroll
                    for (int i = 0; i < 5; ++i) if (i < 4 || lane < 32) *(LAS u32x4*)(pws + i * 1024) = wv[b][i];
                    CFENCE();
                    const unsigned ob = WS_WOF + (unsigned)qn * (18u * 18432u) + (unsigned)(u + 32 * b) * 4608u;
#pragma unroll
                    for (int nn = 0; nn < 5; ++nn) {
                        const u32x4 v = __builtin_bit_cast(u32x4, cat8(tr_read(ptw + nn * 32), tr_read(ptw + 2304 + nn * 32)));
                        if (nn < 4) __builtin_amdgcn_raw_buffer_store_b128(v, ws, ob + nn * 1024 + lane * 16, 0, 16);
                        else if (fr < 8) __builtin_amdgcn_raw_buffer_store_b128(v, ws, ob + 4096 + (fq * 8 + fr) * 16, 0, 16);
                    }
                    CFENCE();
                }
            }
        }
        nb = 9;
        mk_tag();
        WAITL0();
        STEPS_TO(10);
        {
            const f32x4 sp = *(const LAS f32x4*)(lds + L_SP + (h * 64 + lane) * 16);
            const float dm = sp[0] - sp[2];
            const f32x2 mine = {(80.f * sp[0] + 64.f * sp[2]) * (1.0f / 144.f), sp[1] + sp[3] + (80.f * 64.f / 144.f) * dm * dm};
            __builtin_amdgcn_raw_buffer_store_b128((u32x4){T[0], __float_as_uint(mine.x), T[1], __float_as_uint(mine.y)}, ws, WS_ST + (unsigned)((pm * 16 + pn) * 128 + h * 64 + lane) * 16u, 0, 16);
            STEP(18);
            const unsigned so_ = WS_ST + (unsigned)(pm * 16 * 128 + h * 64 + lane) * 16u;
            int t0 = 0;
            u32x4 st[16];
            for (;;) {
                CFENCE();
#pragma unroll
                for (int j = 0; j < 16; ++j) st[j] = __builtin_amdgcn_raw_buffer_load_b128(ws, so_ + j * 2048u, 0, 16);
                STEP(18);
                unsigned bad = 0;
#pragma unroll
                for (int j = 0; j < 16; ++j) bad |= (st[j][0] ^ T[0]) | (st[j][2] ^ T[1]);
                if (__builtin_amdgcn_ballot_w64(bad != 0u) == 0ull) break;
                if (++t0 > SPIN_G) break;
            }
            STEP(18);
            float mean = 0.f, m2 = 0.f;
#pragma unroll
            for (int j = 0; j < 16; ++j) mean += __uint_as_float(st[j][1]);
            mean *= (1.0f / 16.f);
#pragma unroll
            for (int j = 0; j < 16; ++j) { const float d = __uint_as_float(st[j][1]) - mean; m2 += __uint_as_float(st[j][3]) + 144.f * d * d; }
            const float rstd = __builtin_amdgcn_rsqf(m2 * (1.0f / FH) + 1e-5f);
            *(LAS f32x2*)(lds + L_SP + (h * 64 + lane) * 16) = (f32x2){rstd, -mean * rstd};
            CFENCE();
            if (lane == 0) *(volatile LAS unsigned*)(lds + L_MISC + 4 + h * 4) = 1u;
            const volatile LAS unsigned* zd = (const volatile LAS unsigned*)(lds + L_MISC + 16 + h * 8 + (lane & 1) * 4);
            for (;;) {
                STEP(18);
                if (__builtin_amdgcn_ballot_w64(*zd == 0u) == 0ull) break;
                if (++t0 > SPIN_L) break;
            }
            CFENCE();
            const LAS unsigned char* ptz = opq(lds + Z0 + (h * 64 + 8 * fq + (fr >> 2)) * ZST + (lane & 3) * 8);
            const unsigned zo = WS_Z2F + (unsigned)((pn * 64 + pm * 4 + h * 2) * 9) * 1024u + lane * 16;
            u32x4 zf[18];
#pragma unroll
            for (int i = 0; i < 18; ++i) {
                const LAS unsigned char* pt = ptz + (i / 9) * 32 * ZST + (i % 9) * 32;
                zf[i] = __builtin_bit_cast(u32x4, cat8(tr_read(pt), tr_read(pt + 4 * ZST)));
                __builtin_amdgcn_raw_buffer_store_b128(zf[i], ws, zo + i * 1024, 0, 16);
                if (i == 5 || i == 11) STEP(18);
            }
            STEP(18);
            flag_publish(ws, WS_FZ + (unsigned)(pn * 32 + pm * 2 + h) * 128u, T, lane);
            STEPS_TO(18);
            LAS unsigned char* pr_ = opq(lds + h * STG + BOFF + lane * 16);
#pragma unroll
            for (int i = 0; i < 18; ++i) *(LAS u32x4*)(pr_ + i * 1024) = zf[i];
            WAITL0();
        }
    }

    const char* lin = (const char*)wsp_ + p * 1024 + lane * 16;
    auto issueLA = [&](unsigned off, int s) {
#pragma unroll
        for (int i = 0; i < 8; ++i) GLDS(lin + off + i * 2048, lds + s + (p + 2 * i) * 1024, 16); };
    auto issueLB = [&](unsigned off, int s) {
#pragma unroll
        for (int i = 0; i < 9; ++i) GLDS(lin + off + i * 2048, lds + s + BOFF + (p + 2 * i) * 1024, 16); };
    auto ord3 = [&](int s) { return (s >= 16 || (s & 3) == phi) ? s : ((s - 4) & 15); };
    u32x2 pv = {0u, 0u}; unsigned rdy = 0; bool pend = false;
    const char* fp = (const char*)wsp_ + WS_FZ + ((unsigned)pn * 32u + (lane & 31)) * 128u;
    auto poll_issue = [&]() { asm volatile("global_load_dwordx2 %0, %1, off sc1" : "+v"(pv) : "v"(fp) : "memory"); };
    auto poll_take = [&]() { asm volatile("" : "+v"(pv)); rdy |= (unsigned)__builtin_amdgcn_ballot_w64(pv[0] == T[0] && pv[1] == T[1]); };
    auto need = [&](unsigned bits) {
        if (pend) { poll_take(); pend = false; }
        if ((rdy & bits) != bits) {
            int t0 = 0;
            do { poll_issue(); WAITV(0); poll_take(); } while ((rdy & bits) != bits && ++t0 < SPIN_G);
        }
        if (rdy != ~0u) { poll_issue(); pend = true; }
    };
    const unsigned hb = WS_HF + (unsigned)qm * (18u * 16384u), ob = WS_WOF + (unsigned)qn * (18u * 18432u);
    if (is_prod) {
        mk_tag();
        const unsigned zb = WS_Z2F + (unsigned)pn * (64u * 9216u);
        int so = 2 * STG;
        const int r0 = (ord2(0) >> 1) == pm ? ord2(1) : ord2(0), r1 = (ord2(1) >> 1) == pm ? ord2(0) : ord2(1);
        poll_issue(); WAITV(0); poll_take();
#pragma nounroll
        for (int S = 18; S < 48; ++S) {
            BAR();
            int k = ord2(S - 16); if ((k >> 1) == pm) k = (k & 1) ? r1 : r0;
            need(1u << k); issueS(k, so); issueLB(zb + (unsigned)k * 18432u, so); so = nextbuf(so);
            WAITV(17);
        }
        BAR();
        fp = (const char*)wsp_ + WS_FZ + ((unsigned)(pn ^ 8) * 32u + (lane & 31)) * 128u; WAITV(0); rdy = 0; pend = false;
        need(~0u);
        issueLB(ob + (unsigned)ord3(0) * 18432u, 2 * STG);
        BAR();
        issueLB(ob + (unsigned)ord3(1) * 18432u, 0);
    } else
    if (wid < 4) {
        mk_tag();
        if (wid >> 1) consumer_b<4>(lds, ws, T, zp, bs, pm, pn, wid & 1, 1, lane);
        else consumer_b<5>(lds, ws, T, zp, bs, pm, pn, wid & 1, 0, lane);
    } else {
        __builtin_amdgcn_s_setprio(1);
        {   const unsigned fo = WS_FZ + (unsigned)(pn * 32 + (lane & 31)) * 128u;
            int t0 = 0;
            for (;;) {
                STEP(43);
                CFENCE();
                const u32x2 v = flag_ld(ws, fo);
                STEP(43);
                if (flag_all(v, T, true)) break;
                if (++t0 > SPIN_G) break;
            } }
        u32x4 uu[3][2];
#pragma unroll
        for (int c = 0; c < 3; ++c)
#pragma unroll
            for (int j = 0; j < 2; ++j) uu[c][j] = __builtin_amdgcn_raw_buffer_load_b128(ws, WS_UT + (unsigned)(ch0 + (c < nch ? 16 * c : 0)) * 4096u + h * 2048 + j * 1024 + lane * 16, 0, 16);
#pragma unroll
        for (int c = 0; c < 3; ++c) if (lane < 16) *(LAS u32x4*)(lds + L_UT + c * UT_SZ + h * 4352 + lane * 16) = (u32x4){0u, 0u, 0u, 0u};
        BAR(); BAR(); BAR(); nb += 3;
        LAS unsigned char* pu_ = opq(lds + L_UT + 256 + h * 2048 + lane * 16);
#pragma unroll
        for (int c = 0; c < 3; ++c)
#pragma unroll
            for (int j = 0; j < 2; ++j) *(LAS u32x4*)(pu_ + c * UT_SZ + j * 1024) = uu[c][j];
        CFENCE();
        if (lane == 0) *(volatile LAS unsigned*)(lds + L_MISC + 32 + h * 4) = 1u;
        STEP(50);
        {   const volatile LAS unsigned* uw = (const volatile LAS unsigned*)(lds + L_MISC + 32 + (lane & 1) * 4);
            int t0 = 0;
            while (__builtin_amdgcn_ballot_w64(*uw == 0u) != 0ull && ++t0 < SPIN_L) STEP(50);
            CFENCE(); }
    }

    if (wid < 6) nb = 50;
    while (!is_prod || nch == 3) {
        if (nb > 45) STEPS_TO(50);
        STEP(50);
        __builtin_amdgcn_wave_barrier();
        unsigned tk = 0;
        if (lane == 0) tk = __hip_atomic_fetch_add((LAS unsigned*)(lds + L_MISC), 1u, __ATOMIC_RELAXED, __HIP_MEMORY_SCOPE_WORKGROUP);
        __builtin_amdgcn_wave_barrier();
        tk = __builtin_amdgcn_readfirstlane(tk);
        if (tk >= (unsigned)(4 * nch)) break;
        const int c = tk >> 2, tl = tk & 1;
        if (tk & 2) conv_task<0, 10>(lds, L_KT + c * KT_SZ, L_UT + c * UT_SZ, ytab(c), tl, lane, nb);
        else conv_task<11, 15>(lds, L_KT + c * KT_SZ, L_UT + c * UT_SZ, ytab(c), tl, lane, nb);
    }
    STEPS_TO(50);
    WAITL0();
    if (is_prod) {
        fp = (const char*)wsp_ + WS_FH + ((unsigned)qm * 32u + (lane & 31)) * 128u; rdy = 0;
        need(~0u);
        issueLA(hb + (unsigned)ord3(0) * 16384u, 2 * STG); issueLA(hb + (unsigned)ord3(1) * 16384u, 0);
        int so = STG;
        WAITV(8);
#pragma nounroll
        for (int S = 50; S < 66; ++S) {
            BAR();
            const int k = ord3(S - 48); issueLA(hb + (unsigned)k * 16384u, so); issueLB(ob + (unsigned)k * 18432u, so); so = nextbuf(so);
            WAITV(17);
        }
        BAR();
        WAITV(0); BAR();
        if (bid == 0 && p == 0 && lane == 0) __builtin_amdgcn_raw_buffer_store_b64(T, ws, WS_E, 0, 16);
        return;
    }


    if (wid < 4) {
        f32x4 acc[5];
        struct F3 { s16x4 alo[4], ahi[4]; bf16x8 b[20]; } f0, f1;
        const LAS unsigned char* ph = lds + 2 * STG + wid * 4096 + lane * 16;
        const LAS unsigned char* pw = lds + 2 * STG + BOFF + lane * 16;
        const LAS unsigned char* pw4 = lds + 2 * STG + BOFF + 4096 + (fq * 8 + (fr & 7)) * 16;
        auto rd3 = [&](F3& f, int i, bool single) {
            const int j = i / 6, r = i % 6;
            if (r == 0) {
                if (!single) { const bf16x8 v = *(const LAS bf16x8*)(ph + j * 1024); f.alo[j] = __builtin_shufflevector(v, v, 0, 1, 2, 3); f.ahi[j] = __builtin_shufflevector(v, v, 4, 5, 6, 7); }
                else { f.alo[j] = *(const LAS s16x4*)(ph + j * 1024); f.ahi[j] = *(const LAS s16x4*)(ph + j * 1024 + 512); }
            } else if (r < 5) f.b[j * 5 + r - 1] = *(const LAS bf16x8*)(pw + j * 4608 + (r - 1) * 1024);
            else f.b[j * 5 + 4] = *(const LAS bf16x8*)(pw4 + j * 4608);
        };
        int so = 2 * STG;
        auto step3 = [&](const F3& c, F3& l, int mode  , bool first) {
            const int delta = so == 2 * STG ? -2 * STG : STG; so += delta;
#pragma unroll
            for (int i = 0; i < 20; ++i) {
                const int j = i / 5, n = i % 5;
                acc[n] = MFMA16(c.b[i], cat8(c.alo[j], c.ahi[j]), (first && j == 0) ? ((f32x4){0.f, 0.f, 0.f, 0.f}) : acc[n]);
                if (mode < 2 && i < 12) { rd3(l, 2 * i, mode == 1); rd3(l, 2 * i + 1, mode == 1); }
                if (mode < 2) { if (i == 13) { ph += delta; asm volatile("" : "+v"(ph)); } if (i == 14) { pw += delta; asm volatile("" : "+v"(pw)); } if (i == 15) { pw4 += delta; asm volatile("" : "+v"(pw4)); } }
                SB0();
            }
        };
        __builtin_amdgcn_s_waitcnt(0xC07F);
        BAR();
#pragma unroll
        for (int i = 0; i < 24; ++i) rd3(f0, i, false);
        ph -= 2 * STG; pw -= 2 * STG; pw4 -= 2 * STG; so = 0;
        WAITL0(); BAR(); step3(f0, f1, 0, true);
#pragma nounroll
        for (int j = 0; j < 7; ++j) {
            WAITL0(); BAR(); step3(f1, f0, 0, false);
            WAITL0(); BAR(); step3(f0, f1, 0, false);
        }
        ph = opq(ph - lane * 8);
        WAITL0(); BAR(); step3(f1, f0, 1, false);
        WAITL0(); BAR();
        const float g = *(const LAS float*)(lds + L_MISC + 64 + (16 * wid + fr) * 4);
        const LAS unsigned char* pat = opq(lds + L_AT + (4 * fq + (fr >> 2)) * 128 + (16 * wid + 4 * (lane & 3)) * 2);
        u32x2 av[5];
#pragma unroll
        for (int n = 0; n < 5; ++n) av[n] = __builtin_bit_cast(u32x2, tr_read(pat + n * 2048));
        step3(f0, f1, 1, false);
        step3(f1, f0, 2, false);
        const rsrc_t ors = __builtin_amdgcn_make_buffer_rsrc(out, 0, SEQ * DM * 2, 0x00020000);
        const float g1 = rbf(1.0f - g);
#pragma unroll
        for (int n = 0; n < 5; ++n) {
            const f32x4 b = acc[n]; const u32x2 a = av[n];
            const u32x2 o = {pk_bf16(g * bflo(a[0]) + g1 * b[0], g * bfhi(a[0]) + g1 * b[1]), pk_bf16(g * bflo(a[1]) + g1 * b[2], g * bfhi(a[1]) + g1 * b[3])};
            if (n < 4 || fq < 2) __builtin_amdgcn_raw_buffer_store_b64(o, ors, (unsigned)((qm * 64 + 16 * wid + fr) * 1152 + (qn * 72 + 16 * n + 4 * fq) * 2), 0, 0);
        }
    } else {
        int d = pn * 36 + pm;
#pragma nounroll
        for (int c = 0; c < nch; ++c) {
            const float dsk = bf2f(Dsk[d]);
#pragma unroll
            for (int k = 0; k < 2; ++k) {
                BAR(); ++nb;
                const int l0 = h * 1024 + k * 512 + lane * 8;
                f32x4 cv = *(const LAS f32x4*)(lds + ytab(c) + 8192 + (l0 >= 16 ? (l0 >> 4) - 1 : 0) * 16);
                if ((lane & 1) || l0 == 0) cv = (f32x4){0.f, 0.f, 0.f, 0.f};
                const f32x4 y0 = *(const LAS f32x4*)(lds + ytab(c) + l0 * 4) + cv, y1 = *(const LAS f32x4*)(lds + ytab(c) + l0 * 4 + 16);
                const u32x4 uv = *(const LAS u32x4*)(lds + L_UT + c * UT_SZ + 256 + l0 * 2);
                u32x4 o;
#pragma unroll
                for (int j = 0; j < 4; ++j) {
                    const float ya = j < 2 ? y0[2 * j] : y1[2 * j - 4], yb = j < 2 ? y0[2 * j + 1] : y1[2 * j - 3];
                    const unsigned yp = pk_bf16(ya, yb), up = pk_bf16(bflo(uv[j]) * dsk, bfhi(uv[j]) * dsk);
                    o[j] = pk_bf16(bflo(yp) + bflo(up), bfhi(yp) + bfhi(up));
                }
                __builtin_amdgcn_raw_buffer_store_b128(o, ws, WS_AT + (unsigned)d * 4096u + l0 * 2, 0, 16);
            }
            d += 16;
        }
        STEPS_TO(58);
        flag_publish(ws, WS_FA + (unsigned)((pn >> 1) * 64 + (pn & 1) * 32 + pm * 2 + h) * 128u, T, lane);
        STEPS_TO(61);
        if (h == 0) {
            const unsigned fo = WS_FA + (unsigned)(qn * 64 + lane) * 128u;
            int t0 = 0;
            for (;;) {
                CFENCE();
                const u32x2 v = flag_ld(ws, fo);
                STEP(67); STEP(67);
                if (flag_all(v, T, true)) break;
                if (++t0 > SPIN_G) break;
            }
            u32x4 av[9];
#pragma unroll
            for (int i = 0; i < 9; ++i) av[i] = __builtin_amdgcn_raw_buffer_load_b128(ws, WS_AT + (unsigned)(qn * 72 + i * 8 + (lane >> 3)) * 4096u + qm * 128 + (lane & 7) * 16, 0, 16);
            STEP(67); STEP(67);
            LAS unsigned char* pa_ = opq(lds + L_AT + lane * 16);
#pragma unroll
            for (int i = 0; i < 9; ++i) *(LAS u32x4*)(pa_ + i * 1024) = av[i];
        } else {
            STEPS_TO(64);
            const unsigned gv = __builtin_amdgcn_raw_buffer_load_b32(ws, WS_GATE + (unsigned)(qm * 64 + lane) * 4u, 0, 16);
            BAR(); BAR(); nb += 2;
            *(LAS unsigned*)(lds + L_MISC + 64 + lane * 4) = gv;
        }
        STEPS_TO(67);
        WAITL0();
        BAR();
    }
}
}

extern "C" void kernel_launch(void* const* d_in, const int* in_sizes, int n_in, void* d_out, int out_size, void* d_ws, size_t ws_size, hipStream_t stream) {
    (void)in_sizes; (void)n_in; (void)out_size; (void)ws_size;
    (void)hipFuncSetAttribute((const void*)gm::gm_kernel, hipFuncAttributeMaxDynamicSharedMemorySize, gm::LDS_BYTES);
    hipLaunchKernelGGL(gm::gm_kernel, dim3(256), dim3(512), gm::LDS_BYTES, stream, (const u16*)d_in[0], (const u16*)d_in[1], (const u16*)d_in[2], (const u16*)d_in[3],
                       (const u16*)d_in[4], (const u16*)d_in[5], (const u16*)d_in[6], (const u16*)d_in[8], (const u16*)d_in[9], (const u16*)d_in[10],
                       (const u16*)d_in[11], (const u16*)d_in[12], (u16*)d_out, (unsigned char*)d_ws);
}
```

```cpp
#include <hip/hip_runtime.h>
#include <stdint.h>
#include <type_traits>

#define LAS __attribute__((address_space(3)))
typedef unsigned short u16;
typedef unsigned long long u64;
typedef short bf16x8 __attribute__((ext_vector_type(8)));
typedef short s16x4 __attribute__((ext_vector_type(4)));
typedef _Float16 f16x8 __attribute__((ext_vector_type(8)));
typedef _Float16 f16x2 __attribute__((ext_vector_type(2)));
typedef __bf16 bf16x2 __attribute__((ext_vector_type(2)));
typedef float f32x4 __attribute__((ext_vector_type(4)));
typedef float f32x2 __attribute__((ext_vector_type(2)));
typedef unsigned u32x4 __attribute__((ext_vector_type(4)));
typedef unsigned u32x2 __attribute__((ext_vector_type(2)));
typedef __amdgpu_buffer_rsrc_t rsrc_t;

namespace gm {
constexpr int SEQ = 2048, DM = 576, FH = 2304;
constexpr unsigned WS_E = 0, WS_FZ = 4096, WS_FH = WS_FZ + 65536, WS_FA = WS_FH + 131072;
constexpr unsigned WS_ST = 0x60000, WS_GATE = 0xE0000, WS_Z2F = 0x100000, WS_HF = 0xA00000, WS_WOF = 0x1300000, WS_UT = 0x1600000, WS_AT = 0x1900000, WS_END = 0x1C00000;
constexpr int STG = 34816, BOFF = 16384, Z0 = 3 * STG, ZST = 296, T0 = Z0 + 128 * ZST;
constexpr int KT_SZ = 4352, UT_SZ = 4608, Y_SZ = 10240;
constexpr int L_MISC = 163840 - 320;
constexpr int L_KT = L_MISC - 3 * KT_SZ, L_SP = T0, L_KD = Z0 + 3 * 8448, L_UT = Z0 + 3 * Y_SZ;
constexpr int L_AT = Z0;
constexpr int SPIN_G = 8192, SPIN_L = 1 << 20;
constexpr int LDS_BYTES = 163840;
static_assert(L_UT + 3 * UT_SZ <= L_KT && L_KD + 3 * 3072 <= T0 && L_SP + 2048 <= L_KT, "lds map");
constexpr int CODE_LINES = 13, CODE_BYTES = 50176;
constexpr int CONV_SLICE = 11;

__device__ __forceinline__ float bf2f(unsigned v) { return __uint_as_float(v << 16); }
__device__ __forceinline__ float bflo(unsigned w) { return __uint_as_float(w << 16); }
__device__ __forceinline__ float bfhi(unsigned w) { return __uint_as_float(w & 0xffff0000u); }
__device__ __forceinline__ unsigned pk_bf16(float a, float b) { f32x2 v = {a, b}; return __builtin_bit_cast(unsigned, __builtin_convertvector(v, bf16x2)); }
__device__ __forceinline__ unsigned pk_f16(float a, float b) { f32x2 v = {a, b}; return __builtin_bit_cast(unsigned, __builtin_convertvector(v, f16x2)); }
__device__ __forceinline__ float rbf(float a) { return bflo(pk_bf16(a, a)); }
__device__ __forceinline__ f32x2 gelu2(f32x2 x) {
    const f32x2 w = (x * x) * (-0.10294324f) + (-2.30220819f);
    const f32x2 a = w * x;
    f32x2 e; e.x = __builtin_amdgcn_exp2f(a.x); e.y = __builtin_amdgcn_exp2f(a.y);
    e = e + 1.0f;
    f32x2 r; r.x = __builtin_amdgcn_rcpf(e.x); r.y = __builtin_amdgcn_rcpf(e.y);
    return x * r;
}
__device__ __forceinline__ f32x4 gelu4(f32x4 v) { const f32x2 a = gelu2((f32x2){v[0], v[1]}), b = gelu2((f32x2){v[2], v[3]}); return (f32x4){a.x, a.y, b.x, b.y}; }
__device__ __forceinline__ float allred_q(float v) {
    unsigned u = __float_as_uint(v);
    auto r = __builtin_amdgcn_permlane16_swap(u, u, false, false);
    v = __uint_as_float(r[0]) + __uint_as_float(r[1]);
    u = __float_as_uint(v);
    auto s = __builtin_amdgcn_permlane32_swap(u, u, false, false);
    return __uint_as_float(s[0]) + __uint_as_float(s[1]);
}
#define BAR() do { asm volatile("" ::: "memory"); __builtin_amdgcn_s_barrier(); asm volatile("" ::: "memory"); } while (0)
#define WAITV(n) asm volatile("s_waitcnt vmcnt(" #n ")" ::: "memory")
#define WAITL0() asm volatile("s_waitcnt lgkmcnt(0)" ::: "memory")
#define CFENCE() asm volatile("" ::: "memory")
#define MFMA16(a, b, c) __builtin_amdgcn_mfma_f32_16x16x32_bf16(a, b, c, 0, 0, 0)
__device__ __forceinline__ s16x4 tr_read(const LAS unsigned char* p) { return __builtin_amdgcn_ds_read_tr16_b64_v4i16((LAS s16x4*)p); }
__device__ __forceinline__ bf16x8 cat8(s16x4 a, s16x4 b) { return __builtin_shufflevector(a, b, 0, 1, 2, 3, 4, 5, 6, 7); }
template <class P> __device__ __forceinline__ P opq(P p) { asm volatile("" : "+v"(p)); return p; }
__device__ __forceinline__ int nextbuf(int so) { return so == 2 * STG ? 0 : so + STG; }

__device__ __forceinline__ u32x2 flag_ld(rsrc_t ws, unsigned off) { return __builtin_amdgcn_raw_buffer_load_b64(ws, off, 0, 16); }
__device__ __forceinline__ bool flag_all(u32x2 v, u32x2 T, bool active) { return __builtin_amdgcn_ballot_w64(active && (v[0] != T[0] || v[1] != T[1])) == 0ull; }
__device__ __forceinline__ void flag_publish(rsrc_t ws, unsigned off, u32x2 T, int lane) {
    WAITV(0);
    if (lane == 0) __builtin_amdgcn_raw_buffer_store_b64(T, ws, off, 0, 16);
}
#define SB0() __builtin_amdgcn_sched_barrier(0)
template <int NT> struct Frag { bf16x8 a[4]; s16x4 lo[NT], hi[NT]; };
template <int NT, bool TR, int HALF>
__device__ __forceinline__ void frag_read(Frag<NT>& f, int i, const LAS unsigned char* pa, const LAS unsigned char* pb) {
    { constexpr int B0 = TR ? 2 : 1; i = i == 0 ? 0 : i <= B0 ? i + 3 : i <= B0 + 3 ? i - B0 : i; }
    if (i < 4) f.a[i] = *(const LAS bf16x8*)(pa + i * 2048);
    else if (TR) { const int n = (i - 4) >> 1; if ((i - 4) & 1) f.hi[n] = tr_read(pb + HALF * 9216 + 4608 + n * 32); else f.lo[n] = tr_read(pb + HALF * 9216 + n * 32); }
    else { const int n = i - 4; const bf16x8 v = *(const LAS bf16x8*)(pb + HALF * 9216 + n * 1024);
           f.lo[n] = __builtin_shufflevector(v, v, 0, 1, 2, 3); f.hi[n] = __builtin_shufflevector(v, v, 4, 5, 6, 7); }
}
template <int NT, bool TR, int LHALF, int S0, int BAT, bool FIRST, bool UPD>
__device__ __forceinline__ void half_step(f32x4 (&acc)[NT][4], const Frag<NT>& c, Frag<NT>& l, const LAS unsigned char*& pa0, const LAS unsigned char*& pa1, const LAS unsigned char*& pb, int delta,
                                          const float* ini, const volatile LAS unsigned* rdy, unsigned& rv) {
    constexpr int NRD = TR ? 4 + 2 * NT : 4 + NT, NM = 4 * NT, NS = NM - S0;
#pragma unroll
    for (int i = 0; i < NM; ++i) {
        const int n = i >> 2, m = i & 3;
        acc[n][m] = MFMA16(cat8(c.lo[n], c.hi[n]), c.a[m], FIRST ? (TR ? ((f32x4){0.f, 0.f, 0.f, 0.f}) : ((f32x4){ini[m], ini[m], ini[m], ini[m]})) : acc[n][m]);
        SB0();
        if (i == BAT) { WAITL0(); BAR(); if (TR) rv = *rdy; }
        if (LHALF >= 0 && i >= S0) {
#pragma unroll
            for (int r = 0; r < NRD; ++r) if ((r * NS) / NRD == i - S0) frag_read<NT, TR, (LHALF > 0 ? 1 : 0)>(l, r, LHALF > 0 ? pa1 : pa0, pb);
        }
        if (UPD) { if (i == NM - 3) { pa0 += delta; asm volatile("" : "+v"(pa0)); } if (i == NM - 2) { pa1 += delta; asm volatile("" : "+v"(pa1)); } if (i == NM - 1) { pb += delta; asm volatile("" : "+v"(pb)); } }
        SB0();
    }
}
template <int NT, bool TR, class HK>
__device__ __forceinline__ void gemm_phase(int nsteps, f32x4 (&acc)[NT][4], int& so, const LAS unsigned char* pa0, const LAS unsigned char* pa1, const LAS unsigned char* pb,
                                           const float* ini, const volatile LAS unsigned* rdy, HK hook) {
    constexpr int NRD = TR ? 4 + 2 * NT : 4 + NT;
    Frag<NT> f0, f1;
    unsigned rv = 0;
    pa0 += so; pa1 += so; pb += so;
    asm volatile("" : "+v"(pa0), "+v"(pa1), "+v"(pb));
    __builtin_amdgcn_s_waitcnt(0xC07F);
    BAR();
#pragma unroll
    for (int i = 0; i < NRD; ++i) frag_read<NT, TR, 0>(f0, i, pa0, pb);
    SB0();
    int delta = so == 2 * STG ? -2 * STG : STG; so += delta;
    half_step<NT, TR, 1, 2, -1, true, true>(acc, f0, f1, pa0, pa1, pb, delta, ini, rdy, rv);
#pragma nounroll
    for (int s = 1; s < nsteps; ++s) {
        delta = so == 2 * STG ? -2 * STG : STG; so += delta;
        half_step<NT, TR, 0, 6, 5, false, false>(acc, f1, f0, pa0, pa1, pb, 0, ini, rdy, rv);
        half_step<NT, TR, 1, 2, -1, false, true>(acc, f0, f1, pa0, pa1, pb, delta, ini, rdy, rv);
        if (TR) hook(rv);
    }
    if (TR) half_step<NT, TR, -1, 0, -1, false, false>(acc, f1, f0, pa0, pa1, pb, 0, ini, rdy, rv);
    else {
#pragma unroll
        for (int m = 0; m <= 4; ++m) {
            if (m < 4) {
#pragma unroll
                for (int n = 0; n < NT; ++n) acc[n][m] = MFMA16(cat8(f1.lo[n], f1.hi[n]), f1.a[m], acc[n][m]);
            }
            SB0();
            if (m > 0) hook(m - 1);
            SB0();
        }
    }
}

template <int NT, class LT>
__device__ __forceinline__ void consumer_a(LAS unsigned char* lds, u32x2 (&zp)[5][4], LT late, int wm, int wn, int lane) {
    const int fr = lane & 15, fq = lane >> 4;
    constexpr int N0 = (NT == 5) ? 0 : 5;
    f32x4 acc[NT][4];
    int so = 0;
    const int sw = (fr >> 1) & 7;
    const int a0 = (wm * 64 + fr) * 128 + ((fq ^ sw) << 4), a1 = (wm * 64 + fr) * 128 + (((4 + fq) ^ sw) << 4);
    const int bw = BOFF + (4 * fq + (fr >> 2)) * 288 + (lane & 3) * 8 + N0 * 32;
    const volatile LAS unsigned* rdy = (const volatile LAS unsigned*)(lds + L_MISC + 4 + wm * 4);
    int done = 0;
    auto normalise = [&](auto m0) {
        const LAS unsigned char* ps = opq(lds + L_SP + (wm * 64 + fr) * 16);
        LAS unsigned char* pz = opq(lds + Z0 + (wm * 64 + fr) * ZST + N0 * 32 + fq * 8);
#pragma unroll
        for (int m = m0.value; m < m0.value + 2; ++m) {
            const f32x2 sr = *(const LAS f32x2*)(ps + m * 256);
#pragma unroll
            for (int n = 0; n < NT; ++n) {
                const f32x2 u = (f32x2){bflo(zp[n][m][0]), bfhi(zp[n][m][0])} * sr.x + sr.y, v = (f32x2){bflo(zp[n][m][1]), bfhi(zp[n][m][1])} * sr.x + sr.y;
                *(LAS u32x2*)(pz + m * 16 * ZST + n * 32) = (u32x2){pk_bf16(u.x, u.y), pk_bf16(v.x, v.y)};
            }
        }
        CFENCE();
        if (m0.value == 2 && lane == 0) *(volatile LAS unsigned*)(lds + L_MISC + 16 + (wm * 2 + wn) * 4) = 1u;
        __builtin_amdgcn_wave_barrier();
        ++done;
    };
    auto hook = [&](unsigned rv) {
        if (done == 1) normalise(std::integral_constant<int, 2>{});
        else if (done == 0 && __builtin_amdgcn_readfirstlane(rv) != 0u) normalise(std::integral_constant<int, 0>{}); };
#pragma unroll
    for (int n = 0; n < NT; ++n)
#pragma unroll
        for (int m = 0; m < 4; ++m) zp[n][m] = (u32x2){0u, 0u};
#pragma nounroll
    for (int it = 0; it < 2; ++it) {
        gemm_phase<NT, true>(9, acc, so, lds + a0, lds + a1, lds + bw, nullptr, rdy, hook);
        if (it == 1) late();
        if (it == 1 && done < 2) {
            int t0 = 0;
            while (__builtin_amdgcn_readfirstlane(*rdy) == 0u && ++t0 < SPIN_L) __builtin_amdgcn_s_sleep(1);
            hook(1u); hook(1u);
        }
#pragma unroll
        for (int m = 0; m < 4; ++m)
#pragma unroll
            for (int n = 0; n < NT; ++n) { acc[n][m] = gelu4(acc[n][m]); zp[n][m] = (u32x2){pk_bf16(acc[n][m][0], acc[n][m][1]), pk_bf16(acc[n][m][2], acc[n][m][3])}; }
        if (it == 0) {
            LAS unsigned char* ps = opq(lds + L_SP + (wm * 64 + fr) * 16 + wn * 8);
#pragma unroll
            for (int m = 0; m < 4; ++m) {
                f32x4 s4 = acc[0][m];
#pragma unroll
                for (int n = 1; n < NT; ++n) s4 += acc[n][m];
                const float mean = opq(allred_q((s4[0] + s4[1]) + (s4[2] + s4[3])) * (1.0f / (16 * NT)));
                f32x4 q4 = {0.f, 0.f, 0.f, 0.f};
#pragma unroll
                for (int n = 0; n < NT; ++n) { const f32x4 d = acc[n][m] - mean; q4 += d * d; }
                const float m2 = allred_q((q4[0] + q4[1]) + (q4[2] + q4[3]));
                if (fq == 0) *(LAS f32x2*)(ps + m * 256) = (f32x2){mean, m2};
            }
        }
    }
}

template <int NT>
__device__ __forceinline__ void consumer_b(LAS unsigned char* lds, rsrc_t ws, u32x2 T, const u32x2 (&z1)[5][4], const float (&bs_)[4], int pm, int pn, int wm, int wn, int lane) {
    float bs[4] = {bf2f(__float_as_uint(opq(bs_[0]))), bf2f(__float_as_uint(opq(bs_[1]))), bf2f(__float_as_uint(opq(bs_[2]))), bf2f(__float_as_uint(opq(bs_[3])))};
    const int fr = lane & 15, fq = lane >> 4;
    constexpr int N0 = (NT == 5) ? 0 : 5;
    f32x4 acc[NT][4];
    int so = 0;
    const int sw = (fr >> 1) & 7;
    const int a0 = (wm * 64 + fr) * 128 + ((fq ^ sw) << 4), a1 = (wm * 64 + fr) * 128 + (((4 + fq) ^ sw) << 4);
    const int bz = BOFF + N0 * 1024 + lane * 16;
    const int qm = 2 * pm + wm;
    const unsigned hb = WS_HF + (unsigned)qm * (18u * 16384u);
    auto epi = [&](int m) {
        if (m == 0) __builtin_amdgcn_s_setprio(3);
        u32x2 hp[NT];
#pragma unroll
        for (int n = 0; n < NT; ++n) {
            const f32x2 u = (f32x2){bflo(z1[n][m][0]), bfhi(z1[n][m][0])} * (f32x2){acc[n][m][0], acc[n][m][1]}, v = (f32x2){bflo(z1[n][m][1]), bfhi(z1[n][m][1])} * (f32x2){acc[n][m][2], acc[n][m][3]};
            hp[n] = (u32x2){pk_bf16(u.x, u.y), pk_bf16(v.x, v.y)};
        }
#pragma unroll
        for (int j = 0; j < 2; ++j)
            __builtin_amdgcn_raw_buffer_store_b128((u32x4){hp[2 * j][0], hp[2 * j][1], hp[2 * j + 1][0], hp[2 * j + 1][1]}, ws,
                                                   hb + (unsigned)pn * 16384u + (unsigned)(m * 4 + wn * 2 + j) * 1024u + lane * 16, 0, 16);
        if (NT == 5) __builtin_amdgcn_raw_buffer_store_b64(hp[NT - 1], ws, hb + (unsigned)(16 + (pn >> 3)) * 16384u + m * 4096u + (unsigned)(pn & 7) * 512u + lane * 8, 0, 16);
    };
    gemm_phase<NT, false>(32, acc, so, lds + a0, lds + a1, lds + bz, bs, nullptr, epi);
    flag_publish(ws, WS_FH + (unsigned)(qm * 32 + pn * 2 + wn) * 128u, T, lane);
    __builtin_amdgcn_s_setprio(0);
}

template <int TLO, int THI>
__device__ __forceinline__ void conv_task(LAS unsigned char* lds, int kt, int ut, int yt, int tl, int lane, int& nb) {
#define CSL() do { if (nb < 50) { BAR(); ++nb; } } while (0)
    constexpr int NTL = THI - TLO + 1, ND = THI + 2;
    const int i = lane & 15, g = lane >> 4;
    const LAS unsigned char* pk = opq(lds + kt + (i & 3) * 1088 + ((i >> 2) + g) * 16);
    const LAS unsigned char* pu = opq(lds + ut + (128 + 16 * tl - 32 * g - 4 * (i >> 2) + 4 * (i & 3)) * 2 - 32);
    bf16x8 ub[ND], kf[ND];
#pragma unroll
    for (int d = 0; d < ND; ++d) { if (d % 6 == 0) CSL(); ub[ND - 1 - d] = cat8(tr_read(pu + 256 * (ND - 1 - d) + 32), tr_read(pu + 256 * (ND - 1 - d))); kf[d] = *(const LAS bf16x8*)(pk + 64 * d); }
    f32x4 acc[NTL];
    int cnt = CONV_SLICE;
#pragma unroll
    for (int s = -1; s <= THI; ++s) {
#pragma unroll
        for (int t = TLO; t <= THI; ++t) if (t >= s) {
            if (cnt == CONV_SLICE) { CSL(); cnt = 0; }
            ++cnt;
            acc[t - TLO] = MFMA16(kf[s + 1], ub[t - s], s == -1 ? ((f32x4){0.f, 0.f, 0.f, 0.f}) : acc[t - TLO]);
        }
    }
    LAS float* y = opq((LAS float*)(lds + yt) + 32 * g + 16 * tl + i);
    LAS float* cy = opq((LAS float*)(lds + yt + 8192) + (2 * g + tl) * 4 + (i & 3));
#define DPPF(v, ctl) __int_as_float(__builtin_amdgcn_update_dpp(0, __float_as_int(v), ctl, 0xf, 0xf, true))
#pragma unroll
    for (int t = TLO; t <= THI; ++t) {
        if (((t - TLO) % 3) == 0) CSL();
        const f32x4 a = acc[t - TLO];
        y[128 * t] = (a[0] + DPPF(a[1], 0x111)) + (DPPF(a[2], 0x112) + DPPF(a[3], 0x113));
        const float cv = DPPF(a[1], 0x10f) + (DPPF(a[2], 0x10e) + DPPF(a[3], 0x10d));
        if (i < 4) cy[32 * t] = cv;
    }
#undef DPPF
#undef CSL
}
__device__ __forceinline__ int ytab(int c) { return Z0 + c * Y_SZ; }

__global__ __launch_bounds__(512, 2) void gm_kernel(const u16* __restrict__ x, const u16* __restrict__ logA, const u16* __restrict__ Aim, const u16* __restrict__ Cre,
                                                    const u16* __restrict__ Cim, const u16* __restrict__ logdt, const u16* __restrict__ Dsk, const u16* __restrict__ win,
                                                    const u16* __restrict__ wsp, const u16* __restrict__ bsp, const u16* __restrict__ wout, const u16* __restrict__ gw,
                                                    u16* __restrict__ out, unsigned char* wsp_) {
    extern __shared__ __attribute__((aligned(16))) unsigned char lds_[];
    LAS unsigned char* lds = (LAS unsigned char*)lds_;
    const int wid = __builtin_amdgcn_readfirstlane(threadIdx.x >> 6), lane = threadIdx.x & 63;
    const int bid = blockIdx.x, xg = bid & 7, rr = bid >> 3, bi = rr >> 2, bj = rr & 3;
    const int pm = (xg >> 2) * 8 + bi, pn = (xg & 3) * 4 + bj;
    const int qm = 2 * pm + (pn >> 3), qn = pn & 7;
    const int nch = (pm & 3) == 0 ? 3 : 2;
    const rsrc_t ws = __builtin_amdgcn_make_buffer_rsrc(wsp_, 0, WS_END, 0x00020000);
    unsigned pfw = 0;
    if (wid == 6) {
        u64 pc0; asm volatile("s_getpc_b64 %0" : "=s"(pc0));
        const u64 q_ = (pc0 & ~127ull) + (unsigned)min((rr * CODE_LINES + (lane >> 1)) * 128 + (lane & 1) * 64, CODE_BYTES);
        if (lane < 2 * CODE_LINES) asm volatile("global_load_dword %0, %1, off" : "+v"(pfw) : "v"(q_) : "memory");
    }

    const int fr = lane & 15, fq = lane >> 4;
    const int h = wid - 6, p = wid - 4;
    const bool is_prod = (wid >> 1) == 2;
    auto chan = [&](int c) { return pn * 36 + (c < 2 ? pm + 16 * c : 32 + (pm >> 2)); };
    float bs[4];
    auto late = [&]() {
#pragma unroll
        for (int m = 0; m < 4; ++m) bs[m] = __uint_as_float((unsigned)bsp[pm * 128 + (wid & 1) * 64 + m * 16 + fr]);
    };
    const int phi = (-(bi + bj)) & 3;
    const unsigned rsw = (unsigned)(((lane & 7) ^ ((4 * p + (lane >> 4)) & 7)) * 16);
#define GLDS(src, dst, aux) __builtin_amdgcn_global_load_lds((const unsigned*)(src), (LAS unsigned*)(dst), 16, 0, aux)
    auto issueS = [&](int k, int s) {
        const char* sb = (const char*)wsp + (size_t)(pm * 128 + p * 8) * 4096 + k * 128; const unsigned vs = (unsigned)(lane >> 3) * 4096u + rsw;
#pragma unroll
        for (int i = 0; i < 8; ++i) GLDS(sb + (size_t)i * (16 * 4096) + vs, lds + s + (p + 2 * i) * 1024, 0); };
    auto ord2 = [&](int s) { return ((s & 3) == phi) ? s : ((s - 4) & 31); };
    u32x2 E, T;
    auto mk_tag = [&]() { u64 z = (((u64)__builtin_amdgcn_readfirstlane(E[1]) << 32) | __builtin_amdgcn_readfirstlane(E[0])) + 0x9E3779B97F4A7C15ull;
        z = (z ^ (z >> 30)) * 0xBF58476D1CE4E5B9ull; z = (z ^ (z >> 27)) * 0x94D049BB133111EBull; z ^= z >> 31; T = (u32x2){(unsigned)z, (unsigned)(z >> 32)}; };
    u32x2 zp[5][4];
#define STEP(lim) do { if (nb < (lim)) { BAR(); ++nb; } } while (0)
#define STEPS_TO(lim) _Pragma("nounroll") while (nb < (lim)) { BAR(); ++nb; }
    int nb = 0;
    const int kc = wid == 7 ? 2 : wid == 1 || wid == 2 ? wid - 1 : 3;
    if (kc < nch) {
        const int kch = chan(kc), ki = kch * 64 + lane;
        const unsigned pA = ((const unsigned*)logA)[ki], pI = ((const unsigned*)Aim)[ki], pR = ((const unsigned*)Cre)[ki], pC = ((const unsigned*)Cim)[ki], pT = logdt[kch];
        LAS u32x2* tw = opq((LAS u32x2*)(lds + Z0 + kc * 8448) + lane);
        const LAS unsigned char* trd = opq(lds + Z0 + kc * 8448 + fr * 528 + fq * 16);
        const float dt = rbf(__expf(bf2f(pT)));
        const f32x2 ar = (f32x2){rbf(__expf(bflo(pA))), rbf(__expf(bfhi(pA)))} * dt * -1.44269504f, ai = (f32x2){bflo(pI), bfhi(pI)} * dt * 0.15915494f;
        const f32x2 cr = (f32x2){bflo(pR), bfhi(pR)} * dt, ci = (f32x2){bflo(pC), bfhi(pC)} * dt;
        float cm = fmaxf(fmaxf(fabsf(cr.x), fabsf(ci.x)), fmaxf(fabsf(cr.y), fabsf(ci.y)));
#define DPPF(v, ctl) __int_as_float(__builtin_amdgcn_update_dpp(0, __float_as_int(v), ctl, 0xf, 0xf, true))
        cm = fmaxf(cm, DPPF(cm, 0x121)); cm = fmaxf(cm, DPPF(cm, 0x122)); cm = fmaxf(cm, DPPF(cm, 0x124)); cm = fmaxf(cm, DPPF(cm, 0x128));
#undef DPPF
        { auto r = __builtin_amdgcn_permlane16_swap(__float_as_uint(cm), __float_as_uint(cm), false, false); cm = fmaxf(__uint_as_float(r[0]), __uint_as_float(r[1]));
          auto q = __builtin_amdgcn_permlane32_swap(__float_as_uint(cm), __float_as_uint(cm), false, false); cm = fmaxf(fmaxf(__uint_as_float(q[0]), __uint_as_float(q[1])), 1e-30f); }
        struct C2 { f32x2 x, y; };
        auto cexp = [&](float p_) { const f32x2 e = ar * p_, t = ai * p_;
            const f32x2 mg = {__builtin_amdgcn_exp2f(e.x), __builtin_amdgcn_exp2f(e.y)}; const float t0 = __builtin_amdgcn_fractf(t.x), t1 = __builtin_amdgcn_fractf(t.y);
            return C2{mg * (f32x2){__builtin_amdgcn_cosf(t0), __builtin_amdgcn_cosf(t1)}, mg * (f32x2){__builtin_amdgcn_sinf(t0), __builtin_amdgcn_sinf(t1)}}; };
        auto cmul = [](C2 a, C2 b) { return C2{__builtin_elementwise_fma(a.x, b.x, -(a.y * b.y)), __builtin_elementwise_fma(a.x, b.y, a.y * b.x)}; };
        auto put = [&](int row, C2 v, bool conj) { tw[row * 66] = conj ? (u32x2){pk_f16(v.x.x, -v.y.x), pk_f16(v.x.y, -v.y.y)} : (u32x2){pk_f16(v.x.x, v.y.x), pk_f16(v.x.y, v.y.y)}; };
        C2 wp[16], zr[8], vp[4], cv[17];
        wp[1] = cexp(8.f); zr[1] = cexp(1.f); vp[0] = cexp(128.f);
        tw[0] = (u32x2){pk_f16(1.f, 0.f), pk_f16(1.f, 0.f)};
#pragma unroll
        for (int j = 1; j < 16; ++j) { if (j > 1) wp[j] = cmul(wp[j >> 1], wp[j - (j >> 1)]); put(j, wp[j], true); }
        CFENCE();
        u32x4 fb[8];
#pragma unroll
        for (int ks = 0; ks < 8; ++ks) fb[ks] = *(const LAS u32x4*)(trd + ks * 64);
        CFENCE();
#pragma unroll
        for (int r = 2; r < 8; ++r) zr[r] = cmul(zr[r >> 1], zr[r - (r >> 1)]);
#pragma unroll
        for (int k = 1; k < 4; ++k) vp[k] = cmul(vp[k - 1], vp[k - 1]);
        const float rc = __builtin_amdgcn_rcpf(cm);
        cv[0] = C2{cr * rc, ci * rc};
#pragma unroll
        for (int k = 1; k < 8; ++k) { const int lg = k >= 4 ? 2 : k >= 2 ? 1 : 0; cv[k] = cmul(cv[k - (1 << lg)], vp[lg]); }
        LAS float* kb_ = (LAS float*)(lds + L_KD + kc * 3072);
        const float sc = 2.0f * cm;
        u32x4 fa[3][8];
#pragma unroll
        for (int tt = 0; tt < 3; ++tt) {
#pragma unroll
            for (int i = 0; i < (tt == 2 ? 9 : 16); ++i) {
                const int r = tt == 0 ? (i & 7) : tt == 1 ? (i < 8 ? 2 * (i & 3) : 4 * (i & 1)) : 0;
                const int hh = tt == 0 ? (i >> 3) : tt == 1 ? (i < 8 ? 2 + (i >> 2) : 4 + ((i - 8) >> 1)) : 8 + i;
                if (tt == 2) cv[hh] = cmul(cv[hh - 8], vp[3]);
                put(i, r == 0 ? cv[hh] : cmul(cv[hh], zr[r]), false);
            }
            CFENCE();
#pragma unroll
            for (int ks = 0; ks < 8; ++ks) fa[tt][ks] = *(const LAS u32x4*)(trd + ks * 64);
            CFENCE();
            SB0();
        }
        f32x4 kd[3] = {{0.f, 0.f, 0.f, 0.f}, {0.f, 0.f, 0.f, 0.f}, {0.f, 0.f, 0.f, 0.f}};
#pragma unroll
        for (int ks = 0; ks < 8; ++ks)
#pragma unroll
            for (int tt = 0; tt < 3; ++tt) kd[tt] = __builtin_amdgcn_mfma_f32_16x16x32_f16(__builtin_bit_cast(f16x8, fa[tt][ks]), __builtin_bit_cast(f16x8, fb[ks]), kd[tt], 0, 0, 0);
#pragma unroll
        for (int tt = 0; tt < 3; ++tt) {
            LAS float* kw = opq(kb_ + (tt == 0 ? 128 * (fq >> 1) + 4 * (fq & 1) + 8 * fr : tt == 1 ? 256 + 64 * fq + (fq < 2 ? 4 : 2) * fr : 512 + 64 * fq + fr));
            LAS float* kwb = tt == 1 ? opq(kw + (fq < 2 ? 2 : 32)) : kw + (tt == 0 ? 2 : 32);
            kw[0] = kd[tt][0] * sc; kw[tt == 2 ? 16 : 1] = kd[tt][1] * sc; kwb[0] = kd[tt][2] * sc; kwb[tt == 2 ? 16 : 1] = kd[tt][3] * sc;
        }
        WAITL0();
    }
    if (wid == 6) { WAITV(0); asm volatile("" :: "v"(pfw)); }
    if (is_prod) {
        const unsigned vx = (unsigned)(lane >> 3) * 1152u + rsw;
        unsigned wv[9];
        const char* xb = (const char*)x + (size_t)(pm * 128 + p * 8) * 1152;
        const char* wb = (const char*)win + pn * 288;
        auto issueX = [&](int k, int s) {
#pragma unroll
            for (int i = 0; i < 8; ++i) GLDS(xb + (size_t)i * (16 * 1152) + k * 128 + vx, lds + s + (p + 2 * i) * 1024, 0); };
        auto issueW = [&](int k, int coloff, int s) {
            const char* b = wb + (size_t)k * (64 * 9216) + coloff;
#pragma unroll
            for (int i = 0; i < 9; ++i) GLDS(b + wv[i], lds + s + BOFF + (p + 2 * i) * 1024, 0); };
        auto ord1 = [&](int s) { return ((s & 3) == phi) ? s : (s >= 4 ? s - 4 : (s == 0 ? 8 : s + 4)); };
        issueX(ord1(0), 0);
        SB0();
#pragma unroll
        for (int i = 0; i < 9; ++i) { const int c = (p + 2 * i) * 64 + lane, rho = c / 18, ch = c - rho * 18;
            const int kap = (rho & 32) + 8 * ((rho >> 2) & 3) + 4 * ((rho >> 4) & 1) + (rho & 3); wv[i] = (unsigned)kap * 9216u + (unsigned)ch * 16u; }
        issueW(ord1(0), FH * 2, 0); issueX(ord1(1), STG); issueW(ord1(1), FH * 2, STG);
        issueX(ord1(2), 2 * STG); issueW(ord1(2), FH * 2, 2 * STG);
        E = flag_ld(ws, WS_E);
        unsigned wm_ = 0;
        auto touch = [&](const void* base, unsigned off, unsigned last) { const char* q_ = (const char*)base + min(off + lane * 64u, last); asm volatile("global_load_dword %0, %1, off" : "+v"(wm_) : "v"(q_) : "memory"); };
        {
#pragma unroll
            for (int i = 0; i < 2; ++i) touch(x, bid * 9216u + p * 4608u + i * 4096u, bid * 9216u + p * 4608u + 4544u);
#pragma unroll
            for (int i = 0; i < 3; ++i) touch(win, bid * 20736u + p * 10368u + i * 4096u, bid * 20736u + p * 10368u + 10304u);
        }
        int so = 0;
        WAITV(35); BAR();
#pragma nounroll
        for (int S = 1; S < 16; ++S) {
            if (S >= 8 && S <= 10) WAITV(9); else WAITV(17);
            BAR();
            const int t = S + 2, k = ord1(t < 9 ? t : t < 12 ? t - 3 : t - 12);
            if (t < 9 || t >= 12) issueX(k, so);
            issueW(k, t >= 9 ? 0 : FH * 2, so); so = nextbuf(so);
            if (S == 8) {
#pragma unroll
                for (int i = 0; i < 4; ++i) touch(wsp, bid * 32768u + p * 16384u + i * 4096u, 0x7fffffffu);
            }
        }
        WAITV(17); BAR(); issueS(2 * pm, 0);
        WAITV(8); BAR(); issueS(2 * pm + 1, STG);
        asm volatile("" :: "v"(wm_));
    } else if (wid < 4) {
        E = flag_ld(ws, WS_E);
        if (wid >> 1) consumer_a<4>(lds, zp, late, wid & 1, 1, lane);
        else consumer_a<5>(lds, zp, late, wid & 1, 0, lane);
    } else {
        if (h == 0 && lane < 12) *(LAS unsigned*)(lds + L_MISC + lane * 4) = 0u;
        const int row0 = lane / 9, chk0 = lane - row0 * 9;
        const int u = (pn >> 3) * 16 + pm, nblk = u < 8 ? 3 : 2;
        u32x2 uc[18]; u32x4 xv[9], gv[9], wv[3][5];
        if (h == 0) {
            { const char* xb = (const char*)x + (size_t)pm * 128 * 1152 + pn * 72; unsigned off = row0 * 1152 + chk0 * 8; int ch = chk0;
#pragma unroll
              for (int i = 0; i < 9; ++i) { uc[i] = *(const u32x2*)(xb + off); uc[9 + i] = *(const u32x2*)(xb + 64 * 1152 + off); off += 7 * 1152 + 8; if (++ch == 9) { ch = 0; off += 1152 - 72; } } }
            { const char* xr = (const char*)x + (size_t)(pm * 128 + pn * 8 + (lane >> 3)) * 1152 + (lane & 7) * 16; const char* gr = (const char*)gw + (lane & 7) * 16;
#pragma unroll
              for (int i = 0; i < 9; ++i) { xv[i] = *(const u32x4*)(xr + i * 128); gv[i] = *(const u32x4*)(gr + i * 128); } }
        } else {
            unsigned wo_[5], hi_[5];
            { int row = row0, ch = chk0;
#pragma unroll
              for (int i = 0; i < 5; ++i) { const int rw = row < 32 ? row : 31; wo_[i] = (rw & 15) * 1152 + ch * 16; hi_[i] = rw >> 4; row += 7; if (++ch == 9) { ch = 0; ++row; } } }
#pragma unroll
            for (int b = 0; b < 3; ++b) {
                const int blk = (b < nblk) ? u + 32 * b : u, st = blk >> 2, jb = blk & 3;
                const int fA = st < 16 ? st * 144 + (jb < 2 ? 32 * jb : 16 + 32 * jb) : ((st - 16) * 8 + 2 * jb) * 144 + 64, dB = (st < 16 ? 16 : 144) * 1152;
                const char* wb_ = (const char*)wout + (size_t)fA * 1152 + qn * 144;
#pragma unroll
                for (int i = 0; i < 5; ++i) wv[b][i] = *(const u32x4*)(wb_ + (wo_[i] + hi_[i] * dB));
            }
        }
        E = flag_ld(ws, WS_E);
        if (lane < 24) *(LAS u32x4*)(lds + L_KT + (lane >> 3) * KT_SZ + (2 * h + ((lane >> 2) & 1)) * 1088 + (lane & 3) * 16) = (u32x4){0u, 0u, 0u, 0u};
        WAITL0();
        __builtin_amdgcn_s_setprio(1);
        {
            float kv[2][3];
#pragma nounroll
            for (int ei = 0; ei < 9; ++ei) {
                BAR();
                if (ei > 0) {
                    LAS unsigned char* kt = opq(lds + L_KT + (2 * h + ((ei - 1) >> 2)) * 1088 + 64 + ((ei - 1) & 3) * 256 + lane * 2);
#pragma unroll
                    for (int j = 0; j < 2; ++j)
#pragma unroll
                        for (int c = 0; c < 3; ++c) *(LAS u16*)(kt + c * KT_SZ + j * 128) = (u16)pk_bf16(kv[j][c], 0.f);
                }
#pragma unroll
                for (int j = 0; j < 2; ++j) {
                    const int jj = (ei & 3) * 2 + j, e = jj >= 4 ? 3 : jj >= 2 ? 2 : jj, lag = 256 * jj + 4 * lane + 2 * h + ((ei >> 2) & 1);
                    const int idx = e == 0 ? lag : 128 * e + ((lag + (1 << (e - 1)) - 1 + ((lag >> e) & 1)) >> e);
                    const LAS float* q = opq((const LAS float*)(lds + L_KD) + idx);
#pragma unroll
                    for (int c = 0; c < 3; ++c) kv[j][c] = q[c * 768];
                }
            }
        }
        if (h == 0) {
            LAS unsigned char* pzs = opq(lds + Z0 + lane * 8);
#pragma unroll
            for (int i = 0; i < 18; ++i) *(LAS u32x2*)(pzs + i * 512) = uc[i];
            CFENCE();
            { const LAS unsigned char* pt = opq(lds + Z0 + (8 * (4 * fq + (lane & 3)) + (fr >> 2)) * 72);
              const unsigned ub = WS_UT + (unsigned)(pn * 36 + (lane & 3)) * 4096u + (unsigned)(pm * 128 + 8 * (4 * fq + (fr >> 2))) * 2u;
#pragma unroll
              for (int cc = 0; cc < 9; ++cc) __builtin_amdgcn_raw_buffer_store_b128(__builtin_bit_cast(u32x4, cat8(tr_read(pt + cc * 8), tr_read(pt + 288 + cc * 8))), ws, ub + cc * 16384u, 0, 16); }
            float a = 0.f;
#pragma unroll
            for (int i = 0; i < 9; ++i)
#pragma unroll
                for (int j = 0; j < 4; ++j) { const unsigned xa = xv[i][j], ga = gv[i][j]; a = __builtin_amdgcn_fdot2_f32_bf16(__builtin_bit_cast(bf16x2, xa), __builtin_bit_cast(bf16x2, ga), a, false); }
#define DPPF(v, ctl) __int_as_float(__builtin_amdgcn_update_dpp(0, __float_as_int(v), ctl, 0xf, 0xf, true))
            a += DPPF(a, 0xb1); a += DPPF(a, 0x4e); a += DPPF(a, 0x141);
#undef DPPF
            const float gt = rbf(__builtin_amdgcn_rcpf(1.0f + __expf(-rbf(a))));
            if ((lane & 7) == 0) __builtin_amdgcn_raw_buffer_store_b32(__float_as_uint(gt), ws, WS_GATE + (unsigned)(pm * 128 + pn * 8 + (lane >> 3)) * 4u, 0, 16);
        } else {
            LAS unsigned char* pws = opq(lds + Z0 + 9216 + lane * 16);
            const LAS unsigned char* ptw = opq(lds + Z0 + 9216 + (4 * fq + (fr >> 2)) * 144 + (lane & 3) * 8);
#pragma unroll
            for (int b = 0; b < 3; ++b) {
                if (b < nblk) {
#pragma unroll
                    for (int i = 0; i < 5; ++i) if (i < 4 || lane < 32) *(LAS u32x4*)(pws + i * 1024) = wv[b][i];
                    CFENCE();
                    const unsigned ob = WS_WOF + (unsigned)qn * (18u * 18432u) + (unsigned)(u + 32 * b) * 4608u;
#pragma unroll
                    for (int nn = 0; nn < 5; ++nn) {
                        const u32x4 v = __builtin_bit_cast(u32x4, cat8(tr_read(ptw + nn * 32), tr_read(ptw + 2304 + nn * 32)));
                        if (nn < 4) __builtin_amdgcn_raw_buffer_store_b128(v, ws, ob + nn * 1024 + lane * 16, 0, 16);
                        else if (fr < 8) __builtin_amdgcn_raw_buffer_store_b128(v, ws, ob + 4096 + (fq * 8 + fr) * 16, 0, 16);
                    }
                    CFENCE();
                }
            }
        }
        nb = 9;
        mk_tag();
        WAITL0();
        STEPS_TO(10);
        {
            const f32x4 sp = *(const LAS f32x4*)(lds + L_SP + (h * 64 + lane) * 16);
            const float dm = sp[0] - sp[2];
            const f32x2 mine = {(80.f * sp[0] + 64.f * sp[2]) * (1.0f / 144.f), sp[1] + sp[3] + (80.f * 64.f / 144.f) * dm * dm};
            __builtin_amdgcn_raw_buffer_store_b128((u32x4){T[0], __float_as_uint(mine.x), T[1], __float_as_uint(mine.y)}, ws, WS_ST + (unsigned)((pm * 16 + pn) * 128 + h * 64 + lane) * 16u, 0, 16);
            STEP(18);
            const unsigned so_ = WS_ST + (unsigned)(pm * 16 * 128 + h * 64 + lane) * 16u;
            int t0 = 0;
            u32x4 st[16];
            for (;;) {
                CFENCE();
#pragma unroll
                for (int j = 0; j < 16; ++j) st[j] = __builtin_amdgcn_raw_buffer_load_b128(ws, so_ + j * 2048u, 0, 16);
                STEP(18);
                unsigned bad = 0;
#pragma unroll
                for (int j = 0; j < 16; ++j) bad |= (st[j][0] ^ T[0]) | (st[j][2] ^ T[1]);
                if (__builtin_amdgcn_ballot_w64(bad != 0u) == 0ull) break;
                if (++t0 > SPIN_G) break;
            }
            STEP(18);
            float mean = 0.f, m2 = 0.f;
#pragma unroll
            for (int j = 0; j < 16; ++j) mean += __uint_as_float(st[j][1]);
            mean *= (1.0f / 16.f);
#pragma unroll
            for (int j = 0; j < 16; ++j) { const float d = __uint_as_float(st[j][1]) - mean; m2 += __uint_as_float(st[j][3]) + 144.f * d * d; }
            const float rstd = __builtin_amdgcn_rsqf(m2 * (1.0f / FH) + 1e-5f);
            *(LAS f32x2*)(lds + L_SP + (h * 64 + lane) * 16) = (f32x2){rstd, -mean * rstd};
            CFENCE();
            if (lane == 0) *(volatile LAS unsigned*)(lds + L_MISC + 4 + h * 4) = 1u;
            const volatile LAS unsigned* zd = (const volatile LAS unsigned*)(lds + L_MISC + 16 + h * 8 + (lane & 1) * 4);
            for (;;) {
                STEP(18);
                if (__builtin_amdgcn_ballot_w64(*zd == 0u) == 0ull) break;
                if (++t0 > SPIN_L) break;
            }
            CFENCE();
            const LAS unsigned char* ptz = opq(lds + Z0 + (h * 64 + 8 * fq + (fr >> 2)) * ZST + (lane & 3) * 8);
            const unsigned zo = WS_Z2F + (unsigned)((pn * 64 + pm * 4 + h * 2) * 9) * 1024u + lane * 16;
            u32x4 zf[18];
#pragma unroll
            for (int i = 0; i < 18; ++i) {
                const LAS unsigned char* pt = ptz + (i / 9) * 32 * ZST + (i % 9) * 32;
                zf[i] = __builtin_bit_cast(u32x4, cat8(tr_read(pt), tr_read(pt + 4 * ZST)));
                __builtin_amdgcn_raw_buffer_store_b128(zf[i], ws, zo + i * 1024, 0, 16);
                if (i == 5 || i == 11) STEP(18);
            }
            STEP(18);
            flag_publish(ws, WS_FZ + (unsigned)(pn * 32 + pm * 2 + h) * 128u, T, lane);
            STEPS_TO(18);
            LAS unsigned char* pr_ = opq(lds + h * STG + BOFF + lane * 16);
#pragma unroll
            for (int i = 0; i < 18; ++i) *(LAS u32x4*)(pr_ + i * 1024) = zf[i];
            WAITL0();
        }
    }

    const char* lin = (const char*)wsp_ + p * 1024 + lane * 16;
    auto issueLA = [&](unsigned off, int s) {
#pragma unroll
        for (int i = 0; i < 8; ++i) GLDS(lin + off + i * 2048, lds + s + (p + 2 * i) * 1024, 16); };
    auto issueLB = [&](unsigned off, int s) {
#pragma unroll
        for (int i = 0; i < 9; ++i) GLDS(lin + off + i * 2048, lds + s + BOFF + (p + 2 * i) * 1024, 16); };
    auto ord3 = [&](int s) { return (s >= 16 || (s & 3) == phi) ? s : ((s - 4) & 15); };
    u32x2 pv = {0u, 0u}; unsigned rdy = 0; bool pend = false;
    const char* fp = (const char*)wsp_ + WS_FZ + ((unsigned)pn * 32u + (lane & 31)) * 128u;
    auto poll_issue = [&]() { asm volatile("global_load_dwordx2 %0, %1, off sc1" : "+v"(pv) : "v"(fp) : "memory"); };
    auto poll_take = [&]() { asm volatile("" : "+v"(pv)); rdy |= (unsigned)__builtin_amdgcn_ballot_w64(pv[0] == T[0] && pv[1] == T[1]); };
    auto need = [&](unsigned bits) {
        if (pend) { poll_take(); pend = false; }
        if ((rdy & bits) != bits) {
            int t0 = 0;
            do { poll_issue(); WAITV(0); poll_take(); } while ((rdy & bits) != bits && ++t0 < SPIN_G);
        }
        if (rdy != ~0u) { poll_issue(); pend = true; }
    };
    const unsigned hb = WS_HF + (unsigned)qm * (18u * 16384u), ob = WS_WOF + (unsigned)qn * (18u * 18432u);
    if (is_prod) {
        mk_tag();
        const unsigned zb = WS_Z2F + (unsigned)pn * (64u * 9216u);
        int so = 2 * STG;
        const int r0 = (ord2(0) >> 1) == pm ? ord2(1) : ord2(0), r1 = (ord2(1) >> 1) == pm ? ord2(0) : ord2(1);
        poll_issue(); WAITV(0); poll_take();
#pragma nounroll
        for (int S = 18; S < 48; ++S) {
            BAR();
            int k = ord2(S - 16); if ((k >> 1) == pm) k = (k & 1) ? r1 : r0;
            need(1u << k); issueS(k, so); issueLB(zb + (unsigned)k * 18432u, so); so = nextbuf(so);
            WAITV(17);
        }
        BAR();
        fp = (const char*)wsp_ + WS_FZ + ((unsigned)(pn ^ 8) * 32u + (lane & 31)) * 128u; WAITV(0); rdy = 0; pend = false;
        need(~0u);
        issueLB(ob + (unsigned)ord3(0) * 18432u, 2 * STG);
        BAR();
        issueLB(ob + (unsigned)ord3(1) * 18432u, 0);
    } else
    if (wid < 4) {
        mk_tag();
        if (wid >> 1) consumer_b<4>(lds, ws, T, zp, bs, pm, pn, wid & 1, 1, lane);
        else consumer_b<5>(lds, ws, T, zp, bs, pm, pn, wid & 1, 0, lane);
    } else {
        __builtin_amdgcn_s_setprio(1);
        {   const unsigned fo = WS_FZ + (unsigned)(pn * 32 + (lane & 31)) * 128u;
            int t0 = 0;
            for (;;) {
                STEP(43);
                CFENCE();
                const u32x2 v = flag_ld(ws, fo);
                STEP(43);
                if (flag_all(v, T, true)) break;
                if (++t0 > SPIN_G) break;
            } }
        u32x4 uu[3][2];
#pragma unroll
        for (int c = 0; c < 3; ++c)
#pragma unroll
            for (int j = 0; j < 2; ++j) uu[c][j] = __builtin_amdgcn_raw_buffer_load_b128(ws, WS_UT + (unsigned)chan(c) * 4096u + h * 2048 + j * 1024 + lane * 16, 0, 16);
#pragma unroll
        for (int c = 0; c < 3; ++c) if (lane < 16) *(LAS u32x4*)(lds + L_UT + c * UT_SZ + h * 4352 + lane * 16) = (u32x4){0u, 0u, 0u, 0u};
        BAR(); BAR(); BAR(); nb += 3;
        LAS unsigned char* pu_ = opq(lds + L_UT + 256 + h * 2048 + lane * 16);
#pragma unroll
        for (int c = 0; c < 3; ++c)
#pragma unroll
            for (int j = 0; j < 2; ++j) *(LAS u32x4*)(pu_ + c * UT_SZ + j * 1024) = uu[c][j];
        CFENCE();
        if (lane == 0) *(volatile LAS unsigned*)(lds + L_MISC + 32 + h * 4) = 1u;
        STEP(50);
        {   const volatile LAS unsigned* uw = (const volatile LAS unsigned*)(lds + L_MISC + 32 + (lane & 1) * 4);
            int t0 = 0;
            while (__builtin_amdgcn_ballot_w64(*uw == 0u) != 0ull && ++t0 < SPIN_L) STEP(50);
            CFENCE(); }
    }

    if (wid < 6) nb = 50;
    while (!is_prod || nch == 3) {
        if (nb > 45) STEPS_TO(50);
        STEP(50);
        __builtin_amdgcn_wave_barrier();
        unsigned tk = 0;
        if (lane == 0) tk = __hip_atomic_fetch_add((LAS unsigned*)(lds + L_MISC), 1u, __ATOMIC_RELAXED, __HIP_MEMORY_SCOPE_WORKGROUP);
        __builtin_amdgcn_wave_barrier();
        tk = __builtin_amdgcn_readfirstlane(tk);
        if (tk >= (unsigned)(4 * nch)) break;
        const int c = tk >> 2, tl = tk & 1;
        if (tk & 2) conv_task<0, 10>(lds, L_KT + c * KT_SZ, L_UT + c * UT_SZ, ytab(c), tl, lane, nb);
        else conv_task<11, 15>(lds, L_KT + c * KT_SZ, L_UT + c * UT_SZ, ytab(c), tl, lane, nb);
    }
    STEPS_TO(50);
    WAITL0();
    if (is_prod) {
        fp = (const char*)wsp_ + WS_FH + ((unsigned)qm * 32u + (lane & 31)) * 128u; rdy = 0;
        need(~0u);
        issueLA(hb + (unsigned)ord3(0) * 16384u, 2 * STG); issueLA(hb + (unsigned)ord3(1) * 16384u, 0);
        int so = STG;
        WAITV(8);
#pragma nounroll
        for (int S = 50; S < 66; ++S) {
            BAR();
            const int k = ord3(S - 48); issueLA(hb + (unsigned)k * 16384u, so); issueLB(ob + (unsigned)k * 18432u, so); so = nextbuf(so);
            WAITV(17);
        }
        BAR();
        WAITV(0); BAR();
        if (bid == 0 && p == 0 && lane == 0) __builtin_amdgcn_raw_buffer_store_b64(T, ws, WS_E, 0, 16);
        return;
    }


    if (wid < 4) {
        f32x4 acc[5];
        struct F3 { s16x4 alo[4], ahi[4]; bf16x8 b[20]; } f0, f1;
        const LAS unsigned char* ph = lds + 2 * STG + wid * 4096 + lane * 16;
        const LAS unsigned char* pw = lds + 2 * STG + BOFF + lane * 16;
        const LAS unsigned char* pw4 = lds + 2 * STG + BOFF + 4096 + (fq * 8 + (fr & 7)) * 16;
        auto rd3 = [&](F3& f, int i, bool single) {
            const int j = i / 6, r = i % 6;
            if (r == 0) {
                if (!single) { const bf16x8 v = *(const LAS bf16x8*)(ph + j * 1024); f.alo[j] = __builtin_shufflevector(v, v, 0, 1, 2, 3); f.ahi[j] = __builtin_shufflevector(v, v, 4, 5, 6, 7); }
                else { f.alo[j] = *(const LAS s16x4*)(ph + j * 1024); f.ahi[j] = *(const LAS s16x4*)(ph + j * 1024 + 512); }
            } else if (r < 5) f.b[j * 5 + r - 1] = *(const LAS bf16x8*)(pw + j * 4608 + (r - 1) * 1024);
            else f.b[j * 5 + 4] = *(const LAS bf16x8*)(pw4 + j * 4608);
        };
        int so = 2 * STG;
        auto step3 = [&](const F3& c, F3& l, int mode  , bool first) {
            const int delta = so == 2 * STG ? -2 * STG : STG; so += delta;
#pragma unroll
            for (int i = 0; i < 20; ++i) {
                const int j = i / 5, n = i % 5;
                acc[n] = MFMA16(c.b[i], cat8(c.alo[j], c.ahi[j]), (first && j == 0) ? ((f32x4){0.f, 0.f, 0.f, 0.f}) : acc[n]);
                if (mode < 2 && i < 12) { rd3(l, 2 * i, mode == 1); rd3(l, 2 * i + 1, mode == 1); }
                if (mode < 2) { if (i == 13) { ph += delta; asm volatile("" : "+v"(ph)); } if (i == 14) { pw += delta; asm volatile("" : "+v"(pw)); } if (i == 15) { pw4 += delta; asm volatile("" : "+v"(pw4)); } }
                SB0();
            }
        };
        __builtin_amdgcn_s_waitcnt(0xC07F);
        BAR();
#pragma unroll
        for (int i = 0; i < 24; ++i) rd3(f0, i, false);
        ph -= 2 * STG; pw -= 2 * STG; pw4 -= 2 * STG; so = 0;
        WAITL0(); BAR(); step3(f0, f1, 0, true);
#pragma nounroll
        for (int j = 0; j < 7; ++j) {
            WAITL0(); BAR(); step3(f1, f0, 0, false);
            WAITL0(); BAR(); step3(f0, f1, 0, false);
        }
        ph = opq(ph - lane * 8);
        WAITL0(); BAR(); step3(f1, f0, 1, false);
        WAITL0(); BAR();
        const float g = *(const LAS float*)(lds + L_MISC + 64 + (16 * wid + fr) * 4);
        const LAS unsigned char* pat = opq(lds + L_AT + (4 * fq + (fr >> 2)) * 128 + (16 * wid + 4 * (lane & 3)) * 2);
        u32x2 av[5];
#pragma unroll
        for (int n = 0; n < 5; ++n) av[n] = __builtin_bit_cast(u32x2, tr_read(pat + n * 2048));
        step3(f0, f1, 1, false);
        step3(f1, f0, 2, false);
        const rsrc_t ors = __builtin_amdgcn_make_buffer_rsrc(out, 0, SEQ * DM * 2, 0x00020000);
        const float g1 = rbf(1.0f - g);
#pragma unroll
        for (int n = 0; n < 5; ++n) {
            const f32x4 b = acc[n]; const u32x2 a = av[n];
            const u32x2 o = {pk_bf16(g * bflo(a[0]) + g1 * b[0], g * bfhi(a[0]) + g1 * b[1]), pk_bf16(g * bflo(a[1]) + g1 * b[2], g * bfhi(a[1]) + g1 * b[3])};
            if (n < 4 || fq < 2) __builtin_amdgcn_raw_buffer_store_b64(o, ors, (unsigned)((qm * 64 + 16 * wid + fr) * 1152 + (qn * 72 + 16 * n + 4 * fq) * 2), 0, 0);
        }
    } else {
#pragma nounroll
        for (int c = 0; c < nch; ++c) {
            const int d = chan(c);
            const float dsk = bf2f(Dsk[d]);
#pragma unroll
            for (int k = 0; k < 2; ++k) {
                BAR(); ++nb;
                const int l0 = h * 1024 + k * 512 + lane * 8;
                f32x4 cv = *(const LAS f32x4*)(lds + ytab(c) + 8192 + (l0 >= 16 ? (l0 >> 4) - 1 : 0) * 16);
                if ((lane & 1) || l0 == 0) cv = (f32x4){0.f, 0.f, 0.f, 0.f};
                const f32x4 y0 = *(const LAS f32x4*)(lds + ytab(c) + l0 * 4) + cv, y1 = *(const LAS f32x4*)(lds + ytab(c) + l0 * 4 + 16);
                const u32x4 uv = *(const LAS u32x4*)(lds + L_UT + c * UT_SZ + 256 + l0 * 2);
                u32x4 o;
#pragma unroll
                for (int j = 0; j < 4; ++j) {
                    const float ya = j < 2 ? y0[2 * j] : y1[2 * j - 4], yb = j < 2 ? y0[2 * j + 1] : y1[2 * j - 3];
                    const unsigned yp = pk_bf16(ya, yb), up = pk_bf16(bflo(uv[j]) * dsk, bfhi(uv[j]) * dsk);
                    o[j] = pk_bf16(bflo(yp) + bflo(up), bfhi(yp) + bfhi(up));
                }
                __builtin_amdgcn_raw_buffer_store_b128(o, ws, WS_AT + (unsigned)d * 4096u + l0 * 2, 0, 16);
            }
        }
        STEPS_TO(58);
        flag_publish(ws, WS_FA + (unsigned)((pn >> 1) * 64 + (pn & 1) * 32 + pm * 2 + h) * 128u, T, lane);
        STEPS_TO(61);
        if (h == 0) {
            const unsigned fo = WS_FA + (unsigned)(qn * 64 + lane) * 128u;
            int t0 = 0;
            for (;;) {
                CFENCE();
                const u32x2 v = flag_ld(ws, fo);
                STEP(67); STEP(67);
                if (flag_all(v, T, true)) break;
                if (++t0 > SPIN_G) break;
            }
            u32x4 av[9];
#pragma unroll
            for (int i = 0; i < 9; ++i) av[i] = __builtin_amdgcn_raw_buffer_load_b128(ws, WS_AT + (unsigned)(qn * 72 + i * 8 + (lane >> 3)) * 4096u + qm * 128 + (lane & 7) * 16, 0, 16);
            STEP(67); STEP(67);
            LAS unsigned char* pa_ = opq(lds + L_AT + lane * 16);
#pragma unroll
            for (int i = 0; i < 9; ++i) *(LAS u32x4*)(pa_ + i * 1024) = av[i];
        } else {
            STEPS_TO(64);
            const unsigned gv = __builtin_amdgcn_raw_buffer_load_b32(ws, WS_GATE + (unsigned)(qm * 64 + lane) * 4u, 0, 16);
            BAR(); BAR(); nb += 2;
            *(LAS unsigned*)(lds + L_MISC + 64 + lane * 4) = gv;
        }
        STEPS_TO(67);
        WAITL0();
        BAR();
    }
}
}

extern "C" void kernel_launch(void* const* d_in, const int* in_sizes, int n_in, void* d_out, int out_size, void* d_ws, size_t ws_size, hipStream_t stream) {
    (void)in_sizes; (void)n_in; (void)out_size; (void)ws_size;
    (void)hipFuncSetAttribute((const void*)gm::gm_kernel, hipFuncAttributeMaxDynamicSharedMemorySize, gm::LDS_BYTES);
    hipLaunchKernelGGL(gm::gm_kernel, dim3(256), dim3(512), gm::LDS_BYTES, stream, (const u16*)d_in[0], (const u16*)d_in[1], (const u16*)d_in[2], (const u16*)d_in[3],
                       (const u16*)d_in[4], (const u16*)d_in[5], (const u16*)d_in[6], (const u16*)d_in[8], (const u16*)d_in[9], (const u16*)d_in[10],
                       (const u16*)d_in[11], (const u16*)d_in[12], (u16*)d_out, (unsigned char*)d_ws);
}
```

```cpp
#include <hip/hip_runtime.h>
#include <stdint.h>

#define LAS __attribute__((address_space(3)))
typedef unsigned short u16;
typedef unsigned long long u64;
typedef short bf16x8 __attribute__((ext_vector_type(8)));
typedef short s16x4 __attribute__((ext_vector_type(4)));
typedef _Float16 f16x8 __attribute__((ext_vector_type(8)));
typedef _Float16 f16x2 __attribute__((ext_vector_type(2)));
typedef __bf16 bf16x2 __attribute__((ext_vector_type(2)));
typedef float f32x4 __attribute__((ext_vector_type(4)));
typedef float f32x2 __attribute__((ext_vector_type(2)));
typedef unsigned u32x4 __attribute__((ext_vector_type(4)));
typedef unsigned u32x2 __attribute__((ext_vector_type(2)));
typedef __amdgpu_buffer_rsrc_t rsrc_t;

namespace gm {
constexpr int SEQ = 2048, DM = 576, FH = 2304;
constexpr unsigned WS_E = 0, WS_FZ = 4096, WS_FH = WS_FZ + 65536, WS_FA = WS_FH + 131072;
constexpr unsigned WS_ST = 0x60000, WS_GATE = 0xE0000, WS_Z2F = 0x100000, WS_HF = 0xA00000, WS_WOF = 0x1300000, WS_UT = 0x1600000, WS_AT = 0x1900000, WS_END = 0x1C00000;
constexpr int STG = 34816, BOFF = 16384, Z0 = 3 * STG, ZST = 296, T0 = Z0 + 128 * ZST;
constexpr int KT_SZ = 4352, UT_SZ = 4608, Y_SZ = 10240;
constexpr int L_MISC = 163840 - 320;
constexpr int L_KT = L_MISC - 3 * KT_SZ, L_SP = T0, L_KD = Z0 + 3 * 8448, L_UT = Z0 + 3 * Y_SZ;
constexpr int L_AT = Z0;
constexpr int SPIN_G = 8192, SPIN_L = 1 << 20;
constexpr int LDS_BYTES = 163840;
static_assert(L_UT + 3 * UT_SZ <= L_KT && L_KD + 3 * 3072 <= T0 && L_SP + 2048 <= L_KT, "lds map");
constexpr int CODE_LINES = 13, CODE_BYTES = 50176;
constexpr int CONV_SLICE = 11;

__device__ __forceinline__ float bf2f(unsigned v) { return __uint_as_float(v << 16); }
__device__ __forceinline__ float bflo(unsigned w) { return __uint_as_float(w << 16); }
__device__ __forceinline__ float bfhi(unsigned w) { return __uint_as_float(w & 0xffff0000u); }
__device__ __forceinline__ unsigned pk_bf16(float a, float b) { f32x2 v = {a, b}; return __builtin_bit_cast(unsigned, __builtin_convertvector(v, bf16x2)); }
__device__ __forceinline__ unsigned pk_f16(float a, float b) { f32x2 v = {a, b}; return __builtin_bit_cast(unsigned, __builtin_convertvector(v, f16x2)); }
__device__ __forceinline__ float rbf(float a) { return bflo(pk_bf16(a, a)); }
__device__ __forceinline__ f32x2 gelu2(f32x2 x) {
    const f32x2 w = (x * x) * (-0.10294324f) + (-2.30220819f);
    const f32x2 a = w * x;
    f32x2 e; e.x = __builtin_amdgcn_exp2f(a.x); e.y = __builtin_amdgcn_exp2f(a.y);
    e = e + 1.0f;
    f32x2 r; r.x = __builtin_amdgcn_rcpf(e.x); r.y = __builtin_amdgcn_rcpf(e.y);
    return x * r;
}
__device__ __forceinline__ f32x4 gelu4(f32x4 v) { const f32x2 a = gelu2((f32x2){v[0], v[1]}), b = gelu2((f32x2){v[2], v[3]}); return (f32x4){a.x, a.y, b.x, b.y}; }
__device__ __forceinline__ float allred_q(float v) {
    unsigned u = __float_as_uint(v);
    auto r = __builtin_amdgcn_permlane16_swap(u, u, false, false);
    v = __uint_as_float(r[0]) + __uint_as_float(r[1]);
    u = __float_as_uint(v);
    auto s = __builtin_amdgcn_permlane32_swap(u, u, false, false);
    return __uint_as_float(s[0]) + __uint_as_float(s[1]);
}
#define BAR() do { asm volatile("" ::: "memory"); __builtin_amdgcn_s_barrier(); asm volatile("" ::: "memory"); } while (0)
#define WAITV(n) asm volatile("s_waitcnt vmcnt(" #n ")" ::: "memory")
#define WAITL0() asm volatile("s_waitcnt lgkmcnt(0)" ::: "memory")
#define CFENCE() asm volatile("" ::: "memory")
#define MFMA16(a, b, c) __builtin_amdgcn_mfma_f32_16x16x32_bf16(a, b, c, 0, 0, 0)
__device__ __forceinline__ s16x4 tr_read(const LAS unsigned char* p) { return __builtin_amdgcn_ds_read_tr16_b64_v4i16((LAS s16x4*)p); }
__device__ __forceinline__ bf16x8 cat8(s16x4 a, s16x4 b) { return __builtin_shufflevector(a, b, 0, 1, 2, 3, 4, 5, 6, 7); }
template <class P> __device__ __forceinline__ P opq(P p) { asm volatile("" : "+v"(p)); return p; }
__device__ __forceinline__ int nextbuf(int so) { return so == 2 * STG ? 0 : so + STG; }

__device__ __forceinline__ u32x2 flag_ld(rsrc_t ws, unsigned off) { return __builtin_amdgcn_raw_buffer_load_b64(ws, off, 0, 16); }
__device__ __forceinline__ bool flag_all(u32x2 v, u32x2 T, bool active) { return __builtin_amdgcn_ballot_w64(active && (v[0] != T[0] || v[1] != T[1])) == 0ull; }
__device__ __forceinline__ void flag_publish(rsrc_t ws, unsigned off, u32x2 T, int lane) {
    WAITV(0);
    if (lane == 0) __builtin_amdgcn_raw_buffer_store_b64(T, ws, off, 0, 16);
}
#define SB0() __builtin_amdgcn_sched_barrier(0)
template <int NT> struct Frag { bf16x8 a[4]; s16x4 lo[NT], hi[NT]; };
template <int NT, bool TR, int HALF>
__device__ __forceinline__ void frag_read(Frag<NT>& f, int i, const LAS unsigned char* pa, const LAS unsigned char* pb) {
    { constexpr int B0 = TR ? 2 : 1; i = i == 0 ? 0 : i <= B0 ? i + 3 : i <= B0 + 3 ? i - B0 : i; }
    if (i < 4) f.a[i] = *(const LAS bf16x8*)(pa + i * 2048);
    else if (TR) { const int n = (i - 4) >> 1; if ((i - 4) & 1) f.hi[n] = tr_read(pb + HALF * 9216 + 4608 + n * 32); else f.lo[n] = tr_read(pb + HALF * 9216 + n * 32); }
    else { const int n = i - 4; const bf16x8 v = *(const LAS bf16x8*)(pb + HALF * 9216 + n * 1024);
           f.lo[n] = __builtin_shufflevector(v, v, 0, 1, 2, 3); f.hi[n] = __builtin_shufflevector(v, v, 4, 5, 6, 7); }
}
template <int NT, bool TR, int LHALF, int S0, int BAT, bool FIRST, bool UPD>
__device__ __forceinline__ void half_step(f32x4 (&acc)[NT][4], const Frag<NT>& c, Frag<NT>& l, const LAS unsigned char*& pa0, const LAS unsigned char*& pa1, const LAS unsigned char*& pb, int delta,
                                          const float* ini, const volatile LAS unsigned* rdy, unsigned& rv) {
    constexpr int NRD = TR ? 4 + 2 * NT : 4 + NT, NM = 4 * NT, NS = NM - S0;
#pragma unroll
    for (int i = 0; i < NM; ++i) {
        const int n = i >> 2, m = i & 3;
        acc[n][m] = MFMA16(cat8(c.lo[n], c.hi[n]), c.a[m], FIRST ? (TR ? ((f32x4){0.f, 0.f, 0.f, 0.f}) : ((f32x4){ini[m], ini[m], ini[m], ini[m]})) : acc[n][m]);
        SB0();
        if (i == BAT) { WAITL0(); BAR(); if (TR) rv = *rdy; }
        if (LHALF >= 0 && i >= S0) {
#pragma unroll
            for (int r = 0; r < NRD; ++r) if ((r * NS) / NRD == i - S0) frag_read<NT, TR, (LHALF > 0 ? 1 : 0)>(l, r, LHALF > 0 ? pa1 : pa0, pb);
        }
        if (UPD) { if (i == NM - 3) { pa0 += delta; asm volatile("" : "+v"(pa0)); } if (i == NM - 2) { pa1 += delta; asm volatile("" : "+v"(pa1)); } if (i == NM - 1) { pb += delta; asm volatile("" : "+v"(pb)); } }
        SB0();
    }
}
template <int NT, bool TR, class HK>
__device__ __forceinline__ void gemm_phase(int nsteps, f32x4 (&acc)[NT][4], int& so, const LAS unsigned char* pa0, const LAS unsigned char* pa1, const LAS unsigned char* pb,
                                           const float* ini, const volatile LAS unsigned* rdy, HK hook) {
    constexpr int NRD = TR ? 4 + 2 * NT : 4 + NT;
    Frag<NT> f0, f1;
    unsigned rv = 0;
    pa0 += so; pa1 += so; pb += so;
    asm volatile("" : "+v"(pa0), "+v"(pa1), "+v"(pb));
    __builtin_amdgcn_s_waitcnt(0xC07F);
    BAR();
#pragma unroll
    for (int i = 0; i < NRD; ++i) frag_read<NT, TR, 0>(f0, i, pa0, pb);
    SB0();
    int delta = so == 2 * STG ? -2 * STG : STG; so += delta;
    half_step<NT, TR, 1, 2, -1, true, true>(acc, f0, f1, pa0, pa1, pb, delta, ini, rdy, rv);
#pragma nounroll
    for (int s = 1; s < nsteps; ++s) {
        delta = so == 2 * STG ? -2 * STG : STG; so += delta;
        half_step<NT, TR, 0, 6, 5, false, false>(acc, f1, f0, pa0, pa1, pb, 0, ini, rdy, rv);
        half_step<NT, TR, 1, 2, -1, false, true>(acc, f0, f1, pa0, pa1, pb, delta, ini, rdy, rv);
        if (TR) hook(rv);
    }
    if (TR) half_step<NT, TR, -1, 0, -1, false, false>(acc, f1, f0, pa0, pa1, pb, 0, ini, rdy, rv);
    else {
#pragma unroll
        for (int m = 0; m <= 4; ++m) {
            if (m < 4) {
#pragma unroll
                for (int n = 0; n < NT; ++n) acc[n][m] = MFMA16(cat8(f1.lo[n], f1.hi[n]), f1.a[m], acc[n][m]);
            }
            SB0();
            if (m > 0) hook(m - 1);
            SB0();
        }
    }
}

template <int NT, class LT>
__device__ __forceinline__ void consumer_a(LAS unsigned char* lds, u32x2 (&zp)[5][4], LT late, int wm, int wn, int lane) {
    const int fr = lane & 15, fq = lane >> 4;
    constexpr int N0 = (NT == 5) ? 0 : 5;
    f32x4 acc[NT][4];
    int so = 0;
    const int sw = (fr >> 1) & 7;
    const int a0 = (wm * 64 + fr) * 128 + ((fq ^ sw) << 4), a1 = (wm * 64 + fr) * 128 + (((4 + fq) ^ sw) << 4);
    const int bw = BOFF + (4 * fq + (fr >> 2)) * 288 + (lane & 3) * 8 + N0 * 32;
    const volatile LAS unsigned* rdy = (const volatile LAS unsigned*)(lds + L_MISC + 4 + wm * 4);
    bool done = false;
    auto normalise = [&]() {
        const LAS unsigned char* ps = opq(lds + L_SP + (wm * 64 + fr) * 16);
        LAS unsigned char* pz = opq(lds + Z0 + (wm * 64 + fr) * ZST + N0 * 32 + fq * 8);
#pragma unroll
        for (int m = 0; m < 4; ++m) {
            const f32x2 sr = *(const LAS f32x2*)(ps + m * 256);
#pragma unroll
            for (int n = 0; n < NT; ++n) {
                const f32x2 u = (f32x2){bflo(zp[n][m][0]), bfhi(zp[n][m][0])} * sr.x + sr.y, v = (f32x2){bflo(zp[n][m][1]), bfhi(zp[n][m][1])} * sr.x + sr.y;
                *(LAS u32x2*)(pz + m * 16 * ZST + n * 32) = (u32x2){pk_bf16(u.x, u.y), pk_bf16(v.x, v.y)};
            }
        }
        CFENCE();
        if (lane == 0) *(volatile LAS unsigned*)(lds + L_MISC + 16 + (wm * 2 + wn) * 4) = 1u;
        done = true;
    };
    auto hook = [&](unsigned rv) { if (__builtin_amdgcn_readfirstlane(rv) != 0u && !done) normalise(); };
#pragma unroll
    for (int n = 0; n < NT; ++n)
#pragma unroll
        for (int m = 0; m < 4; ++m) zp[n][m] = (u32x2){0u, 0u};
#pragma nounroll
    for (int it = 0; it < 2; ++it) {
        gemm_phase<NT, true>(9, acc, so, lds + a0, lds + a1, lds + bw, nullptr, rdy, hook);
        if (it == 1) late();
        if (it == 1 && !done) {
            int t0 = 0;
            while (__builtin_amdgcn_readfirstlane(*rdy) == 0u && ++t0 < SPIN_L) __builtin_amdgcn_s_sleep(1);
            normalise();
        }
#pragma unroll
        for (int m = 0; m < 4; ++m)
#pragma unroll
            for (int n = 0; n < NT; ++n) { acc[n][m] = gelu4(acc[n][m]); zp[n][m] = (u32x2){pk_bf16(acc[n][m][0], acc[n][m][1]), pk_bf16(acc[n][m][2], acc[n][m][3])}; }
        if (it == 0) {
            LAS unsigned char* ps = opq(lds + L_SP + (wm * 64 + fr) * 16 + wn * 8);
#pragma unroll
            for (int m = 0; m < 4; ++m) {
                f32x4 s4 = acc[0][m];
#pragma unroll
                for (int n = 1; n < NT; ++n) s4 += acc[n][m];
                const float mean = opq(allred_q((s4[0] + s4[1]) + (s4[2] + s4[3])) * (1.0f / (16 * NT)));
                f32x4 q4 = {0.f, 0.f, 0.f, 0.f};
#pragma unroll
                for (int n = 0; n < NT; ++n) { const f32x4 d = acc[n][m] - mean; q4 += d * d; }
                const float m2 = allred_q((q4[0] + q4[1]) + (q4[2] + q4[3]));
                if (fq == 0) *(LAS f32x2*)(ps + m * 256) = (f32x2){mean, m2};
            }
        }
    }
}

template <int NT>
__device__ __forceinline__ void consumer_b(LAS unsigned char* lds, rsrc_t ws, u32x2 T, const u32x2 (&z1)[5][4], const float (&bs_)[4], int pm, int pn, int wm, int wn, int lane) {
    float bs[4] = {bf2f(__float_as_uint(opq(bs_[0]))), bf2f(__float_as_uint(opq(bs_[1]))), bf2f(__float_as_uint(opq(bs_[2]))), bf2f(__float_as_uint(opq(bs_[3])))};
    const int fr = lane & 15, fq = lane >> 4;
    constexpr int N0 = (NT == 5) ? 0 : 5;
    f32x4 acc[NT][4];
    int so = 0;
    const int sw = (fr >> 1) & 7;
    const int a0 = (wm * 64 + fr) * 128 + ((fq ^ sw) << 4), a1 = (wm * 64 + fr) * 128 + (((4 + fq) ^ sw) << 4);
    const int bz = BOFF + N0 * 1024 + lane * 16;
    const int qm = 2 * pm + wm;
    const unsigned hb = WS_HF + (unsigned)qm * (18u * 16384u);
    auto epi = [&](int m) {
        if (m == 0) __builtin_amdgcn_s_setprio(3);
        u32x2 hp[NT];
#pragma unroll
        for (int n = 0; n < NT; ++n) {
            const f32x2 u = (f32x2){bflo(z1[n][m][0]), bfhi(z1[n][m][0])} * (f32x2){acc[n][m][0], acc[n][m][1]}, v = (f32x2){bflo(z1[n][m][1]), bfhi(z1[n][m][1])} * (f32x2){acc[n][m][2], acc[n][m][3]};
            hp[n] = (u32x2){pk_bf16(u.x, u.y), pk_bf16(v.x, v.y)};
        }
#pragma unroll
        for (int j = 0; j < 2; ++j)
            __builtin_amdgcn_raw_buffer_store_b128((u32x4){hp[2 * j][0], hp[2 * j][1], hp[2 * j + 1][0], hp[2 * j + 1][1]}, ws,
                                                   hb + (unsigned)pn * 16384u + (unsigned)(m * 4 + wn * 2 + j) * 1024u + lane * 16, 0, 16);
        if (NT == 5) __builtin_amdgcn_raw_buffer_store_b64(hp[NT - 1], ws, hb + (unsigned)(16 + (pn >> 3)) * 16384u + m * 4096u + (unsigned)(pn & 7) * 512u + lane * 8, 0, 16);
    };
    gemm_phase<NT, false>(32, acc, so, lds + a0, lds + a1, lds + bz, bs, nullptr, epi);
    flag_publish(ws, WS_FH + (unsigned)(qm * 32 + pn * 2 + wn) * 128u, T, lane);
    __builtin_amdgcn_s_setprio(0);
}

template <int TLO, int THI>
__device__ __forceinline__ void conv_task(LAS unsigned char* lds, int kt, int ut, int yt, int tl, int lane, int& nb) {
#define CSL() do { if (nb < 50) { BAR(); ++nb; } } while (0)
    constexpr int NTL = THI - TLO + 1, ND = THI + 2;
    const int i = lane & 15, g = lane >> 4;
    const LAS unsigned char* pk = opq(lds + kt + (i & 3) * 1088 + ((i >> 2) + g) * 16);
    const LAS unsigned char* pu = opq(lds + ut + (128 + 16 * tl - 32 * g - 4 * (i >> 2) + 4 * (i & 3)) * 2 - 32);
    bf16x8 ub[ND], kf[ND];
#pragma unroll
    for (int d = 0; d < ND; ++d) { if (d % 6 == 0) CSL(); ub[ND - 1 - d] = cat8(tr_read(pu + 256 * (ND - 1 - d) + 32), tr_read(pu + 256 * (ND - 1 - d))); kf[d] = *(const LAS bf16x8*)(pk + 64 * d); }
    f32x4 acc[NTL];
    int cnt = CONV_SLICE;
#pragma unroll
    for (int s = -1; s <= THI; ++s) {
#pragma unroll
        for (int t = TLO; t <= THI; ++t) if (t >= s) {
            if (cnt == CONV_SLICE) { CSL(); cnt = 0; }
            ++cnt;
            acc[t - TLO] = MFMA16(kf[s + 1], ub[t - s], s == -1 ? ((f32x4){0.f, 0.f, 0.f, 0.f}) : acc[t - TLO]);
        }
    }
    LAS float* y = opq((LAS float*)(lds + yt) + 32 * g + 16 * tl + i);
    LAS float* cy = opq((LAS float*)(lds + yt + 8192) + (2 * g + tl) * 4 + (i & 3));
#define DPPF(v, ctl) __int_as_float(__builtin_amdgcn_update_dpp(0, __float_as_int(v), ctl, 0xf, 0xf, true))
#pragma unroll
    for (int t = TLO; t <= THI; ++t) {
        if (((t - TLO) % 3) == 0) CSL();
        const f32x4 a = acc[t - TLO];
        y[128 * t] = (a[0] + DPPF(a[1], 0x111)) + (DPPF(a[2], 0x112) + DPPF(a[3], 0x113));
        const float cv = DPPF(a[1], 0x10f) + (DPPF(a[2], 0x10e) + DPPF(a[3], 0x10d));
        if (i < 4) cy[32 * t] = cv;
    }
#undef DPPF
#undef CSL
}
__device__ __forceinline__ int ytab(int c) { return Z0 + c * Y_SZ; }

__global__ __launch_bounds__(512, 2) void gm_kernel(const u16* __restrict__ x, const u16* __restrict__ logA, const u16* __restrict__ Aim, const u16* __restrict__ Cre,
                                                    const u16* __restrict__ Cim, const u16* __restrict__ logdt, const u16* __restrict__ Dsk, const u16* __restrict__ win,
                                                    const u16* __restrict__ wsp, const u16* __restrict__ bsp, const u16* __restrict__ wout, const u16* __restrict__ gw,
                                                    u16* __restrict__ out, unsigned char* wsp_) {
    extern __shared__ __attribute__((aligned(16))) unsigned char lds_[];
    LAS unsigned char* lds = (LAS unsigned char*)lds_;
    const int wid = __builtin_amdgcn_readfirstlane(threadIdx.x >> 6), lane = threadIdx.x & 63;
    const int bid = blockIdx.x, xg = bid & 7, rr = bid >> 3, bi = rr >> 2, bj = rr & 3;
    const int pm = (xg >> 2) * 8 + bi, pn = (xg & 3) * 4 + bj;
    const int qm = 2 * pm + (pn >> 3), qn = pn & 7;
    const int nch = pm < 4 ? 3 : 2;
    const rsrc_t ws = __builtin_amdgcn_make_buffer_rsrc(wsp_, 0, WS_END, 0x00020000);
    unsigned pfw = 0;
    if (wid == 6) {
        u64 pc0; asm volatile("s_getpc_b64 %0" : "=s"(pc0));
        const u64 q_ = (pc0 & ~127ull) + (unsigned)min((rr * CODE_LINES + (lane >> 1)) * 128 + (lane & 1) * 64, CODE_BYTES);
        if (lane < 2 * CODE_LINES) asm volatile("global_load_dword %0, %1, off" : "+v"(pfw) : "v"(q_) : "memory");
    }

    const int fr = lane & 15, fq = lane >> 4;
    const int h = wid - 6, p = wid - 4;
    const bool is_prod = (wid >> 1) == 2;
    const int ch0 = pn * 36 + pm;
    float bs[4];
    auto late = [&]() {
#pragma unroll
        for (int m = 0; m < 4; ++m) bs[m] = __uint_as_float((unsigned)bsp[pm * 128 + (wid & 1) * 64 + m * 16 + fr]);
    };
    const int phi = (-(bi + bj)) & 3;
    const unsigned rsw = (unsigned)(((lane & 7) ^ ((4 * p + (lane >> 4)) & 7)) * 16);
#define GLDS(src, dst, aux) __builtin_amdgcn_global_load_lds((const unsigned*)(src), (LAS unsigned*)(dst), 16, 0, aux)
    auto issueS = [&](int k, int s) {
        const char* sb = (const char*)wsp + (size_t)(pm * 128 + p * 8) * 4096 + k * 128; const unsigned vs = (unsigned)(lane >> 3) * 4096u + rsw;
#pragma unroll
        for (int i = 0; i < 8; ++i) GLDS(sb + (size_t)i * (16 * 4096) + vs, lds + s + (p + 2 * i) * 1024, 0); };
    auto ord2 = [&](int s) { return ((s & 3) == phi) ? s : ((s - 4) & 31); };
    u32x2 E, T;
    auto mk_tag = [&]() { u64 z = (((u64)__builtin_amdgcn_readfirstlane(E[1]) << 32) | __builtin_amdgcn_readfirstlane(E[0])) + 0x9E3779B97F4A7C15ull;
        z = (z ^ (z >> 30)) * 0xBF58476D1CE4E5B9ull; z = (z ^ (z >> 27)) * 0x94D049BB133111EBull; z ^= z >> 31; T = (u32x2){(unsigned)z, (unsigned)(z >> 32)}; };
    u32x2 zp[5][4];
#define STEP(lim) do { if (nb < (lim)) { BAR(); ++nb; } } while (0)
#define STEPS_TO(lim) _Pragma("nounroll") while (nb < (lim)) { BAR(); ++nb; }
    int nb = 0;
    const int kc = wid == 7 ? 2 : wid == 1 || wid == 2 ? wid - 1 : 3;
    if (kc < nch) {
        const int kch = ch0 + 16 * kc, ki = kch * 64 + lane;
        const unsigned pA = ((const unsigned*)logA)[ki], pI = ((const unsigned*)Aim)[ki], pR = ((const unsigned*)Cre)[ki], pC = ((const unsigned*)Cim)[ki], pT = logdt[kch];
        LAS u32x2* tw = opq((LAS u32x2*)(lds + Z0 + kc * 8448) + lane);
        const LAS unsigned char* trd = opq(lds + Z0 + kc * 8448 + fr * 528 + fq * 16);
        const float dt = rbf(__expf(bf2f(pT)));
        const f32x2 ar = (f32x2){rbf(__expf(bflo(pA))), rbf(__expf(bfhi(pA)))} * dt * -1.44269504f, ai = (f32x2){bflo(pI), bfhi(pI)} * dt * 0.15915494f;
        const f32x2 cr = (f32x2){bflo(pR), bfhi(pR)} * dt, ci = (f32x2){bflo(pC), bfhi(pC)} * dt;
        float cm = fmaxf(fmaxf(fabsf(cr.x), fabsf(ci.x)), fmaxf(fabsf(cr.y), fabsf(ci.y)));
#define DPPF(v, ctl) __int_as_float(__builtin_amdgcn_update_dpp(0, __float_as_int(v), ctl, 0xf, 0xf, true))
        cm = fmaxf(cm, DPPF(cm, 0x121)); cm = fmaxf(cm, DPPF(cm, 0x122)); cm = fmaxf(cm, DPPF(cm, 0x124)); cm = fmaxf(cm, DPPF(cm, 0x128));
#undef DPPF
        { auto r = __builtin_amdgcn_permlane16_swap(__float_as_uint(cm), __float_as_uint(cm), false, false); cm = fmaxf(__uint_as_float(r[0]), __uint_as_float(r[1]));
          auto q = __builtin_amdgcn_permlane32_swap(__float_as_uint(cm), __float_as_uint(cm), false, false); cm = fmaxf(fmaxf(__uint_as_float(q[0]), __uint_as_float(q[1])), 1e-30f); }
        struct C2 { f32x2 x, y; };
        auto cexp = [&](float p_) { const f32x2 e = ar * p_, t = ai * p_;
            const f32x2 mg = {__builtin_amdgcn_exp2f(e.x), __builtin_amdgcn_exp2f(e.y)}; const float t0 = __builtin_amdgcn_fractf(t.x), t1 = __builtin_amdgcn_fractf(t.y);
            return C2{mg * (f32x2){__builtin_amdgcn_cosf(t0), __builtin_amdgcn_cosf(t1)}, mg * (f32x2){__builtin_amdgcn_sinf(t0), __builtin_amdgcn_sinf(t1)}}; };
        auto cmul = [](C2 a, C2 b) { return C2{__builtin_elementwise_fma(a.x, b.x, -(a.y * b.y)), __builtin_elementwise_fma(a.x, b.y, a.y * b.x)}; };
        auto put = [&](int row, C2 v, bool conj) { tw[row * 66] = conj ? (u32x2){pk_f16(v.x.x, -v.y.x), pk_f16(v.x.y, -v.y.y)} : (u32x2){pk_f16(v.x.x, v.y.x), pk_f16(v.x.y, v.y.y)}; };
        C2 wp[16], zr[8], vp[4], cv[17];
        wp[1] = cexp(8.f); zr[1] = cexp(1.f); vp[0] = cexp(128.f);
        tw[0] = (u32x2){pk_f16(1.f, 0.f), pk_f16(1.f, 0.f)};
#pragma unroll
        for (int j = 1; j < 16; ++j) { if (j > 1) wp[j] = cmul(wp[j >> 1], wp[j - (j >> 1)]); put(j, wp[j], true); }
        CFENCE();
        u32x4 fb[8];
#pragma unroll
        for (int ks = 0; ks < 8; ++ks) fb[ks] = *(const LAS u32x4*)(trd + ks * 64);
        CFENCE();
#pragma unroll
        for (int r = 2; r < 8; ++r) zr[r] = cmul(zr[r >> 1], zr[r - (r >> 1)]);
#pragma unroll
        for (int k = 1; k < 4; ++k) vp[k] = cmul(vp[k - 1], vp[k - 1]);
        const float rc = __builtin_amdgcn_rcpf(cm);
        cv[0] = C2{cr * rc, ci * rc};
#pragma unroll
        for (int k = 1; k < 8; ++k) { const int lg = k >= 4 ? 2 : k >= 2 ? 1 : 0; cv[k] = cmul(cv[k - (1 << lg)], vp[lg]); }
        LAS float* kb_ = (LAS float*)(lds + L_KD + kc * 3072);
        const float sc = 2.0f * cm;
        u32x4 fa[3][8];
#pragma unroll
        for (int tt = 0; tt < 3; ++tt) {
#pragma unroll
            for (int i = 0; i < (tt == 2 ? 9 : 16); ++i) {
                const int r = tt == 0 ? (i & 7) : tt == 1 ? (i < 8 ? 2 * (i & 3) : 4 * (i & 1)) : 0;
                const int hh = tt == 0 ? (i >> 3) : tt == 1 ? (i < 8 ? 2 + (i >> 2) : 4 + ((i - 8) >> 1)) : 8 + i;
                if (tt == 2) cv[hh] = cmul(cv[hh - 8], vp[3]);
                put(i, r == 0 ? cv[hh] : cmul(cv[hh], zr[r]), false);
            }
            CFENCE();
#pragma unroll
            for (int ks = 0; ks < 8; ++ks) fa[tt][ks] = *(const LAS u32x4*)(trd + ks * 64);
            CFENCE();
            SB0();
        }
        f32x4 kd[3] = {{0.f, 0.f, 0.f, 0.f}, {0.f, 0.f, 0.f, 0.f}, {0.f, 0.f, 0.f, 0.f}};
#pragma unroll
        for (int ks = 0; ks < 8; ++ks)
#pragma unroll
            for (int tt = 0; tt < 3; ++tt) kd[tt] = __builtin_amdgcn_mfma_f32_16x16x32_f16(__builtin_bit_cast(f16x8, fa[tt][ks]), __builtin_bit_cast(f16x8, fb[ks]), kd[tt], 0, 0, 0);
#pragma unroll
        for (int tt = 0; tt < 3; ++tt) {
            LAS float* kw = opq(kb_ + (tt == 0 ? 128 * (fq >> 1) + 4 * (fq & 1) + 8 * fr : tt == 1 ? 256 + 64 * fq + (fq < 2 ? 4 : 2) * fr : 512 + 64 * fq + fr));
            LAS float* kwb = tt == 1 ? opq(kw + (fq < 2 ? 2 : 32)) : kw + (tt == 0 ? 2 : 32);
            kw[0] = kd[tt][0] * sc; kw[tt == 2 ? 16 : 1] = kd[tt][1] * sc; kwb[0] = kd[tt][2] * sc; kwb[tt == 2 ? 16 : 1] = kd[tt][3] * sc;
        }
        WAITL0();
    }
    if (wid == 6) { WAITV(0); asm volatile("" :: "v"(pfw)); }
    if (is_prod) {
        const unsigned vx = (unsigned)(lane >> 3) * 1152u + rsw;
        unsigned wv[9];
        const char* xb = (const char*)x + (size_t)(pm * 128 + p * 8) * 1152;
        const char* wb = (const char*)win + pn * 288;
        auto issueX = [&](int k, int s) {
#pragma unroll
            for (int i = 0; i < 8; ++i) GLDS(xb + (size_t)i * (16 * 1152) + k * 128 + vx, lds + s + (p + 2 * i) * 1024, 0); };
        auto issueW = [&](int k, int coloff, int s) {
            const char* b = wb + (size_t)k * (64 * 9216) + coloff;
#pragma unroll
            for (int i = 0; i < 9; ++i) GLDS(b + wv[i], lds + s + BOFF + (p + 2 * i) * 1024, 0); };
        auto ord1 = [&](int s) { return ((s & 3) == phi) ? s : (s >= 4 ? s - 4 : (s == 0 ? 8 : s + 4)); };
        issueX(ord1(0), 0);
        SB0();
#pragma unroll
        for (int i = 0; i < 9; ++i) { const int c = (p + 2 * i) * 64 + lane, rho = c / 18, ch = c - rho * 18;
            const int kap = (rho & 32) + 8 * ((rho >> 2) & 3) + 4 * ((rho >> 4) & 1) + (rho & 3); wv[i] = (unsigned)kap * 9216u + (unsigned)ch * 16u; }
        issueW(ord1(0), FH * 2, 0); issueX(ord1(1), STG); issueW(ord1(1), FH * 2, STG);
        issueX(ord1(2), 2 * STG); issueW(ord1(2), FH * 2, 2 * STG);
        E = flag_ld(ws, WS_E);
        unsigned wm_ = 0;
        auto touch = [&](const void* base, unsigned off, unsigned last) { const char* q_ = (const char*)base + min(off + lane * 64u, last); asm volatile("global_load_dword %0, %1, off" : "+v"(wm_) : "v"(q_) : "memory"); };
        {
#pragma unroll
            for (int i = 0; i < 2; ++i) touch(x, bid * 9216u + p * 4608u + i * 4096u, bid * 9216u + p * 4608u + 4544u);
#pragma unroll
            for (int i = 0; i < 3; ++i) touch(win, bid * 20736u + p * 10368u + i * 4096u, bid * 20736u + p * 10368u + 10304u);
        }
        int so = 0;
        WAITV(35); BAR();
#pragma nounroll
        for (int S = 1; S < 16; ++S) {
            if (S >= 8 && S <= 10) WAITV(9); else WAITV(17);
            BAR();
            const int t = S + 2, k = ord1(t < 9 ? t : t < 12 ? t - 3 : t - 12);
            if (t < 9 || t >= 12) issueX(k, so);
            issueW(k, t >= 9 ? 0 : FH * 2, so); so = nextbuf(so);
            if (S == 8) {
#pragma unroll
                for (int i = 0; i < 4; ++i) touch(wsp, bid * 32768u + p * 16384u + i * 4096u, 0x7fffffffu);
            }
        }
        WAITV(17); BAR(); issueS(2 * pm, 0);
        WAITV(8); BAR(); issueS(2 * pm + 1, STG);
        asm volatile("" :: "v"(wm_));
    } else if (wid < 4) {
        E = flag_ld(ws, WS_E);
        if (wid >> 1) consumer_a<4>(lds, zp, late, wid & 1, 1, lane);
        else consumer_a<5>(lds, zp, late, wid & 1, 0, lane);
    } else {
        if (h == 0 && lane < 12) *(LAS unsigned*)(lds + L_MISC + lane * 4) = 0u;
        const int row0 = lane / 9, chk0 = lane - row0 * 9;
        const int u = (pn >> 3) * 16 + pm, nblk = u < 8 ? 3 : 2;
        u32x2 uc[18]; u32x4 xv[9], gv[9], wv[3][5];
        if (h == 0) {
            { const char* xb = (const char*)x + (size_t)pm * 128 * 1152 + pn * 72; unsigned off = row0 * 1152 + chk0 * 8; int ch = chk0;
#pragma unroll
              for (int i = 0; i < 9; ++i) { uc[i] = *(const u32x2*)(xb + off); uc[9 + i] = *(const u32x2*)(xb + 64 * 1152 + off); off += 7 * 1152 + 8; if (++ch == 9) { ch = 0; off += 1152 - 72; } } }
            { const char* xr = (const char*)x + (size_t)(pm * 128 + pn * 8 + (lane >> 3)) * 1152 + (lane & 7) * 16; const char* gr = (const char*)gw + (lane & 7) * 16;
#pragma unroll
              for (int i = 0; i < 9; ++i) { xv[i] = *(const u32x4*)(xr + i * 128); gv[i] = *(const u32x4*)(gr + i * 128); } }
        } else {
            unsigned wo_[5], hi_[5];
            { int row = row0, ch = chk0;
#pragma unroll
              for (int i = 0; i < 5; ++i) { const int rw = row < 32 ? row : 31; wo_[i] = (rw & 15) * 1152 + ch * 16; hi_[i] = rw >> 4; row += 7; if (++ch == 9) { ch = 0; ++row; } } }
#pragma unroll
            for (int b = 0; b < 3; ++b) {
                const int blk = (b < nblk) ? u + 32 * b : u, st = blk >> 2, jb = blk & 3;
                const int fA = st < 16 ? st * 144 + (jb < 2 ? 32 * jb : 16 + 32 * jb) : ((st - 16) * 8 + 2 * jb) * 144 + 64, dB = (st < 16 ? 16 : 144) * 1152;
                const char* wb_ = (const char*)wout + (size_t)fA * 1152 + qn * 144;
#pragma unroll
                for (int i = 0; i < 5; ++i) wv[b][i] = *(const u32x4*)(wb_ + (wo_[i] + hi_[i] * dB));
            }
        }
        E = flag_ld(ws, WS_E);
        if (lane < 24) *(LAS u32x4*)(lds + L_KT + (lane >> 3) * KT_SZ + (2 * h + ((lane >> 2) & 1)) * 1088 + (lane & 3) * 16) = (u32x4){0u, 0u, 0u, 0u};
        WAITL0();
        __builtin_amdgcn_s_setprio(1);
        {
            float kv[2][3];
#pragma nounroll
            for (int ei = 0; ei < 9; ++ei) {
                BAR();
                if (ei > 0) {
                    LAS unsigned char* kt = opq(lds + L_KT + (2 * h + ((ei - 1) >> 2)) * 1088 + 64 + ((ei - 1) & 3) * 256 + lane * 2);
#pragma unroll
                    for (int j = 0; j < 2; ++j)
#pragma unroll
                        for (int c = 0; c < 3; ++c) *(LAS u16*)(kt + c * KT_SZ + j * 128) = (u16)pk_bf16(kv[j][c], 0.f);
                }
#pragma unroll
                for (int j = 0; j < 2; ++j) {
                    const int jj = (ei & 3) * 2 + j, e = jj >= 4 ? 3 : jj >= 2 ? 2 : jj, lag = 256 * jj + 4 * lane + 2 * h + ((ei >> 2) & 1);
                    const int idx = e == 0 ? lag : 128 * e + ((lag + (1 << (e - 1)) - 1 + ((lag >> e) & 1)) >> e);
                    const LAS float* q = opq((const LAS float*)(lds + L_KD) + idx);
#pragma unroll
                    for (int c = 0; c < 3; ++c) kv[j][c] = q[c * 768];
                }
            }
        }
        if (h == 0) {
            LAS unsigned char* pzs = opq(lds + Z0 + lane * 8);
#pragma unroll
            for (int i = 0; i < 18; ++i) *(LAS u32x2*)(pzs + i * 512) = uc[i];
            CFENCE();
            { const LAS unsigned char* pt = opq(lds + Z0 + (8 * (4 * fq + (lane & 3)) + (fr >> 2)) * 72);
              const unsigned ub = WS_UT + (unsigned)(pn * 36 + (lane & 3)) * 4096u + (unsigned)(pm * 128 + 8 * (4 * fq + (fr >> 2))) * 2u;
#pragma unroll
              for (int cc = 0; cc < 9; ++cc) __builtin_amdgcn_raw_buffer_store_b128(__builtin_bit_cast(u32x4, cat8(tr_read(pt + cc * 8), tr_read(pt + 288 + cc * 8))), ws, ub + cc * 16384u, 0, 16); }
            float a = 0.f;
#pragma unroll
            for (int i = 0; i < 9; ++i)
#pragma unroll
                for (int j = 0; j < 4; ++j) { const unsigned xa = xv[i][j], ga = gv[i][j]; a = __builtin_amdgcn_fdot2_f32_bf16(__builtin_bit_cast(bf16x2, xa), __builtin_bit_cast(bf16x2, ga), a, false); }
#define DPPF(v, ctl) __int_as_float(__builtin_amdgcn_update_dpp(0, __float_as_int(v), ctl, 0xf, 0xf, true))
            a += DPPF(a, 0xb1); a += DPPF(a, 0x4e); a += DPPF(a, 0x141);
#undef DPPF
            const float gt = rbf(__builtin_amdgcn_rcpf(1.0f + __expf(-rbf(a))));
            if ((lane & 7) == 0) __builtin_amdgcn_raw_buffer_store_b32(__float_as_uint(gt), ws, WS_GATE + (unsigned)(pm * 128 + pn * 8 + (lane >> 3)) * 4u, 0, 16);
        } else {
            LAS unsigned char* pws = opq(lds + Z0 + 9216 + lane * 16);
            const LAS unsigned char* ptw = opq(lds + Z0 + 9216 + (4 * fq + (fr >> 2)) * 144 + (lane & 3) * 8);
#pragma unroll
            for (int b = 0; b < 3; ++b) {
                if (b < nblk) {
#pragma unroll
                    for (int i = 0; i < 5; ++i) if (i < 4 || lane < 32) *(LAS u32x4*)(pws + i * 1024) = wv[b][i];
                    CFENCE();
                    const unsigned ob = WS_WOF + (unsigned)qn * (18u * 18432u) + (unsigned)(u + 32 * b) * 4608u;
#pragma unroll
                    for (int nn = 0; nn < 5; ++nn) {
                        const u32x4 v = __builtin_bit_cast(u32x4, cat8(tr_read(ptw + nn * 32), tr_read(ptw + 2304 + nn * 32)));
                        if (nn < 4) __builtin_amdgcn_raw_buffer_store_b128(v, ws, ob + nn * 1024 + lane * 16, 0, 16);
                        else if (fr < 8) __builtin_amdgcn_raw_buffer_store_b128(v, ws, ob + 4096 + (fq * 8 + fr) * 16, 0, 16);
                    }
                    CFENCE();
                }
            }
        }
        nb = 9;
        mk_tag();
        WAITL0();
        STEPS_TO(10);
        {
            const f32x4 sp = *(const LAS f32x4*)(lds + L_SP + (h * 64 + lane) * 16);
            const float dm = sp[0] - sp[2];
            const f32x2 mine = {(80.f * sp[0] + 64.f * sp[2]) * (1.0f / 144.f), sp[1] + sp[3] + (80.f * 64.f / 144.f) * dm * dm};
            __builtin_amdgcn_raw_buffer_store_b128((u32x4){T[0], __float_as_uint(mine.x), T[1], __float_as_uint(mine.y)}, ws, WS_ST + (unsigned)((pm * 16 + pn) * 128 + h * 64 + lane) * 16u, 0, 16);
            STEP(18);
            const unsigned so_ = WS_ST + (unsigned)(pm * 16 * 128 + h * 64 + lane) * 16u;
            int t0 = 0;
            u32x4 st[16];
            for (;;) {
                CFENCE();
#pragma unroll
                for (int j = 0; j < 16; ++j) st[j] = __builtin_amdgcn_raw_buffer_load_b128(ws, so_ + j * 2048u, 0, 16);
                STEP(18);
                unsigned bad = 0;
#pragma unroll
                for (int j = 0; j < 16; ++j) bad |= (st[j][0] ^ T[0]) | (st[j][2] ^ T[1]);
                if (__builtin_amdgcn_ballot_w64(bad != 0u) == 0ull) break;
                if (++t0 > SPIN_G) break;
            }
            STEP(18);
            float mean = 0.f, m2 = 0.f;
#pragma unroll
            for (int j = 0; j < 16; ++j) mean += __uint_as_float(st[j][1]);
            mean *= (1.0f / 16.f);
#pragma unroll
            for (int j = 0; j < 16; ++j) { const float d = __uint_as_float(st[j][1]) - mean; m2 += __uint_as_float(st[j][3]) + 144.f * d * d; }
            const float rstd = __builtin_amdgcn_rsqf(m2 * (1.0f / FH) + 1e-5f);
            *(LAS f32x2*)(lds + L_SP + (h * 64 + lane) * 16) = (f32x2){rstd, -mean * rstd};
            CFENCE();
            if (lane == 0) *(volatile LAS unsigned*)(lds + L_MISC + 4 + h * 4) = 1u;
            const volatile LAS unsigned* zd = (const volatile LAS unsigned*)(lds + L_MISC + 16 + h * 8 + (lane & 1) * 4);
            for (;;) {
                STEP(18);
                if (__builtin_amdgcn_ballot_w64(*zd == 0u) == 0ull) break;
                if (++t0 > SPIN_L) break;
            }
            CFENCE();
            const LAS unsigned char* ptz = opq(lds + Z0 + (h * 64 + 8 * fq + (fr >> 2)) * ZST + (lane & 3) * 8);
            const unsigned zo = WS_Z2F + (unsigned)((pn * 64 + pm * 4 + h * 2) * 9) * 1024u + lane * 16;
            u32x4 zf[18];
#pragma unroll
            for (int i = 0; i < 18; ++i) {
                const LAS unsigned char* pt = ptz + (i / 9) * 32 * ZST + (i % 9) * 32;
                zf[i] = __builtin_bit_cast(u32x4, cat8(tr_read(pt), tr_read(pt + 4 * ZST)));
                __builtin_amdgcn_raw_buffer_store_b128(zf[i], ws, zo + i * 1024, 0, 16);
                if (i == 5 || i == 11) STEP(18);
            }
            STEP(18);
            flag_publish(ws, WS_FZ + (unsigned)(pn * 32 + pm * 2 + h) * 128u, T, lane);
            STEPS_TO(18);
            LAS unsigned char* pr_ = opq(lds + h * STG + BOFF + lane * 16);
#pragma unroll
            for (int i = 0; i < 18; ++i) *(LAS u32x4*)(pr_ + i * 1024) = zf[i];
            WAITL0();
        }
    }

    const char* lin = (const char*)wsp_ + p * 1024 + lane * 16;
    auto issueLA = [&](unsigned off, int s) {
#pragma unroll
        for (int i = 0; i < 8; ++i) GLDS(lin + off + i * 2048, lds + s + (p + 2 * i) * 1024, 16); };
    auto issueLB = [&](unsigned off, int s) {
#pragma unroll
        for (int i = 0; i < 9; ++i) GLDS(lin + off + i * 2048, lds + s + BOFF + (p + 2 * i) * 1024, 16); };
    auto ord3 = [&](int s) { return (s >= 16 || (s & 3) == phi) ? s : ((s - 4) & 15); };
    u32x2 pv = {0u, 0u}; unsigned rdy = 0; bool pend = false;
    const char* fp = (const char*)wsp_ + WS_FZ + ((unsigned)pn * 32u + (lane & 31)) * 128u;
    auto poll_issue = [&]() { asm volatile("global_load_dwordx2 %0, %1, off sc1" : "+v"(pv) : "v"(fp) : "memory"); };
    auto poll_take = [&]() { asm volatile("" : "+v"(pv)); rdy |= (unsigned)__builtin_amdgcn_ballot_w64(pv[0] == T[0] && pv[1] == T[1]); };
    auto need = [&](unsigned bits) {
        if (pend) { poll_take(); pend = false; }
        if ((rdy & bits) != bits) {
            int t0 = 0;
            do { poll_issue(); WAITV(0); poll_take(); } while ((rdy & bits) != bits && ++t0 < SPIN_G);
        }
        if (rdy != ~0u) { poll_issue(); pend = true; }
    };
    const unsigned hb = WS_HF + (unsigned)qm * (18u * 16384u), ob = WS_WOF + (unsigned)qn * (18u * 18432u);
    if (is_prod) {
        mk_tag();
        const unsigned zb = WS_Z2F + (unsigned)pn * (64u * 9216u);
        int so = 2 * STG;
        const int r0 = (ord2(0) >> 1) == pm ? ord2(1) : ord2(0), r1 = (ord2(1) >> 1) == pm ? ord2(0) : ord2(1);
        poll_issue(); WAITV(0); poll_take();
#pragma nounroll
        for (int S = 18; S < 48; ++S) {
            BAR();
            int k = ord2(S - 16); if ((k >> 1) == pm) k = (k & 1) ? r1 : r0;
            need(1u << k); issueS(k, so); issueLB(zb + (unsigned)k * 18432u, so); so = nextbuf(so);
            WAITV(17);
        }
        BAR();
        fp = (const char*)wsp_ + WS_FZ + ((unsigned)(pn ^ 8) * 32u + (lane & 31)) * 128u; WAITV(0); rdy = 0; pend = false;
        need(~0u);
        issueLB(ob + (unsigned)ord3(0) * 18432u, 2 * STG);
        BAR();
        issueLB(ob + (unsigned)ord3(1) * 18432u, 0);
    } else
    if (wid < 4) {
        mk_tag();
        if (wid >> 1) consumer_b<4>(lds, ws, T, zp, bs, pm, pn, wid & 1, 1, lane);
        else consumer_b<5>(lds, ws, T, zp, bs, pm, pn, wid & 1, 0, lane);
    } else {
        __builtin_amdgcn_s_setprio(1);
        {   const unsigned fo = WS_FZ + (unsigned)(pn * 32 + (lane & 31)) * 128u;
            int t0 = 0;
            for (;;) {
                STEP(43);
                CFENCE();
                const u32x2 v = flag_ld(ws, fo);
                STEP(43);
                if (flag_all(v, T, true)) break;
                if (++t0 > SPIN_G) break;
            } }
        u32x4 uu[3][2];
#pragma unroll
        for (int c = 0; c < 3; ++c)
#pragma unroll
            for (int j = 0; j < 2; ++j) uu[c][j] = __builtin_amdgcn_raw_buffer_load_b128(ws, WS_UT + (unsigned)(ch0 + (c < nch ? 16 * c : 0)) * 4096u + h * 2048 + j * 1024 + lane * 16, 0, 16);
#pragma unroll
        for (int c = 0; c < 3; ++c) if (lane < 16) *(LAS u32x4*)(lds + L_UT + c * UT_SZ + h * 4352 + lane * 16) = (u32x4){0u, 0u, 0u, 0u};
        BAR(); BAR(); BAR(); nb += 3;
        LAS unsigned char* pu_ = opq(lds + L_UT + 256 + h * 2048 + lane * 16);
#pragma unroll
        for (int c = 0; c < 3; ++c)
#pragma unroll
            for (int j = 0; j < 2; ++j) *(LAS u32x4*)(pu_ + c * UT_SZ + j * 1024) = uu[c][j];
        CFENCE();
        if (lane == 0) *(volatile LAS unsigned*)(lds + L_MISC + 32 + h * 4) = 1u;
        STEP(50);
        {   const volatile LAS unsigned* uw = (const volatile LAS unsigned*)(lds + L_MISC + 32 + (lane & 1) * 4);
            int t0 = 0;
            while (__builtin_amdgcn_ballot_w64(*uw == 0u) != 0ull && ++t0 < SPIN_L) STEP(50);
            CFENCE(); }
    }

    if (wid < 6) nb = 50;
    STEPS_TO(50);
    while (wid != 5) {
        if (nb > 45) STEPS_TO(50);
        STEP(50);
        __builtin_amdgcn_wave_barrier();
        unsigned tk = 0;
        if (lane == 0) tk = __hip_atomic_fetch_add((LAS unsigned*)(lds + L_MISC), 1u, __ATOMIC_RELAXED, __HIP_MEMORY_SCOPE_WORKGROUP);
        __builtin_amdgcn_wave_barrier();
        tk = __builtin_amdgcn_readfirstlane(tk);
        if (tk >= (unsigned)(4 * nch)) break;
        const int c = tk >> 2, tl = tk & 1;
        if (tk & 2) conv_task<0, 10>(lds, L_KT + c * KT_SZ, L_UT + c * UT_SZ, ytab(c), tl, lane, nb);
        else conv_task<11, 15>(lds, L_KT + c * KT_SZ, L_UT + c * UT_SZ, ytab(c), tl, lane, nb);
    }
    STEPS_TO(50);
    WAITL0();
    if (is_prod) {
        if (p == 1) {
            fp = (const char*)wsp_ + WS_FH + ((unsigned)qm * 32u + (lane & 31)) * 128u; rdy = 0;
            need(~0u);
#pragma unroll
            for (int i = 0; i < 16; ++i) GLDS(lin - 1024 + hb + (unsigned)ord3(0) * 16384u + i * 1024, lds + 2 * STG + i * 1024, 16);
#pragma unroll
            for (int i = 0; i < 16; ++i) GLDS(lin - 1024 + hb + (unsigned)ord3(1) * 16384u + i * 1024, lds + i * 1024, 16);
            WAITV(16);
        } else WAITV(0);
        int so = STG;
#pragma nounroll
        for (int S = 50; S < 66; ++S) {
            BAR();
            const int k = ord3(S - 48); issueLA(hb + (unsigned)k * 16384u, so); issueLB(ob + (unsigned)k * 18432u, so); so = nextbuf(so);
            WAITV(17);
        }
        BAR();
        WAITV(0); BAR();
        if (bid == 0 && p == 0 && lane == 0) __builtin_amdgcn_raw_buffer_store_b64(T, ws, WS_E, 0, 16);
        return;
    }


    if (wid < 4) {
        f32x4 acc[5];
        struct F3 { s16x4 alo[4], ahi[4]; bf16x8 b[20]; } f0, f1;
        const LAS unsigned char* ph = lds + 2 * STG + wid * 4096 + lane * 16;
        const LAS unsigned char* pw = lds + 2 * STG + BOFF + lane * 16;
        const LAS unsigned char* pw4 = lds + 2 * STG + BOFF + 4096 + (fq * 8 + (fr & 7)) * 16;
        auto rd3 = [&](F3& f, int i, bool single) {
            const int j = i / 6, r = i % 6;
            if (r == 0) {
                if (!single) { const bf16x8 v = *(const LAS bf16x8*)(ph + j * 1024); f.alo[j] = __builtin_shufflevector(v, v, 0, 1, 2, 3); f.ahi[j] = __builtin_shufflevector(v, v, 4, 5, 6, 7); }
                else { f.alo[j] = *(const LAS s16x4*)(ph + j * 1024); f.ahi[j] = *(const LAS s16x4*)(ph + j * 1024 + 512); }
            } else if (r < 5) f.b[j * 5 + r - 1] = *(const LAS bf16x8*)(pw + j * 4608 + (r - 1) * 1024);
            else f.b[j * 5 + 4] = *(const LAS bf16x8*)(pw4 + j * 4608);
        };
        int so = 2 * STG;
        auto step3 = [&](const F3& c, F3& l, int mode  , bool first) {
            const int delta = so == 2 * STG ? -2 * STG : STG; so += delta;
#pragma unroll
            for (int i = 0; i < 20; ++i) {
                const int j = i / 5, n = i % 5;
                acc[n] = MFMA16(c.b[i], cat8(c.alo[j], c.ahi[j]), (first && j == 0) ? ((f32x4){0.f, 0.f, 0.f, 0.f}) : acc[n]);
                if (mode < 2 && i < 12) { rd3(l, 2 * i, mode == 1); rd3(l, 2 * i + 1, mode == 1); }
                if (mode < 2) { if (i == 13) { ph += delta; asm volatile("" : "+v"(ph)); } if (i == 14) { pw += delta; asm volatile("" : "+v"(pw)); } if (i == 15) { pw4 += delta; asm volatile("" : "+v"(pw4)); } }
                SB0();
            }
        };
        __builtin_amdgcn_s_waitcnt(0xC07F);
        BAR();
#pragma unroll
        for (int i = 0; i < 24; ++i) rd3(f0, i, false);
        ph -= 2 * STG; pw -= 2 * STG; pw4 -= 2 * STG; so = 0;
        WAITL0(); BAR(); step3(f0, f1, 0, true);
#pragma nounroll
        for (int j = 0; j < 7; ++j) {
            WAITL0(); BAR(); step3(f1, f0, 0, false);
            WAITL0(); BAR(); step3(f0, f1, 0, false);
        }
        ph = opq(ph - lane * 8);
        WAITL0(); BAR(); step3(f1, f0, 1, false);
        WAITL0(); BAR();
        const float g = *(const LAS float*)(lds + L_MISC + 64 + (16 * wid + fr) * 4);
        const LAS unsigned char* pat = opq(lds + L_AT + (4 * fq + (fr >> 2)) * 128 + (16 * wid + 4 * (lane & 3)) * 2);
        u32x2 av[5];
#pragma unroll
        for (int n = 0; n < 5; ++n) av[n] = __builtin_bit_cast(u32x2, tr_read(pat + n * 2048));
        step3(f0, f1, 1, false);
        step3(f1, f0, 2, false);
        const rsrc_t ors = __builtin_amdgcn_make_buffer_rsrc(out, 0, SEQ * DM * 2, 0x00020000);
        const float g1 = rbf(1.0f - g);
#pragma unroll
        for (int n = 0; n < 5; ++n) {
            const f32x4 b = acc[n]; const u32x2 a = av[n];
            const u32x2 o = {pk_bf16(g * bflo(a[0]) + g1 * b[0], g * bfhi(a[0]) + g1 * b[1]), pk_bf16(g * bflo(a[1]) + g1 * b[2], g * bfhi(a[1]) + g1 * b[3])};
            if (n < 4 || fq < 2) __builtin_amdgcn_raw_buffer_store_b64(o, ors, (unsigned)((qm * 64 + 16 * wid + fr) * 1152 + (qn * 72 + 16 * n + 4 * fq) * 2), 0, 0);
        }
    } else {
        int d = pn * 36 + pm;
#pragma nounroll
        for (int c = 0; c < nch; ++c) {
            const float dsk = bf2f(Dsk[d]);
#pragma unroll
            for (int k = 0; k < 2; ++k) {
                BAR(); ++nb;
                const int l0 = h * 1024 + k * 512 + lane * 8;
                f32x4 cv = *(const LAS f32x4*)(lds + ytab(c) + 8192 + (l0 >= 16 ? (l0 >> 4) - 1 : 0) * 16);
                if ((lane & 1) || l0 == 0) cv = (f32x4){0.f, 0.f, 0.f, 0.f};
                const f32x4 y0 = *(const LAS f32x4*)(lds + ytab(c) + l0 * 4) + cv, y1 = *(const LAS f32x4*)(lds + ytab(c) + l0 * 4 + 16);
                const u32x4 uv = *(const LAS u32x4*)(lds + L_UT + c * UT_SZ + 256 + l0 * 2);
                u32x4 o;
#pragma unroll
                for (int j = 0; j < 4; ++j) {
                    const float ya = j < 2 ? y0[2 * j] : y1[2 * j - 4], yb = j < 2 ? y0[2 * j + 1] : y1[2 * j - 3];
                    const unsigned yp = pk_bf16(ya, yb), up = pk_bf16(bflo(uv[j]) * dsk, bfhi(uv[j]) * dsk);
                    o[j] = pk_bf16(bflo(yp) + bflo(up), bfhi(yp) + bfhi(up));
                }
                __builtin_amdgcn_raw_buffer_store_b128(o, ws, WS_AT + (unsigned)d * 4096u + l0 * 2, 0, 16);
            }
            d += 16;
        }
        STEPS_TO(58);
        flag_publish(ws, WS_FA + (unsigned)((pn >> 1) * 64 + (pn & 1) * 32 + pm * 2 + h) * 128u, T, lane);
        STEPS_TO(61);
        if (h == 0) {
            const unsigned fo = WS_FA + (unsigned)(qn * 64 + lane) * 128u;
            int t0 = 0;
            for (;;) {
                CFENCE();
                const u32x2 v = flag_ld(ws, fo);
                STEP(67); STEP(67);
                if (flag_all(v, T, true)) break;
                if (++t0 > SPIN_G) break;
            }
            u32x4 av[9];
#pragma unroll
            for (int i = 0; i < 9; ++i) av[i] = __builtin_amdgcn_raw_buffer_load_b128(ws, WS_AT + (unsigned)(qn * 72 + i * 8 + (lane >> 3)) * 4096u + qm * 128 + (lane & 7) * 16, 0, 16);
            STEP(67); STEP(67);
            LAS unsigned char* pa_ = opq(lds + L_AT + lane * 16);
#pragma unroll
            for (int i = 0; i < 9; ++i) *(LAS u32x4*)(pa_ + i * 1024) = av[i];
        } else {
            STEPS_TO(64);
            const unsigned gv = __builtin_amdgcn_raw_buffer_load_b32(ws, WS_GATE + (unsigned)(qm * 64 + lane) * 4u, 0, 16);
            BAR(); BAR(); nb += 2;
            *(LAS unsigned*)(lds + L_MISC + 64 + lane * 4) = gv;
        }
        STEPS_TO(67);
        WAITL0();
        BAR();
    }
}
}

extern "C" void kernel_launch(void* const* d_in, const int* in_sizes, int n_in, void* d_out, int out_size, void* d_ws, size_t ws_size, hipStream_t stream) {
    (void)in_sizes; (void)n_in; (void)out_size; (void)ws_size;
    (void)hipFuncSetAttribute((const void*)gm::gm_kernel, hipFuncAttributeMaxDynamicSharedMemorySize, gm::LDS_BYTES);
    hipLaunchKernelGGL(gm::gm_kernel, dim3(256), dim3(512), gm::LDS_BYTES, stream, (const u16*)d_in[0], (const u16*)d_in[1], (const u16*)d_in[2], (const u16*)d_in[3],
                       (const u16*)d_in[4], (const u16*)d_in[5], (const u16*)d_in[6], (const u16*)d_in[8], (const u16*)d_in[9], (const u16*)d_in[10],
                       (const u16*)d_in[11], (const u16*)d_in[12], (u16*)d_out, (unsigned char*)d_ws);
}
```

```cpp
#include <hip/hip_runtime.h>
#include <stdint.h>
#include <type_traits>

#define LAS __attribute__((address_space(3)))
typedef unsigned short u16;
typedef unsigned long long u64;
typedef short bf16x8 __attribute__((ext_vector_type(8)));
typedef short s16x4 __attribute__((ext_vector_type(4)));
typedef _Float16 f16x8 __attribute__((ext_vector_type(8)));
typedef _Float16 f16x2 __attribute__((ext_vector_type(2)));
typedef __bf16 bf16x2 __attribute__((ext_vector_type(2)));
typedef float f32x4 __attribute__((ext_vector_type(4)));
typedef float f32x2 __attribute__((ext_vector_type(2)));
typedef unsigned u32x4 __attribute__((ext_vector_type(4)));
typedef unsigned u32x2 __attribute__((ext_vector_type(2)));
typedef __amdgpu_buffer_rsrc_t rsrc_t;

namespace gm {
constexpr int SEQ = 2048, DM = 576, FH = 2304;
constexpr unsigned WS_E = 0, WS_FS = 4096, WS_FZ = WS_FS + 65536, WS_FH = WS_FZ + 65536, WS_FA = WS_FH + 131072;
constexpr unsigned WS_ST = 0x60000, WS_GATE = 0xE0000, WS_DBG = 0x1E00000, WS_Z2F = 0x100000, WS_HF = 0xA00000, WS_WOF = 0x1300000, WS_UT = 0x1600000, WS_AT = 0x1900000, WS_END = 0x1C00000, WS_TRC = 0x1C00000;
constexpr int STG = 34816, BOFF = 16384, Z0 = 3 * STG, ZST = 304, T0 = Z0 + 128 * ZST;
constexpr int KT_SZ = 4352, UT_SZ = 4608, Y_SZ = 10240;
constexpr int L_MISC = 163840 - 320;
constexpr int L_KT = L_MISC - 3 * KT_SZ, L_SP = T0, L_KD = Z0 + 3 * 8448, L_UT = Z0 + 3 * Y_SZ;
constexpr int L_AT = Z0, L_R = Z0 + 10240;
constexpr int SPIN_G = 8192, SPIN_L = 1 << 20;
constexpr int LDS_BYTES = 163840;
static_assert(L_UT + 3 * UT_SZ <= L_KT && L_KD + 3 * 3072 <= T0 && L_SP + 2048 <= L_KT, "lds map");
#define GM_HPRIO 1
#define GM_OAUX 0
#define GM_PFW(w) ((w) == 6)
#define GM_PFL 11
#define GM_CODE 40960
#define GM_ORD 0
#define GM_PW 3
#define GM_PWS 8
#define GM_DRY 0
#define GM_CSM 11
#define GM_PCONV 1
#define GM_PREPOLL 1
#define GM_BAT 4
#define GM_CV 0
#define GM_LAG 4

__device__ __forceinline__ float bf2f(unsigned v) { return __uint_as_float(v << 16); }
__device__ __forceinline__ float bflo(unsigned w) { return __uint_as_float(w << 16); }
__device__ __forceinline__ float bfhi(unsigned w) { return __uint_as_float(w & 0xffff0000u); }
__device__ __forceinline__ unsigned pk_bf16(float a, float b) { f32x2 v = {a, b}; return __builtin_bit_cast(unsigned, __builtin_convertvector(v, bf16x2)); }
__device__ __forceinline__ unsigned pk_f16(float a, float b) { f32x2 v = {a, b}; return __builtin_bit_cast(unsigned, __builtin_convertvector(v, f16x2)); }
__device__ __forceinline__ float rbf(float a) { return bflo(pk_bf16(a, a)); }
__device__ __forceinline__ f32x2 gelu2(f32x2 x) {
    const f32x2 w = (x * x) * (-0.10294324f) + (-2.30220819f);
    const f32x2 a = w * x;
    f32x2 e; e.x = __builtin_amdgcn_exp2f(a.x); e.y = __builtin_amdgcn_exp2f(a.y);
    e = e + 1.0f;
    f32x2 r; r.x = __builtin_amdgcn_rcpf(e.x); r.y = __builtin_amdgcn_rcpf(e.y);
    return x * r;
}
__device__ __forceinline__ f32x4 gelu4(f32x4 v) { const f32x2 a = gelu2((f32x2){v[0], v[1]}), b = gelu2((f32x2){v[2], v[3]}); return (f32x4){a.x, a.y, b.x, b.y}; }
__device__ __forceinline__ float allred_q(float v) {
    unsigned u = __float_as_uint(v);
    auto r = __builtin_amdgcn_permlane16_swap(u, u, false, false);
    v = __uint_as_float(r[0]) + __uint_as_float(r[1]);
    u = __float_as_uint(v);
    auto s = __builtin_amdgcn_permlane32_swap(u, u, false, false);
    return __uint_as_float(s[0]) + __uint_as_float(s[1]);
}
#define ACQ() __builtin_amdgcn_fence(__ATOMIC_ACQUIRE, "wavefront")
#define TRP
#define TRA
#define TRDUMP() do { } while (0)
#define BAR() do { asm volatile("" ::: "memory"); __builtin_amdgcn_s_barrier(); asm volatile("" ::: "memory"); } while (0)
#define PBAR() BAR()
#define PDUMP() TRDUMP()
#define WAITV(n) asm volatile("s_waitcnt vmcnt(" #n ")" ::: "memory")
#define WAITL0() asm volatile("s_waitcnt lgkmcnt(0)" ::: "memory")
#define CFENCE() asm volatile("" ::: "memory")
#define SGB(mask, n, id) __builtin_amdgcn_sched_group_barrier(mask, n, id)
#define MFMA16(a, b, c) __builtin_amdgcn_mfma_f32_16x16x32_bf16(a, b, c, 0, 0, 0)
__device__ __forceinline__ s16x4 tr_read(const LAS unsigned char* p) { return __builtin_amdgcn_ds_read_tr16_b64_v4i16((LAS s16x4*)p); }
__device__ __forceinline__ bf16x8 cat8(s16x4 a, s16x4 b) { return __builtin_shufflevector(a, b, 0, 1, 2, 3, 4, 5, 6, 7); }
template <class P> __device__ __forceinline__ P opq(P p) { asm volatile("" : "+v"(p)); return p; }
__device__ __forceinline__ int nextbuf(int so) { return so == 2 * STG ? 0 : so + STG; }

#define STAMP(i) do { } while (0)

__device__ __forceinline__ u32x2 flag_ld(rsrc_t ws, unsigned off) { return __builtin_amdgcn_raw_buffer_load_b64(ws, off, 0, 16); }
__device__ __forceinline__ bool flag_all(u32x2 v, u32x2 T, bool active) { return __builtin_amdgcn_ballot_w64(active && (v[0] != T[0] || v[1] != T[1])) == 0ull; }
__device__ __forceinline__ void flag_publish(rsrc_t ws, unsigned off, u32x2 T, int lane) {
    WAITV(0);
    if (lane == 0) __builtin_amdgcn_raw_buffer_store_b64(T, ws, off, 0, 16);
}
#define SB0() __builtin_amdgcn_sched_barrier(0)
struct Frag { bf16x8 a[4]; s16x4 lo[5], hi[5]; };
struct Ptrs { const LAS unsigned char *a0l, *a0h, *a1l, *a1h, *b, *b9; };
template <bool TR, int HALF>
__device__ __forceinline__ void frag_read(Frag& f, int i, const Ptrs& p) {
    { constexpr int B0 = TR ? 2 : 1; i = i == 0 ? 0 : i <= B0 ? i + 3 : i <= B0 + 3 ? i - B0 : i; }
    if (i < 4) f.a[i] = *(const LAS bf16x8*)((i < 2 ? (HALF ? p.a1l : p.a0l) : (HALF ? p.a1h : p.a0h)) + (i & 1) * 2048);
    else if (TR) { const int n = (i - 4) >> 1; const LAS unsigned char* q = (n < 4 ? p.b + n * 32 : p.b9) + HALF * 9216; if ((i - 4) & 1) f.hi[n] = tr_read(q + 4608); else f.lo[n] = tr_read(q); }
    else { const int n = i - 4; const bf16x8 v = *(const LAS bf16x8*)((n < 4 ? p.b + n * 1024 : p.b9) + HALF * 9216);
           f.lo[n] = __builtin_shufflevector(v, v, 0, 1, 2, 3); f.hi[n] = __builtin_shufflevector(v, v, 4, 5, 6, 7); }
}
template <bool TR, int LHALF, int S0, int BAT, bool FIRST>
__device__ __forceinline__ void half_step(f32x4 (&acc)[18], const Frag& c, Frag& l, Ptrs& p, int delta, const float* ini, const volatile LAS unsigned* rdy, unsigned& rv TRP) {
    constexpr int NRD = TR ? 14 : 9, NS = 18 - S0;
#pragma unroll
    for (int i = 0; i < 18; ++i) {
        const int n = i >> 2, m = i & 3;
        acc[i] = MFMA16(cat8(c.lo[n], c.hi[n]), c.a[m], FIRST ? (TR ? ((f32x4){0.f, 0.f, 0.f, 0.f}) : ((f32x4){ini[m], ini[m], ini[m], ini[m]})) : acc[i]);
        SB0();
        if (i == BAT) { WAITL0(); BAR(); if (TR) rv = *rdy; }
        if (LHALF >= 0 && i >= S0) {
#pragma unroll
            for (int r = 0; r < NRD; ++r) if ((r * NS) / NRD == i - S0) frag_read<TR, (LHALF > 0 ? 1 : 0)>(l, r, p);
        }
#define GM_UPD(q) do { q += delta; asm volatile("" : "+v"(q)); } while (0)
        if (LHALF == 1) { if (i == 0) GM_UPD(p.a0l); if (i == 1) GM_UPD(p.a0h); if (i == 12) GM_UPD(p.a1l); if (i == 14) GM_UPD(p.a1h); if (i == 17) { GM_UPD(p.b); GM_UPD(p.b9); } }
#undef GM_UPD
        SB0();
    }
}
template <bool TR, class HK>
__device__ __forceinline__ void gemm_phase(int nsteps, f32x4 (&acc)[18], int& so, LAS unsigned char* lds, int arow, int wn, int boff, int b9off, int lane,
                                           const float* ini, const volatile LAS unsigned* rdy, HK hook TRP) {
    constexpr int NRD = TR ? 14 : 9;
    const int fr = lane & 15, fq = lane >> 4, sw = (fr >> 1) & 7;
    const int a0 = so + (arow + fr) * 128 + ((fq ^ sw) << 4), a1 = so + (arow + fr) * 128 + (((4 + fq) ^ sw) << 4);
    Ptrs p = {opq(lds + a0 + wn * 4096), opq(lds + a0 + (wn ^ 1) * 4096), opq(lds + a1 + wn * 4096), opq(lds + a1 + (wn ^ 1) * 4096), opq(lds + so + boff), opq(lds + so + b9off)};
    Frag f0, f1;
    unsigned rv = 0;
    __builtin_amdgcn_s_waitcnt(0xC07F);
    BAR();
#pragma unroll
    for (int i = 0; i < NRD; ++i) frag_read<TR, 0>(f0, i, p);
    SB0();
    int delta = so == 2 * STG ? -2 * STG : STG; so += delta;
    half_step<TR, 1, 2, -1, true>(acc, f0, f1, p, delta, ini, rdy, rv TRA);
#pragma nounroll
    for (int s = 1; s < nsteps; ++s) {
        delta = so == 2 * STG ? -2 * STG : STG; so += delta;
        half_step<TR, 0, GM_BAT + 1, GM_BAT, false>(acc, f1, f0, p, 0, ini, rdy, rv TRA);
        half_step<TR, 1, 2, -1, false>(acc, f0, f1, p, delta, ini, rdy, rv TRA);
        if (TR) hook(rv);
    }
    if (TR) half_step<TR, -1, 0, -1, false>(acc, f1, f0, p, 0, ini, rdy, rv TRA);
    else {
#pragma unroll
        for (int m = 0; m <= 4; ++m) {
#pragma unroll
            for (int n = 0; n < 5; ++n) if (m < 4 && (n < 4 || m < 2)) acc[n * 4 + m] = MFMA16(cat8(f1.lo[n], f1.hi[n]), f1.a[m], acc[n * 4 + m]);
            SB0();
            if (m > 0) hook(m - 1);
            SB0();
        }
    }
}

template <class LT>
__device__ __forceinline__ void consumer_a(LAS unsigned char* lds, u32x2 (&zp)[18], LT late, int wm, int wn, int lane, unsigned char* wsp_ TRP) {
    const int fr = lane & 15, fq = lane >> 4;
    f32x4 acc[18];
    int so = 0;
    const int bw = BOFF + (4 * fq + (fr >> 2)) * 288 + (lane & 3) * 8;
    const volatile LAS unsigned* rdy = (const volatile LAS unsigned*)(lds + L_MISC + 4 + wm * 4);
    bool done = false;
    auto normalise = [&]() {
        f32x2 srs[4];
#pragma unroll
        for (int mh = 0; mh < 2; ++mh) { const LAS unsigned char* ps = opq(lds + L_SP + (wm * 64 + ((mh ^ wn) << 5) + fr) * 16); srs[2 * mh] = *(const LAS f32x2*)ps; srs[2 * mh + 1] = *(const LAS f32x2*)(ps + 256); }
        CFENCE();
#pragma unroll
        for (int mh = 0; mh < 2; ++mh) {
            const int row = wm * 64 + ((mh ^ wn) << 5) + fr;
            LAS unsigned char* pz = opq(lds + Z0 + row * ZST + wn * 160 + fq * 16);
            LAS unsigned char* pz9 = opq(lds + Z0 + row * ZST + 128 + fq * 8);
#pragma unroll
            for (int mm = 0; mm < 2; ++mm) {
                const f32x2 sr = srs[2 * mh + mm];
                u32x2 o[5];
#pragma unroll
                for (int n = 0; n < (mh == 0 ? 5 : 4); ++n) {
                    const u32x2 z = zp[n * 4 + mh * 2 + mm];
                    const f32x2 u = (f32x2){bflo(z[0]), bfhi(z[0])} * sr.x + sr.y, v = (f32x2){bflo(z[1]), bfhi(z[1])} * sr.x + sr.y;
                    o[n] = (u32x2){pk_bf16(u.x, u.y), pk_bf16(v.x, v.y)};
                }
#pragma unroll
                for (int j = 0; j < 2; ++j) *(LAS u32x4*)(pz + mm * 16 * ZST + j * 64) = (u32x4){o[2 * j][0], o[2 * j][1], o[2 * j + 1][0], o[2 * j + 1][1]};
                if (mh == 0) *(LAS u32x2*)(pz9 + mm * 16 * ZST) = o[4];
            }
        }
        CFENCE();
        if (lane == 0) *(volatile LAS unsigned*)(lds + L_MISC + 16 + (wm * 2 + wn) * 4) = 1u;
        done = true;
    };
    auto hook = [&](unsigned rv) { if (__builtin_amdgcn_readfirstlane(rv) != 0u && !done) normalise(); };
#pragma unroll
    for (int i = 0; i < 18; ++i) zp[i] = (u32x2){0u, 0u};
#pragma nounroll
    for (int it = 0; it < 2; ++it) {
        gemm_phase<true>(9, acc, so, lds, wm * 64, wn, bw + wn * 160, bw + 128, lane, nullptr, rdy, hook TRA);
        STAMP(1 + 2 * it);
        if (it == 1) late();
        if (it == 1 && !done) {
            int t0 = 0;
            while (__builtin_amdgcn_readfirstlane(*rdy) == 0u && ++t0 < SPIN_L) __builtin_amdgcn_s_sleep(1);
            normalise();
        }
#pragma unroll
        for (int i = 0; i < 18; ++i) { acc[i] = gelu4(acc[i]); zp[i] = (u32x2){pk_bf16(acc[i][0], acc[i][1]), pk_bf16(acc[i][2], acc[i][3])}; }
        if (it == 0) {
#pragma unroll
            for (int m = 0; m < 4; ++m) {
                const int nt = m < 2 ? 5 : 4;
                f32x4 s4 = acc[m];
#pragma unroll
                for (int n = 1; n < nt; ++n) s4 += acc[n * 4 + m];
                const float mean = opq(allred_q((s4[0] + s4[1]) + (s4[2] + s4[3])) * (1.0f / (16 * nt)));
                f32x4 q4 = {0.f, 0.f, 0.f, 0.f};
#pragma unroll
                for (int n = 0; n < nt; ++n) { const f32x4 d = acc[n * 4 + m] - mean; q4 += d * d; }
                const float m2 = allred_q((q4[0] + q4[1]) + (q4[2] + q4[3]));
                if (fq == 0) *(LAS f32x2*)(lds + L_SP + (wm * 64 + ((m ^ (2 * wn)) << 4) + fr) * 16 + (m >> 1) * 8) = (f32x2){mean, m2};
            }
            STAMP(2);
        }
    }
    STAMP(4);
}

__device__ __forceinline__ void consumer_b(LAS unsigned char* lds, rsrc_t ws, u32x2 T, const u32x2 (&z1)[18], const float (&bs_)[4], int pm, int pn, int wm, int wn, int lane, unsigned char* wsp_ TRP) {
    float bs[4] = {bf2f(__float_as_uint(opq(bs_[0]))), bf2f(__float_as_uint(opq(bs_[1]))), bf2f(__float_as_uint(opq(bs_[2]))), bf2f(__float_as_uint(opq(bs_[3])))};
    f32x4 acc[18];
    int so = 0;
    const int qm = 2 * pm + wm;
    const unsigned hb = WS_HF + (unsigned)qm * (18u * 16384u);
    auto epi = [&](int m) {
        if (m == 0) __builtin_amdgcn_s_setprio(3);
        const unsigned mo = (unsigned)(m ^ (2 * wn)) * 4096u;
        u32x2 hp[5];
#pragma unroll
        for (int n = 0; n < (m < 2 ? 5 : 4); ++n) {
            const u32x2 z = z1[n * 4 + m]; const f32x4 g = acc[n * 4 + m];
            const f32x2 u = (f32x2){bflo(z[0]), bfhi(z[0])} * (f32x2){g[0], g[1]}, v = (f32x2){bflo(z[1]), bfhi(z[1])} * (f32x2){g[2], g[3]};
            hp[n] = (u32x2){pk_bf16(u.x, u.y), pk_bf16(v.x, v.y)};
        }
#pragma unroll
        for (int j = 0; j < 2; ++j)
            __builtin_amdgcn_raw_buffer_store_b128((u32x4){hp[2 * j][0], hp[2 * j][1], hp[2 * j + 1][0], hp[2 * j + 1][1]}, ws,
                                                   lane * 16 + j * 1024, hb + (unsigned)pn * 16384u + mo + (unsigned)wn * 2048u, 16);
        if (m < 2) __builtin_amdgcn_raw_buffer_store_b64(hp[4], ws, lane * 8, hb + (unsigned)(16 + (pn >> 3)) * 16384u + mo + (unsigned)(pn & 7) * 512u, 16);
    };
    gemm_phase<false>(32, acc, so, lds, wm * 64, wn, BOFF + wn * 5120 + lane * 16, BOFF + 4096 + lane * 16, lane, bs, nullptr, epi TRA);
    STAMP(5);
    flag_publish(ws, WS_FH + (unsigned)(qm * 32 + pn * 2 + wn) * 128u, T, lane);
    __builtin_amdgcn_s_setprio(0);
    STAMP(6);
}

template <int TLO, int THI>
__device__ __forceinline__ void conv_task(LAS unsigned char* lds, int kt, int ut, int yt, int tl, int lane) {
    constexpr int NTL = THI - TLO + 1, ND = THI + 2;
    const int i = lane & 15, g = lane >> 4;
    const LAS unsigned char* pk = opq(lds + kt + (i & 3) * 1088 + ((i >> 2) + g) * 16);
    const LAS unsigned char* pu = opq(lds + ut + (128 + 16 * tl - 32 * g - 4 * (i >> 2) + 4 * (i & 3)) * 2 - 32);
    bf16x8 ub[ND], kf[ND];
#pragma unroll
    for (int d = 0; d < ND; ++d) { ub[ND - 1 - d] = cat8(tr_read(pu + 256 * (ND - 1 - d) + 32), tr_read(pu + 256 * (ND - 1 - d))); kf[d] = *(const LAS bf16x8*)(pk + 64 * d); }
    f32x4 acc[NTL];
#pragma unroll
    for (int s = -1; s <= THI; ++s) {
#pragma unroll
        for (int t = TLO; t <= THI; ++t) if (t >= s) {
            acc[t - TLO] = MFMA16(kf[s + 1], ub[t - s], s == -1 ? ((f32x4){0.f, 0.f, 0.f, 0.f}) : acc[t - TLO]);
        }
    }
    LAS float* y = opq((LAS float*)(lds + yt) + 32 * g + 16 * tl + i);
    LAS float* cy = opq((LAS float*)(lds + yt + 8192) + (2 * g + tl) * 4 + (i & 3));
#define DPPF(v, ctl) __int_as_float(__builtin_amdgcn_update_dpp(0, __float_as_int(v), ctl, 0xf, 0xf, true))
#pragma unroll
    for (int t = TLO; t <= THI; ++t) {
        const f32x4 a = acc[t - TLO];
        y[128 * t] = (a[0] + DPPF(a[1], 0x111)) + (DPPF(a[2], 0x112) + DPPF(a[3], 0x113));
        const float cv = DPPF(a[1], 0x10f) + (DPPF(a[2], 0x10e) + DPPF(a[3], 0x10d));
        if (i < 4) cy[32 * t] = cv;
    }
#undef DPPF
}
__device__ __forceinline__ int ytab(int c) { return Z0 + c * Y_SZ; }

__global__ __launch_bounds__(512, 2) void gm_kernel(const u16* __restrict__ x, const u16* __restrict__ logA, const u16* __restrict__ Aim, const u16* __restrict__ Cre,
                                                    const u16* __restrict__ Cim, const u16* __restrict__ logdt, const u16* __restrict__ Dsk, const u16* __restrict__ win,
                                                    const u16* __restrict__ wsp, const u16* __restrict__ bsp, const u16* __restrict__ wout, const u16* __restrict__ gw,
                                                    u16* __restrict__ out, unsigned char* wsp_) {
    extern __shared__ __attribute__((aligned(16))) unsigned char lds_[];
    LAS unsigned char* lds = (LAS unsigned char*)lds_;
    const int wid = __builtin_amdgcn_readfirstlane(threadIdx.x >> 6), lane = threadIdx.x & 63;
    const int bid = blockIdx.x, xg = bid & 7, rr = bid >> 3, bi = rr >> 2, bj = rr & 3;
    const int pm = (xg >> 2) * 8 + bi, pn = (xg & 3) * 4 + bj;
    const int qm = 2 * pm + (pn >> 3), qn = pn & 7;
    const int nch = pm < 4 ? 3 : 2;
    const rsrc_t ws = __builtin_amdgcn_make_buffer_rsrc(wsp_, 0, WS_END, 0x00020000);
    STAMP(0);
    unsigned pfw = 0;
    if (GM_PFW(wid)) {
        u64 pc0; asm volatile("s_getpc_b64 %0" : "=s"(pc0));
        const u64 q_ = (pc0 & ~127ull) + (unsigned)min((rr * GM_PFL + (lane >> 1)) * 128 + (lane & 1) * 64, GM_CODE);
        if (lane < 2 * GM_PFL) asm volatile("global_load_dword %0, %1, off" : "+v"(pfw) : "v"(q_) : "memory");
    }

    const int fr = lane & 15, fq = lane >> 4;
    const int h = wid - 6, p = wid - 4;
    const bool is_prod = (wid >> 1) == 2;
    const int ch0 = pn * 36 + pm;
    float bs[4];
    auto late = [&]() {
#pragma unroll
        for (int m = 0; m < 4; ++m) bs[m] = __uint_as_float((unsigned)bsp[pm * 128 + (wid & 1) * 64 + ((m ^ (wid & 2)) << 4) + fr]);
    };
    const int phi = (-(bi + bj)) & 3;
    const unsigned rsw = (unsigned)(((lane & 7) ^ ((4 * p + (lane >> 4)) & 7)) * 16);
#define GLDS(src, dst, aux) __builtin_amdgcn_global_load_lds((const unsigned*)(src), (LAS unsigned*)(dst), 16, 0, aux)
    auto issueS = [&](int k, int s) {
        const char* sb = (const char*)wsp + (size_t)(pm * 128 + p * 8) * 4096 + k * 128; const unsigned vs = (unsigned)(lane >> 3) * 4096u + rsw;
#pragma unroll
        for (int i = 0; i < 8; ++i) GLDS(sb + (size_t)i * (16 * 4096) + vs, lds + s + (p + 2 * i) * 1024, 0); };
    auto ord2 = [&](int s) { return GM_LAG == 0 ? s : (((s & 3) == phi) ? s : ((s - GM_LAG) & 31)); };
    u32x2 E, T;
    auto mk_tag = [&]() { u64 z = (((u64)__builtin_amdgcn_readfirstlane(E[1]) << 32) | __builtin_amdgcn_readfirstlane(E[0])) + 0x9E3779B97F4A7C15ull;
        z = (z ^ (z >> 30)) * 0xBF58476D1CE4E5B9ull; z = (z ^ (z >> 27)) * 0x94D049BB133111EBull; z ^= z >> 31; T = (u32x2){(unsigned)z, (unsigned)(z >> 32)}; };
    u32x2 zp[18];
#define STEP(lim) do { if (nb < (lim)) { BAR(); ++nb; } } while (0)
#define STEPS_TO(lim) _Pragma("nounroll") while (nb < (lim)) { BAR(); ++nb; }
    int nb = 0;
    const int kc = wid == 7 ? 2 : wid == 1 || wid == 2 ? wid - 1 : 3;
    if (kc < nch) {
        const int kch = ch0 + 16 * kc, ki = kch * 64 + lane;
        const unsigned pA = ((const unsigned*)logA)[ki], pI = ((const unsigned*)Aim)[ki], pR = ((const unsigned*)Cre)[ki], pC = ((const unsigned*)Cim)[ki], pT = logdt[kch];
        LAS u32x2* tw = opq((LAS u32x2*)(lds + Z0 + kc * 8448) + lane);
        const LAS unsigned char* trd = opq(lds + Z0 + kc * 8448 + fr * 528 + fq * 16);
        const float dt = rbf(__expf(bf2f(pT)));
        const f32x2 ar = (f32x2){rbf(__expf(bflo(pA))), rbf(__expf(bfhi(pA)))} * dt * -1.44269504f, ai = (f32x2){bflo(pI), bfhi(pI)} * dt * 0.15915494f;
        const f32x2 cr = (f32x2){bflo(pR), bfhi(pR)} * dt, ci = (f32x2){bflo(pC), bfhi(pC)} * dt;
        float cm = fmaxf(fmaxf(fabsf(cr.x), fabsf(ci.x)), fmaxf(fabsf(cr.y), fabsf(ci.y)));
#define DPPF(v, ctl) __int_as_float(__builtin_amdgcn_update_dpp(0, __float_as_int(v), ctl, 0xf, 0xf, true))
        cm = fmaxf(cm, DPPF(cm, 0x121)); cm = fmaxf(cm, DPPF(cm, 0x122)); cm = fmaxf(cm, DPPF(cm, 0x124)); cm = fmaxf(cm, DPPF(cm, 0x128));
#undef DPPF
        { auto r = __builtin_amdgcn_permlane16_swap(__float_as_uint(cm), __float_as_uint(cm), false, false); cm = fmaxf(__uint_as_float(r[0]), __uint_as_float(r[1]));
          auto q = __builtin_amdgcn_permlane32_swap(__float_as_uint(cm), __float_as_uint(cm), false, false); cm = fmaxf(fmaxf(__uint_as_float(q[0]), __uint_as_float(q[1])), 1e-30f); }
        struct C2 { f32x2 x, y; };
        auto cexp = [&](float p_) { const f32x2 e = ar * p_, t = ai * p_;
            const f32x2 mg = {__builtin_amdgcn_exp2f(e.x), __builtin_amdgcn_exp2f(e.y)}; const float t0 = __builtin_amdgcn_fractf(t.x), t1 = __builtin_amdgcn_fractf(t.y);
            return C2{mg * (f32x2){__builtin_amdgcn_cosf(t0), __builtin_amdgcn_cosf(t1)}, mg * (f32x2){__builtin_amdgcn_sinf(t0), __builtin_amdgcn_sinf(t1)}}; };
        auto cmul = [](C2 a, C2 b) { return C2{__builtin_elementwise_fma(a.x, b.x, -(a.y * b.y)), __builtin_elementwise_fma(a.x, b.y, a.y * b.x)}; };
        auto put = [&](int row, C2 v, bool conj) { tw[row * 66] = conj ? (u32x2){pk_f16(v.x.x, -v.y.x), pk_f16(v.x.y, -v.y.y)} : (u32x2){pk_f16(v.x.x, v.y.x), pk_f16(v.x.y, v.y.y)}; };
        C2 wp[16], zr[8], vp[4], cv[17];
        wp[1] = cexp(8.f); zr[1] = cexp(1.f); vp[0] = cexp(128.f);
        tw[0] = (u32x2){pk_f16(1.f, 0.f), pk_f16(1.f, 0.f)};
#pragma unroll
        for (int j = 1; j < 16; ++j) { if (j > 1) wp[j] = cmul(wp[j >> 1], wp[j - (j >> 1)]); put(j, wp[j], true); }
        CFENCE();
        u32x4 fb[8];
#pragma unroll
        for (int ks = 0; ks < 8; ++ks) fb[ks] = *(const LAS u32x4*)(trd + ks * 64);
        CFENCE();
#pragma unroll
        for (int r = 2; r < 8; ++r) zr[r] = cmul(zr[r >> 1], zr[r - (r >> 1)]);
#pragma unroll
        for (int k = 1; k < 4; ++k) vp[k] = cmul(vp[k - 1], vp[k - 1]);
        const float rc = __builtin_amdgcn_rcpf(cm);
        cv[0] = C2{cr * rc, ci * rc};
#pragma unroll
        for (int k = 1; k < 8; ++k) { const int lg = k >= 4 ? 2 : k >= 2 ? 1 : 0; cv[k] = cmul(cv[k - (1 << lg)], vp[lg]); }
        LAS float* kb_ = (LAS float*)(lds + L_KD + kc * 3072);
        const float sc = 2.0f * cm;
        u32x4 fa[3][8];
#pragma unroll
        for (int tt = 0; tt < 3; ++tt) {
#pragma unroll
            for (int i = 0; i < (tt == 2 ? 9 : 16); ++i) {
                const int r = tt == 0 ? (i & 7) : tt == 1 ? (i < 8 ? 2 * (i & 3) : 4 * (i & 1)) : 0;
                const int hh = tt == 0 ? (i >> 3) : tt == 1 ? (i < 8 ? 2 + (i >> 2) : 4 + ((i - 8) >> 1)) : 8 + i;
                if (tt == 2) cv[hh] = cmul(cv[hh - 8], vp[3]);
                put(i, r == 0 ? cv[hh] : cmul(cv[hh], zr[r]), false);
            }
            CFENCE();
#pragma unroll
            for (int ks = 0; ks < 8; ++ks) fa[tt][ks] = *(const LAS u32x4*)(trd + ks * 64);
            CFENCE();
            SB0();
        }
        f32x4 kd[3] = {{0.f, 0.f, 0.f, 0.f}, {0.f, 0.f, 0.f, 0.f}, {0.f, 0.f, 0.f, 0.f}};
#pragma unroll
        for (int ks = 0; ks < 8; ++ks)
#pragma unroll
            for (int tt = 0; tt < 3; ++tt) kd[tt] = __builtin_amdgcn_mfma_f32_16x16x32_f16(__builtin_bit_cast(f16x8, fa[tt][ks]), __builtin_bit_cast(f16x8, fb[ks]), kd[tt], 0, 0, 0);
#pragma unroll
        for (int tt = 0; tt < 3; ++tt) {
            LAS float* kw = opq(kb_ + (tt == 0 ? 128 * (fq >> 1) + 4 * (fq & 1) + 8 * fr : tt == 1 ? 256 + 64 * fq + (fq < 2 ? 4 : 2) * fr : 512 + 64 * fq + fr));
            LAS float* kwb = tt == 1 ? opq(kw + (fq < 2 ? 2 : 32)) : kw + (tt == 0 ? 2 : 32);
            kw[0] = kd[tt][0] * sc; kw[tt == 2 ? 16 : 1] = kd[tt][1] * sc; kwb[0] = kd[tt][2] * sc; kwb[tt == 2 ? 16 : 1] = kd[tt][3] * sc;
        }
        WAITL0();
    }
#define IC(n) std::integral_constant<int, n>{}
    if (GM_PFW(wid)) { WAITV(0); asm volatile("" :: "v"(pfw)); }
    if (wid < 6) STAMP(10);
    if (is_prod) {
        const unsigned vx = (unsigned)(lane >> 3) * 1152u + rsw;
        unsigned wv[9];
        const char* xb = (const char*)x + (size_t)(pm * 128 + p * 8) * 1152;
        const char* wb = (const char*)win + pn * 288;
        auto issueX = [&](int k, int s) {
#pragma unroll
            for (int i = 0; i < 8; ++i) GLDS(xb + (size_t)i * (16 * 1152) + k * 128 + vx, lds + s + (p + 2 * i) * 1024, 0); };
        auto issueW = [&](int k, int coloff, int s) {
            const char* b = wb + (size_t)k * (64 * 9216) + coloff;
#pragma unroll
            for (int i = 0; i < 9; ++i) GLDS(b + wv[i], lds + s + BOFF + (p + 2 * i) * 1024, 0); };
        auto ord1 = [&](int s) { return (GM_LAG == 0 || (s & 3) == phi) ? s : (s >= 4 ? s - 4 : (s == 0 ? 8 : s + 4)); };
        issueX(ord1(0), 0);
        SB0();
#pragma unroll
        for (int i = 0; i < 9; ++i) { const int c = (p + 2 * i) * 64 + lane, rho = c / 18, ch = c - rho * 18;
            const int kap = (rho & 32) + 8 * ((rho >> 2) & 3) + 4 * ((rho >> 4) & 1) + (rho & 3); wv[i] = (unsigned)kap * 9216u + (unsigned)ch * 16u; }
        issueW(ord1(0), FH * 2, 0); issueX(ord1(1), STG); issueW(ord1(1), FH * 2, STG);
        issueX(ord1(2), 2 * STG); issueW(ord1(2), FH * 2, 2 * STG);
        E = flag_ld(ws, WS_E);
        unsigned wm_ = 0;
        auto touch = [&](const void* base, unsigned off, unsigned last) { const char* q_ = (const char*)base + min(off + lane * 64u, last); asm volatile("global_load_dword %0, %1, off" : "+v"(wm_) : "v"(q_) : "memory"); };
        if (GM_PW & 1) {
#pragma unroll
            for (int i = 0; i < 2; ++i) touch(x, bid * 9216u + p * 4608u + i * 4096u, bid * 9216u + p * 4608u + 4544u);
#pragma unroll
            for (int i = 0; i < 3; ++i) touch(win, bid * 20736u + p * 10368u + i * 4096u, bid * 20736u + p * 10368u + 10304u);
        }
        int so = 0;
        WAITV(35); PBAR();
#pragma nounroll
        for (int S = 1; S < 16; ++S) {
            if (S >= 8 && S <= 10) WAITV(9); else WAITV(17);
            PBAR();
            const int t = S + 2, k = ord1(t < 9 ? t : t < 12 ? t - 3 : t - 12);
            if (t < 9 || t >= 12) issueX(k, so);
            issueW(k, t >= 9 ? 0 : FH * 2, so); so = nextbuf(so);
            if ((GM_PW & 2) && S == GM_PWS) {
#pragma unroll
                for (int i = 0; i < 4; ++i) touch(wsp, bid * 32768u + p * 16384u + i * 4096u, 0x7fffffffu);
            }
        }
        WAITV(17); PBAR(); issueS(2 * pm, 0);
        WAITV(8); PBAR(); issueS(2 * pm + 1, STG);
        asm volatile("" :: "v"(wm_));
        STAMP(1);
    } else if (wid < 4) {
        E = flag_ld(ws, WS_E);
        consumer_a(lds, zp, late, wid & 1, wid >> 1, lane, wsp_ TRA);
    } else {
        if (h == 0 && lane < 12) *(LAS unsigned*)(lds + L_MISC + lane * 4) = 0u;
        const int row0 = lane / 9, chk0 = lane - row0 * 9;
        const int u = (pn >> 3) * 16 + pm, nblk = u < 8 ? 3 : 2;
        u32x2 uc[18]; u32x4 xv[9], gv[9], wv[3][5];
        if (h == 0) {
            { const char* xb = (const char*)x + (size_t)pm * 128 * 1152 + pn * 72; unsigned off = row0 * 1152 + chk0 * 8; int ch = chk0;
#pragma unroll
              for (int i = 0; i < 9; ++i) { uc[i] = *(const u32x2*)(xb + off); uc[9 + i] = *(const u32x2*)(xb + 64 * 1152 + off); off += 7 * 1152 + 8; if (++ch == 9) { ch = 0; off += 1152 - 72; } } }
            { const char* xr = (const char*)x + (size_t)(pm * 128 + pn * 8 + (lane >> 3)) * 1152 + (lane & 7) * 16; const char* gr = (const char*)gw + (lane & 7) * 16;
#pragma unroll
              for (int i = 0; i < 9; ++i) { xv[i] = *(const u32x4*)(xr + i * 128); gv[i] = *(const u32x4*)(gr + i * 128); } }
        } else {
            unsigned wo_[5], hi_[5];
            { int row = row0, ch = chk0;
#pragma unroll
              for (int i = 0; i < 5; ++i) { const int rw = row < 32 ? row : 31; wo_[i] = (rw & 15) * 1152 + ch * 16; hi_[i] = rw >> 4; row += 7; if (++ch == 9) { ch = 0; ++row; } } }
#pragma unroll
            for (int b = 0; b < 3; ++b) {
                const int blk = (b < nblk) ? u + 32 * b : u, st = blk >> 2, jb = blk & 3;
                const int fA = st < 16 ? st * 144 + (jb < 2 ? 32 * jb : 16 + 32 * jb) : ((st - 16) * 8 + 2 * jb) * 144 + 64, dB = (st < 16 ? 16 : 144) * 1152;
                const char* wb_ = (const char*)wout + (size_t)fA * 1152 + qn * 144;
#pragma unroll
                for (int i = 0; i < 5; ++i) wv[b][i] = *(const u32x4*)(wb_ + (wo_[i] + hi_[i] * dB));
            }
        }
        E = flag_ld(ws, WS_E);
        if (lane < 24) *(LAS u32x4*)(lds + L_KT + (lane >> 3) * KT_SZ + (2 * h + ((lane >> 2) & 1)) * 1088 + (lane & 3) * 16) = (u32x4){0u, 0u, 0u, 0u};
        WAITL0();
        STAMP(10);
        __builtin_amdgcn_s_setprio(GM_HPRIO);
        {
            float kv[2][3];
#pragma nounroll
            for (int ei = 0; ei < 9; ++ei) {
                BAR();
                if (ei > 0) {
                    LAS unsigned char* kt = opq(lds + L_KT + (2 * h + ((ei - 1) >> 2)) * 1088 + 64 + ((ei - 1) & 3) * 256 + lane * 2);
#pragma unroll
                    for (int j = 0; j < 2; ++j)
#pragma unroll
                        for (int c = 0; c < 3; ++c) *(LAS u16*)(kt + c * KT_SZ + j * 128) = (u16)pk_bf16(kv[j][c], 0.f);
                }
#pragma unroll
                for (int j = 0; j < 2; ++j) {
                    const int jj = (ei & 3) * 2 + j, e = jj >= 4 ? 3 : jj >= 2 ? 2 : jj, lag = 256 * jj + 4 * lane + 2 * h + ((ei >> 2) & 1);
                    const int idx = e == 0 ? lag : 128 * e + ((lag + (1 << (e - 1)) - 1 + ((lag >> e) & 1)) >> e);
                    const LAS float* q = opq((const LAS float*)(lds + L_KD) + idx);
#pragma unroll
                    for (int c = 0; c < 3; ++c) kv[j][c] = q[c * 768];
                }
            }
        }
        __builtin_amdgcn_s_setprio(0);
        if (h == 0) {
            LAS unsigned char* pzs = opq(lds + Z0 + lane * 8);
#pragma unroll
            for (int i = 0; i < 18; ++i) *(LAS u32x2*)(pzs + i * 512) = uc[i];
            CFENCE();
            { const LAS unsigned char* pt = opq(lds + Z0 + (8 * (4 * fq + (lane & 3)) + (fr >> 2)) * 72);
              const unsigned ub = WS_UT + (unsigned)(pn * 36 + (lane & 3)) * 4096u + (unsigned)(pm * 128 + 8 * (4 * fq + (fr >> 2))) * 2u;
#pragma unroll
              for (int cc = 0; cc < 9; ++cc) __builtin_amdgcn_raw_buffer_store_b128(__builtin_bit_cast(u32x4, cat8(tr_read(pt + cc * 8), tr_read(pt + 288 + cc * 8))), ws, ub + cc * 16384u, 0, 16); }
            float a = 0.f;
#pragma unroll
            for (int i = 0; i < 9; ++i)
#pragma unroll
                for (int j = 0; j < 4; ++j) { const unsigned xa = xv[i][j], ga = gv[i][j]; a = __builtin_amdgcn_fdot2_f32_bf16(__builtin_bit_cast(bf16x2, xa), __builtin_bit_cast(bf16x2, ga), a, false); }
#define DPPF(v, ctl) __int_as_float(__builtin_amdgcn_update_dpp(0, __float_as_int(v), ctl, 0xf, 0xf, true))
            a += DPPF(a, 0xb1); a += DPPF(a, 0x4e); a += DPPF(a, 0x141);
#undef DPPF
            const float gt = rbf(__builtin_amdgcn_rcpf(1.0f + __expf(-rbf(a))));
            if ((lane & 7) == 0) __builtin_amdgcn_raw_buffer_store_b32(__float_as_uint(gt), ws, WS_GATE + (unsigned)(pm * 128 + pn * 8 + (lane >> 3)) * 4u, 0, 16);
        } else {
            LAS unsigned char* pws = opq(lds + Z0 + 9216 + lane * 16);
            const LAS unsigned char* ptw = opq(lds + Z0 + 9216 + (4 * fq + (fr >> 2)) * 144 + (lane & 3) * 8);
#pragma unroll
            for (int b = 0; b < 3; ++b) {
                if (b < nblk) {
#pragma unroll
                    for (int i = 0; i < 5; ++i) if (i < 4 || lane < 32) *(LAS u32x4*)(pws + i * 1024) = wv[b][i];
                    CFENCE();
                    const unsigned ob = WS_WOF + (unsigned)qn * (18u * 18432u) + (unsigned)(u + 32 * b) * 4608u;
#pragma unroll
                    for (int nn = 0; nn < 5; ++nn) {
                        const u32x4 v = __builtin_bit_cast(u32x4, cat8(tr_read(ptw + nn * 32), tr_read(ptw + 2304 + nn * 32)));
                        if (nn < 4) __builtin_amdgcn_raw_buffer_store_b128(v, ws, ob + nn * 1024 + lane * 16, 0, 16);
                        else if (fr < 8) __builtin_amdgcn_raw_buffer_store_b128(v, ws, ob + 4096 + (fq * 8 + fr) * 16, 0, 16);
                    }
                    CFENCE();
                }
            }
        }
        nb = 9;
        mk_tag();
        WAITL0();
        STEPS_TO(10);
        __builtin_amdgcn_s_setprio(GM_HPRIO);
        STAMP(1);
        {
            const f32x4 sp = *(const LAS f32x4*)(lds + L_SP + (h * 64 + lane) * 16);
            const float dm = sp[0] - sp[2];
            const f32x2 mine = {(80.f * sp[0] + 64.f * sp[2]) * (1.0f / 144.f), sp[1] + sp[3] + (80.f * 64.f / 144.f) * dm * dm};
            __builtin_amdgcn_raw_buffer_store_b128((u32x4){T[0], __float_as_uint(mine.x), T[1], __float_as_uint(mine.y)}, ws, WS_ST + (unsigned)((pm * 16 + pn) * 128 + h * 64 + lane) * 16u, 0, 16);
            STEP(18);
            const unsigned so_ = WS_ST + (unsigned)(pm * 16 * 128 + h * 64 + lane) * 16u;
            int t0 = 0;
            u32x4 st[16];
            for (;;) {
                CFENCE();
#pragma unroll
                for (int j = 0; j < 16; ++j) st[j] = __builtin_amdgcn_raw_buffer_load_b128(ws, so_ + j * 2048u, 0, 16);
                STEP(18);
                unsigned bad = 0;
#pragma unroll
                for (int j = 0; j < 16; ++j) bad |= (st[j][0] ^ T[0]) | (st[j][2] ^ T[1]);
                if (__builtin_amdgcn_ballot_w64(bad != 0u) == 0ull) break;
                if (++t0 > SPIN_G) break;
            }
            STAMP(2);
            STEP(18);
            float mean = 0.f, m2 = 0.f;
#pragma unroll
            for (int j = 0; j < 16; ++j) mean += __uint_as_float(st[j][1]);
            mean *= (1.0f / 16.f);
#pragma unroll
            for (int j = 0; j < 16; ++j) { const float d = __uint_as_float(st[j][1]) - mean; m2 += __uint_as_float(st[j][3]) + 144.f * d * d; }
            const float rstd = __builtin_amdgcn_rsqf(m2 * (1.0f / FH) + 1e-5f);
            *(LAS f32x2*)(lds + L_SP + (h * 64 + lane) * 16) = (f32x2){rstd, -mean * rstd};
            CFENCE();
            if (lane == 0) *(volatile LAS unsigned*)(lds + L_MISC + 4 + h * 4) = 1u;
            const volatile LAS unsigned* zd = (const volatile LAS unsigned*)(lds + L_MISC + 16 + h * 8 + (lane & 1) * 4);
            for (;;) {
                STEP(18);
                if (__builtin_amdgcn_ballot_w64(*zd == 0u) == 0ull) break;
                if (++t0 > SPIN_L) break;
            }
            CFENCE();
            const LAS unsigned char* ptz = opq(lds + Z0 + (h * 64 + 8 * fq + (fr >> 2)) * ZST + (lane & 3) * 16);
            const LAS unsigned char* ptz9 = opq(lds + Z0 + (h * 64 + 8 * fq + (fr >> 2)) * ZST + 128 + (lane & 3) * 8);
            const unsigned zo = WS_Z2F + (unsigned)((pn * 64 + pm * 4 + h * 2) * 9) * 1024u + lane * 16;
            u32x4 zf[18];
#pragma unroll
            for (int i = 0; i < 18; ++i) {
                const int n = i % 9, n2 = n < 4 ? n : n - 5;
                const LAS unsigned char* pt = (n == 4 ? ptz9 : ptz + (n > 4 ? 160 : 0) + (n2 >> 1) * 64 + (n2 & 1) * 8) + (i / 9) * 32 * ZST;
                zf[i] = __builtin_bit_cast(u32x4, cat8(tr_read(pt), tr_read(pt + 4 * ZST)));
                __builtin_amdgcn_raw_buffer_store_b128(zf[i], ws, zo + i * 1024, 0, 16);
                if (i == 5 || i == 11) STEP(18);
            }
            STEP(18);
            flag_publish(ws, WS_FZ + (unsigned)(pn * 32 + pm * 2 + h) * 128u, T, lane);
            STAMP(3);
            STEPS_TO(18);
            LAS unsigned char* pr_ = opq(lds + h * STG + BOFF + lane * 16);
#pragma unroll
            for (int i = 0; i < 18; ++i) *(LAS u32x4*)(pr_ + i * 1024) = zf[i];
            WAITL0();
        }
    }

    float dks[3];
    const char* lin = (const char*)wsp_ + p * 1024 + lane * 16;
    auto issueLA = [&](unsigned off, int s) {
#pragma unroll
        for (int i = 0; i < 8; ++i) GLDS(lin + off + i * 2048, lds + s + (p + 2 * i) * 1024, 16); };
    auto issueLB = [&](unsigned off, int s) {
#pragma unroll
        for (int i = 0; i < 9; ++i) GLDS(lin + off + i * 2048, lds + s + BOFF + (p + 2 * i) * 1024, 16); };
    auto ord3 = [&](int s) { return (GM_LAG == 0 || s >= 16) ? s : (((s & 3) == phi) ? s : ((s - GM_LAG) & 15)); };
    u32x2 pv = {0u, 0u}; unsigned rdy = 0; bool pend = false;
    const char* fp = (const char*)wsp_ + WS_FZ + ((unsigned)pn * 32u + (lane & 31)) * 128u;
    auto poll_issue = [&]() { asm volatile("global_load_dwordx2 %0, %1, off sc1" : "+v"(pv) : "v"(fp) : "memory"); };
    auto poll_take = [&]() { asm volatile("" : "+v"(pv)); rdy |= (unsigned)__builtin_amdgcn_ballot_w64(pv[0] == T[0] && pv[1] == T[1]); };
    auto need = [&](unsigned bits) {
        if (pend) { poll_take(); pend = false; }
        if ((rdy & bits) != bits) {
            int t0 = 0;
            do { poll_issue(); WAITV(0); poll_take(); } while ((rdy & bits) != bits && ++t0 < SPIN_G);
        }
        if (rdy != ~0u) { poll_issue(); pend = true; }
    };
    const unsigned hb = WS_HF + (unsigned)qm * (18u * 16384u), ob = WS_WOF + (unsigned)qn * (18u * 18432u);
    if (is_prod) {
        mk_tag();
        const unsigned zb = WS_Z2F + (unsigned)pn * (64u * 9216u);
        int so = 2 * STG;
        const int r0 = (ord2(0) >> 1) == pm ? ord2(1) : ord2(0), r1 = (ord2(1) >> 1) == pm ? ord2(0) : ord2(1);
        { int k2 = ord2(2), k3 = ord2(3); if ((k2 >> 1) == pm) k2 = (k2 & 1) ? r1 : r0; if ((k3 >> 1) == pm) k3 = (k3 & 1) ? r1 : r0;
          const unsigned b_ = (1u << k2) | (1u << k3);
#pragma nounroll
          for (int i = 0; i < GM_PREPOLL && (rdy & b_) != b_; ++i) { poll_issue(); WAITV(0); poll_take(); }
          WAITV(0); }
        STAMP(2);
        bool second = false;
#pragma nounroll
        for (int S = 18; S < 48; ++S) {
            PBAR();
            int k = ord2(S - 16); if ((k >> 1) == pm) k = (k & 1) ? r1 : r0;
            if (!second && !pend && rdy == ~0u) { second = true; rdy = 0; fp = (const char*)wsp_ + WS_FZ + ((unsigned)(pn ^ 8) * 32u + (lane & 31)) * 128u; }
            need(second ? 0u : 1u << k); issueS(k, so); issueLB(zb + (unsigned)k * 18432u, so); so = nextbuf(so);
            WAITV(17);
        }
        PBAR();
        if (!second) { WAITV(0); need(~0u); rdy = 0; fp = (const char*)wsp_ + WS_FZ + ((unsigned)(pn ^ 8) * 32u + (lane & 31)) * 128u; }
        need(~0u);
        issueLB(ob + (unsigned)ord3(0) * 18432u, 2 * STG);
        WAITV(9); PBAR();
        issueLB(ob + (unsigned)ord3(1) * 18432u, 0);
        STAMP(3);
    } else
    if (wid < 4) {
        mk_tag();
        consumer_b(lds, ws, T, zp, bs, pm, pn, wid & 1, wid >> 1, lane, wsp_ TRA);
    } else {
        __builtin_amdgcn_s_setprio(GM_HPRIO);
        {   const unsigned fo = WS_FZ + (unsigned)(pn * 32 + (lane & 31)) * 128u;
            int t0 = 0;
            for (;;) {
                STEP(43);
                CFENCE();
                const u32x2 v = flag_ld(ws, fo);
                STEP(43);
                if (flag_all(v, T, true)) break;
                if (++t0 > SPIN_G) break;
            } }
#pragma unroll
        for (int c = 0; c < 3; ++c) {
            const char* us = (const char*)wsp_ + WS_UT + (unsigned)(ch0 + (c < nch ? 16 * c : 0)) * 4096u + h * 2048 + lane * 16;
#pragma unroll
            for (int j = 0; j < 2; ++j) GLDS(us + j * 1024, lds + L_UT + c * UT_SZ + 256 + h * 2048 + j * 1024, 16);
        }
#pragma unroll
        for (int c = 0; c < 3; ++c) dks[c] = __uint_as_float((unsigned)Dsk[ch0 + (c < nch ? 16 * c : 0)]);
        STEPS_TO(47);
        WAITV(0);
#pragma unroll
        for (int c = 0; c < 3; ++c) dks[c] = opq(bf2f(__float_as_uint(dks[c])));
        if (lane < 48) *(LAS u32x4*)(lds + L_UT + (lane >> 4) * UT_SZ + h * 4352 + (lane & 15) * 16) = (u32x4){0u, 0u, 0u, 0u};
        WAITL0();
        STEPS_TO(50);
        STAMP(4);
    }

    while (wid != 5) {
        __builtin_amdgcn_wave_barrier();
        unsigned tk = 0;
        if (lane == 0) tk = __hip_atomic_fetch_add((LAS unsigned*)(lds + L_MISC), 1u, __ATOMIC_RELAXED, __HIP_MEMORY_SCOPE_WORKGROUP);
        __builtin_amdgcn_wave_barrier();
        tk = __builtin_amdgcn_readfirstlane(tk);
        if (tk >= (unsigned)(4 * nch)) break;
        const int c = tk >> 2, tl = tk & 1;
        if (tk & 2) conv_task<0, 10>(lds, L_KT + c * KT_SZ, L_UT + c * UT_SZ, ytab(c), tl, lane);
        else conv_task<11, 15>(lds, L_KT + c * KT_SZ, L_UT + c * UT_SZ, ytab(c), tl, lane);
    }
    WAITL0();
    STAMP(7);
    if (is_prod) {
        if (p == 1) {
            fp = (const char*)wsp_ + WS_FH + ((unsigned)qm * 32u + (lane & 31)) * 128u; rdy = 0;
            need(~0u);
            STAMP(4);
#pragma unroll
            for (int i = 0; i < 16; ++i) GLDS(lin - 1024 + hb + (unsigned)ord3(0) * 16384u + i * 1024, lds + 2 * STG + i * 1024, 16);
#pragma unroll
            for (int i = 0; i < 16; ++i) GLDS(lin - 1024 + hb + (unsigned)ord3(1) * 16384u + i * 1024, lds + i * 1024, 16);
            WAITV(16);
        } else WAITV(0);
        int so = STG;
#pragma nounroll
        for (int S = 50; S < 66; ++S) {
            PBAR();
            const int k = ord3(S - 48); issueLA(hb + (unsigned)k * 16384u, so); issueLB(ob + (unsigned)k * 18432u, so); so = nextbuf(so);
            WAITV(17);
        }
        PBAR();
        WAITV(0); PBAR();
        STAMP(5); PDUMP();
        if (bid == 0 && p == 0 && lane == 0) __builtin_amdgcn_raw_buffer_store_b64(T, ws, WS_E, 0, 16);
        return;
    }

    if (wid < 4) {
        f32x4 acc[5];
        struct F3 { s16x4 alo[4], ahi[4]; bf16x8 b[20]; } f0, f1;
        const LAS unsigned char* ph = lds + 2 * STG + wid * 4096 + lane * 16;
        const LAS unsigned char* pw = lds + 2 * STG + BOFF + lane * 16;
        const LAS unsigned char* pw4 = lds + 2 * STG + BOFF + 4096 + (fq * 8 + (fr & 7)) * 16;
        auto rd3 = [&](F3& f, int i, bool single) {
            const int j = i / 6, r = i % 6;
            if (r == 0) {
                if (!single) { const bf16x8 v = *(const LAS bf16x8*)(ph + j * 1024); f.alo[j] = __builtin_shufflevector(v, v, 0, 1, 2, 3); f.ahi[j] = __builtin_shufflevector(v, v, 4, 5, 6, 7); }
                else { f.alo[j] = *(const LAS s16x4*)(ph + j * 1024); f.ahi[j] = *(const LAS s16x4*)(ph + j * 1024 + 512); }
            } else if (r < 5) f.b[j * 5 + r - 1] = *(const LAS bf16x8*)(pw + j * 4608 + (r - 1) * 1024);
            else f.b[j * 5 + 4] = *(const LAS bf16x8*)(pw4 + j * 4608);
        };
        int so = 2 * STG;
        auto step3 = [&](const F3& c, F3& l, int mode  , bool first) {
            const int delta = so == 2 * STG ? -2 * STG : STG; so += delta;
#pragma unroll
            for (int i = 0; i < 20; ++i) {
                const int j = i / 5, n = i % 5;
                acc[n] = MFMA16(c.b[i], cat8(c.alo[j], c.ahi[j]), (first && j == 0) ? ((f32x4){0.f, 0.f, 0.f, 0.f}) : acc[n]);
                if (mode < 2 && i < 12) { rd3(l, 2 * i, mode == 1); rd3(l, 2 * i + 1, mode == 1); }
                if (mode < 2) { if (i == 13) { ph += delta; asm volatile("" : "+v"(ph)); } if (i == 14) { pw += delta; asm volatile("" : "+v"(pw)); } if (i == 15) { pw4 += delta; asm volatile("" : "+v"(pw4)); } }
                SB0();
            }
        };
        __builtin_amdgcn_s_waitcnt(0xC07F);
        BAR();
#pragma unroll
        for (int i = 0; i < 24; ++i) rd3(f0, i, false);
        ph -= 2 * STG; pw -= 2 * STG; pw4 -= 2 * STG; so = 0;
        WAITL0(); BAR(); step3(f0, f1, 0, true);
#pragma nounroll
        for (int j = 0; j < 7; ++j) {
            WAITL0(); BAR(); step3(f1, f0, 0, false);
            WAITL0(); BAR(); step3(f0, f1, 0, false);
        }
        ph = opq(ph - lane * 8);
        WAITL0(); BAR(); step3(f1, f0, 1, false);
        WAITL0(); BAR();
        const float g = *(const LAS float*)(lds + L_MISC + 64 + (16 * wid + fr) * 4);
        const LAS unsigned char* pat = opq(lds + L_AT + (4 * fq + (fr >> 2)) * 128 + (16 * wid + 4 * (lane & 3)) * 2);
        u32x2 av[5];
#pragma unroll
        for (int n = 0; n < 5; ++n) av[n] = __builtin_bit_cast(u32x2, tr_read(pat + n * 2048));
        step3(f0, f1, 1, false);
        step3(f1, f0, 2, false);
        STAMP(8);
        const rsrc_t ors = __builtin_amdgcn_make_buffer_rsrc(out, 0, SEQ * DM * 2, 0x00020000);
        const float g1 = rbf(1.0f - g);
#pragma unroll
        for (int n = 0; n < 5; ++n) {
            const f32x4 b = acc[n]; const u32x2 a = av[n];
            const u32x2 o = {pk_bf16(g * bflo(a[0]) + g1 * b[0], g * bfhi(a[0]) + g1 * b[1]), pk_bf16(g * bflo(a[1]) + g1 * b[2], g * bfhi(a[1]) + g1 * b[3])};
            if (n < 4 || fq < 2) __builtin_amdgcn_raw_buffer_store_b64(o, ors, (unsigned)((qm * 64 + 16 * wid + fr) * 1152 + (qn * 72 + 16 * n + 4 * fq) * 2), 0, GM_OAUX);
        }
    } else {
        int d = pn * 36 + pm;
#pragma nounroll
        for (int c = 0; c < nch; ++c) {
            const float dsk = c == 0 ? dks[0] : c == 1 ? dks[1] : dks[2];
#pragma unroll
            for (int k = 0; k < 2; ++k) {
                BAR(); ++nb;
                const int l0 = h * 1024 + k * 512 + lane * 8;
                f32x4 cv = *(const LAS f32x4*)(lds + ytab(c) + 8192 + (l0 >= 16 ? (l0 >> 4) - 1 : 0) * 16);
                if ((lane & 1) || l0 == 0) cv = (f32x4){0.f, 0.f, 0.f, 0.f};
                const f32x4 y0 = *(const LAS f32x4*)(lds + ytab(c) + l0 * 4) + cv, y1 = *(const LAS f32x4*)(lds + ytab(c) + l0 * 4 + 16);
                const u32x4 uv = *(const LAS u32x4*)(lds + L_UT + c * UT_SZ + 256 + l0 * 2);
                u32x4 o;
#pragma unroll
                for (int j = 0; j < 4; ++j) {
                    const float ya = j < 2 ? y0[2 * j] : y1[2 * j - 4], yb = j < 2 ? y0[2 * j + 1] : y1[2 * j - 3];
                    const unsigned yp = pk_bf16(ya, yb), up = pk_bf16(bflo(uv[j]) * dsk, bfhi(uv[j]) * dsk);
                    o[j] = pk_bf16(bflo(yp) + bflo(up), bfhi(yp) + bfhi(up));
                }
                __builtin_amdgcn_raw_buffer_store_b128(o, ws, WS_AT + (unsigned)d * 4096u + l0 * 2, 0, 16);
            }
            d += 16;
        }
        STEPS_TO(58);
        flag_publish(ws, WS_FA + (unsigned)((pn >> 1) * 64 + (pn & 1) * 32 + pm * 2 + h) * 128u, T, lane);
        STAMP(5);
        STEPS_TO(61);
        if (h == 0) {
            const unsigned fo = WS_FA + (unsigned)(qn * 64 + lane) * 128u;
            int t0 = 0;
            for (;;) {
                CFENCE();
                const u32x2 v = flag_ld(ws, fo);
                STEP(67); STEP(67);
                if (flag_all(v, T, true)) break;
                if (++t0 > SPIN_G) break;
            }
            const char* as = (const char*)wsp_ + WS_AT + (unsigned)(qn * 72 + (lane >> 3)) * 4096u + qm * 128 + (lane & 7) * 16;
#pragma unroll
            for (int i = 0; i < 9; ++i) GLDS(as + i * 32768, lds + L_AT + i * 1024, 16);
        } else __builtin_amdgcn_global_load_lds((const unsigned*)((const char*)wsp_ + WS_GATE + (unsigned)(qm * 64 + lane) * 4u), (LAS unsigned*)(lds + L_MISC + 64), 4, 0, 16);
        STEPS_TO(67);
        WAITV(0);
        BAR();
    }
    STAMP(9); TRDUMP();
}
}

extern "C" void kernel_launch(void* const* d_in, const int* in_sizes, int n_in, void* d_out, int out_size, void* d_ws, size_t ws_size, hipStream_t stream) {
    (void)in_sizes; (void)n_in; (void)out_size; (void)ws_size;
    (void)hipFuncSetAttribute((const void*)gm::gm_kernel, hipFuncAttributeMaxDynamicSharedMemorySize, gm::LDS_BYTES);
    hipLaunchKernelGGL(gm::gm_kernel, dim3(256), dim3(512), gm::LDS_BYTES, stream, (const u16*)d_in[0], (const u16*)d_in[1], (const u16*)d_in[2], (const u16*)d_in[3],
                       (const u16*)d_in[4], (const u16*)d_in[5], (const u16*)d_in[6], (const u16*)d_in[8], (const u16*)d_in[9], (const u16*)d_in[10],
                       (const u16*)d_in[11], (const u16*)d_in[12], (u16*)d_out, (unsigned char*)d_ws);
}
```

```cpp
#include <hip/hip_runtime.h>
#include <stdint.h>

#define LAS __attribute__((address_space(3)))
typedef unsigned short u16;
typedef unsigned long long u64;
typedef short bf16x8 __attribute__((ext_vector_type(8)));
typedef short s16x4 __attribute__((ext_vector_type(4)));
typedef _Float16 f16x8 __attribute__((ext_vector_type(8)));
typedef _Float16 f16x2 __attribute__((ext_vector_type(2)));
typedef __bf16 bf16x2 __attribute__((ext_vector_type(2)));
typedef float f32x4 __attribute__((ext_vector_type(4)));
typedef float f32x2 __attribute__((ext_vector_type(2)));
typedef unsigned u32x4 __attribute__((ext_vector_type(4)));
typedef unsigned u32x2 __attribute__((ext_vector_type(2)));
typedef __amdgpu_buffer_rsrc_t rsrc_t;

namespace gm {
constexpr int SEQ = 2048, DM = 576, FH = 2304;
constexpr unsigned WS_E = 0, WS_FZ = 4096, WS_FH = WS_FZ + 65536, WS_FA = WS_FH + 131072;
constexpr unsigned WS_ST = 0x60000, WS_GATE = 0xE0000, WS_Z2F = 0x100000, WS_HF = 0xA00000, WS_WOF = 0x1300000, WS_UT = 0x1600000, WS_AT = 0x1900000, WS_END = 0x1C00000;
constexpr int STG = 34816, BOFF = 16384, Z0 = 3 * STG, ZST = 304, T0 = Z0 + 128 * ZST;
constexpr int KT_SZ = 4352, UT_SZ = 4608, Y_SZ = 10240;
constexpr int L_MISC = 163840 - 320;
constexpr int L_KT = L_MISC - 3 * KT_SZ, L_SP = T0, L_KD = Z0 + 3 * 8448, L_UT = Z0 + 3 * Y_SZ;
constexpr int L_AT = Z0;
constexpr int SPIN_G = 8192, SPIN_L = 1 << 20;
constexpr int LDS_BYTES = 163840;
static_assert(L_UT + 3 * UT_SZ <= L_KT && L_KD + 3 * 3072 <= T0 && L_SP + 2048 <= L_KT, "lds map");
constexpr int CODE_LINES = 11, CODE_BYTES = 40960;

__device__ __forceinline__ float bf2f(unsigned v) { return __uint_as_float(v << 16); }
__device__ __forceinline__ float bflo(unsigned w) { return __uint_as_float(w << 16); }
__device__ __forceinline__ float bfhi(unsigned w) { return __uint_as_float(w & 0xffff0000u); }
__device__ __forceinline__ unsigned pk_bf16(float a, float b) { f32x2 v = {a, b}; return __builtin_bit_cast(unsigned, __builtin_convertvector(v, bf16x2)); }
__device__ __forceinline__ unsigned pk_f16(float a, float b) { f32x2 v = {a, b}; return __builtin_bit_cast(unsigned, __builtin_convertvector(v, f16x2)); }
__device__ __forceinline__ float rbf(float a) { return bflo(pk_bf16(a, a)); }
__device__ __forceinline__ f32x2 gelu2(f32x2 x) {
    const f32x2 w = (x * x) * (-0.10294324f) + (-2.30220819f);
    const f32x2 a = w * x;
    f32x2 e; e.x = __builtin_amdgcn_exp2f(a.x); e.y = __builtin_amdgcn_exp2f(a.y);
    e = e + 1.0f;
    f32x2 r; r.x = __builtin_amdgcn_rcpf(e.x); r.y = __builtin_amdgcn_rcpf(e.y);
    return x * r;
}
__device__ __forceinline__ f32x4 gelu4(f32x4 v) { const f32x2 a = gelu2((f32x2){v[0], v[1]}), b = gelu2((f32x2){v[2], v[3]}); return (f32x4){a.x, a.y, b.x, b.y}; }
__device__ __forceinline__ float allred_q(float v) {
    unsigned u = __float_as_uint(v);
    auto r = __builtin_amdgcn_permlane16_swap(u, u, false, false);
    v = __uint_as_float(r[0]) + __uint_as_float(r[1]);
    u = __float_as_uint(v);
    auto s = __builtin_amdgcn_permlane32_swap(u, u, false, false);
    return __uint_as_float(s[0]) + __uint_as_float(s[1]);
}
#define BAR() do { asm volatile("" ::: "memory"); __builtin_amdgcn_s_barrier(); asm volatile("" ::: "memory"); } while (0)
#define WAITV(n) asm volatile("s_waitcnt vmcnt(" #n ")" ::: "memory")
#define WAITL0() asm volatile("s_waitcnt lgkmcnt(0)" ::: "memory")
#define CFENCE() asm volatile("" ::: "memory")
#define MFMA16(a, b, c) __builtin_amdgcn_mfma_f32_16x16x32_bf16(a, b, c, 0, 0, 0)
__device__ __forceinline__ s16x4 tr_read(const LAS unsigned char* p) { return __builtin_amdgcn_ds_read_tr16_b64_v4i16((LAS s16x4*)p); }
__device__ __forceinline__ bf16x8 cat8(s16x4 a, s16x4 b) { return __builtin_shufflevector(a, b, 0, 1, 2, 3, 4, 5, 6, 7); }
template <class P> __device__ __forceinline__ P opq(P p) { asm volatile("" : "+v"(p)); return p; }
__device__ __forceinline__ int nextbuf(int so) { return so == 2 * STG ? 0 : so + STG; }

__device__ __forceinline__ u32x2 flag_ld(rsrc_t ws, unsigned off) { return __builtin_amdgcn_raw_buffer_load_b64(ws, off, 0, 16); }
__device__ __forceinline__ bool flag_all(u32x2 v, u32x2 T, bool active) { return __builtin_amdgcn_ballot_w64(active && (v[0] != T[0] || v[1] != T[1])) == 0ull; }
__device__ __forceinline__ void flag_publish(rsrc_t ws, unsigned off, u32x2 T, int lane) {
    WAITV(0);
    if (lane == 0) __builtin_amdgcn_raw_buffer_store_b64(T, ws, off, 0, 16);
}
#define SB0() __builtin_amdgcn_sched_barrier(0)
struct Frag { bf16x8 a[4]; s16x4 lo[5], hi[5]; };
struct Ptrs { const LAS unsigned char *a0l, *a0h, *a1l, *a1h, *b, *b9; };
template <bool TR, int HALF>
__device__ __forceinline__ void frag_read(Frag& f, int i, const Ptrs& p) {
    { constexpr int B0 = TR ? 2 : 1; i = i == 0 ? 0 : i <= B0 ? i + 3 : i <= B0 + 3 ? i - B0 : i; }
    if (i < 4) f.a[i] = *(const LAS bf16x8*)((i < 2 ? (HALF ? p.a1l : p.a0l) : (HALF ? p.a1h : p.a0h)) + (i & 1) * 2048);
    else if (TR) { const int n = (i - 4) >> 1; const LAS unsigned char* q = (n < 4 ? p.b + n * 32 : p.b9) + HALF * 9216; if ((i - 4) & 1) f.hi[n] = tr_read(q + 4608); else f.lo[n] = tr_read(q); }
    else { const int n = i - 4; const bf16x8 v = *(const LAS bf16x8*)((n < 4 ? p.b + n * 1024 : p.b9) + HALF * 9216);
           f.lo[n] = __builtin_shufflevector(v, v, 0, 1, 2, 3); f.hi[n] = __builtin_shufflevector(v, v, 4, 5, 6, 7); }
}
template <bool TR, int LHALF, int S0, int BAT, bool FIRST>
__device__ __forceinline__ void half_step(f32x4 (&acc)[18], const Frag& c, Frag& l, Ptrs& p, int delta, const float* ini, const volatile LAS unsigned* rdy, unsigned& rv) {
    constexpr int NRD = TR ? 14 : 9, NS = 18 - S0;
#pragma unroll
    for (int i = 0; i < 18; ++i) {
        const int n = i >> 2, m = i & 3;
        acc[i] = MFMA16(cat8(c.lo[n], c.hi[n]), c.a[m], FIRST ? (TR ? ((f32x4){0.f, 0.f, 0.f, 0.f}) : ((f32x4){ini[m], ini[m], ini[m], ini[m]})) : acc[i]);
        SB0();
        if (i == BAT) { WAITL0(); BAR(); if (TR) rv = *rdy; }
        if (LHALF >= 0 && i >= S0) {
#pragma unroll
            for (int r = 0; r < NRD; ++r) if ((r * NS) / NRD == i - S0) frag_read<TR, (LHALF > 0 ? 1 : 0)>(l, r, p);
        }
#define UPD(q) do { q += delta; asm volatile("" : "+v"(q)); } while (0)
        if (LHALF == 1) { if (i == 0) UPD(p.a0l); if (i == 1) UPD(p.a0h); if (i == 12) UPD(p.a1l); if (i == 14) UPD(p.a1h); if (i == 17) { UPD(p.b); UPD(p.b9); } }
#undef UPD
        SB0();
    }
}
template <bool TR, class HK>
__device__ __forceinline__ void gemm_phase(int nsteps, f32x4 (&acc)[18], int& so, LAS unsigned char* lds, int arow, int wn, int boff, int b9off, int lane,
                                           const float* ini, const volatile LAS unsigned* rdy, HK hook) {
    constexpr int NRD = TR ? 14 : 9;
    const int fr = lane & 15, fq = lane >> 4, sw = (fr >> 1) & 7;
    const int a0 = so + (arow + fr) * 128 + ((fq ^ sw) << 4), a1 = so + (arow + fr) * 128 + (((4 + fq) ^ sw) << 4);
    Ptrs p = {opq(lds + a0 + wn * 4096), opq(lds + a0 + (wn ^ 1) * 4096), opq(lds + a1 + wn * 4096), opq(lds + a1 + (wn ^ 1) * 4096), opq(lds + so + boff), opq(lds + so + b9off)};
    Frag f0, f1;
    unsigned rv = 0;
    __builtin_amdgcn_s_waitcnt(0xC07F);
    BAR();
#pragma unroll
    for (int i = 0; i < NRD; ++i) frag_read<TR, 0>(f0, i, p);
    SB0();
    int delta = so == 2 * STG ? -2 * STG : STG; so += delta;
    half_step<TR, 1, 2, -1, true>(acc, f0, f1, p, delta, ini, rdy, rv);
#pragma nounroll
    for (int s = 1; s < nsteps; ++s) {
        delta = so == 2 * STG ? -2 * STG : STG; so += delta;
        half_step<TR, 0, 5, 4, false>(acc, f1, f0, p, 0, ini, rdy, rv);
        half_step<TR, 1, 2, -1, false>(acc, f0, f1, p, delta, ini, rdy, rv);
        if (TR) hook(rv);
    }
    if (TR) half_step<TR, -1, 0, -1, false>(acc, f1, f0, p, 0, ini, rdy, rv);
    else {
#pragma unroll
        for (int m = 0; m <= 4; ++m) {
#pragma unroll
            for (int n = 0; n < 5; ++n) if (m < 4 && (n < 4 || m < 2)) acc[n * 4 + m] = MFMA16(cat8(f1.lo[n], f1.hi[n]), f1.a[m], acc[n * 4 + m]);
            SB0();
            if (m > 0) hook(m - 1);
            SB0();
        }
    }
}

template <class LT>
__device__ __forceinline__ void consumer_a(LAS unsigned char* lds, u32x2 (&zp)[18], LT late, int wm, int wn, int lane) {
    const int fr = lane & 15, fq = lane >> 4;
    f32x4 acc[18];
    int so = 0;
    const int bw = BOFF + (4 * fq + (fr >> 2)) * 288 + (lane & 3) * 8;
    const volatile LAS unsigned* rdy = (const volatile LAS unsigned*)(lds + L_MISC + 4 + wm * 4);
    bool done = false;
    auto normalise = [&]() {
        f32x2 srs[4];
#pragma unroll
        for (int mh = 0; mh < 2; ++mh) { const LAS unsigned char* ps = opq(lds + L_SP + (wm * 64 + ((mh ^ wn) << 5) + fr) * 16); srs[2 * mh] = *(const LAS f32x2*)ps; srs[2 * mh + 1] = *(const LAS f32x2*)(ps + 256); }
        CFENCE();
#pragma unroll
        for (int mh = 0; mh < 2; ++mh) {
            const int row = wm * 64 + ((mh ^ wn) << 5) + fr;
            LAS unsigned char* pz = opq(lds + Z0 + row * ZST + wn * 160 + fq * 16);
            LAS unsigned char* pz9 = opq(lds + Z0 + row * ZST + 128 + fq * 8);
#pragma unroll
            for (int mm = 0; mm < 2; ++mm) {
                const f32x2 sr = srs[2 * mh + mm];
                u32x2 o[5];
#pragma unroll
                for (int n = 0; n < (mh == 0 ? 5 : 4); ++n) {
                    const u32x2 z = zp[n * 4 + mh * 2 + mm];
                    const f32x2 u = (f32x2){bflo(z[0]), bfhi(z[0])} * sr.x + sr.y, v = (f32x2){bflo(z[1]), bfhi(z[1])} * sr.x + sr.y;
                    o[n] = (u32x2){pk_bf16(u.x, u.y), pk_bf16(v.x, v.y)};
                }
#pragma unroll
                for (int j = 0; j < 2; ++j) *(LAS u32x4*)(pz + mm * 16 * ZST + j * 64) = (u32x4){o[2 * j][0], o[2 * j][1], o[2 * j + 1][0], o[2 * j + 1][1]};
                if (mh == 0) *(LAS u32x2*)(pz9 + mm * 16 * ZST) = o[4];
            }
        }
        CFENCE();
        if (lane == 0) *(volatile LAS unsigned*)(lds + L_MISC + 16 + (wm * 2 + wn) * 4) = 1u;
        done = true;
    };
    auto hook = [&](unsigned rv) { if (__builtin_amdgcn_readfirstlane(rv) != 0u && !done) normalise(); };
#pragma unroll
    for (int i = 0; i < 18; ++i) zp[i] = (u32x2){0u, 0u};
#pragma nounroll
    for (int it = 0; it < 2; ++it) {
        gemm_phase<true>(9, acc, so, lds, wm * 64, wn, bw + wn * 160, bw + 128, lane, nullptr, rdy, hook);
        if (it == 1) late();
        if (it == 1 && !done) {
            int t0 = 0;
            while (__builtin_amdgcn_readfirstlane(*rdy) == 0u && ++t0 < SPIN_L) __builtin_amdgcn_s_sleep(1);
            normalise();
        }
#pragma unroll
        for (int i = 0; i < 18; ++i) { acc[i] = gelu4(acc[i]); zp[i] = (u32x2){pk_bf16(acc[i][0], acc[i][1]), pk_bf16(acc[i][2], acc[i][3])}; }
        if (it == 0) {
#pragma unroll
            for (int m = 0; m < 4; ++m) {
                const int nt = m < 2 ? 5 : 4;
                f32x4 s4 = acc[m];
#pragma unroll
                for (int n = 1; n < nt; ++n) s4 += acc[n * 4 + m];
                const float mean = opq(allred_q((s4[0] + s4[1]) + (s4[2] + s4[3])) * (1.0f / (16 * nt)));
                f32x4 q4 = {0.f, 0.f, 0.f, 0.f};
#pragma unroll
                for (int n = 0; n < nt; ++n) { const f32x4 d = acc[n * 4 + m] - mean; q4 += d * d; }
                const float m2 = allred_q((q4[0] + q4[1]) + (q4[2] + q4[3]));
                if (fq == 0) *(LAS f32x2*)(lds + L_SP + (wm * 64 + ((m ^ (2 * wn)) << 4) + fr) * 16 + (m >> 1) * 8) = (f32x2){mean, m2};
            }
        }
    }
}

__device__ __forceinline__ void consumer_b(LAS unsigned char* lds, rsrc_t ws, u32x2 T, const u32x2 (&z1)[18], const float (&bs_)[4], int pm, int pn, int wm, int wn, int lane) {
    float bs[4] = {bf2f(__float_as_uint(opq(bs_[0]))), bf2f(__float_as_uint(opq(bs_[1]))), bf2f(__float_as_uint(opq(bs_[2]))), bf2f(__float_as_uint(opq(bs_[3])))};
    f32x4 acc[18];
    int so = 0;
    const int qm = 2 * pm + wm;
    const unsigned hb = WS_HF + (unsigned)qm * (18u * 16384u);
    auto epi = [&](int m) {
        if (m == 0) __builtin_amdgcn_s_setprio(3);
        const unsigned mo = (unsigned)(m ^ (2 * wn)) * 4096u;
        u32x2 hp[5];
#pragma unroll
        for (int n = 0; n < (m < 2 ? 5 : 4); ++n) {
            const u32x2 z = z1[n * 4 + m]; const f32x4 g = acc[n * 4 + m];
            const f32x2 u = (f32x2){bflo(z[0]), bfhi(z[0])} * (f32x2){g[0], g[1]}, v = (f32x2){bflo(z[1]), bfhi(z[1])} * (f32x2){g[2], g[3]};
            hp[n] = (u32x2){pk_bf16(u.x, u.y), pk_bf16(v.x, v.y)};
        }
#pragma unroll
        for (int j = 0; j < 2; ++j)
            __builtin_amdgcn_raw_buffer_store_b128((u32x4){hp[2 * j][0], hp[2 * j][1], hp[2 * j + 1][0], hp[2 * j + 1][1]}, ws,
                                                   lane * 16 + j * 1024, hb + (unsigned)pn * 16384u + mo + (unsigned)wn * 2048u, 16);
        if (m < 2) __builtin_amdgcn_raw_buffer_store_b64(hp[4], ws, lane * 8, hb + (unsigned)(16 + (pn >> 3)) * 16384u + mo + (unsigned)(pn & 7) * 512u, 16);
    };
    gemm_phase<false>(32, acc, so, lds, wm * 64, wn, BOFF + wn * 5120 + lane * 16, BOFF + 4096 + lane * 16, lane, bs, nullptr, epi);
    flag_publish(ws, WS_FH + (unsigned)(qm * 32 + pn * 2 + wn) * 128u, T, lane);
    __builtin_amdgcn_s_setprio(0);
}

template <int TLO, int THI>
__device__ __forceinline__ void conv_task(LAS unsigned char* lds, int kt, int ut, int yt, int tl, int lane) {
    constexpr int NTL = THI - TLO + 1, ND = THI + 2;
    const int i = lane & 15, g = lane >> 4;
    const LAS unsigned char* pk = opq(lds + kt + (i & 3) * 1088 + ((i >> 2) + g) * 16);
    const LAS unsigned char* pu = opq(lds + ut + (128 + 16 * tl - 32 * g - 4 * (i >> 2) + 4 * (i & 3)) * 2 - 32);
    bf16x8 ub[ND], kf[ND];
#pragma unroll
    for (int d = 0; d < ND; ++d) { ub[ND - 1 - d] = cat8(tr_read(pu + 256 * (ND - 1 - d) + 32), tr_read(pu + 256 * (ND - 1 - d))); kf[d] = *(const LAS bf16x8*)(pk + 64 * d); }
    f32x4 acc[NTL];
#pragma unroll
    for (int s = -1; s <= THI; ++s) {
#pragma unroll
        for (int t = TLO; t <= THI; ++t) if (t >= s) {
            acc[t - TLO] = MFMA16(kf[s + 1], ub[t - s], s == -1 ? ((f32x4){0.f, 0.f, 0.f, 0.f}) : acc[t - TLO]);
        }
    }
    LAS float* y = opq((LAS float*)(lds + yt) + 32 * g + 16 * tl + i);
    LAS float* cy = opq((LAS float*)(lds + yt + 8192) + (2 * g + tl) * 4 + (i & 3));
#define DPPF(v, ctl) __int_as_float(__builtin_amdgcn_update_dpp(0, __float_as_int(v), ctl, 0xf, 0xf, true))
#pragma unroll
    for (int t = TLO; t <= THI; ++t) {
        const f32x4 a = acc[t - TLO];
        y[128 * t] = (a[0] + DPPF(a[1], 0x111)) + (DPPF(a[2], 0x112) + DPPF(a[3], 0x113));
        const float cv = DPPF(a[1], 0x10f) + (DPPF(a[2], 0x10e) + DPPF(a[3], 0x10d));
        if (i < 4) cy[32 * t] = cv;
    }
#undef DPPF
}
__device__ __forceinline__ int ytab(int c) { return Z0 + c * Y_SZ; }

__global__ __launch_bounds__(512, 2) void gm_kernel(const u16* __restrict__ x, const u16* __restrict__ logA, const u16* __restrict__ Aim, const u16* __restrict__ Cre,
                                                    const u16* __restrict__ Cim, const u16* __restrict__ logdt, const u16* __restrict__ Dsk, const u16* __restrict__ win,
                                                    const u16* __restrict__ wsp, const u16* __restrict__ bsp, const u16* __restrict__ wout, const u16* __restrict__ gw,
                                                    u16* __restrict__ out, unsigned char* wsp_) {
    extern __shared__ __attribute__((aligned(16))) unsigned char lds_[];
    LAS unsigned char* lds = (LAS unsigned char*)lds_;
    const int wid = __builtin_amdgcn_readfirstlane(threadIdx.x >> 6), lane = threadIdx.x & 63;
    const int bid = blockIdx.x, xg = bid & 7, rr = bid >> 3, bi = rr >> 2, bj = rr & 3;
    const int pm = (xg >> 2) * 8 + bi, pn = (xg & 3) * 4 + bj;
    const int qm = 2 * pm + (pn >> 3), qn = pn & 7;
    const int nch = pm < 4 ? 3 : 2;
    const rsrc_t ws = __builtin_amdgcn_make_buffer_rsrc(wsp_, 0, WS_END, 0x00020000);
    unsigned pfw = 0;
    if (wid == 6) {
        u64 pc0; asm volatile("s_getpc_b64 %0" : "=s"(pc0));
        const u64 q_ = (pc0 & ~127ull) + (unsigned)min((rr * CODE_LINES + (lane >> 1)) * 128 + (lane & 1) * 64, CODE_BYTES);
        if (lane < 2 * CODE_LINES) asm volatile("global_load_dword %0, %1, off" : "+v"(pfw) : "v"(q_) : "memory");
    }

    const int fr = lane & 15, fq = lane >> 4;
    const int h = wid - 6, p = wid - 4;
    const bool is_prod = (wid >> 1) == 2;
    const int ch0 = pn * 36 + pm;
    float bs[4];
    auto late = [&]() {
#pragma unroll
        for (int m = 0; m < 4; ++m) bs[m] = __uint_as_float((unsigned)bsp[pm * 128 + (wid & 1) * 64 + ((m ^ (wid & 2)) << 4) + fr]);
    };
    const int phi = (-(bi + bj)) & 3;
    const unsigned rsw = (unsigned)(((lane & 7) ^ ((4 * p + (lane >> 4)) & 7)) * 16);
#define GLDS(src, dst, aux) __builtin_amdgcn_global_load_lds((const unsigned*)(src), (LAS unsigned*)(dst), 16, 0, aux)
    auto issueS = [&](int k, int s) {
        const char* sb = (const char*)wsp + (size_t)(pm * 128 + p * 8) * 4096 + k * 128; const unsigned vs = (unsigned)(lane >> 3) * 4096u + rsw;
#pragma unroll
        for (int i = 0; i < 8; ++i) GLDS(sb + (size_t)i * (16 * 4096) + vs, lds + s + (p + 2 * i) * 1024, 0); };
    auto ord2 = [&](int s) { return ((s & 3) == phi) ? s : ((s - 4) & 31); };
    u32x2 E, T;
    auto mk_tag = [&]() { u64 z = (((u64)__builtin_amdgcn_readfirstlane(E[1]) << 32) | __builtin_amdgcn_readfirstlane(E[0])) + 0x9E3779B97F4A7C15ull;
        z = (z ^ (z >> 30)) * 0xBF58476D1CE4E5B9ull; z = (z ^ (z >> 27)) * 0x94D049BB133111EBull; z ^= z >> 31; T = (u32x2){(unsigned)z, (unsigned)(z >> 32)}; };
    u32x2 zp[18];
#define STEP(lim) do { if (nb < (lim)) { BAR(); ++nb; } } while (0)
#define STEPS_TO(lim) _Pragma("nounroll") while (nb < (lim)) { BAR(); ++nb; }
    int nb = 0;
    const int kc = wid == 7 ? 2 : wid == 1 || wid == 2 ? wid - 1 : 3;
    if (kc < nch) {
        const int kch = ch0 + 16 * kc, ki = kch * 64 + lane;
        const unsigned pA = ((const unsigned*)logA)[ki], pI = ((const unsigned*)Aim)[ki], pR = ((const unsigned*)Cre)[ki], pC = ((const unsigned*)Cim)[ki], pT = logdt[kch];
        LAS u32x2* tw = opq((LAS u32x2*)(lds + Z0 + kc * 8448) + lane);
        const LAS unsigned char* trd = opq(lds + Z0 + kc * 8448 + fr * 528 + fq * 16);
        const float dt = rbf(__expf(bf2f(pT)));
        const f32x2 ar = (f32x2){rbf(__expf(bflo(pA))), rbf(__expf(bfhi(pA)))} * dt * -1.44269504f, ai = (f32x2){bflo(pI), bfhi(pI)} * dt * 0.15915494f;
        const f32x2 cr = (f32x2){bflo(pR), bfhi(pR)} * dt, ci = (f32x2){bflo(pC), bfhi(pC)} * dt;
        float cm = fmaxf(fmaxf(fabsf(cr.x), fabsf(ci.x)), fmaxf(fabsf(cr.y), fabsf(ci.y)));
#define DPPF(v, ctl) __int_as_float(__builtin_amdgcn_update_dpp(0, __float_as_int(v), ctl, 0xf, 0xf, true))
        cm = fmaxf(cm, DPPF(cm, 0x121)); cm = fmaxf(cm, DPPF(cm, 0x122)); cm = fmaxf(cm, DPPF(cm, 0x124)); cm = fmaxf(cm, DPPF(cm, 0x128));
#undef DPPF
        { auto r = __builtin_amdgcn_permlane16_swap(__float_as_uint(cm), __float_as_uint(cm), false, false); cm = fmaxf(__uint_as_float(r[0]), __uint_as_float(r[1]));
          auto q = __builtin_amdgcn_permlane32_swap(__float_as_uint(cm), __float_as_uint(cm), false, false); cm = fmaxf(fmaxf(__uint_as_float(q[0]), __uint_as_float(q[1])), 1e-30f); }
        struct C2 { f32x2 x, y; };
        auto cexp = [&](float p_) { const f32x2 e = ar * p_, t = ai * p_;
            const f32x2 mg = {__builtin_amdgcn_exp2f(e.x), __builtin_amdgcn_exp2f(e.y)}; const float t0 = __builtin_amdgcn_fractf(t.x), t1 = __builtin_amdgcn_fractf(t.y);
            return C2{mg * (f32x2){__builtin_amdgcn_cosf(t0), __builtin_amdgcn_cosf(t1)}, mg * (f32x2){__builtin_amdgcn_sinf(t0), __builtin_amdgcn_sinf(t1)}}; };
        auto cmul = [](C2 a, C2 b) { return C2{__builtin_elementwise_fma(a.x, b.x, -(a.y * b.y)), __builtin_elementwise_fma(a.x, b.y, a.y * b.x)}; };
        auto put = [&](int row, C2 v, bool conj) { tw[row * 66] = conj ? (u32x2){pk_f16(v.x.x, -v.y.x), pk_f16(v.x.y, -v.y.y)} : (u32x2){pk_f16(v.x.x, v.y.x), pk_f16(v.x.y, v.y.y)}; };
        C2 wp[16], zr[8], vp[4], cv[17];
        wp[1] = cexp(8.f); zr[1] = cexp(1.f); vp[0] = cexp(128.f);
        tw[0] = (u32x2){pk_f16(1.f, 0.f), pk_f16(1.f, 0.f)};
#pragma unroll
        for (int j = 1; j < 16; ++j) { if (j > 1) wp[j] = cmul(wp[j >> 1], wp[j - (j >> 1)]); put(j, wp[j], true); }
        CFENCE();
        u32x4 fb[8];
#pragma unroll
        for (int ks = 0; ks < 8; ++ks) fb[ks] = *(const LAS u32x4*)(trd + ks * 64);
        CFENCE();
#pragma unroll
        for (int r = 2; r < 8; ++r) zr[r] = cmul(zr[r >> 1], zr[r - (r >> 1)]);
#pragma unroll
        for (int k = 1; k < 4; ++k) vp[k] = cmul(vp[k - 1], vp[k - 1]);
        const float rc = __builtin_amdgcn_rcpf(cm);
        cv[0] = C2{cr * rc, ci * rc};
#pragma unroll
        for (int k = 1; k < 8; ++k) { const int lg = k >= 4 ? 2 : k >= 2 ? 1 : 0; cv[k] = cmul(cv[k - (1 << lg)], vp[lg]); }
        LAS float* kb_ = (LAS float*)(lds + L_KD + kc * 3072);
        const float sc = 2.0f * cm;
        u32x4 fa[3][8];
#pragma unroll
        for (int tt = 0; tt < 3; ++tt) {
#pragma unroll
            for (int i = 0; i < (tt == 2 ? 9 : 16); ++i) {
                const int r = tt == 0 ? (i & 7) : tt == 1 ? (i < 8 ? 2 * (i & 3) : 4 * (i & 1)) : 0;
                const int hh = tt == 0 ? (i >> 3) : tt == 1 ? (i < 8 ? 2 + (i >> 2) : 4 + ((i - 8) >> 1)) : 8 + i;
                if (tt == 2) cv[hh] = cmul(cv[hh - 8], vp[3]);
                put(i, r == 0 ? cv[hh] : cmul(cv[hh], zr[r]), false);
            }
            CFENCE();
#pragma unroll
            for (int ks = 0; ks < 8; ++ks) fa[tt][ks] = *(const LAS u32x4*)(trd + ks * 64);
            CFENCE();
            SB0();
        }
        f32x4 kd[3] = {{0.f, 0.f, 0.f, 0.f}, {0.f, 0.f, 0.f, 0.f}, {0.f, 0.f, 0.f, 0.f}};
#pragma unroll
        for (int ks = 0; ks < 8; ++ks)
#pragma unroll
            for (int tt = 0; tt < 3; ++tt) kd[tt] = __builtin_amdgcn_mfma_f32_16x16x32_f16(__builtin_bit_cast(f16x8, fa[tt][ks]), __builtin_bit_cast(f16x8, fb[ks]), kd[tt], 0, 0, 0);
#pragma unroll
        for (int tt = 0; tt < 3; ++tt) {
            LAS float* kw = opq(kb_ + (tt == 0 ? 128 * (fq >> 1) + 4 * (fq & 1) + 8 * fr : tt == 1 ? 256 + 64 * fq + (fq < 2 ? 4 : 2) * fr : 512 + 64 * fq + fr));
            LAS float* kwb = tt == 1 ? opq(kw + (fq < 2 ? 2 : 32)) : kw + (tt == 0 ? 2 : 32);
            kw[0] = kd[tt][0] * sc; kw[tt == 2 ? 16 : 1] = kd[tt][1] * sc; kwb[0] = kd[tt][2] * sc; kwb[tt == 2 ? 16 : 1] = kd[tt][3] * sc;
        }
        WAITL0();
    }
    if (wid == 6) { WAITV(0); asm volatile("" :: "v"(pfw)); }
    if (is_prod) {
        const unsigned vx = (unsigned)(lane >> 3) * 1152u + rsw;
        unsigned wv[9];
        const char* xb = (const char*)x + (size_t)(pm * 128 + p * 8) * 1152;
        const char* wb = (const char*)win + pn * 288;
        auto issueX = [&](int k, int s) {
#pragma unroll
            for (int i = 0; i < 8; ++i) GLDS(xb + (size_t)i * (16 * 1152) + k * 128 + vx, lds + s + (p + 2 * i) * 1024, 0); };
        auto issueW = [&](int k, int coloff, int s) {
            const char* b = wb + (size_t)k * (64 * 9216) + coloff;
#pragma unroll
            for (int i = 0; i < 9; ++i) GLDS(b + wv[i], lds + s + BOFF + (p + 2 * i) * 1024, 0); };
        auto ord1 = [&](int s) { return ((s & 3) == phi) ? s : (s >= 4 ? s - 4 : (s == 0 ? 8 : s + 4)); };
        issueX(ord1(0), 0);
        SB0();
#pragma unroll
        for (int i = 0; i < 9; ++i) { const int c = (p + 2 * i) * 64 + lane, rho = c / 18, ch = c - rho * 18;
            const int kap = (rho & 32) + 8 * ((rho >> 2) & 3) + 4 * ((rho >> 4) & 1) + (rho & 3); wv[i] = (unsigned)kap * 9216u + (unsigned)ch * 16u; }
        issueW(ord1(0), FH * 2, 0); issueX(ord1(1), STG); issueW(ord1(1), FH * 2, STG);
        issueX(ord1(2), 2 * STG); issueW(ord1(2), FH * 2, 2 * STG);
        E = flag_ld(ws, WS_E);
        unsigned wm_ = 0;
        auto touch = [&](const void* base, unsigned off, unsigned last) { const char* q_ = (const char*)base + min(off + lane * 64u, last); asm volatile("global_load_dword %0, %1, off" : "+v"(wm_) : "v"(q_) : "memory"); };
        {
#pragma unroll
            for (int i = 0; i < 2; ++i) touch(x, bid * 9216u + p * 4608u + i * 4096u, bid * 9216u + p * 4608u + 4544u);
#pragma unroll
            for (int i = 0; i < 3; ++i) touch(win, bid * 20736u + p * 10368u + i * 4096u, bid * 20736u + p * 10368u + 10304u);
        }
        int so = 0;
        WAITV(35); BAR();
#pragma nounroll
        for (int S = 1; S < 16; ++S) {
            if (S >= 8 && S <= 10) WAITV(9); else WAITV(17);
            BAR();
            const int t = S + 2, k = ord1(t < 9 ? t : t < 12 ? t - 3 : t - 12);
            if (t < 9 || t >= 12) issueX(k, so);
            issueW(k, t >= 9 ? 0 : FH * 2, so); so = nextbuf(so);
            if (S == 8) {
#pragma unroll
                for (int i = 0; i < 4; ++i) touch(wsp, bid * 32768u + p * 16384u + i * 4096u, 0x7fffffffu);
            }
        }
        WAITV(17); BAR(); issueS(2 * pm, 0);
        WAITV(8); BAR(); issueS(2 * pm + 1, STG);
        asm volatile("" :: "v"(wm_));
    } else if (wid < 4) {
        E = flag_ld(ws, WS_E);
        consumer_a(lds, zp, late, wid & 1, wid >> 1, lane);
    } else {
        if (h == 0 && lane < 12) *(LAS unsigned*)(lds + L_MISC + lane * 4) = 0u;
        const int row0 = lane / 9, chk0 = lane - row0 * 9;
        const int u = (pn >> 3) * 16 + pm, nblk = u < 8 ? 3 : 2;
        u32x2 uc[18]; u32x4 xv[9], gv[9], wv[3][5];
        if (h == 0) {
            { const char* xb = (const char*)x + (size_t)pm * 128 * 1152 + pn * 72; unsigned off = row0 * 1152 + chk0 * 8; int ch = chk0;
#pragma unroll
              for (int i = 0; i < 9; ++i) { uc[i] = *(const u32x2*)(xb + off); uc[9 + i] = *(const u32x2*)(xb + 64 * 1152 + off); off += 7 * 1152 + 8; if (++ch == 9) { ch = 0; off += 1152 - 72; } } }
            { const char* xr = (const char*)x + (size_t)(pm * 128 + pn * 8 + (lane >> 3)) * 1152 + (lane & 7) * 16; const char* gr = (const char*)gw + (lane & 7) * 16;
#pragma unroll
              for (int i = 0; i < 9; ++i) { xv[i] = *(const u32x4*)(xr + i * 128); gv[i] = *(const u32x4*)(gr + i * 128); } }
        } else {
            unsigned wo_[5], hi_[5];
            { int row = row0, ch = chk0;
#pragma unroll
              for (int i = 0; i < 5; ++i) { const int rw = row < 32 ? row : 31; wo_[i] = (rw & 15) * 1152 + ch * 16; hi_[i] = rw >> 4; row += 7; if (++ch == 9) { ch = 0; ++row; } } }
#pragma unroll
            for (int b = 0; b < 3; ++b) {
                const int blk = (b < nblk) ? u + 32 * b : u, st = blk >> 2, jb = blk & 3;
                const int fA = st < 16 ? st * 144 + (jb < 2 ? 32 * jb : 16 + 32 * jb) : ((st - 16) * 8 + 2 * jb) * 144 + 64, dB = (st < 16 ? 16 : 144) * 1152;
                const char* wb_ = (const char*)wout + (size_t)fA * 1152 + qn * 144;
#pragma unroll
                for (int i = 0; i < 5; ++i) wv[b][i] = *(const u32x4*)(wb_ + (wo_[i] + hi_[i] * dB));
            }
        }
        E = flag_ld(ws, WS_E);
        if (lane < 24) *(LAS u32x4*)(lds + L_KT + (lane >> 3) * KT_SZ + (2 * h + ((lane >> 2) & 1)) * 1088 + (lane & 3) * 16) = (u32x4){0u, 0u, 0u, 0u};
        WAITL0();
        __builtin_amdgcn_s_setprio(1);
        {
            float kv[2][3];
#pragma nounroll
            for (int ei = 0; ei < 9; ++ei) {
                BAR();
                if (ei > 0) {
                    LAS unsigned char* kt = opq(lds + L_KT + (2 * h + ((ei - 1) >> 2)) * 1088 + 64 + ((ei - 1) & 3) * 256 + lane * 2);
#pragma unroll
                    for (int j = 0; j < 2; ++j)
#pragma unroll
                        for (int c = 0; c < 3; ++c) *(LAS u16*)(kt + c * KT_SZ + j * 128) = (u16)pk_bf16(kv[j][c], 0.f);
                }
#pragma unroll
                for (int j = 0; j < 2; ++j) {
                    const int jj = (ei & 3) * 2 + j, e = jj >= 4 ? 3 : jj >= 2 ? 2 : jj, lag = 256 * jj + 4 * lane + 2 * h + ((ei >> 2) & 1);
                    const int idx = e == 0 ? lag : 128 * e + ((lag + (1 << (e - 1)) - 1 + ((lag >> e) & 1)) >> e);
                    const LAS float* q = opq((const LAS float*)(lds + L_KD) + idx);
#pragma unroll
                    for (int c = 0; c < 3; ++c) kv[j][c] = q[c * 768];
                }
            }
        }
        __builtin_amdgcn_s_setprio(0);
        if (h == 0) {
            LAS unsigned char* pzs = opq(lds + Z0 + lane * 8);
#pragma unroll
            for (int i = 0; i < 18; ++i) *(LAS u32x2*)(pzs + i * 512) = uc[i];
            CFENCE();
            { const LAS unsigned char* pt = opq(lds + Z0 + (8 * (4 * fq + (lane & 3)) + (fr >> 2)) * 72);
              const unsigned ub = WS_UT + (unsigned)(pn * 36 + (lane & 3)) * 4096u + (unsigned)(pm * 128 + 8 * (4 * fq + (fr >> 2))) * 2u;
#pragma unroll
              for (int cc = 0; cc < 9; ++cc) __builtin_amdgcn_raw_buffer_store_b128(__builtin_bit_cast(u32x4, cat8(tr_read(pt + cc * 8), tr_read(pt + 288 + cc * 8))), ws, ub + cc * 16384u, 0, 16); }
            float a = 0.f;
#pragma unroll
            for (int i = 0; i < 9; ++i)
#pragma unroll
                for (int j = 0; j < 4; ++j) { const unsigned xa = xv[i][j], ga = gv[i][j]; a = __builtin_amdgcn_fdot2_f32_bf16(__builtin_bit_cast(bf16x2, xa), __builtin_bit_cast(bf16x2, ga), a, false); }
#define DPPF(v, ctl) __int_as_float(__builtin_amdgcn_update_dpp(0, __float_as_int(v), ctl, 0xf, 0xf, true))
            a += DPPF(a, 0xb1); a += DPPF(a, 0x4e); a += DPPF(a, 0x141);
#undef DPPF
            const float gt = rbf(__builtin_amdgcn_rcpf(1.0f + __expf(-rbf(a))));
            if ((lane & 7) == 0) __builtin_amdgcn_raw_buffer_store_b32(__float_as_uint(gt), ws, WS_GATE + (unsigned)(pm * 128 + pn * 8 + (lane >> 3)) * 4u, 0, 16);
        } else {
            LAS unsigned char* pws = opq(lds + Z0 + 9216 + lane * 16);
            const LAS unsigned char* ptw = opq(lds + Z0 + 9216 + (4 * fq + (fr >> 2)) * 144 + (lane & 3) * 8);
#pragma unroll
            for (int b = 0; b < 3; ++b) {
                if (b < nblk) {
#pragma unroll
                    for (int i = 0; i < 5; ++i) if (i < 4 || lane < 32) *(LAS u32x4*)(pws + i * 1024) = wv[b][i];
                    CFENCE();
                    const unsigned ob = WS_WOF + (unsigned)qn * (18u * 18432u) + (unsigned)(u + 32 * b) * 4608u;
#pragma unroll
                    for (int nn = 0; nn < 5; ++nn) {
                        const u32x4 v = __builtin_bit_cast(u32x4, cat8(tr_read(ptw + nn * 32), tr_read(ptw + 2304 + nn * 32)));
                        if (nn < 4) __builtin_amdgcn_raw_buffer_store_b128(v, ws, ob + nn * 1024 + lane * 16, 0, 16);
                        else if (fr < 8) __builtin_amdgcn_raw_buffer_store_b128(v, ws, ob + 4096 + (fq * 8 + fr) * 16, 0, 16);
                    }
                    CFENCE();
                }
            }
        }
        nb = 9;
        mk_tag();
        WAITL0();
        STEPS_TO(10);
        __builtin_amdgcn_s_setprio(1);
        {
            const f32x4 sp = *(const LAS f32x4*)(lds + L_SP + (h * 64 + lane) * 16);
            const float dm = sp[0] - sp[2];
            const f32x2 mine = {(80.f * sp[0] + 64.f * sp[2]) * (1.0f / 144.f), sp[1] + sp[3] + (80.f * 64.f / 144.f) * dm * dm};
            __builtin_amdgcn_raw_buffer_store_b128((u32x4){T[0], __float_as_uint(mine.x), T[1], __float_as_uint(mine.y)}, ws, WS_ST + (unsigned)((pm * 16 + pn) * 128 + h * 64 + lane) * 16u, 0, 16);
            STEP(18);
            const unsigned so_ = WS_ST + (unsigned)(pm * 16 * 128 + h * 64 + lane) * 16u;
            int t0 = 0;
            u32x4 st[16];
            for (;;) {
                CFENCE();
#pragma unroll
                for (int j = 0; j < 16; ++j) st[j] = __builtin_amdgcn_raw_buffer_load_b128(ws, so_ + j * 2048u, 0, 16);
                STEP(18);
                unsigned bad = 0;
#pragma unroll
                for (int j = 0; j < 16; ++j) bad |= (st[j][0] ^ T[0]) | (st[j][2] ^ T[1]);
                if (__builtin_amdgcn_ballot_w64(bad != 0u) == 0ull) break;
                if (++t0 > SPIN_G) break;
            }
            STEP(18);
            float mean = 0.f, m2 = 0.f;
#pragma unroll
            for (int j = 0; j < 16; ++j) mean += __uint_as_float(st[j][1]);
            mean *= (1.0f / 16.f);
#pragma unroll
            for (int j = 0; j < 16; ++j) { const float d = __uint_as_float(st[j][1]) - mean; m2 += __uint_as_float(st[j][3]) + 144.f * d * d; }
            const float rstd = __builtin_amdgcn_rsqf(m2 * (1.0f / FH) + 1e-5f);
            *(LAS f32x2*)(lds + L_SP + (h * 64 + lane) * 16) = (f32x2){rstd, -mean * rstd};
            CFENCE();
            if (lane == 0) *(volatile LAS unsigned*)(lds + L_MISC + 4 + h * 4) = 1u;
            const volatile LAS unsigned* zd = (const volatile LAS unsigned*)(lds + L_MISC + 16 + h * 8 + (lane & 1) * 4);
            for (;;) {
                STEP(18);
                if (__builtin_amdgcn_ballot_w64(*zd == 0u) == 0ull) break;
                if (++t0 > SPIN_L) break;
            }
            CFENCE();
            const LAS unsigned char* ptz = opq(lds + Z0 + (h * 64 + 8 * fq + (fr >> 2)) * ZST + (lane & 3) * 16);
            const LAS unsigned char* ptz9 = opq(lds + Z0 + (h * 64 + 8 * fq + (fr >> 2)) * ZST + 128 + (lane & 3) * 8);
            const unsigned zo = WS_Z2F + (unsigned)((pn * 64 + pm * 4 + h * 2) * 9) * 1024u + lane * 16;
            u32x4 zf[18];
#pragma unroll
            for (int i = 0; i < 18; ++i) {
                const int n = i % 9, n2 = n < 4 ? n : n - 5;
                const LAS unsigned char* pt = (n == 4 ? ptz9 : ptz + (n > 4 ? 160 : 0) + (n2 >> 1) * 64 + (n2 & 1) * 8) + (i / 9) * 32 * ZST;
                zf[i] = __builtin_bit_cast(u32x4, cat8(tr_read(pt), tr_read(pt + 4 * ZST)));
                __builtin_amdgcn_raw_buffer_store_b128(zf[i], ws, zo + i * 1024, 0, 16);
                if (i == 5 || i == 11) STEP(18);
            }
            STEP(18);
            flag_publish(ws, WS_FZ + (unsigned)(pn * 32 + pm * 2 + h) * 128u, T, lane);
            STEPS_TO(18);
            LAS unsigned char* pr_ = opq(lds + h * STG + BOFF + lane * 16);
#pragma unroll
            for (int i = 0; i < 18; ++i) *(LAS u32x4*)(pr_ + i * 1024) = zf[i];
            WAITL0();
        }
    }

    float dks[3];
    const char* lin = (const char*)wsp_ + p * 1024 + lane * 16;
    auto issueLA = [&](unsigned off, int s) {
#pragma unroll
        for (int i = 0; i < 8; ++i) GLDS(lin + off + i * 2048, lds + s + (p + 2 * i) * 1024, 16); };
    auto issueLB = [&](unsigned off, int s) {
#pragma unroll
        for (int i = 0; i < 9; ++i) GLDS(lin + off + i * 2048, lds + s + BOFF + (p + 2 * i) * 1024, 16); };
    auto ord3 = [&](int s) { return (s >= 16 || (s & 3) == phi) ? s : ((s - 4) & 15); };
    u32x2 pv = {0u, 0u}; unsigned rdy = 0; bool pend = false;
    const char* fp = (const char*)wsp_ + WS_FZ + ((unsigned)pn * 32u + (lane & 31)) * 128u;
    auto poll_issue = [&]() { asm volatile("global_load_dwordx2 %0, %1, off sc1" : "+v"(pv) : "v"(fp) : "memory"); };
    auto poll_take = [&]() { asm volatile("" : "+v"(pv)); rdy |= (unsigned)__builtin_amdgcn_ballot_w64(pv[0] == T[0] && pv[1] == T[1]); };
    auto need = [&](unsigned bits) {
        if (pend) { poll_take(); pend = false; }
        if ((rdy & bits) != bits) {
            int t0 = 0;
            do { poll_issue(); WAITV(0); poll_take(); } while ((rdy & bits) != bits && ++t0 < SPIN_G);
        }
        if (rdy != ~0u) { poll_issue(); pend = true; }
    };
    const unsigned hb = WS_HF + (unsigned)qm * (18u * 16384u), ob = WS_WOF + (unsigned)qn * (18u * 18432u);
    if (is_prod) {
        mk_tag();
        const unsigned zb = WS_Z2F + (unsigned)pn * (64u * 9216u);
        int so = 2 * STG;
        const int r0 = (ord2(0) >> 1) == pm ? ord2(1) : ord2(0), r1 = (ord2(1) >> 1) == pm ? ord2(0) : ord2(1);
        poll_issue(); WAITV(0); poll_take();
        bool second = false;
#pragma nounroll
        for (int S = 18; S < 48; ++S) {
            BAR();
            int k = ord2(S - 16); if ((k >> 1) == pm) k = (k & 1) ? r1 : r0;
            if (!second && !pend && rdy == ~0u) { second = true; rdy = 0; fp = (const char*)wsp_ + WS_FZ + ((unsigned)(pn ^ 8) * 32u + (lane & 31)) * 128u; }
            need(second ? 0u : 1u << k); issueS(k, so); issueLB(zb + (unsigned)k * 18432u, so); so = nextbuf(so);
            WAITV(17);
        }
        BAR();
        if (!second) { WAITV(0); need(~0u); rdy = 0; fp = (const char*)wsp_ + WS_FZ + ((unsigned)(pn ^ 8) * 32u + (lane & 31)) * 128u; }
        need(~0u);
        issueLB(ob + (unsigned)ord3(0) * 18432u, 2 * STG);
        WAITV(9); BAR();
        issueLB(ob + (unsigned)ord3(1) * 18432u, 0);
    } else
    if (wid < 4) {
        mk_tag();
        consumer_b(lds, ws, T, zp, bs, pm, pn, wid & 1, wid >> 1, lane);
    } else {
        __builtin_amdgcn_s_setprio(1);
        {   const unsigned fo = WS_FZ + (unsigned)(pn * 32 + (lane & 31)) * 128u;
            int t0 = 0;
            for (;;) {
                STEP(43);
                CFENCE();
                const u32x2 v = flag_ld(ws, fo);
                STEP(43);
                if (flag_all(v, T, true)) break;
                if (++t0 > SPIN_G) break;
            } }
#pragma unroll
        for (int c = 0; c < 3; ++c) {
            const char* us = (const char*)wsp_ + WS_UT + (unsigned)(ch0 + (c < nch ? 16 * c : 0)) * 4096u + h * 2048 + lane * 16;
#pragma unroll
            for (int j = 0; j < 2; ++j) GLDS(us + j * 1024, lds + L_UT + c * UT_SZ + 256 + h * 2048 + j * 1024, 16);
        }
#pragma unroll
        for (int c = 0; c < 3; ++c) dks[c] = __uint_as_float((unsigned)Dsk[ch0 + (c < nch ? 16 * c : 0)]);
        STEPS_TO(47);
        WAITV(0);
#pragma unroll
        for (int c = 0; c < 3; ++c) dks[c] = opq(bf2f(__float_as_uint(dks[c])));
        if (lane < 48) *(LAS u32x4*)(lds + L_UT + (lane >> 4) * UT_SZ + h * 4352 + (lane & 15) * 16) = (u32x4){0u, 0u, 0u, 0u};
        WAITL0();
        STEPS_TO(50);
    }

    while (wid != 5) {
        __builtin_amdgcn_wave_barrier();
        unsigned tk = 0;
        if (lane == 0) tk = __hip_atomic_fetch_add((LAS unsigned*)(lds + L_MISC), 1u, __ATOMIC_RELAXED, __HIP_MEMORY_SCOPE_WORKGROUP);
        __builtin_amdgcn_wave_barrier();
        tk = __builtin_amdgcn_readfirstlane(tk);
        if (tk >= (unsigned)(4 * nch)) break;
        const int c = tk >> 2, tl = tk & 1;
        if (tk & 2) conv_task<0, 10>(lds, L_KT + c * KT_SZ, L_UT + c * UT_SZ, ytab(c), tl, lane);
        else conv_task<11, 15>(lds, L_KT + c * KT_SZ, L_UT + c * UT_SZ, ytab(c), tl, lane);
    }
    WAITL0();
    if (is_prod) {
        if (p == 1) {
            fp = (const char*)wsp_ + WS_FH + ((unsigned)qm * 32u + (lane & 31)) * 128u; rdy = 0;
            need(~0u);
#pragma unroll
            for (int i = 0; i < 16; ++i) GLDS(lin - 1024 + hb + (unsigned)ord3(0) * 16384u + i * 1024, lds + 2 * STG + i * 1024, 16);
#pragma unroll
            for (int i = 0; i < 16; ++i) GLDS(lin - 1024 + hb + (unsigned)ord3(1) * 16384u + i * 1024, lds + i * 1024, 16);
            WAITV(16);
        } else WAITV(0);
        int so = STG;
#pragma nounroll
        for (int S = 50; S < 66; ++S) {
            BAR();
            const int k = ord3(S - 48); issueLA(hb + (unsigned)k * 16384u, so); issueLB(ob + (unsigned)k * 18432u, so); so = nextbuf(so);
            WAITV(17);
        }
        BAR();
        WAITV(0); BAR();
        if (bid == 0 && p == 0 && lane == 0) __builtin_amdgcn_raw_buffer_store_b64(T, ws, WS_E, 0, 16);
        return;
    }


    if (wid < 4) {
        f32x4 acc[5];
        struct F3 { s16x4 alo[4], ahi[4]; bf16x8 b[20]; } f0, f1;
        const LAS unsigned char* ph = lds + 2 * STG + wid * 4096 + lane * 16;
        const LAS unsigned char* pw = lds + 2 * STG + BOFF + lane * 16;
        const LAS unsigned char* pw4 = lds + 2 * STG + BOFF + 4096 + (fq * 8 + (fr & 7)) * 16;
        auto rd3 = [&](F3& f, int i, bool single) {
            const int j = i / 6, r = i % 6;
            if (r == 0) {
                if (!single) { const bf16x8 v = *(const LAS bf16x8*)(ph + j * 1024); f.alo[j] = __builtin_shufflevector(v, v, 0, 1, 2, 3); f.ahi[j] = __builtin_shufflevector(v, v, 4, 5, 6, 7); }
                else { f.alo[j] = *(const LAS s16x4*)(ph + j * 1024); f.ahi[j] = *(const LAS s16x4*)(ph + j * 1024 + 512); }
            } else if (r < 5) f.b[j * 5 + r - 1] = *(const LAS bf16x8*)(pw + j * 4608 + (r - 1) * 1024);
            else f.b[j * 5 + 4] = *(const LAS bf16x8*)(pw4 + j * 4608);
        };
        int so = 2 * STG;
        auto step3 = [&](const F3& c, F3& l, int mode  , bool first) {
            const int delta = so == 2 * STG ? -2 * STG : STG; so += delta;
#pragma unroll
            for (int i = 0; i < 20; ++i) {
                const int j = i / 5, n = i % 5;
                acc[n] = MFMA16(c.b[i], cat8(c.alo[j], c.ahi[j]), (first && j == 0) ? ((f32x4){0.f, 0.f, 0.f, 0.f}) : acc[n]);
                if (mode < 2 && i < 12) { rd3(l, 2 * i, mode == 1); rd3(l, 2 * i + 1, mode == 1); }
                if (mode < 2) { if (i == 13) { ph += delta; asm volatile("" : "+v"(ph)); } if (i == 14) { pw += delta; asm volatile("" : "+v"(pw)); } if (i == 15) { pw4 += delta; asm volatile("" : "+v"(pw4)); } }
                SB0();
            }
        };
        __builtin_amdgcn_s_waitcnt(0xC07F);
        BAR();
#pragma unroll
        for (int i = 0; i < 24; ++i) rd3(f0, i, false);
        ph -= 2 * STG; pw -= 2 * STG; pw4 -= 2 * STG; so = 0;
        WAITL0(); BAR(); step3(f0, f1, 0, true);
#pragma nounroll
        for (int j = 0; j < 7; ++j) {
            WAITL0(); BAR(); step3(f1, f0, 0, false);
            WAITL0(); BAR(); step3(f0, f1, 0, false);
        }
        ph = opq(ph - lane * 8);
        WAITL0(); BAR(); step3(f1, f0, 1, false);
        WAITL0(); BAR();
        const float g = *(const LAS float*)(lds + L_MISC + 64 + (16 * wid + fr) * 4);
        const LAS unsigned char* pat = opq(lds + L_AT + (4 * fq + (fr >> 2)) * 128 + (16 * wid + 4 * (lane & 3)) * 2);
        u32x2 av[5];
#pragma unroll
        for (int n = 0; n < 5; ++n) av[n] = __builtin_bit_cast(u32x2, tr_read(pat + n * 2048));
        step3(f0, f1, 1, false);
        step3(f1, f0, 2, false);
        const rsrc_t ors = __builtin_amdgcn_make_buffer_rsrc(out, 0, SEQ * DM * 2, 0x00020000);
        const float g1 = rbf(1.0f - g);
#pragma unroll
        for (int n = 0; n < 5; ++n) {
            const f32x4 b = acc[n]; const u32x2 a = av[n];
            const u32x2 o = {pk_bf16(g * bflo(a[0]) + g1 * b[0], g * bfhi(a[0]) + g1 * b[1]), pk_bf16(g * bflo(a[1]) + g1 * b[2], g * bfhi(a[1]) + g1 * b[3])};
            if (n < 4 || fq < 2) __builtin_amdgcn_raw_buffer_store_b64(o, ors, (unsigned)((qm * 64 + 16 * wid + fr) * 1152 + (qn * 72 + 16 * n + 4 * fq) * 2), 0, 0);
        }
    } else {
        int d = pn * 36 + pm;
#pragma nounroll
        for (int c = 0; c < nch; ++c) {
            const float dsk = c == 0 ? dks[0] : c == 1 ? dks[1] : dks[2];
#pragma unroll
            for (int k = 0; k < 2; ++k) {
                BAR(); ++nb;
                const int l0 = h * 1024 + k * 512 + lane * 8;
                f32x4 cv = *(const LAS f32x4*)(lds + ytab(c) + 8192 + (l0 >= 16 ? (l0 >> 4) - 1 : 0) * 16);
                if ((lane & 1) || l0 == 0) cv = (f32x4){0.f, 0.f, 0.f, 0.f};
                const f32x4 y0 = *(const LAS f32x4*)(lds + ytab(c) + l0 * 4) + cv, y1 = *(const LAS f32x4*)(lds + ytab(c) + l0 * 4 + 16);
                const u32x4 uv = *(const LAS u32x4*)(lds + L_UT + c * UT_SZ + 256 + l0 * 2);
                u32x4 o;
#pragma unroll
                for (int j = 0; j < 4; ++j) {
                    const float ya = j < 2 ? y0[2 * j] : y1[2 * j - 4], yb = j < 2 ? y0[2 * j + 1] : y1[2 * j - 3];
                    const unsigned yp = pk_bf16(ya, yb), up = pk_bf16(bflo(uv[j]) * dsk, bfhi(uv[j]) * dsk);
                    o[j] = pk_bf16(bflo(yp) + bflo(up), bfhi(yp) + bfhi(up));
                }
                __builtin_amdgcn_raw_buffer_store_b128(o, ws, WS_AT + (unsigned)d * 4096u + l0 * 2, 0, 16);
            }
            d += 16;
        }
        STEPS_TO(58);
        flag_publish(ws, WS_FA + (unsigned)((pn >> 1) * 64 + (pn & 1) * 32 + pm * 2 + h) * 128u, T, lane);
        STEPS_TO(61);
        if (h == 0) {
            const unsigned fo = WS_FA + (unsigned)(qn * 64 + lane) * 128u;
            int t0 = 0;
            for (;;) {
                CFENCE();
                const u32x2 v = flag_ld(ws, fo);
                STEP(67); STEP(67);
                if (flag_all(v, T, true)) break;
                if (++t0 > SPIN_G) break;
            }
            const char* as = (const char*)wsp_ + WS_AT + (unsigned)(qn * 72 + (lane >> 3)) * 4096u + qm * 128 + (lane & 7) * 16;
#pragma unroll
            for (int i = 0; i < 9; ++i) GLDS(as + i * 32768, lds + L_AT + i * 1024, 16);
        } else __builtin_amdgcn_global_load_lds((const unsigned*)((const char*)wsp_ + WS_GATE + (unsigned)(qm * 64 + lane) * 4u), (LAS unsigned*)(lds + L_MISC + 64), 4, 0, 16);
        STEPS_TO(67);
        WAITV(0);
        BAR();
    }
}
}

extern "C" void kernel_launch(void* const* d_in, const int* in_sizes, int n_in, void* d_out, int out_size, void* d_ws, size_t ws_size, hipStream_t stream) {
    (void)in_sizes; (void)n_in; (void)out_size; (void)ws_size;
    (void)hipFuncSetAttribute((const void*)gm::gm_kernel, hipFuncAttributeMaxDynamicSharedMemorySize, gm::LDS_BYTES);
    hipLaunchKernelGGL(gm::gm_kernel, dim3(256), dim3(512), gm::LDS_BYTES, stream, (const u16*)d_in[0], (const u16*)d_in[1], (const u16*)d_in[2], (const u16*)d_in[3],
                       (const u16*)d_in[4], (const u16*)d_in[5], (const u16*)d_in[6], (const u16*)d_in[8], (const u16*)d_in[9], (const u16*)d_in[10],
                       (const u16*)d_in[11], (const u16*)d_in[12], (u16*)d_out, (unsigned char*)d_ws);
}
```
